```python
import torch

def _softmax(x, axis=-1):
    return torch.softmax(x, dim=axis)
def _gelu(x):
    return torch.nn.functional.gelu(x, approximate="tanh")
def _silu(x):
    return torch.nn.functional.silu(x)
def _sigmoid(x):
    return torch.sigmoid(x)
def _layernorm(x, axis=-1, eps=1e-5):
    m = x.mean(dim=axis, keepdim=True)
    v = x.var(dim=axis, keepdim=True, unbiased=False)
    return (x - m) / torch.sqrt(v + eps)
def _scatter_add(tgt, idx, src, axis=0):
    return tgt.clone().index_add_(axis, idx, src)
def _segment_sum(src, seg, n):
    out = torch.zeros((n,) + src.shape[1:], dtype=src.dtype)
    return out.index_add_(0, seg, src)
def _pad(x, pw):
    flat = tuple(v for pair in reversed(pw) for v in pair)
    return torch.nn.functional.pad(x, flat)

kernel_name = 'recurrent_scan__sliding_block_cached__dense_activation_gelu__prompt'

def setup_inputs():

    _w = (lambda a: (lambda t: t.to(torch.bfloat16) if t.is_floating_point() else t)(torch.as_tensor(a)))
    def _A_setup():
        B, Sq, D = 1, 2048, 448
        Hq, Hkv, Sk, Dh = 16, 8, 2048, 256
        assert Hq % Hkv == 0, (Hq, Hkv)
        return {
            "x": _w((lambda *s: torch.randn(*s, dtype=torch.bfloat16) * 0.05)(B, Sq, D)),
            "k_cache": _w((lambda *s: torch.randn(*s, dtype=torch.bfloat16) * 3.2)(B, Hkv, Sk, Dh)),
            "v_cache": _w((lambda *s: torch.randn(*s, dtype=torch.bfloat16) * 3.2)(B, Hkv, Sk, Dh)),
            "wq": _w((lambda *s: torch.randn(*s, dtype=torch.bfloat16) * 3.2)(D, Hq * Dh) * (D ** -0.5)),
            "wo": _w((lambda *s: torch.randn(*s, dtype=torch.bfloat16) * 3.2)(Hq * Dh, D) * ((Hq * Dh) ** -0.5)),
        }
    _A_in = _A_setup()

    def _B_setup():
        B, S, D, F = 1, 2048, 448, 1792
        return {
            "x": _w((lambda *s: torch.randn(*s, dtype=torch.bfloat16) * 0.05)(B, S, D)),
            "w1": _w((lambda *s: torch.randn(*s, dtype=torch.bfloat16) * 3.2)(D, F) * (D ** -0.5)),
            "b1": _w((lambda *s: torch.randn(*s, dtype=torch.bfloat16) * 3.2)(F) * 0.01),
            "w2": _w((lambda *s: torch.randn(*s, dtype=torch.bfloat16) * 0.4)(F, D) * (F ** -0.5)),
            "b2": _w((lambda *s: torch.randn(*s, dtype=torch.bfloat16) * 0.4)(D) * 0.01),
        }
    _B_in = _B_setup()

    return {
        "A_x": _A_in["x"],
        "A_k_cache": _A_in["k_cache"],
        "A_v_cache": _A_in["v_cache"],
        "A_wq": _A_in["wq"],
        "A_wo": _A_in["wo"],
        "B_x": _B_in["x"],
        "B_w1": _B_in["w1"],
        "B_b1": _B_in["b1"],
        "B_w2": _B_in["w2"],
        "B_b2": _B_in["b2"],
    }

def reference(A_x, A_k_cache, A_v_cache, A_wq, A_wo, B_x, B_w1, B_b1, B_w2, B_b2):
    def _A_ref(x, k_cache, v_cache, wq, wo):
        B, Sq, D = x.shape
        Hq, Dh = 16, 256
        Hkv, Sk = k_cache.shape[1], k_cache.shape[2]
        W = min(4096, max(256, Sk // 4))
        rep = Hq // Hkv
        q = (lambda x, *d: x.permute(*d))((x @ wq).reshape(B, Sq, Hq, Dh), 0, 2, 1, 3)
        k = (lambda x, n, axis: torch.repeat_interleave(x, n, dim=axis))(k_cache[:, :, Sk - W:, :], rep, axis=1)
        v = (lambda x, n, axis: torch.repeat_interleave(x, n, dim=axis))(v_cache[:, :, Sk - W:, :], rep, axis=1)
        scores = torch.einsum("bhqd,bhkd->bhqk", q, k) * (Dh ** -0.5)
        p = _softmax(scores, axis=-1)
        o = torch.einsum("bhqk,bhkd->bhqd", p, v)
        return (lambda x, *d: x.permute(*d))(o, 0, 2, 1, 3).reshape(B, Sq, Hq * Dh) @ wo

    def _B_ref(x, w1, b1, w2, b2):
        h = torch.einsum("bsd,df->bsf", x, w1) + b1
        h = _gelu(h)
        return torch.einsum("bsf,fd->bsd", h, w2) + b2

    x = A_x
    xn = _layernorm(x, axis=-1)
    s = _A_ref(xn, A_k_cache, A_v_cache, A_wq, A_wo)
    g = _silu(_B_ref(xn, B_w1, B_b1, B_w2, B_b2))
    return x + s * g
```

```cpp
#include <hip/hip_runtime.h>
#include <hip/hip_bf16.h>
#include <stdint.h>
#define MIMIC 0
typedef unsigned short u16;
__device__ __forceinline__ float bf2f(u16 v){ return __uint_as_float(((unsigned)v)<<16); }
__device__ __forceinline__ u16 f2bf(float f){ __hip_bfloat16 b = __float2bfloat16(f); return *(u16*)&b; }
__device__ __forceinline__ float rb(float f){ return bf2f(f2bf(f)); }
__device__ __forceinline__ float rm(float f){
  return f;
}
constexpr int S=2048, D=448, HQ=16, HKV=8, SK=2048, DH=256, W=512, F=1792;

__global__ void k_ln(const u16* x, float* xn){
  int r = blockIdx.x; int t = threadIdx.x;
  float v[7]; float s=0;
  for(int i=0;i<7;i++){ v[i]=bf2f(x[r*D+t+64*i]); s+=v[i]; }
  for(int o=32;o;o>>=1) s+=__shfl_xor(s,o);
  float mean=s/D;
  float q=0; for(int i=0;i<7;i++){ float d=v[i]-mean; q+=d*d; }
  for(int o=32;o;o>>=1) q+=__shfl_xor(q,o);
  float var=q/D;
  float m=rm(mean), vv=rm(var);
  float den=rm(sqrtf(rm(vv+1e-5f)));
  for(int i=0;i<7;i++){ float d=rm(v[i]-m); xn[r*D+t+64*i]=rb(d/den); }
}
template<int MODE>
__global__ void k_gemm(const float* A, const u16* B, const u16* bias, float* C, int M,int N,int K){
  __shared__ float As[32][33], Bs[32][33];
  int tx=threadIdx.x, ty=threadIdx.y; int row=blockIdx.y*32+ty, col=blockIdx.x*32+tx;
  float acc=0;
  for(int k0=0;k0<K;k0+=32){
    As[ty][tx]=A[row*K+k0+tx]; Bs[ty][tx]=bf2f(B[(k0+ty)*N+col]);
    __syncthreads();
    for(int k=0;k<32;k++) acc+=As[ty][k]*Bs[k][tx];
    __syncthreads();
  }
  if(MODE==0) C[row*N+col]=rb(acc);
  if(MODE==1){ float h=rm(rm(acc)+bf2f(bias[col])); float u=0.7978845608028654f*(h+0.044715f*h*h*h); C[row*N+col]=rb(0.5f*h*(1.f+tanhf(u))); }
  if(MODE==2){ float f=rm(rm(acc)+bf2f(bias[col])); C[row*N+col]=rm(f/(1.f+expf(-f))); }
  if(MODE==3) C[row*N+col]=rm(acc);
}
__global__ void k_attn(const float* q, const u16* kc, const u16* vc, float* o){
  __shared__ float qs[DH]; __shared__ float p[W]; __shared__ float red[4];
  int r=blockIdx.x, h=blockIdx.y, t=threadIdx.x; int kh=h/2;
  qs[t]=q[r*(HQ*DH)+h*DH+t]; __syncthreads();
  float sc[2];
  for(int i=0;i<2;i++){ int key=t+256*i; const u16* kp=kc+((size_t)kh*SK+(SK-W)+key)*DH; float a=0; for(int d=0;d<DH;d++) a+=qs[d]*bf2f(kp[d]); sc[i]=rm(a)*0.0625f; }
  float m=fmaxf(sc[0],sc[1]); for(int of=32;of;of>>=1) m=fmaxf(m,__shfl_xor(m,of));
  if((t&63)==0) red[t>>6]=m; __syncthreads(); m=fmaxf(fmaxf(red[0],red[1]),fmaxf(red[2],red[3])); __syncthreads();
  float e0=expf(sc[0]-m), e1=expf(sc[1]-m); float s=e0+e1; for(int of=32;of;of>>=1) s+=__shfl_xor(s,of);
  if((t&63)==0) red[t>>6]=s; __syncthreads(); s=red[0]+red[1]+red[2]+red[3];
  p[t]=rb(e0); p[t+256]=rb(e1); float inv=1.f/s;
  __syncthreads();
  float a=0; const u16* vp=vc+((size_t)kh*SK+(SK-W))*DH+t; for(int k=0;k<W;k++) a+=p[k]*bf2f(vp[(size_t)k*DH]);
  o[r*(HQ*DH)+h*DH+t]=rb(a*inv);
}
__global__ void k_fin(const u16* x,const float* s,const float* g,u16* out){
  int i=blockIdx.x*256+threadIdx.x; out[i]=f2bf(bf2f(x[i])+rm(s[i]*g[i]));
}
extern "C" void kernel_launch(void* const* d_in, const int* in_sizes, int n_in, void* d_out, int out_size, void* d_ws, size_t ws_size, hipStream_t stream){
  const u16* x=(const u16*)d_in[0]; const u16* kc=(const u16*)d_in[1]; const u16* vc=(const u16*)d_in[2];
  const u16* wq=(const u16*)d_in[3]; const u16* wo=(const u16*)d_in[4];
  const u16* w1=(const u16*)d_in[6]; const u16* b1=(const u16*)d_in[7]; const u16* w2=(const u16*)d_in[8]; const u16* b2=(const u16*)d_in[9];
  float* ws=(float*)d_ws;
  float* xn=ws; float* q=xn+S*D; float* o=q+S*HQ*DH; float* hh=o+S*HQ*DH; float* s=hh+S*F; float* g=s+S*D;
  k_ln<<<S,64,0,stream>>>(x,xn);
  dim3 b(32,32);
  k_gemm<0><<<dim3(HQ*DH/32,S/32),b,0,stream>>>(xn,wq,nullptr,q,S,HQ*DH,D);
  k_gemm<1><<<dim3(F/32,S/32),b,0,stream>>>(xn,w1,b1,hh,S,F,D);
  k_attn<<<dim3(S,HQ),256,0,stream>>>(q,kc,vc,o);
  k_gemm<3><<<dim3(D/32,S/32),b,0,stream>>>(o,wo,nullptr,s,S,D,HQ*DH);
  k_gemm<2><<<dim3(D/32,S/32),b,0,stream>>>(hh,w2,b2,g,S,D,F);
  k_fin<<<S*D/256,256,0,stream>>>(x,s,g,(u16*)d_out);
}
```

```cpp
#include <hip/hip_runtime.h>
#define M_ "v_mfma_f32_16x16x32_bf16 "
#define T_ "ds_read_b64_tr_b16 "
#define R_ "ds_read_b128 "
#define W_ "s_waitcnt "
#define L_ "buffer_load_dwordx4 "
#define C_ "v_cvt_pk_bf16_f32 "
#define X_ "v_fma_mix_f32 "
#define E_ "v_exp_f32 "

extern "C" __global__ __launch_bounds__(512, 2) __attribute__((aligned(4096))) void fused_kernel(const void* x, const void* k, const void* v, const void* wq,
    const void* wo, const void* w1, const void* b1, const void* w2, const void* b2, void* out, void* ws) {
  __shared__ __attribute__((aligned(16))) char lds[163840];
  asm volatile(
"s_getpc_b64 s[64:65]\n"
"KPC:\n"
"s_load_dwordx16 s[8:23], %[ka], 0\n"
"s_load_dwordx4 s[24:27], %[ka], 64\n"
"s_load_dwordx2 s[28:29], %[ka], 80\n"
"v_lshrrev_b32 v1, 6, v0\n"
"v_and_b32 v2, 63, v0\n"
"v_readfirstlane_b32 s7, v1\n"
"s_and_b32 s30, %[bid], 3\n"
"s_lshl_b32 s30, s30, 1\n"
"s_bfe_u32 s4, %[bid], 0x10003\n"
"s_add_u32 s30, s30, s4\n"
"s_bfe_u32 s31, %[bid], 0x10002\n"
"s_lshl_b32 s31, s31, 4\n"
"s_lshr_b32 s32, %[bid], 4\n"
"s_add_u32 s31, s31, s32\n"
"s_mov_b32 s66, KEND-KPC\n"
"s_mov_b32 s67, 0x20000\n"
"s_lshl3_add_u32 s6, s32, s7\n"
"s_and_b32 s88, %[bid], 1\n"
"s_mul_i32 s4, s6, 5\n"
"v_add_u32 v60, s4, v2\n"
"v_lshlrev_b32 v60, 7, v60\n"
"s_mul_i32 s4, s88, VARB-VARA\n"
"v_mov_b32 v61, s4\n"
"v_cmp_le_u32 vcc, VARA-KPC, v60\n"
"s_nop 1\n"
"v_cndmask_b32 v61, 0, v61, vcc\n"
"v_add_u32 v60, v61, v60\n"
"s_bfm_b64 exec, 5, 0\n"
"buffer_load_dword v59, v60, s[64:67], 0 offen\n"
"s_mov_b64 exec, -1\n"
"s_mul_i32 s4, s6, 6\n"
"v_add_u32 v61, s4, v2\n"
"v_lshrrev_b32 v62, 3, v61\n"
"v_and_b32 v61, 7, v61\n"
"v_mul_u32_u24 v62, 8192, v62\n"
"v_lshl_add_u32 v61, v61, 7, v62\n"
"v_lshrrev_b32 v3, 3, v0\n"
"v_and_b32 v4, 7, v0\n"
"v_mul_u32_u24 v3, 896, v3\n"
"v_lshl_add_u32 v3, v4, 4, v3\n"
"s_and_b32 s33, s7, 3\n"
"s_and_b32 s34, s7, 1\n"
"s_bfe_u32 s35, s7, 0x10001\n"
"s_lshl2_add_u32 s32, s32, s33\n"
"s_mul_i32 s4, s31, 57344\n"
"s_lshl_b32 s5, s30, 10\n"
"s_sub_u32 s6, 3670528, s5\n"
W_"lgkmcnt(0)\n"
"s_sub_u32 s14, s14, 512\n"
"s_subb_u32 s15, s15, 0\n"
"s_add_u32 s36, s14, s5\n"
"s_addc_u32 s37, s15, 0\n"
"s_mov_b32 s38, s6\n"
"s_mov_b32 s39, 0x20000\n"
"s_bfm_b64 exec, 6, 0\n"
"buffer_load_dword v59, v61, s[36:39], 0 offen offset:512\n"
"s_mov_b64 exec, -1\n"
"s_add_u32 s68, s8, s4\n"
"s_addc_u32 s69, s9, 0\n"
"s_mov_b32 s70, 57344\n"
"s_mov_b32 s71, 0x20000\n"
L_"v[200:203],v3,s[68:71],0 offen\n"
L_"v[204:207],v3,s[68:71],0 offen offset:128\n"
L_"v[208:211],v3,s[68:71],0 offen offset:256\n"
L_"v[212:215],v3,s[68:71],0 offen offset:384\n"
L_"v[216:219],v3,s[68:71],0 offen offset:512\n"
L_"v[220:223],v3,s[68:71],0 offen offset:640\n"
L_"v[224:227],v3,s[68:71],0 offen offset:768\n"
"s_mul_i32 s84, s33, 2056\n"
"v_bfe_u32 v16, v2, 2, 2\n"
"s_lshl_b32 s4, s33, 3\n"
"v_add_u32 v16, s4, v16\n"
"v_lshrrev_b32 v17, 4, v2\n"
"v_and_b32 v18, 3, v2\n"
"v_lshlrev_b32 v18, 4, v18\n"
"v_lshl_add_u32 v17, v17, 6, v18\n"
"v_lshl_add_u32 v9, v16, 13, v17\n"
"v_add_u32 v8, 512, v9\n"
"s_lshl_b32 s5, s30, 20\n"
"s_add_u32 s5, s5, 786368\n"
"s_add_u32 s40, s10, s5\n"
"s_addc_u32 s41, s11, 0\n"
"s_mov_b32 s42, 262208\n"
"s_mov_b32 s43, 0x20000\n"
"s_mul_i32 s5, s31, 917504\n"
"s_add_u32 s5, s5, 65536\n"
"s_add_u32 s60, s28, s5\n"
"s_addc_u32 s61, s29, 0\n"
"s_mov_b32 s62, 917504\n"
"s_mov_b32 s63, 0x20000\n"
"s_lshl2_add_u32 s5, s34, s35\n"
"s_sub_u32 s5, s30, s5\n"
"s_sub_u32 s6, s5, 0\n"
"s_and_b32 s6, s6, 7\n"
"s_lshl_b32 s6, s6, 10\n"
"s_cselect_b32 s90, s6, 0x40000000\n"
"s_sub_u32 s6, s5, 2\n"
"s_and_b32 s6, s6, 7\n"
"s_lshl_b32 s6, s6, 10\n"
"s_cselect_b32 s91, s6, 0x40000000\n"
"s_lshl_b32 s5, s30, 20\n"
"s_add_u32 s5, s5, 786432\n"
"s_add_u32 s44, s12, s5\n"
"s_addc_u32 s45, s13, 0\n"
"s_mov_b32 s46, 262144\n"
"s_mov_b32 s47, 0x20000\n"
"s_mul_i32 s5, s30, 458752\n"
"s_add_u32 s48, s16, s5\n"
"s_addc_u32 s49, s17, 0\n"
"s_mov_b32 s50, 458752\n"
"s_mov_b32 s51, 0x20000\n"
"s_lshr_b32 s89, s30, 2\n"
"s_mul_i32 s89, s89, 896\n"
"s_and_b32 s5, s30, 3\n"
"s_mul_i32 s6, s5, 256\n"
"s_cmp_eq_u32 s5, 3\n"
"s_cselect_b32 s6, 704, s6\n"
"s_add_u32 s89, s89, s6\n"
"s_mul_i32 s5, s89, 896\n"
"s_mul_i32 s6, s88, 57344\n"
"s_sub_u32 s6, 229376, s6\n"
"s_add_u32 s56, s22, s5\n"
"s_addc_u32 s57, s23, 0\n"
"s_mov_b32 s58, s6\n"
"s_mov_b32 s59, 0x20000\n"
"s_lshl_b32 s89, s89, 1\n"
"s_sub_u32 s6, 1605632, s89\n"
"s_add_u32 s52, s18, s89\n"
"s_addc_u32 s53, s19, 0\n"
"s_mov_b32 s54, s6\n"
"s_mov_b32 s55, 0x20000\n"
"s_sub_u32 s6, 3584, s89\n"
"s_add_u32 s76, s20, s89\n"
"s_addc_u32 s77, s21, 0\n"
"s_mov_b32 s78, s6\n"
"s_mov_b32 s79, 0x20000\n"
"s_mul_i32 s85, s33, 2112\n"
"s_lshl_b32 s5, s31, 10\n"
"s_add_u32 s80, s28, s5\n"
"s_addc_u32 s81, s29, 0\n"
"s_mov_b32 s82, 33792\n"
"s_mov_b32 s83, 0x20000\n"
"s_cmp_ge_u32 s7, 4\n"
"s_cbranch_scc0 ASETUP\n"
"v_lshl_add_u32 v246, v16, 9, v17\n"
"v_add_u32 v247, 1024, v246\n"
"v_bfe_u32 v16, v2, 2, 3\n"
"v_add_u32 v19, s4, v16\n"
"v_lshrrev_b32 v17, 5, v2\n"
"v_lshl_add_u32 v20, v17, 6, v18\n"
"v_mul_u32_u24 v21, 896, v19\n"
"v_add_u32 v124, v21, v20\n"
"v_add_u32 v125, 27648, v124\n"
"v_mul_u32_u24 v21, 3584, v19\n"
"v_add_u32 v12, v21, v20\n"
"v_add_u32 v13, 113664, v12\n"
"v_xor_b32 v21, v2, v17\n"
"s_lshl_b32 s5, s33, 12\n"
"s_add_u32 s5, s5, 64\n"
"v_lshl_add_u32 v10, v21, 4, s5\n"
"v_subrev_u32 v11, 32, v10\n"
"v_and_b32 v19, 7, v2\n"
"v_lshlrev_b32 v242, 7, v19\n"
"s_lshl2_add_u32 s4, s34, s35\n"
"s_mul_i32 s4, s4, 114688\n"
"v_lshl_add_u32 v248, v2, 4, s4\n"
"v_lshrrev_b32 v16, 4, v2\n"
"v_xor_b32 v17, v2, v16\n"
"s_lshl_b32 s4, s34, 11\n"
"s_add_u32 s4, s4, 101376\n"
"v_lshl_add_u32 v6, v17, 4, s4\n"
"v_lshrrev_b32 v16, 4, v2\n"
"s_lshl_b32 s4, s35, 6\n"
"v_lshl_add_u32 v15, v16, 4, s4\n"
"v_lshrrev_b32 v16, 4, v2\n"
"v_and_b32 v17, 15, v2\n"
"v_mul_u32_u24 v16, 2056, v16\n"
"v_lshl_add_u32 v19, v17, 4, v16\n"
"v_mov_b32 v240, v2\n"
"s_lshl_b32 s4, s35, 9\n"
"v_add_u32 v1, s4, v19\n"
"v_add_u32 v2, 0x10000, v1\n"
"v_add_u32 v3, 0x20000, v1\n"
"s_branch LNORM\n"
"ASETUP:\n"
"v_lshrrev_b32 v16, 4, v2\n"
"v_xor_b32 v17, v2, v16\n"
"s_lshl_b32 s4, s34, 11\n"
"s_add_u32 s4, s4, 101376\n"
"v_lshl_add_u32 v4, v17, 4, s4\n"
"v_bfe_u32 v16, v2, 1, 3\n"
"v_mul_u32_u24 v16, 1056, v16\n"
"v_and_b32 v17, 1, v2\n"
"v_lshl_add_u32 v16, v17, 9, v16\n"
"v_lshrrev_b32 v18, 4, v2\n"
"v_xor_b32 v17, v17, v18\n"
"v_lshl_add_u32 v7, v17, 4, v16\n"
"v_lshrrev_b32 v16, 4, v2\n"
"v_and_b32 v17, 15, v2\n"
"v_mul_u32_u24 v16, 2056, v16\n"
"v_lshl_add_u32 v11, v17, 4, v16\n"
"s_mul_i32 s4, s32, 44\n"
"v_add_u32 v20, s4, v2\n"
"v_lshrrev_b32 v22, 3, v20\n"
"v_and_b32 v20, 7, v20\n"
"v_mul_u32_u24 v22, 8192, v22\n"
"v_lshl_add_u32 v20, v20, 7, v22\n"
"v_add_u32 v20, 786944, v20\n"
"s_mul_i32 s4, s32, 8\n"
"v_add_u32 v21, s4, v2\n"
"v_lshlrev_b32 v21, 7, v21\n"
"v_mov_b32 v63, v2\n"
"s_mul_i32 s4, s35, 1024\n"
"v_add_u32 v1, s4, v11\n"
"v_add_u32 v2, 0x10000, v1\n"
"v_add_u32 v3, 0x20000, v1\n"
"s_mov_b32 s94, 0x44b8aa3b\n"
"s_bfe_u32 s4, s32, 0x30002\n"
"s_min_u32 s4, s4, 5\n"
"s_lshl_b32 m0, s4, 2\n"
"v_lshlrev_b32 v24, 16, v63\n"
"s_movrels_b64 s[72:73], s[40:41]\n"
"s_movrels_b64 s[74:75], s[42:43]\n"
"s_cmp_eq_u32 s33, 0\n"
"s_cselect_b32 s74, s74, 0\n"
"LNORM:\n"
"s_mov_b64 exec, 0\n"
"s_lshl3_add_u32 s4, s88, s7\n"
"s_cmp_eq_u32 s4, 0\n"
"s_cbranch_scc1 PTH0\n"
"s_cmp_eq_u32 s4, 1\n"
"s_cbranch_scc1 PTH1\n"
"s_cmp_eq_u32 s4, 2\n"
"s_cbranch_scc1 PTH2\n"
"s_cmp_eq_u32 s4, 3\n"
"s_cbranch_scc1 PTH3\n"
"s_cmp_eq_u32 s4, 4\n"
"s_cbranch_scc1 PTH4\n"
"s_cmp_eq_u32 s4, 5\n"
"s_cbranch_scc1 PTH5\n"
"s_cmp_eq_u32 s4, 6\n"
"s_cbranch_scc1 PTH6\n"
"s_cmp_eq_u32 s4, 7\n"
"s_cbranch_scc1 PTH7\n"
"s_cmp_eq_u32 s4, 8\n"
"s_cbranch_scc1 PTL0\n"
"s_cmp_eq_u32 s4, 9\n"
"s_cbranch_scc1 PTL1\n"
"s_cmp_eq_u32 s4, 10\n"
"s_cbranch_scc1 PTL2\n"
"s_cmp_eq_u32 s4, 11\n"
"s_cbranch_scc1 PTL3\n"
"s_cmp_eq_u32 s4, 12\n"
"s_cbranch_scc1 PTL4\n"
"s_cmp_eq_u32 s4, 13\n"
"s_cbranch_scc1 PTL5\n"
"s_cmp_eq_u32 s4, 14\n"
"s_cbranch_scc1 PTL6\n"
"s_branch PTL7\n"
"PTR:\n"
"s_mov_b64 exec, -1\n"
"s_mov_b32 s92, 0x3f803f80\n"
"s_mov_b32 s93, 0xffff0000\n"
"v_bfe_u32 v190, v0, 3, 4\n"
"v_and_b32 v191, 3, v0\n"
"v_xor_b32 v190, v190, v191\n"
"v_lshlrev_b32 v190, 4, v190\n"
"v_lshl_add_u32 v190, v191, 8, v190\n"
"v_bfe_u32 v191, v0, 2, 1\n"
"v_lshl_add_u32 v190, v191, 12, v190\n"
"v_lshrrev_b32 v191, 7, v0\n"
"v_lshl_add_u32 v190, v191, 10, v190\n"
"v_add_u32 v5, 101376, v190\n"
W_"vmcnt(6)\n"
"v_dot2_f32_bf16 v184, v200, s92, 0\n"
"v_dot2_f32_bf16 v185, v201, s92, 0\n"
"v_dot2_f32_bf16 v184, v202, s92, v184\n"
"v_dot2_f32_bf16 v185, v203, s92, v185\n"
"v_dot2_f32_bf16 v186, v200, v200, 0\n"
"v_dot2_f32_bf16 v187, v201, v201, 0\n"
"v_dot2_f32_bf16 v186, v202, v202, v186\n"
"v_dot2_f32_bf16 v187, v203, v203, v187\n"
"v_lshlrev_b32 v128, 16, v200\n"
"v_and_b32 v129, s93, v200\n"
"v_lshlrev_b32 v130, 16, v201\n"
"v_and_b32 v131, s93, v201\n"
"v_lshlrev_b32 v132, 16, v202\n"
"v_and_b32 v133, s93, v202\n"
"v_lshlrev_b32 v134, 16, v203\n"
"v_and_b32 v135, s93, v203\n"
W_"vmcnt(5)\n"
"v_dot2_f32_bf16 v184, v204, s92, v184\n"
"v_dot2_f32_bf16 v185, v205, s92, v185\n"
"v_dot2_f32_bf16 v184, v206, s92, v184\n"
"v_dot2_f32_bf16 v185, v207, s92, v185\n"
"v_dot2_f32_bf16 v186, v204, v204, v186\n"
"v_dot2_f32_bf16 v187, v205, v205, v187\n"
"v_dot2_f32_bf16 v186, v206, v206, v186\n"
"v_dot2_f32_bf16 v187, v207, v207, v187\n"
"v_lshlrev_b32 v136, 16, v204\n"
"v_and_b32 v137, s93, v204\n"
"v_lshlrev_b32 v138, 16, v205\n"
"v_and_b32 v139, s93, v205\n"
"v_lshlrev_b32 v140, 16, v206\n"
"v_and_b32 v141, s93, v206\n"
"v_lshlrev_b32 v142, 16, v207\n"
"v_and_b32 v143, s93, v207\n"
W_"vmcnt(4)\n"
"v_dot2_f32_bf16 v184, v208, s92, v184\n"
"v_dot2_f32_bf16 v185, v209, s92, v185\n"
"v_dot2_f32_bf16 v184, v210, s92, v184\n"
"v_dot2_f32_bf16 v185, v211, s92, v185\n"
"v_dot2_f32_bf16 v186, v208, v208, v186\n"
"v_dot2_f32_bf16 v187, v209, v209, v187\n"
"v_dot2_f32_bf16 v186, v210, v210, v186\n"
"v_dot2_f32_bf16 v187, v211, v211, v187\n"
"v_lshlrev_b32 v144, 16, v208\n"
"v_and_b32 v145, s93, v208\n"
"v_lshlrev_b32 v146, 16, v209\n"
"v_and_b32 v147, s93, v209\n"
"v_lshlrev_b32 v148, 16, v210\n"
"v_and_b32 v149, s93, v210\n"
"v_lshlrev_b32 v150, 16, v211\n"
"v_and_b32 v151, s93, v211\n"
W_"vmcnt(3)\n"
"v_dot2_f32_bf16 v184, v212, s92, v184\n"
"v_dot2_f32_bf16 v185, v213, s92, v185\n"
"v_dot2_f32_bf16 v184, v214, s92, v184\n"
"v_dot2_f32_bf16 v185, v215, s92, v185\n"
"v_dot2_f32_bf16 v186, v212, v212, v186\n"
"v_dot2_f32_bf16 v187, v213, v213, v187\n"
"v_dot2_f32_bf16 v186, v214, v214, v186\n"
"v_dot2_f32_bf16 v187, v215, v215, v187\n"
"v_lshlrev_b32 v152, 16, v212\n"
"v_and_b32 v153, s93, v212\n"
"v_lshlrev_b32 v154, 16, v213\n"
"v_and_b32 v155, s93, v213\n"
"v_lshlrev_b32 v156, 16, v214\n"
"v_and_b32 v157, s93, v214\n"
"v_lshlrev_b32 v158, 16, v215\n"
"v_and_b32 v159, s93, v215\n"
W_"vmcnt(2)\n"
"v_dot2_f32_bf16 v184, v216, s92, v184\n"
"v_dot2_f32_bf16 v185, v217, s92, v185\n"
"v_dot2_f32_bf16 v184, v218, s92, v184\n"
"v_dot2_f32_bf16 v185, v219, s92, v185\n"
"v_dot2_f32_bf16 v186, v216, v216, v186\n"
"v_dot2_f32_bf16 v187, v217, v217, v187\n"
"v_dot2_f32_bf16 v186, v218, v218, v186\n"
"v_dot2_f32_bf16 v187, v219, v219, v187\n"
W_"vmcnt(1)\n"
"v_dot2_f32_bf16 v184, v220, s92, v184\n"
"v_dot2_f32_bf16 v185, v221, s92, v185\n"
"v_dot2_f32_bf16 v184, v222, s92, v184\n"
"v_dot2_f32_bf16 v185, v223, s92, v185\n"
"v_dot2_f32_bf16 v186, v220, v220, v186\n"
"v_dot2_f32_bf16 v187, v221, v221, v187\n"
"v_dot2_f32_bf16 v186, v222, v222, v186\n"
"v_dot2_f32_bf16 v187, v223, v223, v187\n"
W_"vmcnt(0)\n"
"v_dot2_f32_bf16 v184, v224, s92, v184\n"
"v_dot2_f32_bf16 v185, v225, s92, v185\n"
"v_dot2_f32_bf16 v184, v226, s92, v184\n"
"v_dot2_f32_bf16 v185, v227, s92, v185\n"
"v_dot2_f32_bf16 v186, v224, v224, v186\n"
"v_dot2_f32_bf16 v187, v225, v225, v187\n"
"v_dot2_f32_bf16 v186, v226, v226, v186\n"
"v_dot2_f32_bf16 v187, v227, v227, v187\n"
"v_lshlrev_b32 v160, 16, v216\n"
"v_and_b32 v161, s93, v216\n"
"v_lshlrev_b32 v162, 16, v217\n"
"v_add_f32 v184, v184, v185\n"
"v_add_f32 v186, v186, v187\n"
"v_and_b32 v163, s93, v217\n"
"v_add_f32_dpp v184, v184, v184 quad_perm:[1,0,3,2] row_mask:0xf bank_mask:0xf\n"
"v_add_f32_dpp v186, v186, v186 quad_perm:[1,0,3,2] row_mask:0xf bank_mask:0xf\n"
"v_lshlrev_b32 v164, 16, v218\n"
"v_add_f32_dpp v184, v184, v184 quad_perm:[2,3,0,1] row_mask:0xf bank_mask:0xf\n"
"v_add_f32_dpp v186, v186, v186 quad_perm:[2,3,0,1] row_mask:0xf bank_mask:0xf\n"
"v_and_b32 v165, s93, v218\n"
"v_add_f32_dpp v184, v184, v184 row_half_mirror row_mask:0xf bank_mask:0xf\n"
"v_add_f32_dpp v186, v186, v186 row_half_mirror row_mask:0xf bank_mask:0xf\n"
"v_mul_f32 v184, 0x3b124925, v184\n"
"v_mul_f32 v186, 0x3b124925, v186\n"
"v_lshlrev_b32 v166, 16, v219\n"
"v_fma_f32 v192, -v184, v184, v186\n"
C_"v188,v184,v184\n"
"v_max_f32 v192, 0, v192\n"
"v_and_b32 v188, s93, v188\n"
C_"v192,v192,v192\n"
"v_and_b32 v167, s93, v219\n"
"v_and_b32 v192, s93, v192\n"
"v_lshlrev_b32 v168, 16, v220\n"
"v_add_f32 v192, 0x3727c5ac, v192\n"
"v_and_b32 v169, s93, v220\n"
C_"v192,v192,v192\n"
"v_lshlrev_b32 v170, 16, v221\n"
"v_and_b32 v192, s93, v192\n"
"v_and_b32 v171, s93, v221\n"
"v_sqrt_f32 v192, v192\n"
"v_lshlrev_b32 v172, 16, v222\n"
"v_and_b32 v173, s93, v222\n"
C_"v192,v192,v192\n"
"v_lshlrev_b32 v174, 16, v223\n"
"v_and_b32 v192, s93, v192\n"
"v_and_b32 v175, s93, v223\n"
"v_rcp_f32 v192, v192\n"
"v_lshlrev_b32 v176, 16, v224\n"
"v_and_b32 v177, s93, v224\n"
"v_mul_f32 v192, 0x38800000, v192\n"
"v_lshlrev_b32 v178, 16, v225\n"
"v_mul_f32 v188, v188, v192\n"
"v_and_b32 v179, s93, v225\n"
"v_lshlrev_b32 v180, 16, v226\n"
"v_and_b32 v181, s93, v226\n"
"v_lshlrev_b32 v182, 16, v227\n"
"v_and_b32 v183, s93, v227\n"
"v_pk_fma_f32 v[128:129], v[128:129], v[192:193], v[188:189] op_sel_hi:[1,0,0] neg_lo:[0,0,1] neg_hi:[0,0,1]\n"
"v_pk_fma_f32 v[130:131], v[130:131], v[192:193], v[188:189] op_sel_hi:[1,0,0] neg_lo:[0,0,1] neg_hi:[0,0,1]\n"
"v_pk_fma_f32 v[132:133], v[132:133], v[192:193], v[188:189] op_sel_hi:[1,0,0] neg_lo:[0,0,1] neg_hi:[0,0,1]\n"
"v_pk_fma_f32 v[134:135], v[134:135], v[192:193], v[188:189] op_sel_hi:[1,0,0] neg_lo:[0,0,1] neg_hi:[0,0,1]\n"
C_"v200,v128,v129\n"
C_"v201,v130,v131\n"
C_"v202,v132,v133\n"
C_"v203,v134,v135\n"
"v_pk_fma_f32 v[136:137], v[136:137], v[192:193], v[188:189] op_sel_hi:[1,0,0] neg_lo:[0,0,1] neg_hi:[0,0,1]\n"
"v_pk_fma_f32 v[138:139], v[138:139], v[192:193], v[188:189] op_sel_hi:[1,0,0] neg_lo:[0,0,1] neg_hi:[0,0,1]\n"
"v_pk_fma_f32 v[140:141], v[140:141], v[192:193], v[188:189] op_sel_hi:[1,0,0] neg_lo:[0,0,1] neg_hi:[0,0,1]\n"
"v_pk_fma_f32 v[142:143], v[142:143], v[192:193], v[188:189] op_sel_hi:[1,0,0] neg_lo:[0,0,1] neg_hi:[0,0,1]\n"
"ds_write_b128 v5, v[200:203] offset:0\n"
C_"v204,v136,v137\n"
C_"v205,v138,v139\n"
C_"v206,v140,v141\n"
C_"v207,v142,v143\n"
"v_pk_fma_f32 v[144:145], v[144:145], v[192:193], v[188:189] op_sel_hi:[1,0,0] neg_lo:[0,0,1] neg_hi:[0,0,1]\n"
"v_pk_fma_f32 v[146:147], v[146:147], v[192:193], v[188:189] op_sel_hi:[1,0,0] neg_lo:[0,0,1] neg_hi:[0,0,1]\n"
"v_pk_fma_f32 v[148:149], v[148:149], v[192:193], v[188:189] op_sel_hi:[1,0,0] neg_lo:[0,0,1] neg_hi:[0,0,1]\n"
"v_pk_fma_f32 v[150:151], v[150:151], v[192:193], v[188:189] op_sel_hi:[1,0,0] neg_lo:[0,0,1] neg_hi:[0,0,1]\n"
"ds_write_b128 v5, v[204:207] offset:8448\n"
C_"v208,v144,v145\n"
C_"v209,v146,v147\n"
C_"v210,v148,v149\n"
C_"v211,v150,v151\n"
"v_pk_fma_f32 v[152:153], v[152:153], v[192:193], v[188:189] op_sel_hi:[1,0,0] neg_lo:[0,0,1] neg_hi:[0,0,1]\n"
"v_pk_fma_f32 v[154:155], v[154:155], v[192:193], v[188:189] op_sel_hi:[1,0,0] neg_lo:[0,0,1] neg_hi:[0,0,1]\n"
"v_pk_fma_f32 v[156:157], v[156:157], v[192:193], v[188:189] op_sel_hi:[1,0,0] neg_lo:[0,0,1] neg_hi:[0,0,1]\n"
"v_pk_fma_f32 v[158:159], v[158:159], v[192:193], v[188:189] op_sel_hi:[1,0,0] neg_lo:[0,0,1] neg_hi:[0,0,1]\n"
"ds_write_b128 v5, v[208:211] offset:16896\n"
C_"v212,v152,v153\n"
C_"v213,v154,v155\n"
C_"v214,v156,v157\n"
C_"v215,v158,v159\n"
"v_pk_fma_f32 v[160:161], v[160:161], v[192:193], v[188:189] op_sel_hi:[1,0,0] neg_lo:[0,0,1] neg_hi:[0,0,1]\n"
"v_pk_fma_f32 v[162:163], v[162:163], v[192:193], v[188:189] op_sel_hi:[1,0,0] neg_lo:[0,0,1] neg_hi:[0,0,1]\n"
"v_pk_fma_f32 v[164:165], v[164:165], v[192:193], v[188:189] op_sel_hi:[1,0,0] neg_lo:[0,0,1] neg_hi:[0,0,1]\n"
"v_pk_fma_f32 v[166:167], v[166:167], v[192:193], v[188:189] op_sel_hi:[1,0,0] neg_lo:[0,0,1] neg_hi:[0,0,1]\n"
"ds_write_b128 v5, v[212:215] offset:25344\n"
C_"v216,v160,v161\n"
C_"v217,v162,v163\n"
C_"v218,v164,v165\n"
C_"v219,v166,v167\n"
"v_pk_fma_f32 v[168:169], v[168:169], v[192:193], v[188:189] op_sel_hi:[1,0,0] neg_lo:[0,0,1] neg_hi:[0,0,1]\n"
"v_pk_fma_f32 v[170:171], v[170:171], v[192:193], v[188:189] op_sel_hi:[1,0,0] neg_lo:[0,0,1] neg_hi:[0,0,1]\n"
"v_pk_fma_f32 v[172:173], v[172:173], v[192:193], v[188:189] op_sel_hi:[1,0,0] neg_lo:[0,0,1] neg_hi:[0,0,1]\n"
"v_pk_fma_f32 v[174:175], v[174:175], v[192:193], v[188:189] op_sel_hi:[1,0,0] neg_lo:[0,0,1] neg_hi:[0,0,1]\n"
"ds_write_b128 v5, v[216:219] offset:33792\n"
C_"v220,v168,v169\n"
C_"v221,v170,v171\n"
C_"v222,v172,v173\n"
C_"v223,v174,v175\n"
"v_pk_fma_f32 v[176:177], v[176:177], v[192:193], v[188:189] op_sel_hi:[1,0,0] neg_lo:[0,0,1] neg_hi:[0,0,1]\n"
"v_pk_fma_f32 v[178:179], v[178:179], v[192:193], v[188:189] op_sel_hi:[1,0,0] neg_lo:[0,0,1] neg_hi:[0,0,1]\n"
"v_pk_fma_f32 v[180:181], v[180:181], v[192:193], v[188:189] op_sel_hi:[1,0,0] neg_lo:[0,0,1] neg_hi:[0,0,1]\n"
"v_pk_fma_f32 v[182:183], v[182:183], v[192:193], v[188:189] op_sel_hi:[1,0,0] neg_lo:[0,0,1] neg_hi:[0,0,1]\n"
"ds_write_b128 v5, v[220:223] offset:42240\n"
C_"v224,v176,v177\n"
C_"v225,v178,v179\n"
C_"v226,v180,v181\n"
C_"v227,v182,v183\n"
"ds_write_b128 v5, v[224:227] offset:50688\n"
"s_cmp_lg_u32 s88, 0\n"
"s_cbranch_scc1 VARB\n"
"VARA:\n"
"s_cmp_ge_u32 s7, 4\n"
"s_cbranch_scc0 AROLE_H\n"
W_"lgkmcnt(6)\n"
"s_barrier\n"
"s_add_u32 m0, s84, 67584\n"
"s_mov_b32 s86, 524288\n"
L_"v8,s[36:39],s86 offen lds\n"
L_"v9,s[36:39],s86 offen offset:1024 lds\n"
"s_setprio 2\n"
R_"v[128:131],v6 offset:0\n"
"s_add_u32 m0, s84, 76032\n"
"s_mov_b32 s86, 557056\n"
"s_setprio 0\n"
L_"v8,s[36:39],s86 offen lds\n"
L_"v9,s[36:39],s86 offen offset:1024 lds\n"
"s_setprio 2\n"
R_"v[132:135],v6 offset:1024\n"
"s_add_u32 m0, s84, 84480\n"
"s_mov_b32 s86, 524544\n"
"s_setprio 0\n"
L_"v8,s[36:39],s86 offen lds\n"
"s_setprio 2\n"
R_"v[136:139],v6 offset:4096\n"
"s_setprio 0\n"
L_"v9,s[36:39],s86 offen offset:1024 lds\n"
"s_add_u32 m0, s84, 92928\n"
"s_mov_b32 s86, 557312\n"
L_"v8,s[36:39],s86 offen lds\n"
"s_setprio 2\n"
R_"v[140:143],v6 offset:5120\n"
"s_setprio 0\n"
L_"v9,s[36:39],s86 offen offset:1024 lds\n"
W_"lgkmcnt(0)\n"
"s_barrier\n"
"s_add_u32 m0, s84, 0\n"
"s_mov_b32 s86, 786432\n"
L_"v8,s[36:39],s86 offen lds\n"
"s_setprio 2\n"
R_"v[144:147],v6 offset:8448\n"
R_"v[148:151],v6 offset:9472\n"
"s_setprio 0\n"
L_"v9,s[36:39],s86 offen offset:1024 lds\n"
"s_setprio 2\n"
R_"v[152:155],v6 offset:12544\n"
R_"v[156:159],v6 offset:13568\n"
"s_mov_b32 s94, 0xd2d2d3e8\n"
"s_add_u32 m0, s84, 8448\n"
"s_mov_b32 s86, 819200\n"
"s_setprio 0\n"
L_"v8,s[36:39],s86 offen lds\n"
"v_mov_b32 v241, 0xc7135761\n"
"s_mov_b32 s95, 0x38800000\n"
"s_mov_b32 s96, 0x46800000\n"
L_"v9,s[36:39],s86 offen offset:1024 lds\n"
"s_mov_b32 s97, 0xbfb8aa3b\n"
"s_lshl3_add_u32 s4, s31, s30\n"
"s_add_u32 m0, s84, 101376\n"
"s_mov_b32 s86, 786688\n"
L_"v8,s[36:39],s86 offen lds\n"
"s_mul_i32 s5, s4, 114688\n"
"s_add_u32 s5, s5, 65536\n"
"s_add_u32 s64, s28, s5\n"
"s_addc_u32 s65, s29, 0\n"
L_"v9,s[36:39],s86 offen offset:1024 lds\n"
"s_mov_b32 s66, 114688\n"
"s_mov_b32 s67, 0x20000\n"
W_"vmcnt(6) lgkmcnt(0)\n"
"s_barrier\n"
"s_add_u32 m0, s84, 16896\n"
"s_mov_b32 s86, 819456\n"
L_"v8,s[36:39],s86 offen lds\n"
"s_setprio 2\n"
R_"v[160:163],v6 offset:16896\n"
"s_setprio 0\n"
L_"v9,s[36:39],s86 offen offset:1024 lds\n"
"s_setprio 2\n"
R_"v[164:167],v6 offset:17920\n"
R_"v[168:171],v6 offset:20992\n"
"s_add_u32 m0, s84, 25344\n"
"s_mov_b32 s86, 1048576\n"
"s_setprio 0\n"
L_"v8,s[36:39],s86 offen lds\n"
"s_setprio 2\n"
R_"v[172:175],v6 offset:22016\n"
"s_setprio 0\n"
L_"v9,s[36:39],s86 offen offset:1024 lds\n"
"s_and_b32 s4, s4, -2\n"
"s_mul_i32 s5, s4, 7168\n"
"s_add_u32 m0, s84, 33792\n"
"s_mov_b32 s86, 1081344\n"
L_"v8,s[36:39],s86 offen lds\n"
"s_add_u32 s68, s8, s5\n"
"s_addc_u32 s69, s9, 0\n"
"s_mov_b32 s70, 14336\n"
L_"v9,s[36:39],s86 offen offset:1024 lds\n"
"s_mov_b32 s71, 0x20000\n"
"s_add_u32 s72, s26, s5\n"
"s_addc_u32 s73, s27, 0\n"
"s_add_u32 m0, s84, 42240\n"
"s_mov_b32 s86, 1048832\n"
L_"v8,s[36:39],s86 offen lds\n"
"s_mov_b32 s74, 14336\n"
"s_mov_b32 s75, 0x20000\n"
L_"v9,s[36:39],s86 offen offset:1024 lds\n"
"v_lshrrev_b32 v16, 4, v240\n"
W_"vmcnt(6) lgkmcnt(0)\n"
"s_barrier\n"
"s_add_u32 m0, s84, 50688\n"
"s_mov_b32 s86, 1081600\n"
L_"v8,s[36:39],s86 offen lds\n"
"s_setprio 2\n"
R_"v[176:179],v6 offset:25344\n"
"s_setprio 0\n"
L_"v9,s[36:39],s86 offen offset:1024 lds\n"
"s_setprio 2\n"
R_"v[180:183],v6 offset:26368\n"
R_"v[184:187],v6 offset:29440\n"
"s_add_u32 m0, s84, 59136\n"
"s_mov_b32 s86, 1310720\n"
"s_setprio 0\n"
L_"v8,s[36:39],s86 offen lds\n"
"s_setprio 2\n"
R_"v[188:191],v6 offset:30464\n"
"s_setprio 0\n"
L_"v9,s[36:39],s86 offen offset:1024 lds\n"
"v_and_b32 v17, 15, v240\n"
"v_lshlrev_b32 v18, 4, v17\n"
"s_add_u32 m0, s84, 67584\n"
"s_mov_b32 s86, 1343488\n"
L_"v8,s[36:39],s86 offen lds\n"
"v_lshl_add_u32 v18, v16, 10, v18\n"
"s_lshl_b32 s4, s34, 9\n"
L_"v9,s[36:39],s86 offen offset:1024 lds\n"
"s_lshl_b32 s5, s35, 13\n"
"s_add_u32 s4, s4, s5\n"
"s_add_u32 m0, s84, 76032\n"
"s_mov_b32 s86, 1310976\n"
L_"v8,s[36:39],s86 offen lds\n"
"v_add_u32 v255, s4, v18\n"
L_"v9,s[36:39],s86 offen offset:1024 lds\n"
"s_and_b32 s4, s30, 1\n"
"s_lshl1_add_u32 s4, s35, s4\n"
"s_add_u32 m0, s84, 109824\n"
"s_mov_b32 s86, 1343744\n"
L_"v8,s[36:39],s86 offen lds\n"
"s_lshl1_add_u32 s4, s4, s34\n"
"s_lshl_b32 s4, s4, 5\n"
L_"v9,s[36:39],s86 offen offset:1024 lds\n"
"v_lshl_add_u32 v244, v16, 3, s4\n"
W_"vmcnt(8) lgkmcnt(0)\n"
"s_barrier\n"
"s_add_u32 m0, s84, 0\n"
"s_mov_b32 s86, 1572864\n"
L_"v8,s[36:39],s86 offen lds\n"
"s_setprio 2\n"
R_"v[192:195],v6 offset:33792\n"
R_"v[196:199],v6 offset:34816\n"
"s_setprio 0\n"
L_"v9,s[36:39],s86 offen offset:1024 lds\n"
"s_setprio 2\n"
R_"v[200:203],v6 offset:37888\n"
R_"v[204:207],v6 offset:38912\n"
"s_add_u32 m0, s84, 8448\n"
"s_mov_b32 s86, 1605632\n"
"s_setprio 0\n"
L_"v8,s[36:39],s86 offen lds\n"
"v_mul_u32_u24 v20, 896, v17\n"
"v_add_u32 v245, v20, v244\n"
L_"v9,s[36:39],s86 offen offset:1024 lds\n"
"v_and_b32 v19, 1, v17\n"
"v_lshlrev_b32 v19, 4, v19\n"
"s_add_u32 m0, s84, 84480\n"
"s_mov_b32 s86, 1573120\n"
L_"v8,s[36:39],s86 offen lds\n"
"v_mov_b32 v20, 0x3f80\n"
"v_lshlrev_b32 v19, v19, v20\n"
L_"v9,s[36:39],s86 offen offset:1024 lds\n"
"v_lshrrev_b32 v20, 1, v17\n"
"v_cmp_eq_u32 vcc, 0, v20\n"
"s_add_u32 m0, s84, 92928\n"
"s_mov_b32 s86, 1605888\n"
L_"v8,s[36:39],s86 offen lds\n"
"v_cndmask_b32 v112, 0, v19, vcc\n"
"v_cmp_eq_u32 vcc, 1, v20\n"
L_"v9,s[36:39],s86 offen offset:1024 lds\n"
"s_nop 0\n"
"v_cndmask_b32 v113, 0, v19, vcc\n"
W_"vmcnt(8) lgkmcnt(0)\n"
"s_barrier\n"
"s_add_u32 m0, s84, 16896\n"
"s_mov_b32 s86, 1835008\n"
L_"v8,s[36:39],s86 offen lds\n"
"s_setprio 2\n"
R_"v[208:211],v6 offset:42240\n"
R_"v[212:215],v6 offset:43264\n"
"s_setprio 0\n"
"PTH0:\n"
"s_cbranch_execz PTH8\n"
L_"v9,s[36:39],s86 offen offset:1024 lds\n"
"s_setprio 2\n"
R_"v[216:219],v6 offset:46336\n"
R_"v[220:223],v6 offset:47360\n"
"s_add_u32 m0, s84, 25344\n"
"s_mov_b32 s86, 1867776\n"
"s_setprio 0\n"
L_"v8,s[36:39],s86 offen lds\n"
"v_cmp_eq_u32 vcc, 2, v20\n"
"s_nop 1\n"
"v_cndmask_b32 v114, 0, v19, vcc\n"
L_"v9,s[36:39],s86 offen offset:1024 lds\n"
"v_cmp_eq_u32 vcc, 3, v20\n"
"s_nop 1\n"
"v_cndmask_b32 v115, 0, v19, vcc\n"
"s_add_u32 m0, s84, 33792\n"
"s_mov_b32 s86, 1835264\n"
L_"v8,s[36:39],s86 offen lds\n"
"v_cmp_eq_u32 vcc, 4, v20\n"
"s_nop 1\n"
"v_cndmask_b32 v116, 0, v19, vcc\n"
L_"v9,s[36:39],s86 offen offset:1024 lds\n"
"v_cmp_eq_u32 vcc, 5, v20\n"
"s_nop 1\n"
"v_cndmask_b32 v117, 0, v19, vcc\n"
"s_add_u32 m0, s84, 101376\n"
"s_mov_b32 s86, 1868032\n"
L_"v8,s[36:39],s86 offen lds\n"
"v_cmp_eq_u32 vcc, 6, v20\n"
"s_nop 1\n"
"v_cndmask_b32 v118, 0, v19, vcc\n"
L_"v9,s[36:39],s86 offen offset:1024 lds\n"
"v_cmp_eq_u32 vcc, 7, v20\n"
"s_nop 1\n"
"v_cndmask_b32 v119, 0, v19, vcc\n"
W_"vmcnt(8) lgkmcnt(0)\n"
"s_barrier\n"
"s_add_u32 m0, s84, 42240\n"
"s_mov_b32 s86, 2097152\n"
L_"v8,s[36:39],s86 offen lds\n"
L_"v9,s[36:39],s86 offen offset:1024 lds\n"
"s_setprio 2\n"
R_"v[224:227],v6 offset:50688\n"
"s_add_u32 m0, s84, 50688\n"
"s_mov_b32 s86, 2129920\n"
"s_setprio 0\n"
L_"v8,s[36:39],s86 offen lds\n"
L_"v9,s[36:39],s86 offen offset:1024 lds\n"
"s_setprio 2\n"
R_"v[228:231],v6 offset:51712\n"
"s_add_u32 m0, s84, 59136\n"
"s_mov_b32 s86, 2097408\n"
"s_setprio 0\n"
L_"v8,s[36:39],s86 offen lds\n"
"s_setprio 2\n"
R_"v[232:235],v6 offset:54784\n"
"s_setprio 0\n"
L_"v9,s[36:39],s86 offen offset:1024 lds\n"
"s_add_u32 m0, s84, 67584\n"
"s_mov_b32 s86, 2130176\n"
L_"v8,s[36:39],s86 offen lds\n"
"s_setprio 2\n"
R_"v[236:239],v6 offset:55808\n"
"s_setprio 0\n"
L_"v9,s[36:39],s86 offen offset:1024 lds\n"
W_"vmcnt(8) lgkmcnt(0)\n"
"s_barrier\n"
"s_add_u32 m0, s84, 0\n"
"s_mov_b32 s86, 2359296\n"
L_"v8,s[36:39],s86 offen lds\n"
L_"v9,s[36:39],s86 offen offset:1024 lds\n"
"s_lshl_b32 s4, s34, 11\n"
"s_add_u32 m0, s84, 8448\n"
"s_mov_b32 s86, 2392064\n"
L_"v8,s[36:39],s86 offen lds\n"
L_"v9,s[36:39],s86 offen offset:1024 lds\n"
"v_lshl_add_u32 v6, v240, 4, s4\n"
"s_add_u32 m0, s84, 76032\n"
"s_mov_b32 s86, 2359552\n"
L_"v8,s[36:39],s86 offen lds\n"
"s_lshl_b32 s4, s35, 12\n"
L_"v9,s[36:39],s86 offen offset:1024 lds\n"
"s_add_u32 m0, s84, 109824\n"
"s_mov_b32 s86, 2392320\n"
L_"v8,s[36:39],s86 offen lds\n"
"v_add_u32 v5, s4, v6\n"
L_"v9,s[36:39],s86 offen offset:1024 lds\n"
W_"vmcnt(8)\n"
"s_barrier\n"
"s_add_u32 m0, s84, 16896\n"
"s_mov_b32 s86, 2621440\n"
L_"v8,s[36:39],s86 offen lds\n"
L_"v9,s[36:39],s86 offen offset:1024 lds\n"
"s_add_u32 m0, s84, 25344\n"
"s_mov_b32 s86, 2654208\n"
L_"v8,s[36:39],s86 offen lds\n"
L_"v9,s[36:39],s86 offen offset:1024 lds\n"
"s_add_u32 m0, s84, 84480\n"
"s_mov_b32 s86, 2621696\n"
L_"v8,s[36:39],s86 offen lds\n"
L_"v9,s[36:39],s86 offen offset:1024 lds\n"
"s_add_u32 m0, s84, 92928\n"
"s_mov_b32 s86, 2654464\n"
L_"v8,s[36:39],s86 offen lds\n"
L_"v9,s[36:39],s86 offen offset:1024 lds\n"
W_"vmcnt(8)\n"
"s_barrier\n"
"s_add_u32 m0, s84, 33792\n"
"s_mov_b32 s86, 2883584\n"
L_"v8,s[36:39],s86 offen lds\n"
L_"v9,s[36:39],s86 offen offset:1024 lds\n"
"s_add_u32 m0, s84, 42240\n"
"s_mov_b32 s86, 2916352\n"
L_"v8,s[36:39],s86 offen lds\n"
L_"v9,s[36:39],s86 offen offset:1024 lds\n"
"s_add_u32 m0, s84, 50688\n"
"s_mov_b32 s86, 2883840\n"
L_"v8,s[36:39],s86 offen lds\n"
L_"v9,s[36:39],s86 offen offset:1024 lds\n"
"s_add_u32 m0, s84, 101376\n"
"s_mov_b32 s86, 2916608\n"
L_"v8,s[36:39],s86 offen lds\n"
L_"v9,s[36:39],s86 offen offset:1024 lds\n"
W_"vmcnt(8)\n"
"s_barrier\n"
"s_add_u32 m0, s84, 0\n"
"s_mov_b32 s86, 3145728\n"
L_"v8,s[36:39],s86 offen lds\n"
L_"v9,s[36:39],s86 offen offset:1024 lds\n"
"s_add_u32 m0, s84, 8448\n"
"s_mov_b32 s86, 3178496\n"
L_"v8,s[36:39],s86 offen lds\n"
L_"v9,s[36:39],s86 offen offset:1024 lds\n"
"s_add_u32 m0, s84, 59136\n"
"s_mov_b32 s86, 3145984\n"
L_"v8,s[36:39],s86 offen lds\n"
L_"v9,s[36:39],s86 offen offset:1024 lds\n"
"s_add_u32 m0, s84, 67584\n"
"s_mov_b32 s86, 3178752\n"
L_"v8,s[36:39],s86 offen lds\n"
L_"v9,s[36:39],s86 offen offset:1024 lds\n"
W_"vmcnt(8)\n"
"s_barrier\n"
"s_add_u32 m0, s84, 16896\n"
"s_mov_b32 s86, 3407872\n"
L_"v8,s[36:39],s86 offen lds\n"
L_"v9,s[36:39],s86 offen offset:1024 lds\n"
"s_add_u32 m0, s84, 25344\n"
"s_mov_b32 s86, 3440640\n"
L_"v8,s[36:39],s86 offen lds\n"
L_"v9,s[36:39],s86 offen offset:1024 lds\n"
"s_add_u32 m0, s84, 76032\n"
"s_mov_b32 s86, 3408128\n"
L_"v8,s[36:39],s86 offen lds\n"
L_"v9,s[36:39],s86 offen offset:1024 lds\n"
"s_add_u32 m0, s84, 109824\n"
"s_mov_b32 s86, 3440896\n"
L_"v8,s[36:39],s86 offen lds\n"
L_"v9,s[36:39],s86 offen offset:1024 lds\n"
W_"vmcnt(8)\n"
"s_barrier\n"
"s_add_u32 m0, s85, 33792\n"
"s_mov_b32 s86, 0\n"
L_"v10,s[40:43],s86 offen lds\n"
L_"v11,s[40:43],s86 offen offset:1056 lds\n"
"s_add_u32 m0, s85, 42240\n"
"s_mov_b32 s86, 2048\n"
L_"v10,s[40:43],s86 offen lds\n"
L_"v11,s[40:43],s86 offen offset:1056 lds\n"
"s_add_u32 m0, s85, 84480\n"
"s_mov_b32 s86, 16384\n"
L_"v10,s[40:43],s86 offen lds\n"
L_"v11,s[40:43],s86 offen offset:1056 lds\n"
"s_add_u32 m0, s85, 92928\n"
"s_mov_b32 s86, 18432\n"
L_"v10,s[40:43],s86 offen lds\n"
L_"v11,s[40:43],s86 offen offset:1056 lds\n"
W_"vmcnt(8)\n"
"s_barrier\n"
"s_add_u32 m0, s85, 0\n"
"s_mov_b32 s86, 32768\n"
L_"v10,s[40:43],s86 offen lds\n"
"s_mul_i32 s87, s32, 28\n"
L_"v11,s[40:43],s86 offen offset:1056 lds\n"
"v_add_u32 v24, s87, v240\n"
"v_lshrrev_b32 v26, 2, v24\n"
"s_add_u32 m0, s85, 8448\n"
"s_mov_b32 s86, 34816\n"
L_"v10,s[40:43],s86 offen lds\n"
"v_and_b32 v24, 3, v24\n"
L_"v11,s[40:43],s86 offen offset:1056 lds\n"
"v_mul_u32_u24 v26, 3584, v26\n"
"v_lshl_add_u32 v24, v24, 7, v26\n"
"s_add_u32 m0, s85, 50688\n"
"s_mov_b32 s86, 49152\n"
L_"v10,s[40:43],s86 offen lds\n"
"s_mul_i32 s87, s32, 24\n"
L_"v11,s[40:43],s86 offen offset:1056 lds\n"
"v_add_u32 v25, s87, v240\n"
"v_lshlrev_b32 v25, 7, v25\n"
"s_add_u32 m0, s85, 101376\n"
"s_mov_b32 s86, 51200\n"
L_"v10,s[40:43],s86 offen lds\n"
"v_add_u32 v25, 65600, v25\n"
L_"v11,s[40:43],s86 offen offset:1056 lds\n"
"s_bfm_b64 exec, 28, 0\n"
"buffer_load_dword v7, v24, s[52:55], 0 offen\n"
"s_mov_b64 exec, -1\n"
"s_bfm_b64 exec, 24, 0\n"
"buffer_load_dword v7, v25, s[40:43], 0 offen\n"
"s_mov_b64 exec, -1\n"
"buffer_load_dword v243, v242, s[80:83], 0 offen sc1\n"
L_"v[48:51],v15,s[76:79],0 offen\n"
L_"v[64:67],v15,s[76:79],0 offen offset:128\n"
L_"v[80:83],v15,s[76:79],0 offen offset:256\n"
L_"v[96:99],v15,s[76:79],0 offen offset:384\n"
W_"vmcnt(19)\n"
"s_barrier\n"
"s_add_u32 m0, s85, 16896\n"
"s_mov_b32 s86, 65536\n"
L_"v10,s[40:43],s86 offen lds\n"
"s_add_u32 s76, s24, 0\n"
"s_addc_u32 s77, s25, 0\n"
"s_mov_b32 s78, 896\n"
L_"v11,s[40:43],s86 offen offset:1056 lds\n"
"s_mov_b32 s79, 0x20000\n"
W_"vmcnt(17)\n"
"s_barrier\n"
"s_add_u32 m0, s85, 25344\n"
"s_mov_b32 s86, 67584\n"
L_"v10,s[40:43],s86 offen lds\n"
L_"v11,s[40:43],s86 offen offset:1056 lds\n"
W_"vmcnt(15)\n"
"s_barrier\n"
"s_add_u32 m0, s84, 33792\n"
"s_mov_b32 s86, 0\n"
L_"v12,s[52:55],s86 offen lds\n"
W_"vmcnt(9)\n"
"v_mov_b32 v243, v243\n"
W_"vmcnt(8)\n"
"v_lshlrev_b32 v52, 16, v48\n"
L_"v13,s[52:55],s86 offen offset:1024 lds\n"
"v_and_b32 v53, s93, v48\n"
"v_lshlrev_b32 v54, 16, v49\n"
"v_and_b32 v55, s93, v49\n"
"s_add_u32 m0, s84, 42240\n"
"s_mov_b32 s86, 229376\n"
L_"v12,s[52:55],s86 offen lds\n"
"v_lshlrev_b32 v60, 16, v50\n"
"v_and_b32 v61, s93, v50\n"
"v_lshlrev_b32 v62, 16, v51\n"
L_"v13,s[52:55],s86 offen offset:1024 lds\n"
"v_and_b32 v63, s93, v51\n"
"v_mul_f32 v52, s95, v52\n"
"v_mul_f32 v53, s95, v53\n"
"s_add_u32 m0, s84, 59136\n"
"s_mov_b32 s86, 458752\n"
L_"v12,s[52:55],s86 offen lds\n"
"v_mul_f32 v54, s95, v54\n"
"v_mul_f32 v55, s95, v55\n"
"v_mul_f32 v60, s95, v60\n"
L_"v13,s[52:55],s86 offen offset:1024 lds\n"
"v_mul_f32 v61, s95, v61\n"
"v_mul_f32 v62, s95, v62\n"
"v_mul_f32 v63, s95, v63\n"
"s_add_u32 m0, s84, 67584\n"
"s_mov_b32 s86, 688128\n"
L_"v12,s[52:55],s86 offen lds\n"
W_"vmcnt(13)\n"
"v_lshlrev_b32 v68, 16, v64\n"
"v_and_b32 v69, s93, v64\n"
"v_lshlrev_b32 v70, 16, v65\n"
L_"v13,s[52:55],s86 offen offset:1024 lds\n"
"v_and_b32 v71, s93, v65\n"
"v_lshlrev_b32 v76, 16, v66\n"
"s_barrier\n"
"s_add_u32 m0, s84, 0\n"
"s_mov_b32 s86, 917504\n"
L_"v12,s[52:55],s86 offen lds\n"
"v_and_b32 v77, s93, v66\n"
"v_lshlrev_b32 v78, 16, v67\n"
"v_and_b32 v79, s93, v67\n"
L_"v13,s[52:55],s86 offen offset:1024 lds\n"
"v_mul_f32 v68, s95, v68\n"
"v_mul_f32 v69, s95, v69\n"
"v_mul_f32 v70, s95, v70\n"
"s_add_u32 m0, s84, 76032\n"
"s_mov_b32 s86, 1146880\n"
L_"v12,s[52:55],s86 offen lds\n"
"v_mul_f32 v71, s95, v71\n"
"v_mul_f32 v76, s95, v76\n"
"v_mul_f32 v77, s95, v77\n"
"v_mul_f32 v78, s95, v78\n"
L_"v13,s[52:55],s86 offen offset:1024 lds\n"
"v_mul_f32 v79, s95, v79\n"
W_"vmcnt(17)\n"
"v_lshlrev_b32 v84, 16, v80\n"
"v_and_b32 v85, s93, v80\n"
"s_add_u32 m0, s84, 84480\n"
"s_mov_b32 s86, 1376256\n"
L_"v12,s[52:55],s86 offen lds\n"
"v_lshlrev_b32 v86, 16, v81\n"
"v_and_b32 v87, s93, v81\n"
"v_lshlrev_b32 v92, 16, v82\n"
L_"v13,s[52:55],s86 offen offset:1024 lds\n"
"v_and_b32 v93, s93, v82\n"
"v_lshlrev_b32 v94, 16, v83\n"
"v_and_b32 v95, s93, v83\n"
"v_mul_f32 v84, s95, v84\n"
"s_add_u32 m0, s84, 92928\n"
"s_mov_b32 s86, 128\n"
L_"v12,s[52:55],s86 offen lds\n"
"v_mul_f32 v85, s95, v85\n"
"v_mul_f32 v86, s95, v86\n"
"v_not_b32 v16, v243\n"
L_"v13,s[52:55],s86 offen offset:1024 lds\n"
"s_lshl2_add_u32 s4, s30, 32768\n"
"s_lshl_b32 s5, s33, 20\n"
"s_add_u32 s4, s4, s5\n"
"s_mov_b64 exec, 0xff\n"
"buffer_store_dword v16, v242, s[80:83], s4 offen sc1\n"
"s_mov_b64 exec, -1\n"
W_"vmcnt(17)\n"
"s_barrier\n"
"s_add_u32 m0, s84, 8448\n"
"s_mov_b32 s86, 229504\n"
L_"v12,s[52:55],s86 offen lds\n"
"v_mul_f32 v87, s95, v87\n"
"v_mul_f32 v92, s95, v92\n"
L_"v13,s[52:55],s86 offen offset:1024 lds\n"
"v_mul_f32 v93, s95, v93\n"
"v_mul_f32 v94, s95, v94\n"
"v_mul_f32 v95, s95, v95\n"
"s_add_u32 m0, s84, 50688\n"
"s_mov_b32 s86, 458880\n"
L_"v12,s[52:55],s86 offen lds\n"
"v_lshlrev_b32 v100, 16, v96\n"
"v_and_b32 v101, s93, v96\n"
"v_lshlrev_b32 v102, 16, v97\n"
L_"v13,s[52:55],s86 offen offset:1024 lds\n"
"v_and_b32 v103, s93, v97\n"
"v_lshlrev_b32 v108, 16, v98\n"
"v_and_b32 v109, s93, v98\n"
"s_add_u32 m0, s84, 109824\n"
"s_mov_b32 s86, 688256\n"
L_"v12,s[52:55],s86 offen lds\n"
"v_lshlrev_b32 v110, 16, v99\n"
"v_and_b32 v111, s93, v99\n"
L_"v13,s[52:55],s86 offen offset:1024 lds\n"
"v_mul_f32 v100, s95, v100\n"
"v_mul_f32 v101, s95, v101\n"
"v_mul_f32 v102, s95, v102\n"
"s_add_u32 m0, s84, 118272\n"
"s_mov_b32 s86, 917632\n"
L_"v12,s[52:55],s86 offen lds\n"
"v_mul_f32 v103, s95, v103\n"
"v_mul_f32 v108, s95, v108\n"
"v_mul_f32 v109, s95, v109\n"
L_"v13,s[52:55],s86 offen offset:1024 lds\n"
"v_mul_f32 v110, s95, v110\n"
"v_mul_f32 v111, s95, v111\n"
"s_setprio 2\n"
W_"vmcnt(17)\n"
"s_barrier\n"
T_"v[16:17],v1 offset:33792\n"
T_"v[18:19],v1 offset:34048\n"
T_"v[20:21],v1 offset:33800\n"
T_"v[22:23],v1 offset:34056\n"
T_"v[24:25],v1 offset:34816\n"
T_"v[26:27],v1 offset:35072\n"
T_"v[28:29],v1 offset:34824\n"
T_"v[30:31],v1 offset:35080\n"
T_"v[32:33],v1 offset:42240\n"
T_"v[34:35],v1 offset:42496\n"
W_"lgkmcnt(8)\n"
M_"v[48:51],v[16:19],v[128:131],v[52:55]\n"
"s_add_u32 m0, s84, 16896\n"
"s_mov_b32 s86, 1147008\n"
T_"v[36:37],v1 offset:42248\n"
M_"v[52:55],v[16:19],v[132:135],v[52:55]\n"
W_"lgkmcnt(7)\n"
T_"v[38:39],v1 offset:42504\n"
L_"v12,s[52:55],s86 offen lds\n"
M_"v[56:59],v[20:23],v[128:131],v[60:63]\n"
T_"v[40:41],v1 offset:43264\n"
M_"v[60:63],v[20:23],v[132:135],v[60:63]\n"
W_"lgkmcnt(7)\n"
T_"v[42:43],v1 offset:43520\n"
L_"v13,s[52:55],s86 offen offset:1024 lds\n"
M_"v[48:51],v[24:27],v[136:139],v[48:51]\n"
"s_add_u32 m0, s84, 101376\n"
"s_mov_b32 s86, 1376384\n"
T_"v[44:45],v1 offset:43272\n"
M_"v[52:55],v[24:27],v[140:143],v[52:55]\n"
W_"lgkmcnt(4)\n"
T_"v[46:47],v1 offset:43528\n"
L_"v12,s[52:55],s86 offen lds\n"
M_"v[56:59],v[28:31],v[136:139],v[56:59]\n"
T_"v[16:17],v1 offset:59136\n"
M_"v[60:63],v[28:31],v[140:143],v[60:63]\n"
T_"v[18:19],v1 offset:59392\n"
L_"v13,s[52:55],s86 offen offset:1024 lds\n"
M_"v[48:51],v[32:35],v[144:147],v[48:51]\n"
"s_add_u32 m0, s84, 126720\n"
"s_mov_b32 s86, 256\n"
T_"v[20:21],v1 offset:59144\n"
M_"v[52:55],v[32:35],v[148:151],v[52:55]\n"
W_"lgkmcnt(4)\n"
T_"v[22:23],v1 offset:59400\n"
L_"v12,s[52:55],s86 offen lds\n"
M_"v[56:59],v[36:39],v[144:147],v[56:59]\n"
T_"v[24:25],v1 offset:60160\n"
M_"v[60:63],v[36:39],v[148:151],v[60:63]\n"
T_"v[26:27],v1 offset:60416\n"
M_"v[48:51],v[40:43],v[152:155],v[48:51]\n"
T_"v[28:29],v1 offset:60168\n"
L_"v13,s[52:55],s86 offen offset:1024 lds\n"
M_"v[52:55],v[40:43],v[156:159],v[52:55]\n"
W_"lgkmcnt(4)\n"
T_"v[30:31],v1 offset:60424\n"
M_"v[56:59],v[44:47],v[152:155],v[56:59]\n"
"s_add_u32 m0, s84, 135168\n"
"s_mov_b32 s86, 229632\n"
T_"v[32:33],v2 offset:2048\n"
L_"v12,s[52:55],s86 offen lds\n"
M_"v[60:63],v[44:47],v[156:159],v[60:63]\n"
T_"v[34:35],v2 offset:2304\n"
M_"v[48:51],v[16:19],v[160:163],v[48:51]\n"
T_"v[36:37],v2 offset:2056\n"
L_"v13,s[52:55],s86 offen offset:1024 lds\n"
M_"v[52:55],v[16:19],v[164:167],v[52:55]\n"
W_"lgkmcnt(4)\n"
T_"v[38:39],v2 offset:2312\n"
M_"v[56:59],v[20:23],v[160:163],v[56:59]\n"
"s_add_u32 m0, s84, 143616\n"
"s_mov_b32 s86, 459008\n"
T_"v[40:41],v2 offset:3072\n"
L_"v12,s[52:55],s86 offen lds\n"
M_"v[60:63],v[20:23],v[164:167],v[60:63]\n"
T_"v[42:43],v2 offset:3328\n"
M_"v[48:51],v[24:27],v[168:171],v[48:51]\n"
T_"v[44:45],v2 offset:3080\n"
L_"v13,s[52:55],s86 offen offset:1024 lds\n"
M_"v[52:55],v[24:27],v[172:175],v[52:55]\n"
W_"lgkmcnt(4)\n"
T_"v[46:47],v2 offset:3336\n"
M_"v[56:59],v[28:31],v[168:171],v[56:59]\n"
W_"vmcnt(19)\n"
"s_barrier\n"
T_"v[16:17],v1 offset:0\n"
M_"v[60:63],v[28:31],v[172:175],v[60:63]\n"
"s_add_u32 m0, s84, 25344\n"
T_"v[18:19],v1 offset:256\n"
M_"v[48:51],v[32:35],v[176:179],v[48:51]\n"
"s_mov_b32 s86, 688384\n"
T_"v[20:21],v1 offset:8\n"
L_"v12,s[52:55],s86 offen lds\n"
M_"v[52:55],v[32:35],v[180:183],v[52:55]\n"
W_"lgkmcnt(4)\n"
T_"v[22:23],v1 offset:264\n"
M_"v[56:59],v[36:39],v[176:179],v[56:59]\n"
T_"v[24:25],v1 offset:1024\n"
M_"v[60:63],v[36:39],v[180:183],v[60:63]\n"
T_"v[26:27],v1 offset:1280\n"
L_"v13,s[52:55],s86 offen offset:1024 lds\n"
M_"v[48:51],v[40:43],v[184:187],v[48:51]\n"
T_"v[28:29],v1 offset:1032\n"
M_"v[52:55],v[40:43],v[188:191],v[52:55]\n"
W_"lgkmcnt(4)\n"
T_"v[30:31],v1 offset:1288\n"
M_"v[56:59],v[44:47],v[184:187],v[56:59]\n"
"s_add_u32 m0, s84, 33792\n"
"s_mov_b32 s86, 917760\n"
T_"v[32:33],v2 offset:10496\n"
L_"v12,s[52:55],s86 offen lds\n"
M_"v[60:63],v[44:47],v[188:191],v[60:63]\n"
T_"v[34:35],v2 offset:10752\n"
M_"v[48:51],v[16:19],v[192:195],v[48:51]\n"
T_"v[36:37],v2 offset:10504\n"
M_"v[52:55],v[16:19],v[196:199],v[52:55]\n"
W_"lgkmcnt(4)\n"
T_"v[38:39],v2 offset:10760\n"
L_"v13,s[52:55],s86 offen offset:1024 lds\n"
M_"v[56:59],v[20:23],v[192:195],v[56:59]\n"
T_"v[40:41],v2 offset:11520\n"
M_"v[60:63],v[20:23],v[196:199],v[60:63]\n"
"s_add_u32 m0, s84, 42240\n"
T_"v[42:43],v2 offset:11776\n"
M_"v[48:51],v[24:27],v[200:203],v[48:51]\n"
"s_mov_b32 s86, 1147136\n"
T_"v[44:45],v2 offset:11528\n"
L_"v12,s[52:55],s86 offen lds\n"
M_"v[52:55],v[24:27],v[204:207],v[52:55]\n"
W_"lgkmcnt(4)\n"
T_"v[46:47],v2 offset:11784\n"
M_"v[56:59],v[28:31],v[200:203],v[56:59]\n"
T_"v[16:17],v2 offset:18944\n"
M_"v[60:63],v[28:31],v[204:207],v[60:63]\n"
T_"v[18:19],v2 offset:19200\n"
L_"v13,s[52:55],s86 offen offset:1024 lds\n"
M_"v[48:51],v[32:35],v[208:211],v[48:51]\n"
T_"v[20:21],v2 offset:18952\n"
M_"v[52:55],v[32:35],v[212:215],v[52:55]\n"
W_"lgkmcnt(4)\n"
T_"v[22:23],v2 offset:19208\n"
M_"v[56:59],v[36:39],v[208:211],v[56:59]\n"
"s_add_u32 m0, s84, 59136\n"
"s_mov_b32 s86, 1376512\n"
T_"v[24:25],v2 offset:19968\n"
L_"v12,s[52:55],s86 offen lds\n"
M_"v[60:63],v[36:39],v[212:215],v[60:63]\n"
T_"v[26:27],v2 offset:20224\n"
M_"v[48:51],v[40:43],v[216:219],v[48:51]\n"
T_"v[28:29],v2 offset:19976\n"
M_"v[52:55],v[40:43],v[220:223],v[52:55]\n"
W_"lgkmcnt(4)\n"
T_"v[30:31],v2 offset:20232\n"
L_"v13,s[52:55],s86 offen offset:1024 lds\n"
M_"v[56:59],v[44:47],v[216:219],v[56:59]\n"
T_"v[32:33],v2 offset:27392\n"
M_"v[60:63],v[44:47],v[220:223],v[60:63]\n"
"s_add_u32 m0, s84, 152064\n"
T_"v[34:35],v2 offset:27648\n"
M_"v[48:51],v[16:19],v[224:227],v[48:51]\n"
"s_mov_b32 s86, 384\n"
T_"v[36:37],v2 offset:27400\n"
L_"v12,s[52:55],s86 offen lds\n"
M_"v[52:55],v[16:19],v[228:231],v[52:55]\n"
W_"lgkmcnt(4)\n"
T_"v[38:39],v2 offset:27656\n"
M_"v[56:59],v[20:23],v[224:227],v[56:59]\n"
T_"v[40:41],v2 offset:28416\n"
M_"v[60:63],v[20:23],v[228:231],v[60:63]\n"
T_"v[42:43],v2 offset:28672\n"
L_"v13,s[52:55],s86 offen offset:1024 lds\n"
M_"v[48:51],v[24:27],v[232:235],v[48:51]\n"
T_"v[44:45],v2 offset:28424\n"
M_"v[52:55],v[24:27],v[236:239],v[52:55]\n"
W_"lgkmcnt(4)\n"
T_"v[46:47],v2 offset:28680\n"
M_"v[56:59],v[28:31],v[232:235],v[56:59]\n"
W_"vmcnt(20)\n"
"s_barrier\n"
T_"v[16:17],v1 offset:8448\n"
M_"v[60:63],v[28:31],v[236:239],v[60:63]\n"
"s_add_u32 m0, s84, 0\n"
T_"v[18:19],v1 offset:8704\n"
M_"v[64:67],v[32:35],v[128:131],v[68:71]\n"
"s_mov_b32 s86, 229760\n"
T_"v[20:21],v1 offset:8456\n"
L_"v12,s[52:55],s86 offen lds\n"
M_"v[68:71],v[32:35],v[132:135],v[68:71]\n"
W_"lgkmcnt(4)\n"
T_"v[22:23],v1 offset:8712\n"
M_"v[72:75],v[36:39],v[128:131],v[76:79]\n"
T_"v[24:25],v1 offset:9472\n"
M_"v[76:79],v[36:39],v[132:135],v[76:79]\n"
T_"v[26:27],v1 offset:9728\n"
L_"v13,s[52:55],s86 offen offset:1024 lds\n"
M_"v[64:67],v[40:43],v[136:139],v[64:67]\n"
T_"v[28:29],v1 offset:9480\n"
M_"v[68:71],v[40:43],v[140:143],v[68:71]\n"
W_"lgkmcnt(4)\n"
T_"v[30:31],v1 offset:9736\n"
M_"v[72:75],v[44:47],v[136:139],v[72:75]\n"
"s_add_u32 m0, s84, 67584\n"
T_"v[32:33],v1 offset:50688\n"
M_"v[76:79],v[44:47],v[140:143],v[76:79]\n"
"s_mov_b32 s86, 459136\n"
T_"v[34:35],v1 offset:50944\n"
L_"v12,s[52:55],s86 offen lds\n"
M_"v[64:67],v[16:19],v[144:147],v[64:67]\n"
T_"v[36:37],v1 offset:50696\n"
"v_mul_f32 v120, v48, v48\n"
M_"v[68:71],v[16:19],v[148:151],v[68:71]\n"
W_"lgkmcnt(4)\n"
T_"v[38:39],v1 offset:50952\n"
M_"v[72:75],v[20:23],v[144:147],v[72:75]\n"
T_"v[40:41],v1 offset:51712\n"
"v_mul_f32 v121, v49, v49\n"
M_"v[76:79],v[20:23],v[148:151],v[76:79]\n"
T_"v[42:43],v1 offset:51968\n"
L_"v13,s[52:55],s86 offen offset:1024 lds\n"
M_"v[64:67],v[24:27],v[152:155],v[64:67]\n"
T_"v[44:45],v1 offset:51720\n"
M_"v[68:71],v[24:27],v[156:159],v[68:71]\n"
W_"lgkmcnt(4)\n"
T_"v[46:47],v1 offset:51976\n"
"v_mul_f32 v122, v50, v50\n"
M_"v[72:75],v[28:31],v[152:155],v[72:75]\n"
"s_add_u32 m0, s84, 76032\n"
T_"v[16:17],v2 offset:44288\n"
M_"v[76:79],v[28:31],v[156:159],v[76:79]\n"
T_"v[18:19],v2 offset:44544\n"
"v_mul_f32 v123, v51, v51\n"
M_"v[64:67],v[32:35],v[160:163],v[64:67]\n"
"s_mov_b32 s86, 688512\n"
T_"v[20:21],v2 offset:44296\n"
L_"v12,s[52:55],s86 offen lds\n"
M_"v[68:71],v[32:35],v[164:167],v[68:71]\n"
W_"lgkmcnt(4)\n"
T_"v[22:23],v2 offset:44552\n"
"PTH1:\n"
"s_cbranch_execz PTH9\n"
M_"v[72:75],v[36:39],v[160:163],v[72:75]\n"
T_"v[24:25],v2 offset:45312\n"
"v_fma_f32 v120, v120, s94, v241\n"
M_"v[76:79],v[36:39],v[164:167],v[76:79]\n"
T_"v[26:27],v2 offset:45568\n"
L_"v13,s[52:55],s86 offen offset:1024 lds\n"
M_"v[64:67],v[40:43],v[168:171],v[64:67]\n"
T_"v[28:29],v2 offset:45320\n"
"v_fma_f32 v121, v121, s94, v241\n"
M_"v[68:71],v[40:43],v[172:175],v[68:71]\n"
W_"lgkmcnt(4)\n"
T_"v[30:31],v2 offset:45576\n"
M_"v[72:75],v[44:47],v[168:171],v[72:75]\n"
"s_add_u32 m0, s84, 84480\n"
T_"v[32:33],v2 offset:52736\n"
M_"v[76:79],v[44:47],v[172:175],v[76:79]\n"
T_"v[34:35],v2 offset:52992\n"
"v_fma_f32 v122, v122, s94, v241\n"
M_"v[64:67],v[16:19],v[176:179],v[64:67]\n"
"s_mov_b32 s86, 917888\n"
T_"v[36:37],v2 offset:52744\n"
L_"v12,s[52:55],s86 offen lds\n"
M_"v[68:71],v[16:19],v[180:183],v[68:71]\n"
W_"lgkmcnt(4)\n"
T_"v[38:39],v2 offset:53000\n"
"v_fma_f32 v123, v123, s94, v241\n"
M_"v[72:75],v[20:23],v[176:179],v[72:75]\n"
T_"v[40:41],v2 offset:53760\n"
M_"v[76:79],v[20:23],v[180:183],v[76:79]\n"
T_"v[42:43],v2 offset:54016\n"
L_"v13,s[52:55],s86 offen offset:1024 lds\n"
M_"v[64:67],v[24:27],v[184:187],v[64:67]\n"
T_"v[44:45],v2 offset:53768\n"
"v_mul_f32 v120, v120, v48\n"
M_"v[68:71],v[24:27],v[188:191],v[68:71]\n"
W_"lgkmcnt(4)\n"
T_"v[46:47],v2 offset:54024\n"
M_"v[72:75],v[28:31],v[184:187],v[72:75]\n"
W_"vmcnt(20)\n"
"s_barrier\n"
T_"v[16:17],v1 offset:16896\n"
"v_mul_f32 v121, v121, v49\n"
M_"v[76:79],v[28:31],v[188:191],v[76:79]\n"
"s_add_u32 m0, s84, 8448\n"
T_"v[18:19],v1 offset:17152\n"
M_"v[64:67],v[32:35],v[192:195],v[64:67]\n"
"s_mov_b32 s86, 1147264\n"
T_"v[20:21],v1 offset:16904\n"
L_"v12,s[52:55],s86 offen lds\n"
M_"v[68:71],v[32:35],v[196:199],v[68:71]\n"
W_"lgkmcnt(4)\n"
T_"v[22:23],v1 offset:17160\n"
"v_mul_f32 v122, v122, v50\n"
M_"v[72:75],v[36:39],v[192:195],v[72:75]\n"
T_"v[24:25],v1 offset:17920\n"
M_"v[76:79],v[36:39],v[196:199],v[76:79]\n"
T_"v[26:27],v1 offset:18176\n"
L_"v13,s[52:55],s86 offen offset:1024 lds\n"
M_"v[64:67],v[40:43],v[200:203],v[64:67]\n"
T_"v[28:29],v1 offset:17928\n"
"v_mul_f32 v123, v123, v51\n"
M_"v[68:71],v[40:43],v[204:207],v[68:71]\n"
W_"lgkmcnt(4)\n"
T_"v[30:31],v1 offset:18184\n"
M_"v[72:75],v[44:47],v[200:203],v[72:75]\n"
"s_add_u32 m0, s84, 50688\n"
T_"v[32:33],v2 offset:35840\n"
T_"v[34:35],v2 offset:36096\n"
M_"v[76:79],v[44:47],v[204:207],v[76:79]\n"
"s_mov_b32 s86, 1376640\n"
L_"v12,s[52:55],s86 offen lds\n"
E_"v120,v120\n"
M_"v[64:67],v[16:19],v[208:211],v[64:67]\n"
T_"v[36:37],v2 offset:35848\n"
T_"v[38:39],v2 offset:36104\n"
M_"v[68:71],v[16:19],v[212:215],v[68:71]\n"
W_"lgkmcnt(5)\n"
E_"v121,v121\n"
M_"v[72:75],v[20:23],v[208:211],v[72:75]\n"
T_"v[40:41],v2 offset:36864\n"
T_"v[42:43],v2 offset:37120\n"
M_"v[76:79],v[20:23],v[212:215],v[76:79]\n"
L_"v13,s[52:55],s86 offen offset:1024 lds\n"
E_"v122,v122\n"
M_"v[64:67],v[24:27],v[216:219],v[64:67]\n"
T_"v[44:45],v2 offset:36872\n"
M_"v[68:71],v[24:27],v[220:223],v[68:71]\n"
W_"lgkmcnt(3)\n"
T_"v[46:47],v2 offset:37128\n"
M_"v[72:75],v[28:31],v[216:219],v[72:75]\n"
"s_add_u32 m0, s85, 92928\n"
T_"v[16:17],v2 offset:61184\n"
T_"v[18:19],v2 offset:61440\n"
M_"v[76:79],v[28:31],v[220:223],v[76:79]\n"
"s_mov_b32 s86, 81920\n"
L_"v10,s[40:43],s86 offen lds\n"
E_"v123,v123\n"
M_"v[64:67],v[32:35],v[224:227],v[64:67]\n"
T_"v[20:21],v2 offset:61192\n"
"v_add_f32 v120, 1.0, v120\n"
M_"v[68:71],v[32:35],v[228:231],v[68:71]\n"
T_"v[22:23],v2 offset:61448\n"
M_"v[72:75],v[36:39],v[224:227],v[72:75]\n"
T_"v[24:25],v2 offset:62208\n"
M_"v[76:79],v[36:39],v[228:231],v[76:79]\n"
W_"lgkmcnt(3)\n"
T_"v[26:27],v2 offset:62464\n"
"v_add_f32 v121, 1.0, v121\n"
M_"v[64:67],v[40:43],v[232:235],v[64:67]\n"
T_"v[28:29],v2 offset:62216\n"
L_"v11,s[40:43],s86 offen offset:1056 lds\n"
M_"v[68:71],v[40:43],v[236:239],v[68:71]\n"
T_"v[30:31],v2 offset:62472\n"
"v_add_f32 v122, 1.0, v122\n"
M_"v[72:75],v[44:47],v[232:235],v[72:75]\n"
"s_add_u32 m0, s85, 109824\n"
T_"v[32:33],v3 offset:4096\n"
M_"v[76:79],v[44:47],v[236:239],v[76:79]\n"
"s_mov_b32 s86, 83968\n"
T_"v[34:35],v3 offset:4352\n"
L_"v10,s[40:43],s86 offen lds\n"
M_"v[80:83],v[16:19],v[128:131],v[84:87]\n"
T_"v[36:37],v3 offset:4104\n"
"v_add_f32 v123, 1.0, v123\n"
M_"v[84:87],v[16:19],v[132:135],v[84:87]\n"
W_"lgkmcnt(4)\n"
T_"v[38:39],v3 offset:4360\n"
M_"v[88:91],v[20:23],v[128:131],v[92:95]\n"
T_"v[40:41],v3 offset:5120\n"
T_"v[42:43],v3 offset:5376\n"
M_"v[92:95],v[20:23],v[132:135],v[92:95]\n"
L_"v11,s[40:43],s86 offen offset:1056 lds\n"
"v_rcp_f32 v120, v120\n"
M_"v[80:83],v[24:27],v[136:139],v[80:83]\n"
T_"v[44:45],v3 offset:5128\n"
T_"v[46:47],v3 offset:5384\n"
M_"v[84:87],v[24:27],v[140:143],v[84:87]\n"
W_"lgkmcnt(5)\n"
"v_rcp_f32 v121, v121\n"
M_"v[88:91],v[28:31],v[136:139],v[88:91]\n"
W_"vmcnt(20)\n"
"s_barrier\n"
T_"v[16:17],v3 offset:12544\n"
M_"v[92:95],v[28:31],v[140:143],v[92:95]\n"
"s_add_u32 m0, s85, 16896\n"
T_"v[18:19],v3 offset:12800\n"
M_"v[80:83],v[32:35],v[144:147],v[80:83]\n"
"s_mov_b32 s86, 98304\n"
T_"v[20:21],v3 offset:12552\n"
T_"v[22:23],v3 offset:12808\n"
M_"v[84:87],v[32:35],v[148:151],v[84:87]\n"
W_"lgkmcnt(4)\n"
L_"v10,s[40:43],s86 offen lds\n"
"v_rcp_f32 v122, v122\n"
M_"v[88:91],v[36:39],v[144:147],v[88:91]\n"
T_"v[24:25],v3 offset:13568\n"
T_"v[26:27],v3 offset:13824\n"
M_"v[92:95],v[36:39],v[148:151],v[92:95]\n"
L_"v11,s[40:43],s86 offen offset:1056 lds\n"
"v_rcp_f32 v123, v123\n"
M_"v[80:83],v[40:43],v[152:155],v[80:83]\n"
T_"v[28:29],v3 offset:13576\n"
M_"v[84:87],v[40:43],v[156:159],v[84:87]\n"
T_"v[30:31],v3 offset:13832\n"
"v_mul_f32 v48, v48, v120\n"
M_"v[88:91],v[44:47],v[152:155],v[88:91]\n"
"s_add_u32 m0, s85, 101376\n"
T_"v[32:33],v1 offset:25344\n"
M_"v[92:95],v[44:47],v[156:159],v[92:95]\n"
W_"lgkmcnt(3)\n"
T_"v[34:35],v1 offset:25600\n"
"v_mul_f32 v49, v49, v121\n"
M_"v[80:83],v[16:19],v[160:163],v[80:83]\n"
"s_mov_b32 s86, 100352\n"
T_"v[36:37],v1 offset:25352\n"
L_"v10,s[40:43],s86 offen lds\n"
M_"v[84:87],v[16:19],v[164:167],v[84:87]\n"
T_"v[38:39],v1 offset:25608\n"
M_"v[88:91],v[20:23],v[160:163],v[88:91]\n"
T_"v[40:41],v1 offset:26368\n"
"v_mul_f32 v50, v50, v122\n"
M_"v[92:95],v[20:23],v[164:167],v[92:95]\n"
T_"v[42:43],v1 offset:26624\n"
L_"v11,s[40:43],s86 offen offset:1056 lds\n"
M_"v[80:83],v[24:27],v[168:171],v[80:83]\n"
T_"v[44:45],v1 offset:26376\n"
"v_mul_f32 v51, v51, v123\n"
M_"v[84:87],v[24:27],v[172:175],v[84:87]\n"
W_"lgkmcnt(4)\n"
T_"v[46:47],v1 offset:26632\n"
M_"v[88:91],v[28:31],v[168:171],v[88:91]\n"
"s_add_u32 m0, s85, 118272\n"
T_"v[16:17],v1 offset:33792\n"
M_"v[92:95],v[28:31],v[172:175],v[92:95]\n"
T_"v[18:19],v1 offset:34048\n"
"v_mul_f32 v120, v56, v56\n"
M_"v[80:83],v[32:35],v[176:179],v[80:83]\n"
"s_mov_b32 s86, 114688\n"
T_"v[20:21],v1 offset:33800\n"
L_"v10,s[40:43],s86 offen lds\n"
M_"v[84:87],v[32:35],v[180:183],v[84:87]\n"
W_"lgkmcnt(4)\n"
T_"v[22:23],v1 offset:34056\n"
M_"v[88:91],v[36:39],v[176:179],v[88:91]\n"
T_"v[24:25],v1 offset:34816\n"
"v_mul_f32 v121, v57, v57\n"
M_"v[92:95],v[36:39],v[180:183],v[92:95]\n"
T_"v[26:27],v1 offset:35072\n"
L_"v11,s[40:43],s86 offen offset:1056 lds\n"
M_"v[80:83],v[40:43],v[184:187],v[80:83]\n"
T_"v[28:29],v1 offset:34824\n"
"v_mul_f32 v122, v58, v58\n"
M_"v[84:87],v[40:43],v[188:191],v[84:87]\n"
W_"lgkmcnt(4)\n"
T_"v[30:31],v1 offset:35080\n"
M_"v[88:91],v[44:47],v[184:187],v[88:91]\n"
"s_add_u32 m0, s85, 126720\n"
T_"v[32:33],v1 offset:42240\n"
M_"v[92:95],v[44:47],v[188:191],v[92:95]\n"
T_"v[34:35],v1 offset:42496\n"
"v_mul_f32 v123, v59, v59\n"
M_"v[80:83],v[16:19],v[192:195],v[80:83]\n"
"s_mov_b32 s86, 116736\n"
T_"v[36:37],v1 offset:42248\n"
L_"v10,s[40:43],s86 offen lds\n"
M_"v[84:87],v[16:19],v[196:199],v[84:87]\n"
W_"lgkmcnt(4)\n"
T_"v[38:39],v1 offset:42504\n"
"v_fma_f32 v120, v120, s94, v241\n"
M_"v[88:91],v[20:23],v[192:195],v[88:91]\n"
T_"v[40:41],v1 offset:43264\n"
M_"v[92:95],v[20:23],v[196:199],v[92:95]\n"
T_"v[42:43],v1 offset:43520\n"
L_"v11,s[40:43],s86 offen offset:1056 lds\n"
M_"v[80:83],v[24:27],v[200:203],v[80:83]\n"
T_"v[44:45],v1 offset:43272\n"
"v_fma_f32 v121, v121, s94, v241\n"
M_"v[84:87],v[24:27],v[204:207],v[84:87]\n"
W_"lgkmcnt(4)\n"
T_"v[46:47],v1 offset:43528\n"
M_"v[88:91],v[28:31],v[200:203],v[88:91]\n"
W_"vmcnt(20)\n"
"s_barrier\n"
T_"v[16:17],v1 offset:59136\n"
"v_fma_f32 v122, v122, s94, v241\n"
M_"v[92:95],v[28:31],v[204:207],v[92:95]\n"
"s_add_u32 m0, s85, 25344\n"
T_"v[18:19],v1 offset:59392\n"
M_"v[80:83],v[32:35],v[208:211],v[80:83]\n"
"s_mov_b32 s86, 131072\n"
T_"v[20:21],v1 offset:59144\n"
L_"v10,s[40:43],s86 offen lds\n"
M_"v[84:87],v[32:35],v[212:215],v[84:87]\n"
W_"lgkmcnt(4)\n"
T_"v[22:23],v1 offset:59400\n"
"v_fma_f32 v123, v123, s94, v241\n"
M_"v[88:91],v[36:39],v[208:211],v[88:91]\n"
T_"v[24:25],v1 offset:60160\n"
M_"v[92:95],v[36:39],v[212:215],v[92:95]\n"
T_"v[26:27],v1 offset:60416\n"
"v_mul_f32 v120, v120, v56\n"
M_"v[80:83],v[40:43],v[216:219],v[80:83]\n"
T_"v[28:29],v1 offset:60168\n"
L_"v11,s[40:43],s86 offen offset:1056 lds\n"
M_"v[84:87],v[40:43],v[220:223],v[84:87]\n"
W_"lgkmcnt(4)\n"
T_"v[30:31],v1 offset:60424\n"
M_"v[88:91],v[44:47],v[216:219],v[88:91]\n"
"s_add_u32 m0, s85, 33792\n"
T_"v[32:33],v3 offset:20992\n"
"v_mul_f32 v121, v121, v57\n"
M_"v[92:95],v[44:47],v[220:223],v[92:95]\n"
"s_mov_b32 s86, 133120\n"
T_"v[34:35],v3 offset:21248\n"
L_"v10,s[40:43],s86 offen lds\n"
M_"v[80:83],v[16:19],v[224:227],v[80:83]\n"
T_"v[36:37],v3 offset:21000\n"
"v_mul_f32 v122, v122, v58\n"
M_"v[84:87],v[16:19],v[228:231],v[84:87]\n"
W_"lgkmcnt(4)\n"
T_"v[38:39],v3 offset:21256\n"
M_"v[88:91],v[20:23],v[224:227],v[88:91]\n"
T_"v[40:41],v3 offset:22016\n"
M_"v[92:95],v[20:23],v[228:231],v[92:95]\n"
T_"v[42:43],v3 offset:22272\n"
"v_mul_f32 v123, v123, v59\n"
M_"v[80:83],v[24:27],v[232:235],v[80:83]\n"
T_"v[44:45],v3 offset:22024\n"
L_"v11,s[40:43],s86 offen offset:1056 lds\n"
M_"v[84:87],v[24:27],v[236:239],v[84:87]\n"
W_"lgkmcnt(4)\n"
T_"v[46:47],v3 offset:22280\n"
M_"v[88:91],v[28:31],v[232:235],v[88:91]\n"
"s_add_u32 m0, s85, 135168\n"
T_"v[16:17],v1 offset:0\n"
T_"v[18:19],v1 offset:256\n"
M_"v[92:95],v[28:31],v[236:239],v[92:95]\n"
"s_mov_b32 s86, 147456\n"
L_"v10,s[40:43],s86 offen lds\n"
E_"v120,v120\n"
M_"v[96:99],v[32:35],v[128:131],v[100:103]\n"
T_"v[20:21],v1 offset:8\n"
T_"v[22:23],v1 offset:264\n"
M_"v[100:103],v[32:35],v[132:135],v[100:103]\n"
W_"lgkmcnt(5)\n"
E_"v121,v121\n"
M_"v[104:107],v[36:39],v[128:131],v[108:111]\n"
T_"v[24:25],v1 offset:1024\n"
T_"v[26:27],v1 offset:1280\n"
M_"v[108:111],v[36:39],v[132:135],v[108:111]\n"
L_"v11,s[40:43],s86 offen offset:1056 lds\n"
E_"v122,v122\n"
M_"v[96:99],v[40:43],v[136:139],v[96:99]\n"
T_"v[28:29],v1 offset:1032\n"
T_"v[30:31],v1 offset:1288\n"
M_"v[100:103],v[40:43],v[140:143],v[100:103]\n"
W_"lgkmcnt(4)\n"
E_"v123,v123\n"
M_"v[104:107],v[44:47],v[136:139],v[104:107]\n"
"s_add_u32 m0, s85, 143616\n"
T_"v[32:33],v2 offset:2048\n"
M_"v[108:111],v[44:47],v[140:143],v[108:111]\n"
"s_mov_b32 s86, 149504\n"
T_"v[34:35],v2 offset:2304\n"
L_"v10,s[40:43],s86 offen lds\n"
M_"v[96:99],v[16:19],v[144:147],v[96:99]\n"
T_"v[36:37],v2 offset:2056\n"
"v_add_f32 v120, 1.0, v120\n"
M_"v[100:103],v[16:19],v[148:151],v[100:103]\n"
T_"v[38:39],v2 offset:2312\n"
M_"v[104:107],v[20:23],v[144:147],v[104:107]\n"
T_"v[40:41],v2 offset:3072\n"
"v_add_f32 v121, 1.0, v121\n"
M_"v[108:111],v[20:23],v[148:151],v[108:111]\n"
W_"lgkmcnt(4)\n"
T_"v[42:43],v2 offset:3328\n"
L_"v11,s[40:43],s86 offen offset:1056 lds\n"
M_"v[96:99],v[24:27],v[152:155],v[96:99]\n"
T_"v[44:45],v2 offset:3080\n"
M_"v[100:103],v[24:27],v[156:159],v[100:103]\n"
T_"v[46:47],v2 offset:3336\n"
"v_add_f32 v122, 1.0, v122\n"
M_"v[104:107],v[28:31],v[152:155],v[104:107]\n"
W_"vmcnt(20)\n"
"s_barrier\n"
T_"v[16:17],v2 offset:10496\n"
M_"v[108:111],v[28:31],v[156:159],v[108:111]\n"
W_"lgkmcnt(4)\n"
T_"v[18:19],v2 offset:10752\n"
"v_add_f32 v123, 1.0, v123\n"
M_"v[96:99],v[32:35],v[160:163],v[96:99]\n"
"s_add_u32 m0, s85, 0\n"
"s_mov_b32 s86, 163840\n"
T_"v[20:21],v2 offset:10504\n"
L_"v10,s[40:43],s86 offen lds\n"
M_"v[100:103],v[32:35],v[164:167],v[100:103]\n"
T_"v[22:23],v2 offset:10760\n"
M_"v[104:107],v[36:39],v[160:163],v[104:107]\n"
T_"v[24:25],v2 offset:11520\n"
T_"v[26:27],v2 offset:11776\n"
M_"v[108:111],v[36:39],v[164:167],v[108:111]\n"
W_"lgkmcnt(5)\n"
L_"v11,s[40:43],s86 offen offset:1056 lds\n"
"v_rcp_f32 v120, v120\n"
M_"v[96:99],v[40:43],v[168:171],v[96:99]\n"
T_"v[28:29],v2 offset:11528\n"
T_"v[30:31],v2 offset:11784\n"
M_"v[100:103],v[40:43],v[172:175],v[100:103]\n"
"v_rcp_f32 v121, v121\n"
M_"v[104:107],v[44:47],v[168:171],v[104:107]\n"
"s_add_u32 m0, s85, 42240\n"
"s_mov_b32 s86, 165888\n"
T_"v[32:33],v2 offset:18944\n"
T_"v[34:35],v2 offset:19200\n"
M_"v[108:111],v[44:47],v[172:175],v[108:111]\n"
W_"lgkmcnt(4)\n"
L_"v10,s[40:43],s86 offen lds\n"
"v_rcp_f32 v122, v122\n"
M_"v[96:99],v[16:19],v[176:179],v[96:99]\n"
T_"v[36:37],v2 offset:18952\n"
M_"v[100:103],v[16:19],v[180:183],v[100:103]\n"
T_"v[38:39],v2 offset:19208\n"
M_"v[104:107],v[20:23],v[176:179],v[104:107]\n"
T_"v[40:41],v2 offset:19968\n"
T_"v[42:43],v2 offset:20224\n"
M_"v[108:111],v[20:23],v[180:183],v[108:111]\n"
L_"v11,s[40:43],s86 offen offset:1056 lds\n"
"v_rcp_f32 v123, v123\n"
M_"v[96:99],v[24:27],v[184:187],v[96:99]\n"
T_"v[44:45],v2 offset:19976\n"
"v_mul_f32 v56, v56, v120\n"
M_"v[100:103],v[24:27],v[188:191],v[100:103]\n"
W_"lgkmcnt(4)\n"
T_"v[46:47],v2 offset:20232\n"
M_"v[104:107],v[28:31],v[184:187],v[104:107]\n"
"s_add_u32 m0, s85, 59136\n"
T_"v[16:17],v1 offset:8448\n"
M_"v[108:111],v[28:31],v[188:191],v[108:111]\n"
T_"v[18:19],v1 offset:8704\n"
"v_mul_f32 v57, v57, v121\n"
M_"v[96:99],v[32:35],v[192:195],v[96:99]\n"
"s_mov_b32 s86, 180224\n"
T_"v[20:21],v1 offset:8456\n"
L_"v10,s[40:43],s86 offen lds\n"
M_"v[100:103],v[32:35],v[196:199],v[100:103]\n"
W_"lgkmcnt(4)\n"
T_"v[22:23],v1 offset:8712\n"
"v_mul_f32 v58, v58, v122\n"
M_"v[104:107],v[36:39],v[192:195],v[104:107]\n"
T_"v[24:25],v1 offset:9472\n"
M_"v[108:111],v[36:39],v[196:199],v[108:111]\n"
T_"v[26:27],v1 offset:9728\n"
L_"v11,s[40:43],s86 offen offset:1056 lds\n"
M_"v[96:99],v[40:43],v[200:203],v[96:99]\n"
T_"v[28:29],v1 offset:9480\n"
"v_mul_f32 v59, v59, v123\n"
M_"v[100:103],v[40:43],v[204:207],v[100:103]\n"
W_"lgkmcnt(4)\n"
T_"v[30:31],v1 offset:9736\n"
M_"v[104:107],v[44:47],v[200:203],v[104:107]\n"
"s_add_u32 m0, s85, 152064\n"
T_"v[32:33],v1 offset:50688\n"
C_"v48,v48,v49\n"
M_"v[108:111],v[44:47],v[204:207],v[108:111]\n"
"s_mov_b32 s86, 182272\n"
T_"v[34:35],v1 offset:50944\n"
L_"v10,s[40:43],s86 offen lds\n"
M_"v[96:99],v[16:19],v[208:211],v[96:99]\n"
T_"v[36:37],v1 offset:50696\n"
M_"v[100:103],v[16:19],v[212:215],v[100:103]\n"
W_"lgkmcnt(4)\n"
T_"v[38:39],v1 offset:50952\n"
C_"v49,v50,v51\n"
M_"v[104:107],v[20:23],v[208:211],v[104:107]\n"
T_"v[40:41],v1 offset:51712\n"
M_"v[108:111],v[20:23],v[212:215],v[108:111]\n"
T_"v[42:43],v1 offset:51968\n"
L_"v11,s[40:43],s86 offen offset:1056 lds\n"
M_"v[96:99],v[24:27],v[216:219],v[96:99]\n"
T_"v[44:45],v1 offset:51720\n"
M_"v[100:103],v[24:27],v[220:223],v[100:103]\n"
W_"lgkmcnt(4)\n"
T_"v[46:47],v1 offset:51976\n"
M_"v[104:107],v[28:31],v[216:219],v[104:107]\n"
W_"vmcnt(24)\n"
"s_barrier\n"
M_"v[108:111],v[28:31],v[220:223],v[108:111]\n"
M_"v[96:99],v[32:35],v[224:227],v[96:99]\n"
M_"v[100:103],v[32:35],v[228:231],v[100:103]\n"
W_"lgkmcnt(1)\n"
M_"v[104:107],v[36:39],v[224:227],v[104:107]\n"
M_"v[108:111],v[36:39],v[228:231],v[108:111]\n"
M_"v[96:99],v[40:43],v[232:235],v[96:99]\n"
M_"v[100:103],v[40:43],v[236:239],v[100:103]\n"
W_"lgkmcnt(0)\n"
M_"v[104:107],v[44:47],v[232:235],v[104:107]\n"
M_"v[108:111],v[44:47],v[236:239],v[108:111]\n"
"s_add_u32 m0, s85, 67584\n"
"s_mov_b32 s86, 196608\n"
"s_setprio 0\n"
L_"v10,s[40:43],s86 offen lds\n"
C_"v50,v56,v57\n"
C_"v51,v58,v59\n"
"s_setprio 2\n"
"ds_write_b128 v5, v[48:51] offset:8448\n"
"s_setprio 0\n"
"v_mul_f32 v120, v52, v52\n"
"v_mul_f32 v121, v53, v53\n"
"v_mul_f32 v122, v54, v54\n"
"v_mul_f32 v123, v55, v55\n"
L_"v11,s[40:43],s86 offen offset:1056 lds\n"
"v_fma_f32 v120, v120, s94, v241\n"
"v_fma_f32 v121, v121, s94, v241\n"
"v_fma_f32 v122, v122, s94, v241\n"
"v_fma_f32 v123, v123, s94, v241\n"
"v_mul_f32 v120, v120, v52\n"
"v_mul_f32 v121, v121, v53\n"
"v_mul_f32 v122, v122, v54\n"
"s_add_u32 m0, s85, 76032\n"
"s_mov_b32 s86, 198656\n"
L_"v10,s[40:43],s86 offen lds\n"
"v_mul_f32 v123, v123, v55\n"
E_"v120,v120\n"
E_"v121,v121\n"
E_"v122,v122\n"
E_"v123,v123\n"
"v_add_f32 v120, 1.0, v120\n"
"v_add_f32 v121, 1.0, v121\n"
L_"v11,s[40:43],s86 offen offset:1056 lds\n"
"v_add_f32 v122, 1.0, v122\n"
"v_add_f32 v123, 1.0, v123\n"
"v_rcp_f32 v120, v120\n"
"v_rcp_f32 v121, v121\n"
"v_rcp_f32 v122, v122\n"
"v_rcp_f32 v123, v123\n"
"v_mul_f32 v52, v52, v120\n"
"s_add_u32 m0, s85, 84480\n"
"s_mov_b32 s86, 212992\n"
L_"v10,s[40:43],s86 offen lds\n"
"v_mul_f32 v53, v53, v121\n"
"v_mul_f32 v54, v54, v122\n"
"v_mul_f32 v55, v55, v123\n"
"s_mul_i32 s87, s32, 28\n"
"v_add_u32 v24, s87, v240\n"
"v_lshlrev_b32 v24, 7, v24\n"
"s_mul_i32 s87, s32, 32\n"
L_"v11,s[40:43],s86 offen offset:1056 lds\n"
"v_add_u32 v25, s87, v240\n"
"v_lshlrev_b32 v25, 7, v25\n"
"s_bfm_b64 exec, 28, 0\n"
"buffer_load_dword v7, v24, s[56:59], 0 offen\n"
"s_mov_b64 exec, -1\n"
"s_bfm_b64 exec, 32, 0\n"
"buffer_load_dword v7, v25, s[44:47], 0 offen\n"
"s_mov_b64 exec, -1\n"
"v_readlane_b32 s8, v243, s30\n"
"v_lshrrev_b32 v126, 5, v242\n"
"s_lshl_b32 s9, s30, 7\n"
"s_add_u32 s9, s9, 32768\n"
"buffer_load_dword v127, v126, s[80:83], s9 offen sc1\n"
"buffer_load_dword v7, v242, s[76:79], 0 offen\n"
W_"vmcnt(30)\n"
"s_barrier\n"
"v_mul_f32 v120, v60, v60\n"
"v_mul_f32 v121, v61, v61\n"
"v_mul_f32 v122, v62, v62\n"
"v_mul_f32 v123, v63, v63\n"
"v_fma_f32 v120, v120, s94, v241\n"
"v_fma_f32 v121, v121, s94, v241\n"
"v_fma_f32 v122, v122, s94, v241\n"
"v_fma_f32 v123, v123, s94, v241\n"
"v_mul_f32 v120, v120, v60\n"
"v_mul_f32 v121, v121, v61\n"
"v_mul_f32 v122, v122, v62\n"
"v_mul_f32 v123, v123, v63\n"
E_"v120,v120\n"
E_"v121,v121\n"
E_"v122,v122\n"
E_"v123,v123\n"
"v_add_f32 v120, 1.0, v120\n"
"v_add_f32 v121, 1.0, v121\n"
"v_add_f32 v122, 1.0, v122\n"
"v_add_f32 v123, 1.0, v123\n"
"v_rcp_f32 v120, v120\n"
"v_rcp_f32 v121, v121\n"
"v_rcp_f32 v122, v122\n"
"v_rcp_f32 v123, v123\n"
"v_mul_f32 v60, v60, v120\n"
"v_mul_f32 v61, v61, v121\n"
"v_mul_f32 v62, v62, v122\n"
"v_mul_f32 v63, v63, v123\n"
C_"v52,v52,v53\n"
C_"v53,v54,v55\n"
C_"v54,v60,v61\n"
C_"v55,v62,v63\n"
"s_setprio 2\n"
"ds_write_b128 v5, v[52:55] offset:9472\n"
"s_setprio 0\n"
"v_mul_f32 v120, v64, v64\n"
"v_mul_f32 v121, v65, v65\n"
"v_mul_f32 v122, v66, v66\n"
"v_mul_f32 v123, v67, v67\n"
"v_fma_f32 v120, v120, s94, v241\n"
"v_fma_f32 v121, v121, s94, v241\n"
"v_fma_f32 v122, v122, s94, v241\n"
"v_fma_f32 v123, v123, s94, v241\n"
"v_mul_f32 v120, v120, v64\n"
"v_mul_f32 v121, v121, v65\n"
"v_mul_f32 v122, v122, v66\n"
W_"vmcnt(26) lgkmcnt(0)\n"
"s_barrier\n"
"s_setprio 2\n"
R_"v[136:139],v6 offset:12544\n"
R_"v[140:143],v6 offset:13568\n"
R_"v[128:131],v6 offset:8448\n"
R_"v[132:135],v6 offset:9472\n"
"s_setprio 0\n"
"v_mul_f32 v123, v123, v67\n"
E_"v120,v120\n"
E_"v121,v121\n"
E_"v122,v122\n"
E_"v123,v123\n"
"v_add_f32 v120, 1.0, v120\n"
"v_add_f32 v121, 1.0, v121\n"
"v_add_f32 v122, 1.0, v122\n"
"v_add_f32 v123, 1.0, v123\n"
"v_rcp_f32 v120, v120\n"
"v_rcp_f32 v121, v121\n"
"v_rcp_f32 v122, v122\n"
"v_rcp_f32 v123, v123\n"
"v_mul_f32 v64, v64, v120\n"
"v_mul_f32 v65, v65, v121\n"
"v_mul_f32 v66, v66, v122\n"
"v_mul_f32 v67, v67, v123\n"
"v_mul_f32 v120, v72, v72\n"
"v_mul_f32 v121, v73, v73\n"
"v_mul_f32 v122, v74, v74\n"
"v_mul_f32 v123, v75, v75\n"
"v_fma_f32 v120, v120, s94, v241\n"
"v_fma_f32 v121, v121, s94, v241\n"
"v_fma_f32 v122, v122, s94, v241\n"
"v_fma_f32 v123, v123, s94, v241\n"
"v_mul_f32 v120, v120, v72\n"
"v_mul_f32 v121, v121, v73\n"
"v_mul_f32 v122, v122, v74\n"
"v_mul_f32 v123, v123, v75\n"
E_"v120,v120\n"
E_"v121,v121\n"
E_"v122,v122\n"
"PTH2:\n"
"s_cbranch_execz PTH10\n"
E_"v123,v123\n"
"v_add_f32 v120, 1.0, v120\n"
"v_add_f32 v121, 1.0, v121\n"
"v_add_f32 v122, 1.0, v122\n"
"v_add_f32 v123, 1.0, v123\n"
"v_rcp_f32 v120, v120\n"
"v_rcp_f32 v121, v121\n"
"v_rcp_f32 v122, v122\n"
"v_rcp_f32 v123, v123\n"
"v_mul_f32 v72, v72, v120\n"
"v_mul_f32 v73, v73, v121\n"
"v_mul_f32 v74, v74, v122\n"
"v_mul_f32 v75, v75, v123\n"
W_"vmcnt(22) lgkmcnt(0)\n"
"s_barrier\n"
"s_add_u32 m0, s85, 16896\n"
"s_mov_b32 s86, 215040\n"
L_"v10,s[40:43],s86 offen lds\n"
C_"v64,v64,v65\n"
C_"v65,v66,v67\n"
C_"v66,v72,v73\n"
C_"v67,v74,v75\n"
"s_setprio 2\n"
"ds_write_b128 v5, v[64:67] offset:8448\n"
"s_setprio 0\n"
"v_mul_f32 v120, v68, v68\n"
L_"v11,s[40:43],s86 offen offset:1056 lds\n"
"v_mul_f32 v121, v69, v69\n"
"v_mul_f32 v122, v70, v70\n"
"v_mul_f32 v123, v71, v71\n"
"v_fma_f32 v120, v120, s94, v241\n"
"v_fma_f32 v121, v121, s94, v241\n"
"v_fma_f32 v122, v122, s94, v241\n"
"s_add_u32 m0, s85, 50688\n"
"s_mov_b32 s86, 229376\n"
L_"v10,s[40:43],s86 offen lds\n"
"v_fma_f32 v123, v123, s94, v241\n"
"v_mul_f32 v120, v120, v68\n"
"v_mul_f32 v121, v121, v69\n"
"v_mul_f32 v122, v122, v70\n"
"v_mul_f32 v123, v123, v71\n"
E_"v120,v120\n"
L_"v11,s[40:43],s86 offen offset:1056 lds\n"
E_"v121,v121\n"
E_"v122,v122\n"
E_"v123,v123\n"
"v_add_f32 v120, 1.0, v120\n"
"v_add_f32 v121, 1.0, v121\n"
"v_add_f32 v122, 1.0, v122\n"
"s_add_u32 m0, s85, 92928\n"
"s_mov_b32 s86, 231424\n"
L_"v10,s[40:43],s86 offen lds\n"
"v_add_f32 v123, 1.0, v123\n"
"v_rcp_f32 v120, v120\n"
"v_rcp_f32 v121, v121\n"
"v_rcp_f32 v122, v122\n"
"v_rcp_f32 v123, v123\n"
"v_mul_f32 v68, v68, v120\n"
L_"v11,s[40:43],s86 offen offset:1056 lds\n"
"v_mul_f32 v69, v69, v121\n"
"v_mul_f32 v70, v70, v122\n"
"v_mul_f32 v71, v71, v123\n"
"v_mul_f32 v120, v76, v76\n"
"v_mul_f32 v121, v77, v77\n"
"v_mul_f32 v122, v78, v78\n"
"s_add_u32 m0, s85, 101376\n"
"s_mov_b32 s86, 245760\n"
L_"v10,s[40:43],s86 offen lds\n"
"v_mul_f32 v123, v79, v79\n"
"v_fma_f32 v120, v120, s94, v241\n"
"v_fma_f32 v121, v121, s94, v241\n"
"v_fma_f32 v122, v122, s94, v241\n"
"v_fma_f32 v123, v123, s94, v241\n"
"v_mul_f32 v120, v120, v76\n"
L_"v11,s[40:43],s86 offen offset:1056 lds\n"
"v_mul_f32 v121, v121, v77\n"
"v_mul_f32 v122, v122, v78\n"
"v_mov_b32 v44, -1\n"
"v_mov_b32 v45, -1\n"
"v_mov_b32 v46, -1\n"
"v_mov_b32 v47, -1\n"
W_"vmcnt(26)\n"
"s_barrier\n"
"s_add_u32 m0, s85, 25344\n"
"s_mov_b32 s86, 247808\n"
L_"v10,s[40:43],s86 offen lds\n"
"v_mul_f32 v123, v123, v79\n"
E_"v120,v120\n"
E_"v121,v121\n"
E_"v122,v122\n"
E_"v123,v123\n"
"v_add_f32 v120, 1.0, v120\n"
L_"v11,s[40:43],s86 offen offset:1056 lds\n"
"v_add_f32 v121, 1.0, v121\n"
"v_add_f32 v122, 1.0, v122\n"
"v_add_f32 v123, 1.0, v123\n"
"v_rcp_f32 v120, v120\n"
"v_rcp_f32 v121, v121\n"
"v_rcp_f32 v122, v122\n"
"s_add_u32 m0, s84, 109824\n"
"s_mov_b32 s86, 0\n"
L_"v124,s[56:59],s86 offen lds\n"
"v_rcp_f32 v123, v123\n"
"v_mul_f32 v76, v76, v120\n"
"v_mul_f32 v77, v77, v121\n"
"v_mul_f32 v78, v78, v122\n"
"v_mul_f32 v79, v79, v123\n"
C_"v68,v68,v69\n"
L_"v125,s[56:59],s86 offen offset:1024 lds\n"
C_"v69,v70,v71\n"
C_"v70,v76,v77\n"
C_"v71,v78,v79\n"
"s_setprio 2\n"
"ds_write_b128 v5, v[68:71] offset:9472\n"
"s_setprio 0\n"
"v_mul_f32 v120, v80, v80\n"
"v_mul_f32 v121, v81, v81\n"
"s_add_u32 m0, s84, 118272\n"
"s_mov_b32 s86, 57344\n"
L_"v124,s[56:59],s86 offen lds\n"
"v_mul_f32 v122, v82, v82\n"
"v_mul_f32 v123, v83, v83\n"
"v_fma_f32 v120, v120, s94, v241\n"
"v_fma_f32 v121, v121, s94, v241\n"
"v_fma_f32 v122, v122, s94, v241\n"
"v_fma_f32 v123, v123, s94, v241\n"
L_"v125,s[56:59],s86 offen offset:1024 lds\n"
"v_mul_f32 v120, v120, v80\n"
"v_mul_f32 v121, v121, v81\n"
"v_mul_f32 v122, v122, v82\n"
"v_mul_f32 v123, v123, v83\n"
E_"v120,v120\n"
E_"v121,v121\n"
"s_add_u32 m0, s84, 126720\n"
"s_mov_b32 s86, 114688\n"
L_"v124,s[56:59],s86 offen lds\n"
E_"v122,v122\n"
E_"v123,v123\n"
"v_add_f32 v120, 1.0, v120\n"
"v_add_f32 v121, 1.0, v121\n"
"v_add_f32 v122, 1.0, v122\n"
"v_add_f32 v123, 1.0, v123\n"
L_"v125,s[56:59],s86 offen offset:1024 lds\n"
"v_rcp_f32 v120, v120\n"
"v_rcp_f32 v121, v121\n"
"s_add_u32 s87, s90, 98304\n"
"buffer_store_dwordx4 v[44:47], v248, s[60:63], s87 offen sc1\n"
"buffer_load_dwordx2 v[8:9], v245, s[68:71], 0 offen\n"
W_"vmcnt(32) lgkmcnt(0)\n"
"s_barrier\n"
"s_add_u32 m0, s84, 135168\n"
"s_mov_b32 s86, 172032\n"
L_"v124,s[56:59],s86 offen lds\n"
"s_setprio 2\n"
R_"v[152:155],v6 offset:12544\n"
R_"v[156:159],v6 offset:13568\n"
R_"v[144:147],v6 offset:8448\n"
R_"v[148:151],v6 offset:9472\n"
"s_setprio 0\n"
W_"vmcnt(20)\n"
"v_not_b32 v127, v127\n"
"s_mov_b32 s4, 0\n"
"ACKL_H:\n"
"v_cmp_ne_u32 vcc, s8, v127\n"
"s_add_u32 s4, s4, 1\n"
"s_nop 3\n"
"s_cbranch_vccz ACKED_H\n"
"s_cmp_lt_u32 s4, 0x40000\n"
"s_cbranch_scc0 ACKED_H\n"
"s_sleep 2\n"
"buffer_load_dword v127, v126, s[80:83], s9 offen sc1\n"
W_"vmcnt(0)\n"
"v_not_b32 v127, v127\n"
"s_branch ACKL_H\n"
"ACKED_H:\n"
"v_rcp_f32 v122, v122\n"
"v_rcp_f32 v123, v123\n"
"v_mul_f32 v80, v80, v120\n"
"v_mul_f32 v81, v81, v121\n"
"v_mul_f32 v82, v82, v122\n"
"v_mul_f32 v83, v83, v123\n"
"v_mul_f32 v120, v88, v88\n"
"v_mul_f32 v121, v89, v89\n"
"v_mul_f32 v122, v90, v90\n"
"v_mul_f32 v123, v91, v91\n"
"v_fma_f32 v120, v120, s94, v241\n"
"v_fma_f32 v121, v121, s94, v241\n"
"v_fma_f32 v122, v122, s94, v241\n"
"v_fma_f32 v123, v123, s94, v241\n"
"v_mul_f32 v120, v120, v88\n"
"v_mul_f32 v121, v121, v89\n"
"v_mul_f32 v122, v122, v90\n"
"v_mul_f32 v123, v123, v91\n"
E_"v120,v120\n"
E_"v121,v121\n"
E_"v122,v122\n"
E_"v123,v123\n"
L_"v125,s[56:59],s86 offen offset:1024 lds\n"
"v_add_f32 v120, 1.0, v120\n"
"v_add_f32 v121, 1.0, v121\n"
"v_add_f32 v122, 1.0, v122\n"
"v_add_f32 v123, 1.0, v123\n"
"v_rcp_f32 v120, v120\n"
"v_rcp_f32 v121, v121\n"
"v_rcp_f32 v122, v122\n"
"v_rcp_f32 v123, v123\n"
"v_mul_f32 v88, v88, v120\n"
"v_mul_f32 v89, v89, v121\n"
"v_mul_f32 v90, v90, v122\n"
"v_mul_f32 v91, v91, v123\n"
C_"v80,v80,v81\n"
C_"v81,v82,v83\n"
C_"v82,v88,v89\n"
C_"v83,v90,v91\n"
"s_setprio 2\n"
"ds_write_b128 v5, v[80:83] offset:33792\n"
"s_setprio 0\n"
"v_mul_f32 v120, v84, v84\n"
"v_mul_f32 v121, v85, v85\n"
"v_mul_f32 v122, v86, v86\n"
"v_mul_f32 v123, v87, v87\n"
"v_fma_f32 v120, v120, s94, v241\n"
"v_fma_f32 v121, v121, s94, v241\n"
"s_add_u32 s87, s91, 327680\n"
"buffer_store_dwordx4 v[44:47], v248, s[60:63], s87 offen sc1\n"
"buffer_load_dwordx2 v[12:13], v245, s[68:71], 0 offen offset:256\n"
W_"lgkmcnt(0)\n"
"s_barrier\n"
"s_add_u32 m0, s84, 0\n"
"s_mov_b32 s86, 128\n"
L_"v124,s[56:59],s86 offen lds\n"
"v_fma_f32 v122, v122, s94, v241\n"
"v_fma_f32 v123, v123, s94, v241\n"
"v_mul_f32 v120, v120, v84\n"
"v_mul_f32 v121, v121, v85\n"
"v_mul_f32 v122, v122, v86\n"
"v_mul_f32 v123, v123, v87\n"
E_"v120,v120\n"
E_"v121,v121\n"
L_"v125,s[56:59],s86 offen offset:1024 lds\n"
E_"v122,v122\n"
E_"v123,v123\n"
"v_add_f32 v120, 1.0, v120\n"
"v_add_f32 v121, 1.0, v121\n"
"v_add_f32 v122, 1.0, v122\n"
"v_add_f32 v123, 1.0, v123\n"
"v_rcp_f32 v120, v120\n"
"v_rcp_f32 v121, v121\n"
"s_add_u32 m0, s84, 8448\n"
"s_mov_b32 s86, 57472\n"
L_"v124,s[56:59],s86 offen lds\n"
"v_rcp_f32 v122, v122\n"
"v_rcp_f32 v123, v123\n"
"v_mul_f32 v84, v84, v120\n"
"v_mul_f32 v85, v85, v121\n"
"v_mul_f32 v86, v86, v122\n"
"v_mul_f32 v87, v87, v123\n"
"v_mul_f32 v120, v92, v92\n"
"v_mul_f32 v121, v93, v93\n"
L_"v125,s[56:59],s86 offen offset:1024 lds\n"
"v_mul_f32 v122, v94, v94\n"
"v_mul_f32 v123, v95, v95\n"
"v_fma_f32 v120, v120, s94, v241\n"
"v_fma_f32 v121, v121, s94, v241\n"
"v_fma_f32 v122, v122, s94, v241\n"
"v_fma_f32 v123, v123, s94, v241\n"
"v_mul_f32 v120, v120, v92\n"
"v_mul_f32 v121, v121, v93\n"
"s_add_u32 m0, s84, 143616\n"
"s_mov_b32 s86, 114816\n"
L_"v124,s[56:59],s86 offen lds\n"
"v_mul_f32 v122, v122, v94\n"
"v_mul_f32 v123, v123, v95\n"
E_"v120,v120\n"
E_"v121,v121\n"
E_"v122,v122\n"
E_"v123,v123\n"
"v_add_f32 v120, 1.0, v120\n"
"v_add_f32 v121, 1.0, v121\n"
L_"v125,s[56:59],s86 offen offset:1024 lds\n"
"v_add_f32 v122, 1.0, v122\n"
"v_add_f32 v123, 1.0, v123\n"
"v_rcp_f32 v120, v120\n"
"v_rcp_f32 v121, v121\n"
"s_add_u32 s87, s90, 106496\n"
"buffer_store_dwordx4 v[44:47], v248, s[60:63], s87 offen sc1\n"
"buffer_load_dwordx2 v[14:15], v245, s[68:71], 0 offen offset:512\n"
"s_barrier\n"
"s_add_u32 m0, s84, 42240\n"
"s_mov_b32 s86, 172160\n"
L_"v124,s[56:59],s86 offen lds\n"
"v_rcp_f32 v122, v122\n"
"v_rcp_f32 v123, v123\n"
"v_mul_f32 v92, v92, v120\n"
"v_mul_f32 v93, v93, v121\n"
"v_mul_f32 v94, v94, v122\n"
"v_mul_f32 v95, v95, v123\n"
C_"v84,v84,v85\n"
C_"v85,v86,v87\n"
C_"v86,v92,v93\n"
C_"v87,v94,v95\n"
"s_setprio 2\n"
"ds_write_b128 v5, v[84:87] offset:34816\n"
"s_setprio 0\n"
"v_mul_f32 v120, v96, v96\n"
"v_mul_f32 v121, v97, v97\n"
L_"v125,s[56:59],s86 offen offset:1024 lds\n"
"v_mul_f32 v122, v98, v98\n"
"v_mul_f32 v123, v99, v99\n"
"v_fma_f32 v120, v120, s94, v241\n"
"v_fma_f32 v121, v121, s94, v241\n"
"v_fma_f32 v122, v122, s94, v241\n"
"v_fma_f32 v123, v123, s94, v241\n"
"v_mul_f32 v120, v120, v96\n"
"v_mul_f32 v121, v121, v97\n"
"v_mul_f32 v122, v122, v98\n"
"v_mul_f32 v123, v123, v99\n"
E_"v120,v120\n"
E_"v121,v121\n"
E_"v122,v122\n"
"s_add_u32 m0, s84, 59136\n"
"s_mov_b32 s86, 256\n"
L_"v124,s[56:59],s86 offen lds\n"
E_"v123,v123\n"
"v_add_f32 v120, 1.0, v120\n"
"v_add_f32 v121, 1.0, v121\n"
"v_add_f32 v122, 1.0, v122\n"
"v_add_f32 v123, 1.0, v123\n"
"v_rcp_f32 v120, v120\n"
"v_rcp_f32 v121, v121\n"
"v_rcp_f32 v122, v122\n"
"v_rcp_f32 v123, v123\n"
"v_mul_f32 v96, v96, v120\n"
"v_mul_f32 v97, v97, v121\n"
"v_mul_f32 v98, v98, v122\n"
"v_mul_f32 v99, v99, v123\n"
L_"v125,s[56:59],s86 offen offset:1024 lds\n"
"v_mul_f32 v120, v104, v104\n"
"v_mul_f32 v121, v105, v105\n"
"v_mul_f32 v122, v106, v106\n"
"v_mul_f32 v123, v107, v107\n"
"v_fma_f32 v120, v120, s94, v241\n"
"s_add_u32 s87, s91, 335872\n"
"buffer_store_dwordx4 v[44:47], v248, s[60:63], s87 offen sc1\n"
"buffer_load_dwordx2 v[126:127], v245, s[68:71], 0 offen offset:768\n"
"s_mul_i32 s87, s32, 56\n"
"v_add_u32 v24, s87, v240\n"
"v_lshlrev_b32 v24, 7, v24\n"
"s_bfm_b64 exec, 56, 0\n"
"buffer_load_dword v7, v24, s[48:51], 0 offen\n"
"s_mov_b64 exec, -1\n"
W_"vmcnt(35) lgkmcnt(0)\n"
"s_barrier\n"
"s_setprio 2\n"
R_"v[168:171],v6 offset:37888\n"
R_"v[172:175],v6 offset:38912\n"
R_"v[160:163],v6 offset:33792\n"
R_"v[164:167],v6 offset:34816\n"
"s_setprio 0\n"
"v_fma_f32 v121, v121, s94, v241\n"
"v_fma_f32 v122, v122, s94, v241\n"
"v_fma_f32 v123, v123, s94, v241\n"
"v_mul_f32 v120, v120, v104\n"
"v_mul_f32 v121, v121, v105\n"
"v_mul_f32 v122, v122, v106\n"
"v_mul_f32 v123, v123, v107\n"
E_"v120,v120\n"
E_"v121,v121\n"
E_"v122,v122\n"
E_"v123,v123\n"
"v_add_f32 v120, 1.0, v120\n"
"v_add_f32 v121, 1.0, v121\n"
"v_add_f32 v122, 1.0, v122\n"
"v_add_f32 v123, 1.0, v123\n"
"v_rcp_f32 v120, v120\n"
"v_rcp_f32 v121, v121\n"
"v_rcp_f32 v122, v122\n"
"v_rcp_f32 v123, v123\n"
"v_mul_f32 v104, v104, v120\n"
"v_mul_f32 v105, v105, v121\n"
"v_mul_f32 v106, v106, v122\n"
"v_mul_f32 v107, v107, v123\n"
C_"v96,v96,v97\n"
C_"v97,v98,v99\n"
C_"v98,v104,v105\n"
C_"v99,v106,v107\n"
"s_setprio 2\n"
"v_add_u32 v5, 0x10000, v5\n"
"ds_write_b128 v5, v[96:99] offset:2048\n"
"s_setprio 0\n"
"v_mul_f32 v120, v100, v100\n"
"v_mul_f32 v121, v101, v101\n"
"v_mul_f32 v122, v102, v102\n"
"v_mul_f32 v123, v103, v103\n"
"v_fma_f32 v120, v120, s94, v241\n"
"v_fma_f32 v121, v121, s94, v241\n"
"v_fma_f32 v122, v122, s94, v241\n"
"v_fma_f32 v123, v123, s94, v241\n"
"v_mul_f32 v120, v120, v100\n"
"v_mul_f32 v121, v121, v101\n"
"v_mul_f32 v122, v122, v102\n"
"v_mul_f32 v123, v123, v103\n"
E_"v120,v120\n"
E_"v121,v121\n"
E_"v122,v122\n"
E_"v123,v123\n"
"v_add_f32 v120, 1.0, v120\n"
W_"vmcnt(31) lgkmcnt(0)\n"
"s_barrier\n"
"v_add_f32 v121, 1.0, v121\n"
"v_add_f32 v122, 1.0, v122\n"
"v_add_f32 v123, 1.0, v123\n"
"v_rcp_f32 v120, v120\n"
"v_rcp_f32 v121, v121\n"
"v_rcp_f32 v122, v122\n"
"v_rcp_f32 v123, v123\n"
"v_mul_f32 v100, v100, v120\n"
"v_mul_f32 v101, v101, v121\n"
"v_mul_f32 v102, v102, v122\n"
"v_mul_f32 v103, v103, v123\n"
"v_mul_f32 v120, v108, v108\n"
"v_mul_f32 v121, v109, v109\n"
"v_mul_f32 v122, v110, v110\n"
"v_mul_f32 v123, v111, v111\n"
"v_fma_f32 v120, v120, s94, v241\n"
"v_fma_f32 v121, v121, s94, v241\n"
"v_fma_f32 v122, v122, s94, v241\n"
"v_fma_f32 v123, v123, s94, v241\n"
"v_mul_f32 v120, v120, v108\n"
"v_mul_f32 v121, v121, v109\n"
"v_mul_f32 v122, v122, v110\n"
"v_mul_f32 v123, v123, v111\n"
E_"v120,v120\n"
E_"v121,v121\n"
E_"v122,v122\n"
E_"v123,v123\n"
"v_add_f32 v120, 1.0, v120\n"
"v_add_f32 v121, 1.0, v121\n"
"v_add_f32 v122, 1.0, v122\n"
"v_add_f32 v123, 1.0, v123\n"
"v_rcp_f32 v120, v120\n"
"v_rcp_f32 v121, v121\n"
"v_rcp_f32 v122, v122\n"
"v_rcp_f32 v123, v123\n"
"v_mul_f32 v108, v108, v120\n"
"v_mul_f32 v109, v109, v121\n"
"v_mul_f32 v110, v110, v122\n"
"v_mul_f32 v111, v111, v123\n"
C_"v100,v100,v101\n"
C_"v101,v102,v103\n"
C_"v102,v108,v109\n"
C_"v103,v110,v111\n"
"s_setprio 2\n"
"ds_write_b128 v5, v[100:103] offset:3072\n"
W_"vmcnt(27) lgkmcnt(0)\n"
"s_barrier\n"
"s_add_u32 m0, s84, 16896\n"
"s_mov_b32 s86, 57600\n"
"s_setprio 0\n"
L_"v124,s[56:59],s86 offen lds\n"
L_"v125,s[56:59],s86 offen offset:1024 lds\n"
"s_setprio 2\n"
"v_add_u32 v6, 0x10000, v6\n"
R_"v[184:187],v6 offset:6144\n"
"s_add_u32 m0, s84, 33792\n"
"s_mov_b32 s86, 114944\n"
"s_setprio 0\n"
L_"v124,s[56:59],s86 offen lds\n"
L_"v125,s[56:59],s86 offen offset:1024 lds\n"
"s_setprio 2\n"
R_"v[188:191],v6 offset:7168\n"
"s_add_u32 m0, s84, 50688\n"
"s_mov_b32 s86, 172288\n"
"s_setprio 0\n"
L_"v124,s[56:59],s86 offen lds\n"
"s_setprio 2\n"
R_"v[176:179],v6 offset:2048\n"
"s_setprio 0\n"
L_"v125,s[56:59],s86 offen offset:1024 lds\n"
"s_add_u32 m0, s84, 76032\n"
"s_mov_b32 s86, 384\n"
L_"v124,s[56:59],s86 offen lds\n"
"s_setprio 2\n"
R_"v[180:183],v6 offset:3072\n"
"s_setprio 0\n"
L_"v125,s[56:59],s86 offen offset:1024 lds\n"
W_"lgkmcnt(0)\n"
"s_setprio 2\n"
W_"vmcnt(25)\n"
"s_barrier\n"
T_"v[16:17],v2 offset:44288\n"
T_"v[18:19],v2 offset:44544\n"
T_"v[20:21],v2 offset:44296\n"
T_"v[22:23],v2 offset:44552\n"
T_"v[24:25],v2 offset:45312\n"
T_"v[26:27],v2 offset:45568\n"
T_"v[28:29],v2 offset:45320\n"
T_"v[30:31],v2 offset:45576\n"
T_"v[32:33],v2 offset:52736\n"
T_"v[34:35],v2 offset:52992\n"
W_"lgkmcnt(8)\n"
M_"v[48:51],v[16:19],v[128:131],0\n"
"s_add_u32 m0, s84, 67584\n"
"s_mov_b32 s86, 57728\n"
T_"v[36:37],v2 offset:52744\n"
M_"v[52:55],v[16:19],v[132:135],0\n"
W_"lgkmcnt(7)\n"
T_"v[38:39],v2 offset:53000\n"
L_"v124,s[56:59],s86 offen lds\n"
M_"v[56:59],v[20:23],v[128:131],0\n"
T_"v[40:41],v2 offset:53760\n"
M_"v[60:63],v[20:23],v[132:135],0\n"
W_"lgkmcnt(7)\n"
T_"v[42:43],v2 offset:54016\n"
L_"v125,s[56:59],s86 offen offset:1024 lds\n"
M_"v[48:51],v[24:27],v[136:139],v[48:51]\n"
"s_add_u32 m0, s84, 84480\n"
"s_mov_b32 s86, 115072\n"
T_"v[44:45],v2 offset:53768\n"
M_"v[52:55],v[24:27],v[140:143],v[52:55]\n"
W_"lgkmcnt(4)\n"
T_"v[46:47],v2 offset:54024\n"
L_"v124,s[56:59],s86 offen lds\n"
M_"v[56:59],v[28:31],v[136:139],v[56:59]\n"
T_"v[16:17],v2 offset:61184\n"
M_"v[60:63],v[28:31],v[140:143],v[60:63]\n"
T_"v[18:19],v2 offset:61440\n"
L_"v125,s[56:59],s86 offen offset:1024 lds\n"
M_"v[48:51],v[32:35],v[144:147],v[48:51]\n"
"s_add_u32 m0, s84, 92928\n"
"s_mov_b32 s86, 172416\n"
T_"v[20:21],v2 offset:61192\n"
M_"v[52:55],v[32:35],v[148:151],v[52:55]\n"
W_"lgkmcnt(4)\n"
T_"v[22:23],v2 offset:61448\n"
L_"v124,s[56:59],s86 offen lds\n"
M_"v[56:59],v[36:39],v[144:147],v[56:59]\n"
T_"v[24:25],v2 offset:62208\n"
M_"v[60:63],v[36:39],v[148:151],v[60:63]\n"
T_"v[26:27],v2 offset:62464\n"
M_"v[48:51],v[40:43],v[152:155],v[48:51]\n"
T_"v[28:29],v2 offset:62216\n"
L_"v125,s[56:59],s86 offen offset:1024 lds\n"
M_"v[52:55],v[40:43],v[156:159],v[52:55]\n"
W_"lgkmcnt(4)\n"
T_"v[30:31],v2 offset:62472\n"
M_"v[56:59],v[44:47],v[152:155],v[56:59]\n"
"s_add_u32 m0, s84, 101376\n"
"s_mov_b32 s86, 512\n"
T_"v[32:33],v3 offset:4096\n"
L_"v124,s[56:59],s86 offen lds\n"
M_"v[60:63],v[44:47],v[156:159],v[60:63]\n"
T_"v[34:35],v3 offset:4352\n"
M_"v[48:51],v[16:19],v[160:163],v[48:51]\n"
T_"v[36:37],v3 offset:4104\n"
L_"v125,s[56:59],s86 offen offset:1024 lds\n"
M_"v[52:55],v[16:19],v[164:167],v[52:55]\n"
W_"lgkmcnt(4)\n"
T_"v[38:39],v3 offset:4360\n"
M_"v[56:59],v[20:23],v[160:163],v[56:59]\n"
"s_add_u32 m0, s84, 152064\n"
"s_mov_b32 s86, 57856\n"
T_"v[40:41],v3 offset:5120\n"
L_"v124,s[56:59],s86 offen lds\n"
M_"v[60:63],v[20:23],v[164:167],v[60:63]\n"
T_"v[42:43],v3 offset:5376\n"
M_"v[48:51],v[24:27],v[168:171],v[48:51]\n"
T_"v[44:45],v3 offset:5128\n"
L_"v125,s[56:59],s86 offen offset:1024 lds\n"
M_"v[52:55],v[24:27],v[172:175],v[52:55]\n"
W_"lgkmcnt(4)\n"
T_"v[46:47],v3 offset:5384\n"
M_"v[56:59],v[28:31],v[168:171],v[56:59]\n"
W_"vmcnt(23)\n"
"s_barrier\n"
T_"v[16:17],v1 offset:0\n"
M_"v[60:63],v[28:31],v[172:175],v[60:63]\n"
"s_add_u32 m0, s84, 25344\n"
T_"v[18:19],v1 offset:256\n"
M_"v[48:51],v[32:35],v[176:179],v[48:51]\n"
"s_mov_b32 s86, 115200\n"
T_"v[20:21],v1 offset:8\n"
L_"v124,s[56:59],s86 offen lds\n"
M_"v[52:55],v[32:35],v[180:183],v[52:55]\n"
W_"lgkmcnt(4)\n"
T_"v[22:23],v1 offset:264\n"
M_"v[56:59],v[36:39],v[176:179],v[56:59]\n"
T_"v[24:25],v1 offset:1024\n"
M_"v[60:63],v[36:39],v[180:183],v[60:63]\n"
T_"v[26:27],v1 offset:1280\n"
L_"v125,s[56:59],s86 offen offset:1024 lds\n"
M_"v[48:51],v[40:43],v[184:187],v[48:51]\n"
T_"v[28:29],v1 offset:1032\n"
M_"v[52:55],v[40:43],v[188:191],v[52:55]\n"
W_"lgkmcnt(4)\n"
T_"v[30:31],v1 offset:1288\n"
M_"v[56:59],v[44:47],v[184:187],v[56:59]\n"
"s_add_u32 m0, s84, 109824\n"
T_"v[32:33],v1 offset:8448\n"
M_"v[60:63],v[44:47],v[188:191],v[60:63]\n"
"s_mov_b32 s86, 172544\n"
T_"v[34:35],v1 offset:8704\n"
L_"v124,s[56:59],s86 offen lds\n"
M_"v[64:67],v[16:19],v[128:131],0\n"
T_"v[36:37],v1 offset:8456\n"
M_"v[68:71],v[16:19],v[132:135],0\n"
W_"lgkmcnt(4)\n"
T_"v[38:39],v1 offset:8712\n"
M_"v[72:75],v[20:23],v[128:131],0\n"
T_"v[40:41],v1 offset:9472\n"
M_"v[76:79],v[20:23],v[132:135],0\n"
T_"v[42:43],v1 offset:9728\n"
L_"v125,s[56:59],s86 offen offset:1024 lds\n"
M_"v[64:67],v[24:27],v[136:139],v[64:67]\n"
T_"v[44:45],v1 offset:9480\n"
C_"v192,v48,v49\n"
M_"v[68:71],v[24:27],v[140:143],v[68:71]\n"
W_"lgkmcnt(4)\n"
T_"v[46:47],v1 offset:9736\n"
C_"v193,v50,v51\n"
M_"v[72:75],v[28:31],v[136:139],v[72:75]\n"
"s_add_u32 m0, s84, 118272\n"
T_"v[16:17],v3 offset:12544\n"
C_"v194,v56,v57\n"
M_"v[76:79],v[28:31],v[140:143],v[76:79]\n"
"s_mov_b32 s86, 640\n"
T_"v[18:19],v3 offset:12800\n"
L_"v124,s[56:59],s86 offen lds\n"
C_"v195,v58,v59\n"
M_"v[64:67],v[32:35],v[144:147],v[64:67]\n"
T_"v[20:21],v3 offset:12552\n"
C_"v196,v52,v53\n"
M_"v[68:71],v[32:35],v[148:151],v[68:71]\n"
W_"lgkmcnt(4)\n"
T_"v[22:23],v3 offset:12808\n"
C_"v197,v54,v55\n"
M_"v[72:75],v[36:39],v[144:147],v[72:75]\n"
T_"v[24:25],v3 offset:13568\n"
C_"v198,v60,v61\n"
M_"v[76:79],v[36:39],v[148:151],v[76:79]\n"
T_"v[26:27],v3 offset:13824\n"
C_"v199,v62,v63\n"
M_"v[64:67],v[40:43],v[152:155],v[64:67]\n"
T_"v[28:29],v3 offset:13576\n"
L_"v125,s[56:59],s86 offen offset:1024 lds\n"
M_"v[68:71],v[40:43],v[156:159],v[68:71]\n"
W_"lgkmcnt(4)\n"
T_"v[30:31],v3 offset:13832\n"
M_"v[72:75],v[44:47],v[152:155],v[72:75]\n"
"s_add_u32 m0, s84, 126720\n"
T_"v[32:33],v1 offset:42240\n"
M_"v[76:79],v[44:47],v[156:159],v[76:79]\n"
"s_mov_b32 s86, 57984\n"
T_"v[34:35],v1 offset:42496\n"
L_"v124,s[56:59],s86 offen lds\n"
M_"v[64:67],v[16:19],v[160:163],v[64:67]\n"
T_"v[36:37],v1 offset:42248\n"
M_"v[68:71],v[16:19],v[164:167],v[68:71]\n"
W_"lgkmcnt(4)\n"
T_"v[38:39],v1 offset:42504\n"
M_"v[72:75],v[20:23],v[160:163],v[72:75]\n"
T_"v[40:41],v1 offset:43264\n"
M_"v[76:79],v[20:23],v[164:167],v[76:79]\n"
T_"v[42:43],v1 offset:43520\n"
L_"v125,s[56:59],s86 offen offset:1024 lds\n"
M_"v[64:67],v[24:27],v[168:171],v[64:67]\n"
T_"v[44:45],v1 offset:43272\n"
M_"v[68:71],v[24:27],v[172:175],v[68:71]\n"
W_"lgkmcnt(4)\n"
T_"v[46:47],v1 offset:43528\n"
M_"v[72:75],v[28:31],v[168:171],v[72:75]\n"
W_"vmcnt(20)\n"
"s_barrier\n"
T_"v[16:17],v1 offset:59136\n"
M_"v[76:79],v[28:31],v[172:175],v[76:79]\n"
"s_add_u32 m0, s84, 0\n"
T_"v[18:19],v1 offset:59392\n"
M_"v[64:67],v[32:35],v[176:179],v[64:67]\n"
"s_mov_b32 s86, 115328\n"
T_"v[20:21],v1 offset:59144\n"
L_"v124,s[56:59],s86 offen lds\n"
M_"v[68:71],v[32:35],v[180:183],v[68:71]\n"
W_"lgkmcnt(4)\n"
T_"v[22:23],v1 offset:59400\n"
"PTH3:\n"
"s_cbranch_execz PTH11\n"
M_"v[72:75],v[36:39],v[176:179],v[72:75]\n"
T_"v[24:25],v1 offset:60160\n"
M_"v[76:79],v[36:39],v[180:183],v[76:79]\n"
T_"v[26:27],v1 offset:60416\n"
L_"v125,s[56:59],s86 offen offset:1024 lds\n"
M_"v[64:67],v[40:43],v[184:187],v[64:67]\n"
T_"v[28:29],v1 offset:60168\n"
M_"v[68:71],v[40:43],v[188:191],v[68:71]\n"
W_"lgkmcnt(4)\n"
T_"v[30:31],v1 offset:60424\n"
M_"v[72:75],v[44:47],v[184:187],v[72:75]\n"
"s_add_u32 m0, s84, 8448\n"
T_"v[32:33],v1 offset:16896\n"
M_"v[76:79],v[44:47],v[188:191],v[76:79]\n"
"s_mov_b32 s86, 172672\n"
T_"v[34:35],v1 offset:17152\n"
L_"v124,s[56:59],s86 offen lds\n"
M_"v[48:51],v[16:19],v[128:131],0\n"
T_"v[36:37],v1 offset:16904\n"
M_"v[52:55],v[16:19],v[132:135],0\n"
W_"lgkmcnt(4)\n"
T_"v[38:39],v1 offset:17160\n"
M_"v[56:59],v[20:23],v[128:131],0\n"
T_"v[40:41],v1 offset:17920\n"
M_"v[60:63],v[20:23],v[132:135],0\n"
T_"v[42:43],v1 offset:18176\n"
L_"v125,s[56:59],s86 offen offset:1024 lds\n"
M_"v[48:51],v[24:27],v[136:139],v[48:51]\n"
T_"v[44:45],v1 offset:17928\n"
C_"v200,v64,v65\n"
M_"v[52:55],v[24:27],v[140:143],v[52:55]\n"
W_"lgkmcnt(4)\n"
T_"v[46:47],v1 offset:18184\n"
C_"v201,v66,v67\n"
M_"v[56:59],v[28:31],v[136:139],v[56:59]\n"
"s_add_u32 m0, s84, 135168\n"
T_"v[16:17],v1 offset:33792\n"
C_"v202,v72,v73\n"
M_"v[60:63],v[28:31],v[140:143],v[60:63]\n"
"s_mov_b32 s86, 768\n"
T_"v[18:19],v1 offset:34048\n"
L_"v124,s[56:59],s86 offen lds\n"
C_"v203,v74,v75\n"
M_"v[48:51],v[32:35],v[144:147],v[48:51]\n"
T_"v[20:21],v1 offset:33800\n"
C_"v204,v68,v69\n"
M_"v[52:55],v[32:35],v[148:151],v[52:55]\n"
W_"lgkmcnt(4)\n"
T_"v[22:23],v1 offset:34056\n"
C_"v205,v70,v71\n"
M_"v[56:59],v[36:39],v[144:147],v[56:59]\n"
T_"v[24:25],v1 offset:34816\n"
C_"v206,v76,v77\n"
M_"v[60:63],v[36:39],v[148:151],v[60:63]\n"
T_"v[26:27],v1 offset:35072\n"
C_"v207,v78,v79\n"
M_"v[48:51],v[40:43],v[152:155],v[48:51]\n"
T_"v[28:29],v1 offset:34824\n"
L_"v125,s[56:59],s86 offen offset:1024 lds\n"
M_"v[52:55],v[40:43],v[156:159],v[52:55]\n"
W_"lgkmcnt(4)\n"
T_"v[30:31],v1 offset:35080\n"
M_"v[56:59],v[44:47],v[152:155],v[56:59]\n"
"s_add_u32 m0, s84, 143616\n"
T_"v[32:33],v1 offset:50688\n"
M_"v[60:63],v[44:47],v[156:159],v[60:63]\n"
"s_mov_b32 s86, 58112\n"
T_"v[34:35],v1 offset:50944\n"
L_"v124,s[56:59],s86 offen lds\n"
M_"v[48:51],v[16:19],v[160:163],v[48:51]\n"
T_"v[36:37],v1 offset:50696\n"
M_"v[52:55],v[16:19],v[164:167],v[52:55]\n"
W_"lgkmcnt(4)\n"
T_"v[38:39],v1 offset:50952\n"
M_"v[56:59],v[20:23],v[160:163],v[56:59]\n"
T_"v[40:41],v1 offset:51712\n"
M_"v[60:63],v[20:23],v[164:167],v[60:63]\n"
T_"v[42:43],v1 offset:51968\n"
L_"v125,s[56:59],s86 offen offset:1024 lds\n"
M_"v[48:51],v[24:27],v[168:171],v[48:51]\n"
T_"v[44:45],v1 offset:51720\n"
M_"v[52:55],v[24:27],v[172:175],v[52:55]\n"
W_"lgkmcnt(4)\n"
T_"v[46:47],v1 offset:51976\n"
M_"v[56:59],v[28:31],v[168:171],v[56:59]\n"
W_"vmcnt(20)\n"
"s_barrier\n"
T_"v[16:17],v2 offset:10496\n"
M_"v[60:63],v[28:31],v[172:175],v[60:63]\n"
"s_add_u32 m0, s84, 16896\n"
T_"v[18:19],v2 offset:10752\n"
M_"v[48:51],v[32:35],v[176:179],v[48:51]\n"
"s_mov_b32 s86, 115456\n"
T_"v[20:21],v2 offset:10504\n"
L_"v124,s[56:59],s86 offen lds\n"
M_"v[52:55],v[32:35],v[180:183],v[52:55]\n"
W_"lgkmcnt(4)\n"
T_"v[22:23],v2 offset:10760\n"
M_"v[56:59],v[36:39],v[176:179],v[56:59]\n"
T_"v[24:25],v2 offset:11520\n"
M_"v[60:63],v[36:39],v[180:183],v[60:63]\n"
T_"v[26:27],v2 offset:11776\n"
L_"v125,s[56:59],s86 offen offset:1024 lds\n"
M_"v[48:51],v[40:43],v[184:187],v[48:51]\n"
T_"v[28:29],v2 offset:11528\n"
M_"v[52:55],v[40:43],v[188:191],v[52:55]\n"
W_"lgkmcnt(4)\n"
T_"v[30:31],v2 offset:11784\n"
M_"v[56:59],v[44:47],v[184:187],v[56:59]\n"
"s_add_u32 m0, s84, 33792\n"
T_"v[32:33],v2 offset:2048\n"
M_"v[60:63],v[44:47],v[188:191],v[60:63]\n"
"s_mov_b32 s86, 172800\n"
T_"v[34:35],v2 offset:2304\n"
L_"v124,s[56:59],s86 offen lds\n"
M_"v[64:67],v[16:19],v[128:131],0\n"
T_"v[36:37],v2 offset:2056\n"
M_"v[68:71],v[16:19],v[132:135],0\n"
W_"lgkmcnt(4)\n"
T_"v[38:39],v2 offset:2312\n"
M_"v[72:75],v[20:23],v[128:131],0\n"
T_"v[40:41],v2 offset:3072\n"
M_"v[76:79],v[20:23],v[132:135],0\n"
T_"v[42:43],v2 offset:3328\n"
L_"v125,s[56:59],s86 offen offset:1024 lds\n"
M_"v[64:67],v[24:27],v[136:139],v[64:67]\n"
T_"v[44:45],v2 offset:3080\n"
C_"v208,v48,v49\n"
M_"v[68:71],v[24:27],v[140:143],v[68:71]\n"
W_"lgkmcnt(4)\n"
T_"v[46:47],v2 offset:3336\n"
C_"v209,v50,v51\n"
M_"v[72:75],v[28:31],v[136:139],v[72:75]\n"
"s_add_u32 m0, s84, 42240\n"
T_"v[16:17],v2 offset:18944\n"
C_"v210,v56,v57\n"
M_"v[76:79],v[28:31],v[140:143],v[76:79]\n"
"s_mov_b32 s86, 0\n"
T_"v[18:19],v2 offset:19200\n"
L_"v246,s[44:47],s86 offen lds\n"
C_"v211,v58,v59\n"
M_"v[64:67],v[32:35],v[144:147],v[64:67]\n"
T_"v[20:21],v2 offset:18952\n"
C_"v212,v52,v53\n"
M_"v[68:71],v[32:35],v[148:151],v[68:71]\n"
W_"lgkmcnt(4)\n"
T_"v[22:23],v2 offset:19208\n"
C_"v213,v54,v55\n"
M_"v[72:75],v[36:39],v[144:147],v[72:75]\n"
T_"v[24:25],v2 offset:19968\n"
C_"v214,v60,v61\n"
M_"v[76:79],v[36:39],v[148:151],v[76:79]\n"
T_"v[26:27],v2 offset:20224\n"
C_"v215,v62,v63\n"
M_"v[64:67],v[40:43],v[152:155],v[64:67]\n"
T_"v[28:29],v2 offset:19976\n"
L_"v247,s[44:47],s86 offen offset:1024 lds\n"
M_"v[68:71],v[40:43],v[156:159],v[68:71]\n"
W_"lgkmcnt(4)\n"
T_"v[30:31],v2 offset:20232\n"
M_"v[72:75],v[44:47],v[152:155],v[72:75]\n"
"s_add_u32 m0, s84, 59136\n"
T_"v[32:33],v2 offset:27392\n"
M_"v[76:79],v[44:47],v[156:159],v[76:79]\n"
"s_mov_b32 s86, 16384\n"
T_"v[34:35],v2 offset:27648\n"
L_"v246,s[44:47],s86 offen lds\n"
M_"v[64:67],v[16:19],v[160:163],v[64:67]\n"
T_"v[36:37],v2 offset:27400\n"
M_"v[68:71],v[16:19],v[164:167],v[68:71]\n"
W_"lgkmcnt(4)\n"
T_"v[38:39],v2 offset:27656\n"
M_"v[72:75],v[20:23],v[160:163],v[72:75]\n"
T_"v[40:41],v2 offset:28416\n"
M_"v[76:79],v[20:23],v[164:167],v[76:79]\n"
T_"v[42:43],v2 offset:28672\n"
L_"v247,s[44:47],s86 offen offset:1024 lds\n"
M_"v[64:67],v[24:27],v[168:171],v[64:67]\n"
T_"v[44:45],v2 offset:28424\n"
M_"v[68:71],v[24:27],v[172:175],v[68:71]\n"
W_"lgkmcnt(4)\n"
T_"v[46:47],v2 offset:28680\n"
M_"v[72:75],v[28:31],v[168:171],v[72:75]\n"
W_"vmcnt(20)\n"
"s_barrier\n"
T_"v[16:17],v2 offset:35840\n"
M_"v[76:79],v[28:31],v[172:175],v[76:79]\n"
"s_add_u32 m0, s84, 50688\n"
T_"v[18:19],v2 offset:36096\n"
M_"v[64:67],v[32:35],v[176:179],v[64:67]\n"
"s_mov_b32 s86, 32768\n"
T_"v[20:21],v2 offset:35848\n"
L_"v246,s[44:47],s86 offen lds\n"
M_"v[68:71],v[32:35],v[180:183],v[68:71]\n"
W_"lgkmcnt(4)\n"
T_"v[22:23],v2 offset:36104\n"
M_"v[72:75],v[36:39],v[176:179],v[72:75]\n"
T_"v[24:25],v2 offset:36864\n"
M_"v[76:79],v[36:39],v[180:183],v[76:79]\n"
T_"v[26:27],v2 offset:37120\n"
L_"v247,s[44:47],s86 offen offset:1024 lds\n"
M_"v[64:67],v[40:43],v[184:187],v[64:67]\n"
T_"v[28:29],v2 offset:36872\n"
M_"v[68:71],v[40:43],v[188:191],v[68:71]\n"
W_"lgkmcnt(4)\n"
T_"v[30:31],v2 offset:37128\n"
M_"v[72:75],v[44:47],v[184:187],v[72:75]\n"
"s_add_u32 m0, s84, 67584\n"
T_"v[32:33],v3 offset:20992\n"
M_"v[76:79],v[44:47],v[188:191],v[76:79]\n"
"s_mov_b32 s86, 49152\n"
T_"v[34:35],v3 offset:21248\n"
L_"v246,s[44:47],s86 offen lds\n"
M_"v[48:51],v[16:19],v[128:131],0\n"
T_"v[36:37],v3 offset:21000\n"
M_"v[52:55],v[16:19],v[132:135],0\n"
W_"lgkmcnt(4)\n"
T_"v[38:39],v3 offset:21256\n"
M_"v[56:59],v[20:23],v[128:131],0\n"
T_"v[40:41],v3 offset:22016\n"
M_"v[60:63],v[20:23],v[132:135],0\n"
T_"v[42:43],v3 offset:22272\n"
L_"v247,s[44:47],s86 offen offset:1024 lds\n"
M_"v[48:51],v[24:27],v[136:139],v[48:51]\n"
T_"v[44:45],v3 offset:22024\n"
C_"v216,v64,v65\n"
M_"v[52:55],v[24:27],v[140:143],v[52:55]\n"
W_"lgkmcnt(4)\n"
T_"v[46:47],v3 offset:22280\n"
C_"v217,v66,v67\n"
M_"v[56:59],v[28:31],v[136:139],v[56:59]\n"
"s_add_u32 m0, s84, 76032\n"
T_"v[16:17],v1 offset:25344\n"
C_"v218,v72,v73\n"
M_"v[60:63],v[28:31],v[140:143],v[60:63]\n"
"s_mov_b32 s86, 65536\n"
T_"v[18:19],v1 offset:25600\n"
L_"v246,s[44:47],s86 offen lds\n"
C_"v219,v74,v75\n"
M_"v[48:51],v[32:35],v[144:147],v[48:51]\n"
T_"v[20:21],v1 offset:25352\n"
C_"v220,v68,v69\n"
M_"v[52:55],v[32:35],v[148:151],v[52:55]\n"
W_"lgkmcnt(4)\n"
T_"v[22:23],v1 offset:25608\n"
C_"v221,v70,v71\n"
M_"v[56:59],v[36:39],v[144:147],v[56:59]\n"
T_"v[24:25],v1 offset:26368\n"
C_"v222,v76,v77\n"
M_"v[60:63],v[36:39],v[148:151],v[60:63]\n"
T_"v[26:27],v1 offset:26624\n"
C_"v223,v78,v79\n"
M_"v[48:51],v[40:43],v[152:155],v[48:51]\n"
T_"v[28:29],v1 offset:26376\n"
L_"v247,s[44:47],s86 offen offset:1024 lds\n"
M_"v[52:55],v[40:43],v[156:159],v[52:55]\n"
W_"lgkmcnt(4)\n"
T_"v[30:31],v1 offset:26632\n"
M_"v[56:59],v[44:47],v[152:155],v[56:59]\n"
"s_add_u32 m0, s84, 84480\n"
T_"v[32:33],v2 offset:44288\n"
M_"v[60:63],v[44:47],v[156:159],v[60:63]\n"
"s_mov_b32 s86, 81920\n"
T_"v[34:35],v2 offset:44544\n"
L_"v246,s[44:47],s86 offen lds\n"
M_"v[48:51],v[16:19],v[160:163],v[48:51]\n"
T_"v[36:37],v2 offset:44296\n"
M_"v[52:55],v[16:19],v[164:167],v[52:55]\n"
W_"lgkmcnt(4)\n"
T_"v[38:39],v2 offset:44552\n"
M_"v[56:59],v[20:23],v[160:163],v[56:59]\n"
T_"v[40:41],v2 offset:45312\n"
M_"v[60:63],v[20:23],v[164:167],v[60:63]\n"
T_"v[42:43],v2 offset:45568\n"
L_"v247,s[44:47],s86 offen offset:1024 lds\n"
M_"v[48:51],v[24:27],v[168:171],v[48:51]\n"
T_"v[44:45],v2 offset:45320\n"
M_"v[52:55],v[24:27],v[172:175],v[52:55]\n"
W_"lgkmcnt(4)\n"
T_"v[46:47],v2 offset:45576\n"
M_"v[56:59],v[28:31],v[168:171],v[56:59]\n"
W_"vmcnt(20)\n"
"s_barrier\n"
T_"v[16:17],v2 offset:52736\n"
M_"v[60:63],v[28:31],v[172:175],v[60:63]\n"
"s_add_u32 m0, s84, 25344\n"
T_"v[18:19],v2 offset:52992\n"
M_"v[48:51],v[32:35],v[176:179],v[48:51]\n"
"s_mov_b32 s86, 98304\n"
T_"v[20:21],v2 offset:52744\n"
L_"v246,s[44:47],s86 offen lds\n"
M_"v[52:55],v[32:35],v[180:183],v[52:55]\n"
W_"lgkmcnt(4)\n"
T_"v[22:23],v2 offset:53000\n"
M_"v[56:59],v[36:39],v[176:179],v[56:59]\n"
T_"v[24:25],v2 offset:53760\n"
M_"v[60:63],v[36:39],v[180:183],v[60:63]\n"
T_"v[26:27],v2 offset:54016\n"
L_"v247,s[44:47],s86 offen offset:1024 lds\n"
M_"v[48:51],v[40:43],v[184:187],v[48:51]\n"
T_"v[28:29],v2 offset:53768\n"
M_"v[52:55],v[40:43],v[188:191],v[52:55]\n"
W_"lgkmcnt(4)\n"
T_"v[30:31],v2 offset:54024\n"
M_"v[56:59],v[44:47],v[184:187],v[56:59]\n"
"s_add_u32 m0, s84, 92928\n"
T_"v[32:33],v2 offset:61184\n"
M_"v[60:63],v[44:47],v[188:191],v[60:63]\n"
"s_mov_b32 s86, 114688\n"
T_"v[34:35],v2 offset:61440\n"
L_"v246,s[44:47],s86 offen lds\n"
M_"v[64:67],v[16:19],v[128:131],0\n"
T_"v[36:37],v2 offset:61192\n"
M_"v[68:71],v[16:19],v[132:135],0\n"
W_"lgkmcnt(4)\n"
T_"v[38:39],v2 offset:61448\n"
M_"v[72:75],v[20:23],v[128:131],0\n"
T_"v[40:41],v2 offset:62208\n"
M_"v[76:79],v[20:23],v[132:135],0\n"
T_"v[42:43],v2 offset:62464\n"
L_"v247,s[44:47],s86 offen offset:1024 lds\n"
M_"v[64:67],v[24:27],v[136:139],v[64:67]\n"
T_"v[44:45],v2 offset:62216\n"
C_"v224,v48,v49\n"
M_"v[68:71],v[24:27],v[140:143],v[68:71]\n"
W_"lgkmcnt(4)\n"
T_"v[46:47],v2 offset:62472\n"
C_"v225,v50,v51\n"
M_"v[72:75],v[28:31],v[136:139],v[72:75]\n"
"s_add_u32 m0, s84, 101376\n"
T_"v[16:17],v1 offset:0\n"
C_"v226,v56,v57\n"
M_"v[76:79],v[28:31],v[140:143],v[76:79]\n"
"s_mov_b32 s86, 131072\n"
T_"v[18:19],v1 offset:256\n"
L_"v246,s[44:47],s86 offen lds\n"
C_"v227,v58,v59\n"
M_"v[64:67],v[32:35],v[144:147],v[64:67]\n"
T_"v[20:21],v1 offset:8\n"
C_"v228,v52,v53\n"
M_"v[68:71],v[32:35],v[148:151],v[68:71]\n"
W_"lgkmcnt(4)\n"
T_"v[22:23],v1 offset:264\n"
C_"v229,v54,v55\n"
M_"v[72:75],v[36:39],v[144:147],v[72:75]\n"
T_"v[24:25],v1 offset:1024\n"
C_"v230,v60,v61\n"
M_"v[76:79],v[36:39],v[148:151],v[76:79]\n"
T_"v[26:27],v1 offset:1280\n"
C_"v231,v62,v63\n"
M_"v[64:67],v[40:43],v[152:155],v[64:67]\n"
T_"v[28:29],v1 offset:1032\n"
L_"v247,s[44:47],s86 offen offset:1024 lds\n"
M_"v[68:71],v[40:43],v[156:159],v[68:71]\n"
W_"lgkmcnt(4)\n"
T_"v[30:31],v1 offset:1288\n"
M_"v[72:75],v[44:47],v[152:155],v[72:75]\n"
"s_add_u32 m0, s84, 152064\n"
T_"v[32:33],v1 offset:8448\n"
M_"v[76:79],v[44:47],v[156:159],v[76:79]\n"
"s_mov_b32 s86, 147456\n"
T_"v[34:35],v1 offset:8704\n"
L_"v246,s[44:47],s86 offen lds\n"
M_"v[64:67],v[16:19],v[160:163],v[64:67]\n"
T_"v[36:37],v1 offset:8456\n"
M_"v[68:71],v[16:19],v[164:167],v[68:71]\n"
W_"lgkmcnt(4)\n"
T_"v[38:39],v1 offset:8712\n"
M_"v[72:75],v[20:23],v[160:163],v[72:75]\n"
T_"v[40:41],v1 offset:9472\n"
M_"v[76:79],v[20:23],v[164:167],v[76:79]\n"
T_"v[42:43],v1 offset:9728\n"
L_"v247,s[44:47],s86 offen offset:1024 lds\n"
M_"v[64:67],v[24:27],v[168:171],v[64:67]\n"
T_"v[44:45],v1 offset:9480\n"
M_"v[68:71],v[24:27],v[172:175],v[68:71]\n"
W_"lgkmcnt(4)\n"
T_"v[46:47],v1 offset:9736\n"
M_"v[72:75],v[28:31],v[168:171],v[72:75]\n"
W_"vmcnt(20)\n"
"s_barrier\n"
T_"v[16:17],v3 offset:4096\n"
M_"v[76:79],v[28:31],v[172:175],v[76:79]\n"
"s_add_u32 m0, s84, 0\n"
T_"v[18:19],v3 offset:4352\n"
M_"v[64:67],v[32:35],v[176:179],v[64:67]\n"
"s_mov_b32 s86, 163840\n"
T_"v[20:21],v3 offset:4104\n"
L_"v246,s[44:47],s86 offen lds\n"
M_"v[68:71],v[32:35],v[180:183],v[68:71]\n"
W_"lgkmcnt(4)\n"
T_"v[22:23],v3 offset:4360\n"
M_"v[72:75],v[36:39],v[176:179],v[72:75]\n"
T_"v[24:25],v3 offset:5120\n"
M_"v[76:79],v[36:39],v[180:183],v[76:79]\n"
T_"v[26:27],v3 offset:5376\n"
L_"v247,s[44:47],s86 offen offset:1024 lds\n"
M_"v[64:67],v[40:43],v[184:187],v[64:67]\n"
T_"v[28:29],v3 offset:5128\n"
M_"v[68:71],v[40:43],v[188:191],v[68:71]\n"
W_"lgkmcnt(4)\n"
T_"v[30:31],v3 offset:5384\n"
M_"v[72:75],v[44:47],v[184:187],v[72:75]\n"
"s_add_u32 m0, s84, 109824\n"
T_"v[32:33],v3 offset:12544\n"
M_"v[76:79],v[44:47],v[188:191],v[76:79]\n"
"s_mov_b32 s86, 180224\n"
T_"v[34:35],v3 offset:12800\n"
L_"v246,s[44:47],s86 offen lds\n"
M_"v[48:51],v[16:19],v[128:131],0\n"
T_"v[36:37],v3 offset:12552\n"
M_"v[52:55],v[16:19],v[132:135],0\n"
W_"lgkmcnt(4)\n"
T_"v[38:39],v3 offset:12808\n"
M_"v[56:59],v[20:23],v[128:131],0\n"
T_"v[40:41],v3 offset:13568\n"
M_"v[60:63],v[20:23],v[132:135],0\n"
T_"v[42:43],v3 offset:13824\n"
L_"v247,s[44:47],s86 offen offset:1024 lds\n"
M_"v[48:51],v[24:27],v[136:139],v[48:51]\n"
T_"v[44:45],v3 offset:13576\n"
C_"v232,v64,v65\n"
M_"v[52:55],v[24:27],v[140:143],v[52:55]\n"
W_"lgkmcnt(4)\n"
T_"v[46:47],v3 offset:13832\n"
C_"v233,v66,v67\n"
M_"v[56:59],v[28:31],v[136:139],v[56:59]\n"
"s_add_u32 m0, s84, 118272\n"
T_"v[16:17],v1 offset:16896\n"
C_"v234,v72,v73\n"
M_"v[60:63],v[28:31],v[140:143],v[60:63]\n"
"s_mov_b32 s86, 196608\n"
T_"v[18:19],v1 offset:17152\n"
L_"v246,s[44:47],s86 offen lds\n"
C_"v235,v74,v75\n"
M_"v[48:51],v[32:35],v[144:147],v[48:51]\n"
T_"v[20:21],v1 offset:16904\n"
C_"v236,v68,v69\n"
M_"v[52:55],v[32:35],v[148:151],v[52:55]\n"
W_"lgkmcnt(4)\n"
T_"v[22:23],v1 offset:17160\n"
C_"v237,v70,v71\n"
M_"v[56:59],v[36:39],v[144:147],v[56:59]\n"
T_"v[24:25],v1 offset:17920\n"
C_"v238,v76,v77\n"
M_"v[60:63],v[36:39],v[148:151],v[60:63]\n"
T_"v[26:27],v1 offset:18176\n"
C_"v239,v78,v79\n"
M_"v[48:51],v[40:43],v[152:155],v[48:51]\n"
T_"v[28:29],v1 offset:17928\n"
L_"v247,s[44:47],s86 offen offset:1024 lds\n"
M_"v[52:55],v[40:43],v[156:159],v[52:55]\n"
W_"lgkmcnt(4)\n"
T_"v[30:31],v1 offset:18184\n"
M_"v[56:59],v[44:47],v[152:155],v[56:59]\n"
"s_add_u32 m0, s84, 126720\n"
T_"v[32:33],v1 offset:33792\n"
M_"v[60:63],v[44:47],v[156:159],v[60:63]\n"
"s_mov_b32 s86, 212992\n"
T_"v[34:35],v1 offset:34048\n"
L_"v246,s[44:47],s86 offen lds\n"
M_"v[48:51],v[16:19],v[160:163],v[48:51]\n"
T_"v[36:37],v1 offset:33800\n"
M_"v[52:55],v[16:19],v[164:167],v[52:55]\n"
W_"lgkmcnt(4)\n"
T_"v[38:39],v1 offset:34056\n"
M_"v[56:59],v[20:23],v[160:163],v[56:59]\n"
T_"v[40:41],v1 offset:34816\n"
M_"v[60:63],v[20:23],v[164:167],v[60:63]\n"
T_"v[42:43],v1 offset:35072\n"
L_"v247,s[44:47],s86 offen offset:1024 lds\n"
M_"v[48:51],v[24:27],v[168:171],v[48:51]\n"
T_"v[44:45],v1 offset:34824\n"
M_"v[52:55],v[24:27],v[172:175],v[52:55]\n"
W_"lgkmcnt(4)\n"
T_"v[46:47],v1 offset:35080\n"
M_"v[56:59],v[28:31],v[168:171],v[56:59]\n"
W_"vmcnt(24)\n"
"s_barrier\n"
M_"v[60:63],v[28:31],v[172:175],v[60:63]\n"
M_"v[48:51],v[32:35],v[176:179],v[48:51]\n"
M_"v[52:55],v[32:35],v[180:183],v[52:55]\n"
W_"lgkmcnt(1)\n"
M_"v[56:59],v[36:39],v[176:179],v[56:59]\n"
M_"v[60:63],v[36:39],v[180:183],v[60:63]\n"
M_"v[48:51],v[40:43],v[184:187],v[48:51]\n"
M_"v[52:55],v[40:43],v[188:191],v[52:55]\n"
W_"lgkmcnt(0)\n"
M_"v[56:59],v[44:47],v[184:187],v[56:59]\n"
M_"v[60:63],v[44:47],v[188:191],v[60:63]\n"
"s_add_u32 m0, s84, 8448\n"
"s_mov_b32 s86, 229376\n"
"s_setprio 0\n"
L_"v246,s[44:47],s86 offen lds\n"
C_"v80,v48,v49\n"
L_"v247,s[44:47],s86 offen offset:1024 lds\n"
C_"v81,v50,v51\n"
C_"v82,v56,v57\n"
"s_add_u32 m0, s84, 16896\n"
"s_mov_b32 s86, 245760\n"
L_"v246,s[44:47],s86 offen lds\n"
C_"v83,v58,v59\n"
L_"v247,s[44:47],s86 offen offset:1024 lds\n"
C_"v84,v52,v53\n"
C_"v85,v54,v55\n"
"s_add_u32 m0, s84, 135168\n"
"s_mov_b32 s86, 256\n"
L_"v246,s[44:47],s86 offen lds\n"
C_"v86,v60,v61\n"
C_"v87,v62,v63\n"
L_"v247,s[44:47],s86 offen offset:1024 lds\n"
"s_add_u32 s87, s90, 0\n"
"s_add_u32 m0, s84, 143616\n"
"s_mov_b32 s86, 16640\n"
L_"v246,s[44:47],s86 offen lds\n"
"buffer_store_dwordx4 v[192:195], v248, s[60:63], s87 offen sc1\n"
"s_add_u32 s87, s91, 229376\n"
L_"v247,s[44:47],s86 offen offset:1024 lds\n"
"buffer_store_dwordx4 v[196:199], v248, s[60:63], s87 offen sc1\n"
W_"vmcnt(30)\n"
"s_barrier\n"
"s_add_u32 m0, s84, 33792\n"
"s_mov_b32 s86, 33024\n"
L_"v246,s[44:47],s86 offen lds\n"
"s_add_u32 s87, s90, 8192\n"
L_"v247,s[44:47],s86 offen offset:1024 lds\n"
"buffer_store_dwordx4 v[200:203], v248, s[60:63], s87 offen sc1\n"
"s_add_u32 m0, s84, 42240\n"
"s_mov_b32 s86, 49408\n"
L_"v246,s[44:47],s86 offen lds\n"
"s_add_u32 s87, s91, 237568\n"
L_"v247,s[44:47],s86 offen offset:1024 lds\n"
"buffer_store_dwordx4 v[204:207], v248, s[60:63], s87 offen sc1\n"
W_"vmcnt(32)\n"
"s_barrier\n"
"s_add_u32 m0, s84, 50688\n"
"s_mov_b32 s86, 65792\n"
L_"v246,s[44:47],s86 offen lds\n"
"s_add_u32 s87, s90, 16384\n"
"buffer_store_dwordx4 v[208:211], v248, s[60:63], s87 offen sc1\n"
L_"v247,s[44:47],s86 offen offset:1024 lds\n"
"PTH4:\n"
"s_cbranch_execz PTH12\n"
"s_add_u32 s87, s91, 245760\n"
"buffer_store_dwordx4 v[212:215], v248, s[60:63], s87 offen sc1\n"
"s_add_u32 m0, s84, 59136\n"
"s_mov_b32 s86, 82176\n"
L_"v246,s[44:47],s86 offen lds\n"
"s_lshl1_add_u32 s4, s35, s34\n"
"s_lshl_b32 s4, s4, 9\n"
L_"v247,s[44:47],s86 offen offset:1024 lds\n"
"v_lshl_add_u32 v6, v240, 4, s4\n"
W_"vmcnt(34)\n"
"s_barrier\n"
"s_add_u32 m0, s84, 67584\n"
"s_mov_b32 s86, 98560\n"
L_"v246,s[44:47],s86 offen lds\n"
L_"v247,s[44:47],s86 offen offset:1024 lds\n"
"s_add_u32 s87, s90, 24576\n"
"s_add_u32 m0, s84, 76032\n"
"s_mov_b32 s86, 114944\n"
L_"v246,s[44:47],s86 offen lds\n"
"buffer_store_dwordx4 v[216:219], v248, s[60:63], s87 offen sc1\n"
L_"v247,s[44:47],s86 offen offset:1024 lds\n"
W_"vmcnt(35)\n"
"s_barrier\n"
"s_add_u32 m0, s84, 25344\n"
"s_mov_b32 s86, 131328\n"
L_"v246,s[44:47],s86 offen lds\n"
"s_add_u32 s87, s91, 253952\n"
L_"v247,s[44:47],s86 offen offset:1024 lds\n"
"buffer_store_dwordx4 v[220:223], v248, s[60:63], s87 offen sc1\n"
"s_add_u32 m0, s84, 84480\n"
"s_mov_b32 s86, 147712\n"
L_"v246,s[44:47],s86 offen lds\n"
"s_add_u32 s87, s90, 32768\n"
L_"v247,s[44:47],s86 offen offset:1024 lds\n"
"buffer_store_dwordx4 v[224:227], v248, s[60:63], s87 offen sc1\n"
W_"vmcnt(37)\n"
"s_barrier\n"
"s_add_u32 m0, s84, 92928\n"
"s_mov_b32 s86, 164096\n"
L_"v246,s[44:47],s86 offen lds\n"
"s_add_u32 s87, s91, 262144\n"
L_"v247,s[44:47],s86 offen offset:1024 lds\n"
"buffer_store_dwordx4 v[228:231], v248, s[60:63], s87 offen sc1\n"
"s_add_u32 m0, s84, 101376\n"
"s_mov_b32 s86, 180480\n"
L_"v246,s[44:47],s86 offen lds\n"
"s_add_u32 s87, s90, 40960\n"
L_"v247,s[44:47],s86 offen offset:1024 lds\n"
"buffer_store_dwordx4 v[232:235], v248, s[60:63], s87 offen sc1\n"
W_"vmcnt(39)\n"
"s_barrier\n"
"s_add_u32 m0, s84, 0\n"
"s_mov_b32 s86, 196864\n"
L_"v246,s[44:47],s86 offen lds\n"
"s_add_u32 s87, s91, 270336\n"
"buffer_store_dwordx4 v[236:239], v248, s[60:63], s87 offen sc1\n"
L_"v247,s[44:47],s86 offen offset:1024 lds\n"
"s_add_u32 s87, s90, 49152\n"
"buffer_store_dwordx4 v[80:83], v248, s[60:63], s87 offen sc1\n"
W_"vmcnt(39)\n"
"s_barrier\n"
"s_add_u32 s87, s91, 278528\n"
"buffer_store_dwordx4 v[84:87], v248, s[60:63], s87 offen sc1\n"
"v_mbcnt_lo_u32_b32 v5, -1, 0\n"
"v_mbcnt_hi_u32_b32 v5, -1, v5\n"
"s_mov_b32 s4, 109824\n"
"s_setprio 2\n"
"v_lshl_add_u32 v5, v5, 4, s4\n"
"s_bitcmp1_b32 s90, 30\n"
"s_cbranch_scc0 OWF0_H\n"
"ds_write_b128 v5, v[192:195] offset:0\n"
"ds_write_b128 v5, v[200:203] offset:1024\n"
"ds_write_b128 v5, v[208:211] offset:2048\n"
"ds_write_b128 v5, v[216:219] offset:3072\n"
"ds_write_b128 v5, v[224:227] offset:4096\n"
"ds_write_b128 v5, v[232:235] offset:5120\n"
"ds_write_b128 v5, v[80:83] offset:6144\n"
"OWF0_H:\n"
"s_bitcmp1_b32 s91, 30\n"
"s_cbranch_scc0 OWF1_H\n"
"ds_write_b128 v5, v[196:199] offset:0\n"
"ds_write_b128 v5, v[204:207] offset:1024\n"
"ds_write_b128 v5, v[212:215] offset:2048\n"
"ds_write_b128 v5, v[220:223] offset:3072\n"
"ds_write_b128 v5, v[228:231] offset:4096\n"
"ds_write_b128 v5, v[236:239] offset:5120\n"
"ds_write_b128 v5, v[84:87] offset:6144\n"
"OWF1_H:\n"
W_"vmcnt(35) lgkmcnt(0)\n"
"s_barrier\n"
"s_mov_b64 exec, 0xffff\n"
"v_add_u32 v6, 0x10000, v6\n"
R_"v[128:131],v6 offset:44288\n"
R_"v[132:135],v6 offset:44544\n"
R_"v[144:147],v6 offset:46336\n"
R_"v[148:151],v6 offset:46592\n"
R_"v[160:163],v6 offset:48384\n"
R_"v[164:167],v6 offset:48640\n"
R_"v[176:179],v6 offset:50432\n"
R_"v[180:183],v6 offset:50688\n"
"s_mov_b64 exec, -1\n"
W_"lgkmcnt(0)\n"
W_"vmcnt(0)\n"
W_"lgkmcnt(0)\n"
"s_barrier\n"
"s_add_u32 m0, s84, 16896\n"
"s_mov_b32 s86, 213248\n"
"s_setprio 0\n"
L_"v246,s[44:47],s86 offen lds\n"
L_"v247,s[44:47],s86 offen offset:1024 lds\n"
"s_add_u32 m0, s84, 109824\n"
"s_mov_b32 s86, 229632\n"
L_"v246,s[44:47],s86 offen lds\n"
"buffer_load_dwordx2 v[40:41], v244, s[76:79], 0 offen\n"
L_"v247,s[44:47],s86 offen offset:1024 lds\n"
"s_add_u32 m0, s84, 135168\n"
"s_mov_b32 s86, 246016\n"
L_"v246,s[44:47],s86 offen lds\n"
L_"v247,s[44:47],s86 offen offset:1024 lds\n"
"s_barrier\n"
"s_add_u32 m0, s84, 33792\n"
"s_mov_b32 s86, 0\n"
L_"v124,s[48:51],s86 offen lds\n"
"s_lshl_b32 s4, s30, 7\n"
L_"v125,s[48:51],s86 offen offset:1024 lds\n"
"v_cmp_eq_u32 vcc, s4, v242\n"
"s_nop 1\n"
"v_cndmask_b32 v17, 0, 16, vcc\n"
"s_add_u32 m0, s84, 143616\n"
"s_mov_b32 s86, 57344\n"
L_"v124,s[48:51],s86 offen lds\n"
"v_sub_u32 v243, v243, v17\n"
L_"v125,s[48:51],s86 offen offset:1024 lds\n"
"buffer_load_dwordx2 v[42:43], v244, s[76:79], 0 offen offset:256\n"
"s_barrier\n"
"s_add_u32 m0, s84, 42240\n"
"s_mov_b32 s86, 229376\n"
L_"v124,s[48:51],s86 offen lds\n"
L_"v125,s[48:51],s86 offen offset:1024 lds\n"
"buffer_load_dwordx2 v[44:45], v244, s[76:79], 0 offen offset:512\n"
"s_add_u32 m0, s84, 50688\n"
"s_mov_b32 s86, 286720\n"
L_"v124,s[48:51],s86 offen lds\n"
L_"v125,s[48:51],s86 offen offset:1024 lds\n"
"s_barrier\n"
"s_add_u32 m0, s84, 59136\n"
"s_mov_b32 s86, 114688\n"
L_"v124,s[48:51],s86 offen lds\n"
L_"v125,s[48:51],s86 offen offset:1024 lds\n"
"buffer_load_dwordx2 v[46:47], v244, s[76:79], 0 offen offset:768\n"
"s_add_u32 m0, s84, 67584\n"
"s_mov_b32 s86, 172032\n"
L_"v124,s[48:51],s86 offen lds\n"
L_"v125,s[48:51],s86 offen offset:1024 lds\n"
"s_barrier\n"
"s_add_u32 m0, s84, 8448\n"
"s_mov_b32 s86, 344064\n"
L_"v124,s[48:51],s86 offen lds\n"
L_"v125,s[48:51],s86 offen offset:1024 lds\n"
"s_add_u32 m0, s84, 25344\n"
"s_mov_b32 s86, 401408\n"
L_"v124,s[48:51],s86 offen lds\n"
L_"v125,s[48:51],s86 offen offset:1024 lds\n"
"s_add_u32 m0, s84, 76032\n"
"s_mov_b32 s86, 128\n"
L_"v124,s[48:51],s86 offen lds\n"
L_"v125,s[48:51],s86 offen offset:1024 lds\n"
"s_add_u32 m0, s84, 118272\n"
"s_mov_b32 s86, 57472\n"
L_"v124,s[48:51],s86 offen lds\n"
L_"v125,s[48:51],s86 offen offset:1024 lds\n"
W_"vmcnt(28)\n"
"s_barrier\n"
"s_add_u32 m0, s84, 84480\n"
"s_mov_b32 s86, 229504\n"
L_"v124,s[48:51],s86 offen lds\n"
L_"v125,s[48:51],s86 offen offset:1024 lds\n"
"s_add_u32 m0, s84, 92928\n"
"s_mov_b32 s86, 286848\n"
L_"v124,s[48:51],s86 offen lds\n"
L_"v125,s[48:51],s86 offen offset:1024 lds\n"
"buffer_load_dword v18, v242, s[80:83], 0 offen sc1\n"
"s_add_u32 m0, s84, 126720\n"
"s_mov_b32 s86, 114816\n"
L_"v124,s[48:51],s86 offen lds\n"
L_"v125,s[48:51],s86 offen offset:1024 lds\n"
"s_add_u32 m0, s84, 152064\n"
"s_mov_b32 s86, 172160\n"
L_"v124,s[48:51],s86 offen lds\n"
L_"v125,s[48:51],s86 offen offset:1024 lds\n"
W_"vmcnt(32)\n"
"s_barrier\n"
W_"vmcnt(4)\n"
"v_sub_u32 v18, v18, v243\n"
"v_cmp_gt_u32 vcc, 1, v18\n"
"s_mov_b32 s4, 0\n"
"s_nop 3\n"
"s_cbranch_vccz FOK1_H\n"
"FPOLL1_H:\n"
"s_sleep 2\n"
"buffer_load_dword v18, v242, s[80:83], 0 offen sc1\n"
W_"vmcnt(0)\n"
"v_sub_u32 v18, v18, v243\n"
"v_cmp_gt_u32 vcc, 1, v18\n"
"s_add_u32 s4, s4, 1\n"
"s_nop 3\n"
"s_cbranch_vccz FOK1_H\n"
"s_cmp_lt_u32 s4, 0x40000\n"
"s_cbranch_scc1 FPOLL1_H\n"
"FOK1_H:\n"
"s_mov_b32 s87, 0\n"
"s_bfm_b64 exec, 48, 16\n"
L_"v[128:131],v255,s[64:67],s87 offen sc1\n"
L_"v[132:135],v255,s[64:67],s87 offen offset:256 sc1\n"
"s_mov_b64 exec, -1\n"
"s_mov_b32 s87, 4096\n"
L_"v[136:139],v255,s[64:67],s87 offen sc1\n"
L_"v[140:143],v255,s[64:67],s87 offen offset:256 sc1\n"
"s_mov_b32 s87, 16384\n"
"s_bfm_b64 exec, 48, 16\n"
L_"v[144:147],v255,s[64:67],s87 offen sc1\n"
L_"v[148:151],v255,s[64:67],s87 offen offset:256 sc1\n"
"s_mov_b64 exec, -1\n"
"s_mov_b32 s87, 20480\n"
L_"v[152:155],v255,s[64:67],s87 offen sc1\n"
L_"v[156:159],v255,s[64:67],s87 offen offset:256 sc1\n"
"s_barrier\n"
"s_mov_b32 s87, 32768\n"
"s_bfm_b64 exec, 48, 16\n"
L_"v[160:163],v255,s[64:67],s87 offen sc1\n"
L_"v[164:167],v255,s[64:67],s87 offen offset:256 sc1\n"
"s_mov_b64 exec, -1\n"
"s_mov_b32 s87, 36864\n"
L_"v[168:171],v255,s[64:67],s87 offen sc1\n"
L_"v[172:175],v255,s[64:67],s87 offen offset:256 sc1\n"
"s_lshl_b32 s87, s35, 24\n"
"s_add_u32 s87, s87, 49152\n"
"s_bfm_b64 exec, 48, 16\n"
L_"v[176:179],v255,s[64:67],s87 offen sc1\n"
L_"v[180:183],v255,s[64:67],s87 offen offset:256 sc1\n"
"s_mov_b64 exec, -1\n"
"s_lshl_b32 s87, s35, 24\n"
"s_add_u32 s87, s87, 53248\n"
L_"v[184:187],v255,s[64:67],s87 offen sc1\n"
L_"v[188:191],v255,s[64:67],s87 offen offset:256 sc1\n"
"s_barrier\n"
"s_add_u32 m0, s84, 33792\n"
"s_mov_b32 s86, 344192\n"
L_"v124,s[48:51],s86 offen lds\n"
L_"v125,s[48:51],s86 offen offset:1024 lds\n"
"s_add_u32 m0, s84, 42240\n"
"s_mov_b32 s86, 401536\n"
L_"v124,s[48:51],s86 offen lds\n"
L_"v125,s[48:51],s86 offen offset:1024 lds\n"
"s_add_u32 m0, s84, 135168\n"
"s_mov_b32 s86, 256\n"
L_"v124,s[48:51],s86 offen lds\n"
L_"v125,s[48:51],s86 offen offset:1024 lds\n"
"s_add_u32 m0, s84, 143616\n"
"s_mov_b32 s86, 57600\n"
L_"v124,s[48:51],s86 offen lds\n"
L_"v125,s[48:51],s86 offen offset:1024 lds\n"
"s_barrier\n"
"s_add_u32 m0, s84, 0\n"
"s_mov_b32 s86, 229632\n"
L_"v124,s[48:51],s86 offen lds\n"
L_"v125,s[48:51],s86 offen offset:1024 lds\n"
"s_add_u32 m0, s84, 8448\n"
"s_mov_b32 s86, 286976\n"
L_"v124,s[48:51],s86 offen lds\n"
L_"v125,s[48:51],s86 offen offset:1024 lds\n"
"s_add_u32 m0, s84, 16896\n"
"s_mov_b32 s86, 114944\n"
L_"v124,s[48:51],s86 offen lds\n"
L_"v125,s[48:51],s86 offen offset:1024 lds\n"
"s_add_u32 m0, s84, 50688\n"
"s_mov_b32 s86, 172288\n"
L_"v124,s[48:51],s86 offen lds\n"
L_"v125,s[48:51],s86 offen offset:1024 lds\n"
W_"vmcnt(12)\n"
"s_barrier\n"
"s_add_u32 m0, s84, 25344\n"
"s_mov_b32 s86, 344320\n"
L_"v124,s[48:51],s86 offen lds\n"
L_"v125,s[48:51],s86 offen offset:1024 lds\n"
"s_add_u32 m0, s84, 59136\n"
"s_mov_b32 s86, 401664\n"
L_"v124,s[48:51],s86 offen lds\n"
L_"v125,s[48:51],s86 offen offset:1024 lds\n"
"s_add_u32 m0, s84, 67584\n"
"s_mov_b32 s86, 384\n"
L_"v124,s[48:51],s86 offen lds\n"
L_"v125,s[48:51],s86 offen offset:1024 lds\n"
W_"vmcnt(10)\n"
"s_barrier\n"
"s_add_u32 m0, s84, 76032\n"
"s_mov_b32 s86, 57728\n"
L_"v124,s[48:51],s86 offen lds\n"
L_"v125,s[48:51],s86 offen offset:1024 lds\n"
"s_add_u32 m0, s84, 84480\n"
"s_mov_b32 s86, 229760\n"
L_"v124,s[48:51],s86 offen lds\n"
L_"v125,s[48:51],s86 offen offset:1024 lds\n"
"s_add_u32 m0, s84, 92928\n"
"s_mov_b32 s86, 287104\n"
L_"v124,s[48:51],s86 offen lds\n"
L_"v125,s[48:51],s86 offen offset:1024 lds\n"
"s_add_u32 m0, s84, 101376\n"
"s_mov_b32 s86, 115072\n"
L_"v124,s[48:51],s86 offen lds\n"
L_"v125,s[48:51],s86 offen offset:1024 lds\n"
W_"vmcnt(10)\n"
"s_barrier\n"
"s_add_u32 m0, s84, 33792\n"
"s_mov_b32 s86, 172416\n"
L_"v124,s[48:51],s86 offen lds\n"
L_"v125,s[48:51],s86 offen offset:1024 lds\n"
"s_add_u32 m0, s84, 42240\n"
"s_mov_b32 s86, 344448\n"
L_"v124,s[48:51],s86 offen lds\n"
L_"v125,s[48:51],s86 offen offset:1024 lds\n"
"s_add_u32 m0, s84, 109824\n"
"s_mov_b32 s86, 401792\n"
L_"v124,s[48:51],s86 offen lds\n"
L_"v125,s[48:51],s86 offen offset:1024 lds\n"
"s_add_u32 m0, s84, 118272\n"
"s_mov_b32 s86, 512\n"
L_"v124,s[48:51],s86 offen lds\n"
L_"v125,s[48:51],s86 offen offset:1024 lds\n"
W_"vmcnt(10)\n"
"s_barrier\n"
"s_add_u32 m0, s84, 0\n"
"s_mov_b32 s86, 57856\n"
L_"v124,s[48:51],s86 offen lds\n"
L_"v125,s[48:51],s86 offen offset:1024 lds\n"
"s_add_u32 m0, s84, 8448\n"
"s_mov_b32 s86, 229888\n"
L_"v124,s[48:51],s86 offen lds\n"
L_"v125,s[48:51],s86 offen offset:1024 lds\n"
"s_add_u32 m0, s84, 16896\n"
"s_mov_b32 s86, 287232\n"
L_"v124,s[48:51],s86 offen lds\n"
L_"v125,s[48:51],s86 offen offset:1024 lds\n"
W_"vmcnt(8)\n"
"s_barrier\n"
"s_add_u32 m0, s84, 25344\n"
"s_mov_b32 s86, 115200\n"
L_"v124,s[48:51],s86 offen lds\n"
"s_setprio 2\n"
M_"v[48:51],v[112:115],v[128:131],0\n"
M_"v[48:51],v[116:119],v[132:135],v[48:51]\n"
M_"v[48:51],v[112:115],v[136:139],v[48:51]\n"
M_"v[48:51],v[116:119],v[140:143],v[48:51]\n"
"s_setprio 0\n"
L_"v125,s[48:51],s86 offen offset:1024 lds\n"
"v_lshlrev_b32 v64, 16, v8\n"
"v_and_b32 v65, s93, v8\n"
"v_lshlrev_b32 v66, 16, v9\n"
"v_and_b32 v67, s93, v9\n"
"v_lshlrev_b32 v108, 16, v40\n"
"s_add_u32 m0, s84, 50688\n"
"s_mov_b32 s86, 172544\n"
L_"v124,s[48:51],s86 offen lds\n"
"v_and_b32 v109, s93, v40\n"
"v_lshlrev_b32 v110, 16, v41\n"
"v_and_b32 v111, s93, v41\n"
"v_fma_f32 v48, v48, s96, v108\n"
L_"v125,s[48:51],s86 offen offset:1024 lds\n"
"v_fma_f32 v49, v49, s96, v109\n"
"v_fma_f32 v50, v50, s96, v110\n"
"v_fma_f32 v51, v51, s96, v111\n"
"v_mul_f32 v104, s97, v48\n"
"v_mul_f32 v105, s97, v49\n"
"s_add_u32 m0, s84, 59136\n"
"s_mov_b32 s86, 344576\n"
L_"v124,s[48:51],s86 offen lds\n"
"v_mul_f32 v106, s97, v50\n"
"v_mul_f32 v107, s97, v51\n"
E_"v104,v104\n"
E_"v105,v105\n"
E_"v106,v106\n"
L_"v125,s[48:51],s86 offen offset:1024 lds\n"
E_"v107,v107\n"
"v_add_f32 v104, 1.0, v104\n"
"v_add_f32 v105, 1.0, v105\n"
"v_add_f32 v106, 1.0, v106\n"
"s_add_u32 m0, s84, 67584\n"
"s_mov_b32 s86, 401920\n"
L_"v124,s[48:51],s86 offen lds\n"
"v_add_f32 v107, 1.0, v107\n"
"v_rcp_f32 v104, v104\n"
"v_rcp_f32 v105, v105\n"
"v_rcp_f32 v106, v106\n"
"v_rcp_f32 v107, v107\n"
L_"v125,s[48:51],s86 offen offset:1024 lds\n"
"v_mul_f32 v48, v48, v104\n"
"v_mul_f32 v49, v49, v105\n"
"v_mul_f32 v50, v50, v106\n"
"v_mul_f32 v51, v51, v107\n"
W_"vmcnt(8)\n"
"s_barrier\n"
"s_add_u32 m0, s84, 33792\n"
"s_mov_b32 s86, 640\n"
L_"v124,s[48:51],s86 offen lds\n"
"s_setprio 2\n"
M_"v[52:55],v[112:115],v[144:147],0\n"
M_"v[52:55],v[116:119],v[148:151],v[52:55]\n"
M_"v[52:55],v[112:115],v[152:155],v[52:55]\n"
M_"v[52:55],v[116:119],v[156:159],v[52:55]\n"
"s_setprio 0\n"
L_"v125,s[48:51],s86 offen offset:1024 lds\n"
"v_lshlrev_b32 v68, 16, v12\n"
"v_and_b32 v69, s93, v12\n"
"v_lshlrev_b32 v70, 16, v13\n"
"v_and_b32 v71, s93, v13\n"
"v_lshlrev_b32 v108, 16, v42\n"
"s_add_u32 m0, s84, 76032\n"
"s_mov_b32 s86, 57984\n"
L_"v124,s[48:51],s86 offen lds\n"
"v_and_b32 v109, s93, v42\n"
"v_lshlrev_b32 v110, 16, v43\n"
"v_and_b32 v111, s93, v43\n"
"v_fma_f32 v52, v52, s96, v108\n"
L_"v125,s[48:51],s86 offen offset:1024 lds\n"
"v_fma_f32 v53, v53, s96, v109\n"
"v_fma_f32 v54, v54, s96, v110\n"
"v_fma_f32 v55, v55, s96, v111\n"
"v_mul_f32 v104, s97, v52\n"
"v_mul_f32 v105, s97, v53\n"
"s_add_u32 m0, s84, 84480\n"
"s_mov_b32 s86, 230016\n"
L_"v124,s[48:51],s86 offen lds\n"
"v_mul_f32 v106, s97, v54\n"
"v_mul_f32 v107, s97, v55\n"
E_"v104,v104\n"
E_"v105,v105\n"
E_"v106,v106\n"
L_"v125,s[48:51],s86 offen offset:1024 lds\n"
E_"v107,v107\n"
"v_add_f32 v104, 1.0, v104\n"
"v_add_f32 v105, 1.0, v105\n"
"v_add_f32 v106, 1.0, v106\n"
"s_add_u32 m0, s84, 92928\n"
"s_mov_b32 s86, 287360\n"
L_"v124,s[48:51],s86 offen lds\n"
"v_add_f32 v107, 1.0, v107\n"
"v_rcp_f32 v104, v104\n"
"v_rcp_f32 v105, v105\n"
"v_rcp_f32 v106, v106\n"
"v_rcp_f32 v107, v107\n"
L_"v125,s[48:51],s86 offen offset:1024 lds\n"
"v_mul_f32 v52, v52, v104\n"
"v_mul_f32 v53, v53, v105\n"
"v_mul_f32 v54, v54, v106\n"
"v_mul_f32 v55, v55, v107\n"
W_"vmcnt(8)\n"
"s_barrier\n"
"s_add_u32 m0, s84, 0\n"
"s_mov_b32 s86, 115328\n"
L_"v124,s[48:51],s86 offen lds\n"
"s_setprio 2\n"
M_"v[56:59],v[112:115],v[160:163],0\n"
M_"v[56:59],v[116:119],v[164:167],v[56:59]\n"
M_"v[56:59],v[112:115],v[168:171],v[56:59]\n"
M_"v[56:59],v[116:119],v[172:175],v[56:59]\n"
"s_setprio 0\n"
L_"v125,s[48:51],s86 offen offset:1024 lds\n"
"v_lshlrev_b32 v72, 16, v14\n"
"v_and_b32 v73, s93, v14\n"
"v_lshlrev_b32 v74, 16, v15\n"
"v_and_b32 v75, s93, v15\n"
"v_lshlrev_b32 v108, 16, v44\n"
"s_add_u32 m0, s84, 42240\n"
"s_mov_b32 s86, 172672\n"
L_"v124,s[48:51],s86 offen lds\n"
"v_and_b32 v109, s93, v44\n"
"v_lshlrev_b32 v110, 16, v45\n"
"v_and_b32 v111, s93, v45\n"
"v_fma_f32 v56, v56, s96, v108\n"
L_"v125,s[48:51],s86 offen offset:1024 lds\n"
"v_fma_f32 v57, v57, s96, v109\n"
"v_fma_f32 v58, v58, s96, v110\n"
"v_fma_f32 v59, v59, s96, v111\n"
"v_mul_f32 v104, s97, v56\n"
"v_mul_f32 v105, s97, v57\n"
"s_add_u32 m0, s84, 101376\n"
"s_mov_b32 s86, 344704\n"
L_"v124,s[48:51],s86 offen lds\n"
"v_mul_f32 v106, s97, v58\n"
"v_mul_f32 v107, s97, v59\n"
E_"v104,v104\n"
E_"v105,v105\n"
E_"v106,v106\n"
L_"v125,s[48:51],s86 offen offset:1024 lds\n"
E_"v107,v107\n"
"v_add_f32 v104, 1.0, v104\n"
"v_add_f32 v105, 1.0, v105\n"
"v_add_f32 v106, 1.0, v106\n"
"s_add_u32 m0, s84, 109824\n"
"s_mov_b32 s86, 402048\n"
L_"v124,s[48:51],s86 offen lds\n"
"v_add_f32 v107, 1.0, v107\n"
"v_rcp_f32 v104, v104\n"
"v_rcp_f32 v105, v105\n"
"v_rcp_f32 v106, v106\n"
"v_rcp_f32 v107, v107\n"
L_"v125,s[48:51],s86 offen offset:1024 lds\n"
"v_mul_f32 v56, v56, v104\n"
"v_mul_f32 v57, v57, v105\n"
"v_mul_f32 v58, v58, v106\n"
"v_mul_f32 v59, v59, v107\n"
W_"vmcnt(8)\n"
"s_barrier\n"
"s_add_u32 m0, s84, 8448\n"
"s_mov_b32 s86, 768\n"
L_"v124,s[48:51],s86 offen lds\n"
"s_setprio 2\n"
M_"v[60:63],v[112:115],v[176:179],0\n"
M_"v[60:63],v[116:119],v[180:183],v[60:63]\n"
M_"v[60:63],v[112:115],v[184:187],v[60:63]\n"
M_"v[60:63],v[116:119],v[188:191],v[60:63]\n"
"s_setprio 0\n"
L_"v125,s[48:51],s86 offen offset:1024 lds\n"
"v_lshlrev_b32 v76, 16, v126\n"
"v_and_b32 v77, s93, v126\n"
"v_lshlrev_b32 v78, 16, v127\n"
"v_and_b32 v79, s93, v127\n"
"v_lshlrev_b32 v108, 16, v46\n"
"s_add_u32 m0, s84, 16896\n"
"s_mov_b32 s86, 58112\n"
L_"v124,s[48:51],s86 offen lds\n"
"v_and_b32 v109, s93, v46\n"
"v_lshlrev_b32 v110, 16, v47\n"
"v_and_b32 v111, s93, v47\n"
"v_fma_f32 v60, v60, s96, v108\n"
L_"v125,s[48:51],s86 offen offset:1024 lds\n"
"v_fma_f32 v61, v61, s96, v109\n"
"v_fma_f32 v62, v62, s96, v110\n"
"v_fma_f32 v63, v63, s96, v111\n"
"v_mul_f32 v104, s97, v60\n"
"v_mul_f32 v105, s97, v61\n"
"s_add_u32 m0, s84, 25344\n"
"s_mov_b32 s86, 230144\n"
L_"v124,s[48:51],s86 offen lds\n"
"v_mul_f32 v106, s97, v62\n"
"v_mul_f32 v107, s97, v63\n"
E_"v104,v104\n"
E_"v105,v105\n"
E_"v106,v106\n"
L_"v125,s[48:51],s86 offen offset:1024 lds\n"
E_"v107,v107\n"
"v_add_f32 v104, 1.0, v104\n"
"v_add_f32 v105, 1.0, v105\n"
"v_add_f32 v106, 1.0, v106\n"
"s_add_u32 m0, s84, 50688\n"
"s_mov_b32 s86, 287488\n"
L_"v124,s[48:51],s86 offen lds\n"
"v_add_f32 v107, 1.0, v107\n"
"v_rcp_f32 v104, v104\n"
"v_rcp_f32 v105, v105\n"
"v_rcp_f32 v106, v106\n"
"v_rcp_f32 v107, v107\n"
L_"v125,s[48:51],s86 offen offset:1024 lds\n"
"v_mul_f32 v60, v60, v104\n"
"v_mul_f32 v61, v61, v105\n"
"v_mul_f32 v62, v62, v106\n"
"v_mul_f32 v63, v63, v107\n"
W_"vmcnt(8)\n"
"s_barrier\n"
"s_add_u32 m0, s84, 33792\n"
"s_mov_b32 s86, 115456\n"
L_"v124,s[48:51],s86 offen lds\n"
L_"v125,s[48:51],s86 offen offset:1024 lds\n"
"s_add_u32 m0, s84, 59136\n"
"s_mov_b32 s86, 172800\n"
L_"v124,s[48:51],s86 offen lds\n"
L_"v125,s[48:51],s86 offen offset:1024 lds\n"
"buffer_load_dword v18, v242, s[80:83], 0 offen sc1\n"
"s_add_u32 m0, s84, 67584\n"
"s_mov_b32 s86, 344832\n"
L_"v124,s[48:51],s86 offen lds\n"
L_"v125,s[48:51],s86 offen offset:1024 lds\n"
"s_add_u32 m0, s84, 76032\n"
"s_mov_b32 s86, 402176\n"
L_"v124,s[48:51],s86 offen lds\n"
L_"v125,s[48:51],s86 offen offset:1024 lds\n"
W_"vmcnt(9)\n"
"s_barrier\n"
W_"vmcnt(4)\n"
"v_sub_u32 v18, v18, v243\n"
"v_cmp_gt_u32 vcc, 2, v18\n"
"s_mov_b32 s4, 0\n"
"s_nop 3\n"
"s_cbranch_vccz FOK2_H\n"
"FPOLL2_H:\n"
"s_sleep 2\n"
"buffer_load_dword v18, v242, s[80:83], 0 offen sc1\n"
W_"vmcnt(0)\n"
"v_sub_u32 v18, v18, v243\n"
"v_cmp_gt_u32 vcc, 2, v18\n"
"s_add_u32 s4, s4, 1\n"
"s_nop 3\n"
"s_cbranch_vccz FOK2_H\n"
"s_cmp_lt_u32 s4, 0x40000\n"
"s_cbranch_scc1 FPOLL2_H\n"
"FOK2_H:\n"
"s_mov_b32 s87, 57344\n"
"s_bfm_b64 exec, 48, 16\n"
L_"v[128:131],v255,s[64:67],s87 offen sc1\n"
L_"v[132:135],v255,s[64:67],s87 offen offset:256 sc1\n"
"s_mov_b64 exec, -1\n"
"s_mov_b32 s87, 61440\n"
L_"v[136:139],v255,s[64:67],s87 offen sc1\n"
L_"v[140:143],v255,s[64:67],s87 offen offset:256 sc1\n"
W_"vmcnt(4)\n"
"s_barrier\n"
"s_cmp_lg_u32 s35, 0\n"
"s_cbranch_scc1 NQ2_H\n"
"s_mov_b32 s87, 73728\n"
"s_bfm_b64 exec, 48, 16\n"
L_"v[144:147],v255,s[64:67],s87 offen sc1\n"
L_"v[148:151],v255,s[64:67],s87 offen offset:256 sc1\n"
"s_mov_b64 exec, -1\n"
"s_mov_b32 s87, 77824\n"
L_"v[152:155],v255,s[64:67],s87 offen sc1\n"
L_"v[156:159],v255,s[64:67],s87 offen offset:256 sc1\n"
"NQ2_H:\n"
"s_barrier\n"
"buffer_load_dword v18, v242, s[80:83], 0 offen sc1\n"
"s_setprio 2\n"
"s_mov_b64 exec, 0xffff\n"
"v_add_u32 v6, 0xffff0000, v6\n"
R_"v[128:131],v6 offset:0\n"
R_"v[132:135],v6 offset:256\n"
R_"v[144:147],v6 offset:2048\n"
R_"v[148:151],v6 offset:2304\n"
"PTH5:\n"
"s_cbranch_execz PTH13\n"
R_"v[160:163],v6 offset:4096\n"
R_"v[164:167],v6 offset:4352\n"
R_"v[176:179],v6 offset:6144\n"
R_"v[180:183],v6 offset:6400\n"
"s_mov_b64 exec, -1\n"
W_"lgkmcnt(0)\n"
"s_cmp_lg_u32 s35, 0\n"
"s_cbranch_scc1 ODD_H\n"
W_"vmcnt(1)\n"
M_"v[108:111],v[112:115],v[128:131],0\n"
M_"v[100:103],v[112:115],v[144:147],0\n"
M_"v[108:111],v[116:119],v[132:135],v[108:111]\n"
M_"v[100:103],v[116:119],v[148:151],v[100:103]\n"
M_"v[108:111],v[112:115],v[136:139],v[108:111]\n"
M_"v[100:103],v[112:115],v[152:155],v[100:103]\n"
M_"v[108:111],v[116:119],v[140:143],v[108:111]\n"
M_"v[100:103],v[116:119],v[156:159],v[100:103]\n"
"s_setprio 0\n"
"s_nop 5\n"
"v_fma_f32 v64, v108, v48, v64\n"
"v_fma_f32 v65, v109, v49, v65\n"
"v_fma_f32 v66, v110, v50, v66\n"
"v_fma_f32 v67, v111, v51, v67\n"
C_"v64,v64,v65\n"
C_"v65,v66,v67\n"
"buffer_store_dwordx2 v[64:65], v245, s[72:75], 0 offen\n"
"v_fma_f32 v68, v100, v52, v68\n"
"v_fma_f32 v69, v101, v53, v69\n"
"v_fma_f32 v70, v102, v54, v70\n"
"v_fma_f32 v71, v103, v55, v71\n"
C_"v68,v68,v69\n"
C_"v69,v70,v71\n"
"buffer_store_dwordx2 v[68:69], v245, s[72:75], 0 offen offset:256\n"
W_"vmcnt(2)\n"
"v_sub_u32 v18, v18, v243\n"
"v_cmp_gt_u32 vcc, 3, v18\n"
"s_mov_b32 s4, 0\n"
"s_nop 3\n"
"s_cbranch_vccz FOK3_H\n"
"FPOLL3_H:\n"
"s_sleep 2\n"
"buffer_load_dword v18, v242, s[80:83], 0 offen sc1\n"
W_"vmcnt(0)\n"
"v_sub_u32 v18, v18, v243\n"
"v_cmp_gt_u32 vcc, 3, v18\n"
"s_add_u32 s4, s4, 1\n"
"s_nop 3\n"
"s_cbranch_vccz FOK3_H\n"
"s_cmp_lt_u32 s4, 0x40000\n"
"s_cbranch_scc1 FPOLL3_H\n"
"FOK3_H:\n"
"s_mov_b32 s87, 90112\n"
"s_bfm_b64 exec, 48, 16\n"
L_"v[160:163],v255,s[64:67],s87 offen sc1\n"
L_"v[164:167],v255,s[64:67],s87 offen offset:256 sc1\n"
"s_mov_b64 exec, -1\n"
"s_mov_b32 s87, 94208\n"
L_"v[168:171],v255,s[64:67],s87 offen sc1\n"
L_"v[172:175],v255,s[64:67],s87 offen offset:256 sc1\n"
"s_mov_b32 s87, 106496\n"
"s_bfm_b64 exec, 48, 16\n"
L_"v[176:179],v255,s[64:67],s87 offen sc1\n"
L_"v[180:183],v255,s[64:67],s87 offen offset:256 sc1\n"
"s_mov_b64 exec, -1\n"
"s_mov_b32 s87, 110592\n"
L_"v[184:187],v255,s[64:67],s87 offen sc1\n"
L_"v[188:191],v255,s[64:67],s87 offen offset:256 sc1\n"
"s_setprio 2\n"
W_"vmcnt(7)\n"
M_"v[108:111],v[112:115],v[160:163],0\n"
W_"vmcnt(6)\n"
M_"v[108:111],v[116:119],v[164:167],v[108:111]\n"
W_"vmcnt(5)\n"
M_"v[108:111],v[112:115],v[168:171],v[108:111]\n"
W_"vmcnt(4)\n"
M_"v[108:111],v[116:119],v[172:175],v[108:111]\n"
"s_setprio 0\n"
"s_nop 6\n"
"v_fma_f32 v72, v108, v56, v72\n"
"v_fma_f32 v73, v109, v57, v73\n"
"v_fma_f32 v74, v110, v58, v74\n"
"v_fma_f32 v75, v111, v59, v75\n"
C_"v72,v72,v73\n"
C_"v73,v74,v75\n"
"buffer_store_dwordx2 v[72:73], v245, s[72:75], 0 offen offset:512\n"
W_"vmcnt(1)\n"
"v_mov_b32 v191, v191\n"
"s_mov_b32 s4, 0\n"
"SCL_H:\n"
"v_max3_u32 v104, v176, v177, v178\n"
"v_max3_u32 v105, v179, v180, v181\n"
"v_max3_u32 v104, v104, v182, v183\n"
"v_max3_u32 v105, v105, v184, v185\n"
"v_max3_u32 v104, v104, v186, v187\n"
"v_max3_u32 v105, v105, v188, v189\n"
"v_max3_u32 v104, v104, v190, v191\n"
"v_max_u32 v104, v104, v105\n"
"v_cmp_eq_u32 vcc, -1, v104\n"
"s_add_u32 s4, s4, 1\n"
"s_nop 3\n"
"s_cbranch_vccz SCD_H\n"
"s_cmp_lt_u32 s4, 0x40000\n"
"s_cbranch_scc0 SCD_H\n"
"s_sleep 10\n"
"s_mov_b32 s87, 106496\n"
"s_bfm_b64 exec, 48, 16\n"
L_"v[176:179],v255,s[64:67],s87 offen sc1\n"
L_"v[180:183],v255,s[64:67],s87 offen offset:256 sc1\n"
"s_mov_b64 exec, -1\n"
"s_mov_b32 s87, 110592\n"
L_"v[184:187],v255,s[64:67],s87 offen sc1\n"
L_"v[188:191],v255,s[64:67],s87 offen offset:256 sc1\n"
W_"vmcnt(0)\n"
"s_branch SCL_H\n"
"SCD_H:\n"
"s_setprio 2\n"
M_"v[108:111],v[112:115],v[176:179],0\n"
M_"v[108:111],v[116:119],v[180:183],v[108:111]\n"
M_"v[108:111],v[112:115],v[184:187],v[108:111]\n"
M_"v[108:111],v[116:119],v[188:191],v[108:111]\n"
"s_setprio 0\n"
"s_nop 6\n"
"v_fma_f32 v76, v108, v60, v76\n"
"v_fma_f32 v77, v109, v61, v77\n"
"v_fma_f32 v78, v110, v62, v78\n"
"v_fma_f32 v79, v111, v63, v79\n"
C_"v76,v76,v77\n"
C_"v77,v78,v79\n"
"buffer_store_dwordx2 v[76:77], v245, s[72:75], 0 offen offset:768\n"
"s_branch DONE_H\n"
"ODD_H:\n"
"PTH6:\n"
"s_cbranch_execz PTH14\n"
W_"vmcnt(1)\n"
"s_setprio 2\n"
M_"v[108:111],v[112:115],v[128:131],0\n"
M_"v[108:111],v[116:119],v[132:135],v[108:111]\n"
M_"v[108:111],v[112:115],v[136:139],v[108:111]\n"
M_"v[108:111],v[116:119],v[140:143],v[108:111]\n"
"s_setprio 0\n"
"s_nop 6\n"
"v_fma_f32 v64, v108, v48, v64\n"
"v_fma_f32 v65, v109, v49, v65\n"
"v_fma_f32 v66, v110, v50, v66\n"
"v_fma_f32 v67, v111, v51, v67\n"
C_"v64,v64,v65\n"
C_"v65,v66,v67\n"
"buffer_store_dwordx2 v[64:65], v245, s[72:75], 0 offen\n"
W_"vmcnt(1)\n"
"v_sub_u32 v18, v18, v243\n"
"v_cmp_gt_u32 vcc, 3, v18\n"
"s_mov_b32 s4, 0\n"
"s_nop 3\n"
"s_cbranch_vccz FOKB3_H\n"
"FPOLLB3_H:\n"
"s_sleep 2\n"
"buffer_load_dword v18, v242, s[80:83], 0 offen sc1\n"
W_"vmcnt(0)\n"
"v_sub_u32 v18, v18, v243\n"
"v_cmp_gt_u32 vcc, 3, v18\n"
"s_add_u32 s4, s4, 1\n"
"s_nop 3\n"
"s_cbranch_vccz FOKB3_H\n"
"s_cmp_lt_u32 s4, 0x40000\n"
"s_cbranch_scc1 FPOLLB3_H\n"
"FOKB3_H:\n"
"s_mov_b32 s87, 73728\n"
"s_bfm_b64 exec, 48, 16\n"
L_"v[144:147],v255,s[64:67],s87 offen sc1\n"
L_"v[148:151],v255,s[64:67],s87 offen offset:256 sc1\n"
"s_mov_b64 exec, -1\n"
"s_mov_b32 s87, 77824\n"
L_"v[152:155],v255,s[64:67],s87 offen sc1\n"
L_"v[156:159],v255,s[64:67],s87 offen offset:256 sc1\n"
"s_mov_b32 s87, 90112\n"
"s_bfm_b64 exec, 48, 16\n"
L_"v[160:163],v255,s[64:67],s87 offen sc1\n"
L_"v[164:167],v255,s[64:67],s87 offen offset:256 sc1\n"
"s_mov_b64 exec, -1\n"
"s_mov_b32 s87, 94208\n"
L_"v[168:171],v255,s[64:67],s87 offen sc1\n"
L_"v[172:175],v255,s[64:67],s87 offen offset:256 sc1\n"
"s_setprio 2\n"
W_"vmcnt(7)\n"
M_"v[108:111],v[112:115],v[144:147],0\n"
W_"vmcnt(6)\n"
M_"v[108:111],v[116:119],v[148:151],v[108:111]\n"
W_"vmcnt(5)\n"
M_"v[108:111],v[112:115],v[152:155],v[108:111]\n"
W_"vmcnt(4)\n"
M_"v[108:111],v[116:119],v[156:159],v[108:111]\n"
"s_setprio 0\n"
"s_nop 6\n"
"v_fma_f32 v68, v108, v52, v68\n"
"v_fma_f32 v69, v109, v53, v69\n"
"v_fma_f32 v70, v110, v54, v70\n"
"v_fma_f32 v71, v111, v55, v71\n"
C_"v68,v68,v69\n"
C_"v69,v70,v71\n"
"buffer_store_dwordx2 v[68:69], v245, s[72:75], 0 offen offset:256\n"
W_"vmcnt(1)\n"
"v_mov_b32 v175, v175\n"
"s_mov_b32 s4, 0\n"
"SDL_H:\n"
"v_max3_u32 v104, v160, v161, v162\n"
"v_max3_u32 v105, v163, v164, v165\n"
"v_max3_u32 v104, v104, v166, v167\n"
"v_max3_u32 v105, v105, v168, v169\n"
"v_max3_u32 v104, v104, v170, v171\n"
"v_max3_u32 v105, v105, v172, v173\n"
"v_max3_u32 v104, v104, v174, v175\n"
"v_max_u32 v104, v104, v105\n"
"v_cmp_eq_u32 vcc, -1, v104\n"
"s_add_u32 s4, s4, 1\n"
"s_nop 3\n"
"s_cbranch_vccz SDD_H\n"
"s_cmp_lt_u32 s4, 0x40000\n"
"s_cbranch_scc0 SDD_H\n"
"s_sleep 10\n"
"s_mov_b32 s87, 90112\n"
"s_bfm_b64 exec, 48, 16\n"
L_"v[160:163],v255,s[64:67],s87 offen sc1\n"
L_"v[164:167],v255,s[64:67],s87 offen offset:256 sc1\n"
"s_mov_b64 exec, -1\n"
"s_mov_b32 s87, 94208\n"
L_"v[168:171],v255,s[64:67],s87 offen sc1\n"
L_"v[172:175],v255,s[64:67],s87 offen offset:256 sc1\n"
W_"vmcnt(0)\n"
"s_branch SDL_H\n"
"SDD_H:\n"
"s_setprio 2\n"
M_"v[108:111],v[112:115],v[160:163],0\n"
M_"v[108:111],v[116:119],v[164:167],v[108:111]\n"
M_"v[108:111],v[112:115],v[168:171],v[108:111]\n"
M_"v[108:111],v[116:119],v[172:175],v[108:111]\n"
"s_setprio 0\n"
"s_nop 6\n"
"v_fma_f32 v72, v108, v56, v72\n"
"v_fma_f32 v73, v109, v57, v73\n"
"v_fma_f32 v74, v110, v58, v74\n"
"v_fma_f32 v75, v111, v59, v75\n"
C_"v72,v72,v73\n"
C_"v73,v74,v75\n"
"buffer_store_dwordx2 v[72:73], v245, s[72:75], 0 offen offset:512\n"
"DONE_H:\n"
"s_endpgm\n"
".p2align 6\n"
"AROLE_H:\n"
"PTH7:\n"
"s_cbranch_execz PTH15\n"
"s_add_u32 m0, s84, 0\n"
"s_mov_b32 s86, 0\n"
L_"v8,s[36:39],s86 offen lds\n"
L_"v9,s[36:39],s86 offen offset:1024 lds\n"
"s_add_u32 m0, s84, 8448\n"
"s_mov_b32 s86, 32768\n"
L_"v8,s[36:39],s86 offen lds\n"
L_"v9,s[36:39],s86 offen offset:1024 lds\n"
"s_add_u32 m0, s84, 16896\n"
"s_mov_b32 s86, 256\n"
L_"v8,s[36:39],s86 offen lds\n"
L_"v9,s[36:39],s86 offen offset:1024 lds\n"
"s_add_u32 m0, s84, 25344\n"
"s_mov_b32 s86, 33024\n"
L_"v8,s[36:39],s86 offen lds\n"
L_"v9,s[36:39],s86 offen offset:1024 lds\n"
"s_add_u32 m0, s84, 33792\n"
"s_mov_b32 s86, 262144\n"
L_"v8,s[36:39],s86 offen lds\n"
L_"v9,s[36:39],s86 offen offset:1024 lds\n"
"s_add_u32 m0, s84, 42240\n"
"s_mov_b32 s86, 294912\n"
L_"v8,s[36:39],s86 offen lds\n"
L_"v9,s[36:39],s86 offen offset:1024 lds\n"
"s_add_u32 m0, s84, 50688\n"
"s_mov_b32 s86, 262400\n"
L_"v8,s[36:39],s86 offen lds\n"
L_"v9,s[36:39],s86 offen offset:1024 lds\n"
"s_add_u32 m0, s84, 59136\n"
"s_mov_b32 s86, 295168\n"
L_"v8,s[36:39],s86 offen lds\n"
L_"v9,s[36:39],s86 offen offset:1024 lds\n"
"s_bfm_b64 exec, 44, 0\n"
"buffer_load_dword v6, v20, s[36:39], 0 offen\n"
"s_mov_b64 exec, -1\n"
"s_bfm_b64 exec, 8, 0\n"
"buffer_load_dword v6, v21, s[40:43], 0 offen offset:64\n"
"s_mov_b64 exec, -1\n"
"buffer_load_dword v6, v24, s[72:75], 0 offen offset:64\n"
"v_mov_b32 v0, v11\n"
"s_setprio 1\n"
W_"vmcnt(11) lgkmcnt(6)\n"
"s_barrier\n"
R_"v[40:43],v4 offset:0\n"
R_"v[44:47],v4 offset:1024\n"
T_"v[8:9],v1 offset:0\n"
T_"v[10:11],v1 offset:8448\n"
T_"v[12:13],v1 offset:8\n"
T_"v[14:15],v1 offset:8456\n"
T_"v[16:17],v1 offset:256\n"
T_"v[18:19],v1 offset:8704\n"
T_"v[20:21],v1 offset:264\n"
T_"v[22:23],v1 offset:8712\n"
T_"v[24:25],v1 offset:512\n"
T_"v[26:27],v1 offset:8960\n"
T_"v[28:29],v1 offset:520\n"
T_"v[30:31],v1 offset:8968\n"
W_"lgkmcnt(10)\n"
M_"v[128:131],v[8:11],v[40:43],0\n"
T_"v[32:33],v1 offset:768\n"
M_"v[132:135],v[8:11],v[44:47],0\n"
W_"lgkmcnt(9)\n"
T_"v[34:35],v1 offset:9216\n"
M_"v[136:139],v[12:15],v[40:43],0\n"
T_"v[36:37],v1 offset:776\n"
M_"v[140:143],v[12:15],v[44:47],0\n"
W_"lgkmcnt(9)\n"
T_"v[38:39],v1 offset:9224\n"
M_"v[144:147],v[16:19],v[40:43],0\n"
T_"v[8:9],v1 offset:16896\n"
R_"v[48:51],v4 offset:4096\n"
M_"v[148:151],v[16:19],v[44:47],0\n"
W_"lgkmcnt(10)\n"
T_"v[10:11],v1 offset:25344\n"
M_"v[152:155],v[20:23],v[40:43],0\n"
T_"v[12:13],v1 offset:16904\n"
M_"v[156:159],v[20:23],v[44:47],0\n"
W_"lgkmcnt(7)\n"
T_"v[14:15],v1 offset:25352\n"
M_"v[160:163],v[24:27],v[40:43],0\n"
T_"v[16:17],v1 offset:17152\n"
R_"v[52:55],v4 offset:5120\n"
M_"v[164:167],v[24:27],v[44:47],0\n"
T_"v[18:19],v1 offset:25600\n"
M_"v[168:171],v[28:31],v[40:43],0\n"
T_"v[20:21],v1 offset:17160\n"
M_"v[172:175],v[28:31],v[44:47],0\n"
W_"lgkmcnt(7)\n"
T_"v[22:23],v1 offset:25608\n"
M_"v[176:179],v[32:35],v[40:43],0\n"
T_"v[24:25],v1 offset:17408\n"
M_"v[180:183],v[32:35],v[44:47],0\n"
T_"v[26:27],v1 offset:25856\n"
M_"v[184:187],v[36:39],v[40:43],0\n"
T_"v[28:29],v1 offset:17416\n"
M_"v[188:191],v[36:39],v[44:47],0\n"
W_"lgkmcnt(6)\n"
T_"v[30:31],v1 offset:25864\n"
M_"v[192:195],v[8:11],v[40:43],0\n"
T_"v[32:33],v1 offset:17664\n"
M_"v[196:199],v[8:11],v[44:47],0\n"
T_"v[34:35],v1 offset:26112\n"
M_"v[200:203],v[12:15],v[40:43],0\n"
T_"v[36:37],v1 offset:17672\n"
M_"v[204:207],v[12:15],v[44:47],0\n"
W_"lgkmcnt(6)\n"
T_"v[38:39],v1 offset:26120\n"
M_"v[208:211],v[16:19],v[40:43],0\n"
W_"vmcnt(3)\n"
"s_barrier\n"
T_"v[8:9],v1 offset:33792\n"
M_"v[212:215],v[16:19],v[44:47],0\n"
T_"v[10:11],v1 offset:42240\n"
M_"v[216:219],v[20:23],v[40:43],0\n"
T_"v[12:13],v1 offset:33800\n"
M_"v[220:223],v[20:23],v[44:47],0\n"
W_"lgkmcnt(6)\n"
T_"v[14:15],v1 offset:42248\n"
M_"v[224:227],v[24:27],v[40:43],0\n"
T_"v[16:17],v1 offset:34048\n"
M_"v[228:231],v[24:27],v[44:47],0\n"
T_"v[18:19],v1 offset:42496\n"
M_"v[232:235],v[28:31],v[40:43],0\n"
T_"v[20:21],v1 offset:34056\n"
M_"v[236:239],v[28:31],v[44:47],0\n"
W_"lgkmcnt(6)\n"
T_"v[22:23],v1 offset:42504\n"
M_"v[240:243],v[32:35],v[40:43],0\n"
T_"v[24:25],v1 offset:34304\n"
M_"v[244:247],v[32:35],v[44:47],0\n"
T_"v[26:27],v1 offset:42752\n"
M_"v[248:251],v[36:39],v[40:43],0\n"
T_"v[28:29],v1 offset:34312\n"
M_"v[252:255],v[36:39],v[44:47],0\n"
W_"lgkmcnt(6)\n"
T_"v[30:31],v1 offset:42760\n"
M_"v[128:131],v[8:11],v[48:51],v[128:131]\n"
T_"v[32:33],v1 offset:34560\n"
M_"v[132:135],v[8:11],v[52:55],v[132:135]\n"
T_"v[34:35],v1 offset:43008\n"
M_"v[136:139],v[12:15],v[48:51],v[136:139]\n"
T_"v[36:37],v1 offset:34568\n"
M_"v[140:143],v[12:15],v[52:55],v[140:143]\n"
W_"lgkmcnt(6)\n"
T_"v[38:39],v1 offset:43016\n"
M_"v[144:147],v[16:19],v[48:51],v[144:147]\n"
T_"v[8:9],v1 offset:50688\n"
R_"v[40:43],v4 offset:8448\n"
M_"v[148:151],v[16:19],v[52:55],v[148:151]\n"
T_"v[10:11],v1 offset:59136\n"
M_"v[152:155],v[20:23],v[48:51],v[152:155]\n"
T_"v[12:13],v1 offset:50696\n"
M_"v[156:159],v[20:23],v[52:55],v[156:159]\n"
W_"lgkmcnt(7)\n"
T_"v[14:15],v1 offset:59144\n"
M_"v[160:163],v[24:27],v[48:51],v[160:163]\n"
T_"v[16:17],v1 offset:50944\n"
R_"v[44:47],v4 offset:9472\n"
M_"v[164:167],v[24:27],v[52:55],v[164:167]\n"
T_"v[18:19],v1 offset:59392\n"
M_"v[168:171],v[28:31],v[48:51],v[168:171]\n"
T_"v[20:21],v1 offset:50952\n"
M_"v[172:175],v[28:31],v[52:55],v[172:175]\n"
W_"lgkmcnt(7)\n"
T_"v[22:23],v1 offset:59400\n"
M_"v[176:179],v[32:35],v[48:51],v[176:179]\n"
T_"v[24:25],v1 offset:51200\n"
M_"v[180:183],v[32:35],v[52:55],v[180:183]\n"
T_"v[26:27],v1 offset:59648\n"
M_"v[184:187],v[36:39],v[48:51],v[184:187]\n"
T_"v[28:29],v1 offset:51208\n"
M_"v[188:191],v[36:39],v[52:55],v[188:191]\n"
W_"lgkmcnt(6)\n"
T_"v[30:31],v1 offset:59656\n"
M_"v[192:195],v[8:11],v[48:51],v[192:195]\n"
T_"v[32:33],v1 offset:51456\n"
M_"v[196:199],v[8:11],v[52:55],v[196:199]\n"
T_"v[34:35],v1 offset:59904\n"
M_"v[200:203],v[12:15],v[48:51],v[200:203]\n"
T_"v[36:37],v1 offset:51464\n"
M_"v[204:207],v[12:15],v[52:55],v[204:207]\n"
W_"lgkmcnt(6)\n"
T_"v[38:39],v1 offset:59912\n"
M_"v[208:211],v[16:19],v[48:51],v[208:211]\n"
"s_barrier\n"
T_"v[8:9],v2 offset:2048\n"
M_"v[212:215],v[16:19],v[52:55],v[212:215]\n"
T_"v[10:11],v2 offset:10496\n"
M_"v[216:219],v[20:23],v[48:51],v[216:219]\n"
T_"v[12:13],v2 offset:2056\n"
M_"v[220:223],v[20:23],v[52:55],v[220:223]\n"
W_"lgkmcnt(6)\n"
T_"v[14:15],v2 offset:10504\n"
M_"v[224:227],v[24:27],v[48:51],v[224:227]\n"
T_"v[16:17],v2 offset:2304\n"
M_"v[228:231],v[24:27],v[52:55],v[228:231]\n"
T_"v[18:19],v2 offset:10752\n"
M_"v[232:235],v[28:31],v[48:51],v[232:235]\n"
T_"v[20:21],v2 offset:2312\n"
M_"v[236:239],v[28:31],v[52:55],v[236:239]\n"
W_"lgkmcnt(6)\n"
T_"v[22:23],v2 offset:10760\n"
M_"v[240:243],v[32:35],v[48:51],v[240:243]\n"
T_"v[24:25],v2 offset:2560\n"
M_"v[244:247],v[32:35],v[52:55],v[244:247]\n"
T_"v[26:27],v2 offset:11008\n"
M_"v[248:251],v[36:39],v[48:51],v[248:251]\n"
T_"v[28:29],v2 offset:2568\n"
M_"v[252:255],v[36:39],v[52:55],v[252:255]\n"
W_"lgkmcnt(6)\n"
T_"v[30:31],v2 offset:11016\n"
M_"v[128:131],v[8:11],v[40:43],v[128:131]\n"
T_"v[32:33],v2 offset:2816\n"
M_"v[132:135],v[8:11],v[44:47],v[132:135]\n"
T_"v[34:35],v2 offset:11264\n"
M_"v[136:139],v[12:15],v[40:43],v[136:139]\n"
T_"v[36:37],v2 offset:2824\n"
M_"v[140:143],v[12:15],v[44:47],v[140:143]\n"
W_"lgkmcnt(6)\n"
T_"v[38:39],v2 offset:11272\n"
M_"v[144:147],v[16:19],v[40:43],v[144:147]\n"
T_"v[8:9],v2 offset:18944\n"
R_"v[48:51],v4 offset:12544\n"
M_"v[148:151],v[16:19],v[44:47],v[148:151]\n"
T_"v[10:11],v2 offset:27392\n"
M_"v[152:155],v[20:23],v[40:43],v[152:155]\n"
T_"v[12:13],v2 offset:18952\n"
M_"v[156:159],v[20:23],v[44:47],v[156:159]\n"
W_"lgkmcnt(7)\n"
T_"v[14:15],v2 offset:27400\n"
M_"v[160:163],v[24:27],v[40:43],v[160:163]\n"
T_"v[16:17],v2 offset:19200\n"
R_"v[52:55],v4 offset:13568\n"
M_"v[164:167],v[24:27],v[44:47],v[164:167]\n"
T_"v[18:19],v2 offset:27648\n"
M_"v[168:171],v[28:31],v[40:43],v[168:171]\n"
T_"v[20:21],v2 offset:19208\n"
M_"v[172:175],v[28:31],v[44:47],v[172:175]\n"
W_"lgkmcnt(7)\n"
T_"v[22:23],v2 offset:27656\n"
M_"v[176:179],v[32:35],v[40:43],v[176:179]\n"
T_"v[24:25],v2 offset:19456\n"
M_"v[180:183],v[32:35],v[44:47],v[180:183]\n"
T_"v[26:27],v2 offset:27904\n"
M_"v[184:187],v[36:39],v[40:43],v[184:187]\n"
T_"v[28:29],v2 offset:19464\n"
M_"v[188:191],v[36:39],v[44:47],v[188:191]\n"
W_"lgkmcnt(6)\n"
T_"v[30:31],v2 offset:27912\n"
M_"v[192:195],v[8:11],v[40:43],v[192:195]\n"
T_"v[32:33],v2 offset:19712\n"
M_"v[196:199],v[8:11],v[44:47],v[196:199]\n"
T_"v[34:35],v2 offset:28160\n"
M_"v[200:203],v[12:15],v[40:43],v[200:203]\n"
T_"v[36:37],v2 offset:19720\n"
M_"v[204:207],v[12:15],v[44:47],v[204:207]\n"
W_"lgkmcnt(6)\n"
T_"v[38:39],v2 offset:28168\n"
M_"v[208:211],v[16:19],v[40:43],v[208:211]\n"
"s_barrier\n"
T_"v[8:9],v1 offset:0\n"
M_"v[212:215],v[16:19],v[44:47],v[212:215]\n"
T_"v[10:11],v1 offset:8448\n"
M_"v[216:219],v[20:23],v[40:43],v[216:219]\n"
T_"v[12:13],v1 offset:8\n"
M_"v[220:223],v[20:23],v[44:47],v[220:223]\n"
W_"lgkmcnt(6)\n"
T_"v[14:15],v1 offset:8456\n"
M_"v[224:227],v[24:27],v[40:43],v[224:227]\n"
T_"v[16:17],v1 offset:256\n"
M_"v[228:231],v[24:27],v[44:47],v[228:231]\n"
T_"v[18:19],v1 offset:8704\n"
M_"v[232:235],v[28:31],v[40:43],v[232:235]\n"
T_"v[20:21],v1 offset:264\n"
M_"v[236:239],v[28:31],v[44:47],v[236:239]\n"
W_"lgkmcnt(6)\n"
T_"v[22:23],v1 offset:8712\n"
M_"v[240:243],v[32:35],v[40:43],v[240:243]\n"
T_"v[24:25],v1 offset:512\n"
M_"v[244:247],v[32:35],v[44:47],v[244:247]\n"
T_"v[26:27],v1 offset:8960\n"
M_"v[248:251],v[36:39],v[40:43],v[248:251]\n"
T_"v[28:29],v1 offset:520\n"
M_"v[252:255],v[36:39],v[44:47],v[252:255]\n"
W_"lgkmcnt(6)\n"
T_"v[30:31],v1 offset:8968\n"
M_"v[128:131],v[8:11],v[48:51],v[128:131]\n"
T_"v[32:33],v1 offset:768\n"
M_"v[132:135],v[8:11],v[52:55],v[132:135]\n"
T_"v[34:35],v1 offset:9216\n"
M_"v[136:139],v[12:15],v[48:51],v[136:139]\n"
T_"v[36:37],v1 offset:776\n"
M_"v[140:143],v[12:15],v[52:55],v[140:143]\n"
W_"lgkmcnt(6)\n"
T_"v[38:39],v1 offset:9224\n"
M_"v[144:147],v[16:19],v[48:51],v[144:147]\n"
T_"v[8:9],v2 offset:35840\n"
R_"v[40:43],v4 offset:16896\n"
M_"v[148:151],v[16:19],v[52:55],v[148:151]\n"
T_"v[10:11],v1 offset:16896\n"
M_"v[152:155],v[20:23],v[48:51],v[152:155]\n"
T_"v[12:13],v2 offset:35848\n"
M_"v[156:159],v[20:23],v[52:55],v[156:159]\n"
W_"lgkmcnt(7)\n"
T_"v[14:15],v1 offset:16904\n"
M_"v[160:163],v[24:27],v[48:51],v[160:163]\n"
T_"v[16:17],v2 offset:36096\n"
R_"v[44:47],v4 offset:17920\n"
M_"v[164:167],v[24:27],v[52:55],v[164:167]\n"
T_"v[18:19],v1 offset:17152\n"
M_"v[168:171],v[28:31],v[48:51],v[168:171]\n"
T_"v[20:21],v2 offset:36104\n"
M_"v[172:175],v[28:31],v[52:55],v[172:175]\n"
W_"lgkmcnt(7)\n"
T_"v[22:23],v1 offset:17160\n"
M_"v[176:179],v[32:35],v[48:51],v[176:179]\n"
T_"v[24:25],v2 offset:36352\n"
M_"v[180:183],v[32:35],v[52:55],v[180:183]\n"
T_"v[26:27],v1 offset:17408\n"
M_"v[184:187],v[36:39],v[48:51],v[184:187]\n"
T_"v[28:29],v2 offset:36360\n"
M_"v[188:191],v[36:39],v[52:55],v[188:191]\n"
W_"lgkmcnt(6)\n"
T_"v[30:31],v1 offset:17416\n"
M_"v[192:195],v[8:11],v[48:51],v[192:195]\n"
T_"v[32:33],v2 offset:36608\n"
M_"v[196:199],v[8:11],v[52:55],v[196:199]\n"
T_"v[34:35],v1 offset:17664\n"
M_"v[200:203],v[12:15],v[48:51],v[200:203]\n"
T_"v[36:37],v2 offset:36616\n"
M_"v[204:207],v[12:15],v[52:55],v[204:207]\n"
W_"lgkmcnt(6)\n"
T_"v[38:39],v1 offset:17672\n"
M_"v[208:211],v[16:19],v[48:51],v[208:211]\n"
"s_barrier\n"
T_"v[8:9],v1 offset:25344\n"
M_"v[212:215],v[16:19],v[52:55],v[212:215]\n"
"PTH8:\n"
"s_cbranch_execz PTH16\n"
T_"v[10:11],v1 offset:33792\n"
M_"v[216:219],v[20:23],v[48:51],v[216:219]\n"
T_"v[12:13],v1 offset:25352\n"
M_"v[220:223],v[20:23],v[52:55],v[220:223]\n"
W_"lgkmcnt(6)\n"
T_"v[14:15],v1 offset:33800\n"
M_"v[224:227],v[24:27],v[48:51],v[224:227]\n"
T_"v[16:17],v1 offset:25600\n"
M_"v[228:231],v[24:27],v[52:55],v[228:231]\n"
T_"v[18:19],v1 offset:34048\n"
M_"v[232:235],v[28:31],v[48:51],v[232:235]\n"
T_"v[20:21],v1 offset:25608\n"
M_"v[236:239],v[28:31],v[52:55],v[236:239]\n"
W_"lgkmcnt(6)\n"
T_"v[22:23],v1 offset:34056\n"
M_"v[240:243],v[32:35],v[48:51],v[240:243]\n"
T_"v[24:25],v1 offset:25856\n"
M_"v[244:247],v[32:35],v[52:55],v[244:247]\n"
T_"v[26:27],v1 offset:34304\n"
M_"v[248:251],v[36:39],v[48:51],v[248:251]\n"
T_"v[28:29],v1 offset:25864\n"
M_"v[252:255],v[36:39],v[52:55],v[252:255]\n"
W_"lgkmcnt(6)\n"
T_"v[30:31],v1 offset:34312\n"
M_"v[128:131],v[8:11],v[40:43],v[128:131]\n"
T_"v[32:33],v1 offset:26112\n"
M_"v[132:135],v[8:11],v[44:47],v[132:135]\n"
T_"v[34:35],v1 offset:34560\n"
M_"v[136:139],v[12:15],v[40:43],v[136:139]\n"
T_"v[36:37],v1 offset:26120\n"
M_"v[140:143],v[12:15],v[44:47],v[140:143]\n"
W_"lgkmcnt(6)\n"
T_"v[38:39],v1 offset:34568\n"
M_"v[144:147],v[16:19],v[40:43],v[144:147]\n"
T_"v[8:9],v1 offset:42240\n"
R_"v[48:51],v4 offset:20992\n"
M_"v[148:151],v[16:19],v[44:47],v[148:151]\n"
T_"v[10:11],v1 offset:50688\n"
M_"v[152:155],v[20:23],v[40:43],v[152:155]\n"
T_"v[12:13],v1 offset:42248\n"
M_"v[156:159],v[20:23],v[44:47],v[156:159]\n"
W_"lgkmcnt(7)\n"
T_"v[14:15],v1 offset:50696\n"
M_"v[160:163],v[24:27],v[40:43],v[160:163]\n"
T_"v[16:17],v1 offset:42496\n"
R_"v[52:55],v4 offset:22016\n"
M_"v[164:167],v[24:27],v[44:47],v[164:167]\n"
T_"v[18:19],v1 offset:50944\n"
M_"v[168:171],v[28:31],v[40:43],v[168:171]\n"
T_"v[20:21],v1 offset:42504\n"
M_"v[172:175],v[28:31],v[44:47],v[172:175]\n"
W_"lgkmcnt(7)\n"
T_"v[22:23],v1 offset:50952\n"
M_"v[176:179],v[32:35],v[40:43],v[176:179]\n"
T_"v[24:25],v1 offset:42752\n"
M_"v[180:183],v[32:35],v[44:47],v[180:183]\n"
T_"v[26:27],v1 offset:51200\n"
M_"v[184:187],v[36:39],v[40:43],v[184:187]\n"
T_"v[28:29],v1 offset:42760\n"
M_"v[188:191],v[36:39],v[44:47],v[188:191]\n"
W_"lgkmcnt(6)\n"
T_"v[30:31],v1 offset:51208\n"
M_"v[192:195],v[8:11],v[40:43],v[192:195]\n"
T_"v[32:33],v1 offset:43008\n"
M_"v[196:199],v[8:11],v[44:47],v[196:199]\n"
T_"v[34:35],v1 offset:51456\n"
M_"v[200:203],v[12:15],v[40:43],v[200:203]\n"
T_"v[36:37],v1 offset:43016\n"
M_"v[204:207],v[12:15],v[44:47],v[204:207]\n"
W_"lgkmcnt(6)\n"
T_"v[38:39],v1 offset:51464\n"
M_"v[208:211],v[16:19],v[40:43],v[208:211]\n"
"s_barrier\n"
T_"v[8:9],v1 offset:59136\n"
M_"v[212:215],v[16:19],v[44:47],v[212:215]\n"
T_"v[10:11],v2 offset:2048\n"
M_"v[216:219],v[20:23],v[40:43],v[216:219]\n"
T_"v[12:13],v1 offset:59144\n"
M_"v[220:223],v[20:23],v[44:47],v[220:223]\n"
W_"lgkmcnt(6)\n"
T_"v[14:15],v2 offset:2056\n"
M_"v[224:227],v[24:27],v[40:43],v[224:227]\n"
T_"v[16:17],v1 offset:59392\n"
M_"v[228:231],v[24:27],v[44:47],v[228:231]\n"
T_"v[18:19],v2 offset:2304\n"
M_"v[232:235],v[28:31],v[40:43],v[232:235]\n"
T_"v[20:21],v1 offset:59400\n"
M_"v[236:239],v[28:31],v[44:47],v[236:239]\n"
W_"lgkmcnt(6)\n"
T_"v[22:23],v2 offset:2312\n"
M_"v[240:243],v[32:35],v[40:43],v[240:243]\n"
T_"v[24:25],v1 offset:59648\n"
M_"v[244:247],v[32:35],v[44:47],v[244:247]\n"
T_"v[26:27],v2 offset:2560\n"
M_"v[248:251],v[36:39],v[40:43],v[248:251]\n"
T_"v[28:29],v1 offset:59656\n"
M_"v[252:255],v[36:39],v[44:47],v[252:255]\n"
W_"lgkmcnt(6)\n"
T_"v[30:31],v2 offset:2568\n"
M_"v[128:131],v[8:11],v[48:51],v[128:131]\n"
T_"v[32:33],v1 offset:59904\n"
M_"v[132:135],v[8:11],v[52:55],v[132:135]\n"
T_"v[34:35],v2 offset:2816\n"
M_"v[136:139],v[12:15],v[48:51],v[136:139]\n"
T_"v[36:37],v1 offset:59912\n"
M_"v[140:143],v[12:15],v[52:55],v[140:143]\n"
W_"lgkmcnt(6)\n"
T_"v[38:39],v2 offset:2824\n"
M_"v[144:147],v[16:19],v[48:51],v[144:147]\n"
T_"v[8:9],v2 offset:10496\n"
R_"v[40:43],v4 offset:25344\n"
M_"v[148:151],v[16:19],v[52:55],v[148:151]\n"
T_"v[10:11],v2 offset:44288\n"
M_"v[152:155],v[20:23],v[48:51],v[152:155]\n"
T_"v[12:13],v2 offset:10504\n"
M_"v[156:159],v[20:23],v[52:55],v[156:159]\n"
W_"lgkmcnt(7)\n"
T_"v[14:15],v2 offset:44296\n"
M_"v[160:163],v[24:27],v[48:51],v[160:163]\n"
T_"v[16:17],v2 offset:10752\n"
R_"v[44:47],v4 offset:26368\n"
M_"v[164:167],v[24:27],v[52:55],v[164:167]\n"
T_"v[18:19],v2 offset:44544\n"
M_"v[168:171],v[28:31],v[48:51],v[168:171]\n"
T_"v[20:21],v2 offset:10760\n"
M_"v[172:175],v[28:31],v[52:55],v[172:175]\n"
W_"lgkmcnt(7)\n"
T_"v[22:23],v2 offset:44552\n"
M_"v[176:179],v[32:35],v[48:51],v[176:179]\n"
T_"v[24:25],v2 offset:11008\n"
M_"v[180:183],v[32:35],v[52:55],v[180:183]\n"
T_"v[26:27],v2 offset:44800\n"
M_"v[184:187],v[36:39],v[48:51],v[184:187]\n"
T_"v[28:29],v2 offset:11016\n"
M_"v[188:191],v[36:39],v[52:55],v[188:191]\n"
W_"lgkmcnt(6)\n"
T_"v[30:31],v2 offset:44808\n"
M_"v[192:195],v[8:11],v[48:51],v[192:195]\n"
T_"v[32:33],v2 offset:11264\n"
M_"v[196:199],v[8:11],v[52:55],v[196:199]\n"
T_"v[34:35],v2 offset:45056\n"
M_"v[200:203],v[12:15],v[48:51],v[200:203]\n"
T_"v[36:37],v2 offset:11272\n"
M_"v[204:207],v[12:15],v[52:55],v[204:207]\n"
W_"lgkmcnt(6)\n"
T_"v[38:39],v2 offset:45064\n"
M_"v[208:211],v[16:19],v[48:51],v[208:211]\n"
"s_barrier\n"
T_"v[8:9],v1 offset:0\n"
M_"v[212:215],v[16:19],v[52:55],v[212:215]\n"
T_"v[10:11],v1 offset:8448\n"
M_"v[216:219],v[20:23],v[48:51],v[216:219]\n"
T_"v[12:13],v1 offset:8\n"
M_"v[220:223],v[20:23],v[52:55],v[220:223]\n"
W_"lgkmcnt(6)\n"
T_"v[14:15],v1 offset:8456\n"
M_"v[224:227],v[24:27],v[48:51],v[224:227]\n"
T_"v[16:17],v1 offset:256\n"
M_"v[228:231],v[24:27],v[52:55],v[228:231]\n"
T_"v[18:19],v1 offset:8704\n"
M_"v[232:235],v[28:31],v[48:51],v[232:235]\n"
T_"v[20:21],v1 offset:264\n"
M_"v[236:239],v[28:31],v[52:55],v[236:239]\n"
W_"lgkmcnt(6)\n"
T_"v[22:23],v1 offset:8712\n"
M_"v[240:243],v[32:35],v[48:51],v[240:243]\n"
T_"v[24:25],v1 offset:512\n"
M_"v[244:247],v[32:35],v[52:55],v[244:247]\n"
T_"v[26:27],v1 offset:8960\n"
M_"v[248:251],v[36:39],v[48:51],v[248:251]\n"
T_"v[28:29],v1 offset:520\n"
M_"v[252:255],v[36:39],v[52:55],v[252:255]\n"
W_"lgkmcnt(6)\n"
T_"v[30:31],v1 offset:8968\n"
M_"v[128:131],v[8:11],v[40:43],v[128:131]\n"
T_"v[32:33],v1 offset:768\n"
M_"v[132:135],v[8:11],v[44:47],v[132:135]\n"
T_"v[34:35],v1 offset:9216\n"
M_"v[136:139],v[12:15],v[40:43],v[136:139]\n"
T_"v[36:37],v1 offset:776\n"
M_"v[140:143],v[12:15],v[44:47],v[140:143]\n"
W_"lgkmcnt(6)\n"
T_"v[38:39],v1 offset:9224\n"
M_"v[144:147],v[16:19],v[40:43],v[144:147]\n"
T_"v[8:9],v2 offset:18944\n"
R_"v[48:51],v4 offset:29440\n"
M_"v[148:151],v[16:19],v[44:47],v[148:151]\n"
T_"v[10:11],v2 offset:27392\n"
M_"v[152:155],v[20:23],v[40:43],v[152:155]\n"
T_"v[12:13],v2 offset:18952\n"
M_"v[156:159],v[20:23],v[44:47],v[156:159]\n"
W_"lgkmcnt(7)\n"
T_"v[14:15],v2 offset:27400\n"
M_"v[160:163],v[24:27],v[40:43],v[160:163]\n"
T_"v[16:17],v2 offset:19200\n"
R_"v[52:55],v4 offset:30464\n"
M_"v[164:167],v[24:27],v[44:47],v[164:167]\n"
T_"v[18:19],v2 offset:27648\n"
M_"v[168:171],v[28:31],v[40:43],v[168:171]\n"
T_"v[20:21],v2 offset:19208\n"
M_"v[172:175],v[28:31],v[44:47],v[172:175]\n"
W_"lgkmcnt(7)\n"
T_"v[22:23],v2 offset:27656\n"
M_"v[176:179],v[32:35],v[40:43],v[176:179]\n"
T_"v[24:25],v2 offset:19456\n"
M_"v[180:183],v[32:35],v[44:47],v[180:183]\n"
T_"v[26:27],v2 offset:27904\n"
M_"v[184:187],v[36:39],v[40:43],v[184:187]\n"
T_"v[28:29],v2 offset:19464\n"
M_"v[188:191],v[36:39],v[44:47],v[188:191]\n"
W_"lgkmcnt(6)\n"
T_"v[30:31],v2 offset:27912\n"
M_"v[192:195],v[8:11],v[40:43],v[192:195]\n"
T_"v[32:33],v2 offset:19712\n"
M_"v[196:199],v[8:11],v[44:47],v[196:199]\n"
T_"v[34:35],v2 offset:28160\n"
M_"v[200:203],v[12:15],v[40:43],v[200:203]\n"
T_"v[36:37],v2 offset:19720\n"
M_"v[204:207],v[12:15],v[44:47],v[204:207]\n"
W_"lgkmcnt(6)\n"
T_"v[38:39],v2 offset:28168\n"
M_"v[208:211],v[16:19],v[40:43],v[208:211]\n"
"s_barrier\n"
T_"v[8:9],v1 offset:16896\n"
M_"v[212:215],v[16:19],v[44:47],v[212:215]\n"
T_"v[10:11],v1 offset:25344\n"
M_"v[216:219],v[20:23],v[40:43],v[216:219]\n"
T_"v[12:13],v1 offset:16904\n"
M_"v[220:223],v[20:23],v[44:47],v[220:223]\n"
W_"lgkmcnt(6)\n"
T_"v[14:15],v1 offset:25352\n"
M_"v[224:227],v[24:27],v[40:43],v[224:227]\n"
T_"v[16:17],v1 offset:17152\n"
M_"v[228:231],v[24:27],v[44:47],v[228:231]\n"
T_"v[18:19],v1 offset:25600\n"
M_"v[232:235],v[28:31],v[40:43],v[232:235]\n"
T_"v[20:21],v1 offset:17160\n"
M_"v[236:239],v[28:31],v[44:47],v[236:239]\n"
W_"lgkmcnt(6)\n"
T_"v[22:23],v1 offset:25608\n"
M_"v[240:243],v[32:35],v[40:43],v[240:243]\n"
T_"v[24:25],v1 offset:17408\n"
M_"v[244:247],v[32:35],v[44:47],v[244:247]\n"
T_"v[26:27],v1 offset:25856\n"
M_"v[248:251],v[36:39],v[40:43],v[248:251]\n"
T_"v[28:29],v1 offset:17416\n"
M_"v[252:255],v[36:39],v[44:47],v[252:255]\n"
W_"lgkmcnt(6)\n"
T_"v[30:31],v1 offset:25864\n"
M_"v[128:131],v[8:11],v[48:51],v[128:131]\n"
T_"v[32:33],v1 offset:17664\n"
M_"v[132:135],v[8:11],v[52:55],v[132:135]\n"
T_"v[34:35],v1 offset:26112\n"
M_"v[136:139],v[12:15],v[48:51],v[136:139]\n"
T_"v[36:37],v1 offset:17672\n"
M_"v[140:143],v[12:15],v[52:55],v[140:143]\n"
W_"lgkmcnt(6)\n"
T_"v[38:39],v1 offset:26120\n"
M_"v[144:147],v[16:19],v[48:51],v[144:147]\n"
T_"v[8:9],v1 offset:33792\n"
R_"v[40:43],v4 offset:33792\n"
M_"v[148:151],v[16:19],v[52:55],v[148:151]\n"
T_"v[10:11],v2 offset:35840\n"
M_"v[152:155],v[20:23],v[48:51],v[152:155]\n"
T_"v[12:13],v1 offset:33800\n"
M_"v[156:159],v[20:23],v[52:55],v[156:159]\n"
W_"lgkmcnt(7)\n"
T_"v[14:15],v2 offset:35848\n"
M_"v[160:163],v[24:27],v[48:51],v[160:163]\n"
T_"v[16:17],v1 offset:34048\n"
R_"v[44:47],v4 offset:34816\n"
M_"v[164:167],v[24:27],v[52:55],v[164:167]\n"
T_"v[18:19],v2 offset:36096\n"
M_"v[168:171],v[28:31],v[48:51],v[168:171]\n"
T_"v[20:21],v1 offset:34056\n"
M_"v[172:175],v[28:31],v[52:55],v[172:175]\n"
W_"lgkmcnt(7)\n"
T_"v[22:23],v2 offset:36104\n"
M_"v[176:179],v[32:35],v[48:51],v[176:179]\n"
T_"v[24:25],v1 offset:34304\n"
M_"v[180:183],v[32:35],v[52:55],v[180:183]\n"
T_"v[26:27],v2 offset:36352\n"
M_"v[184:187],v[36:39],v[48:51],v[184:187]\n"
T_"v[28:29],v1 offset:34312\n"
M_"v[188:191],v[36:39],v[52:55],v[188:191]\n"
W_"lgkmcnt(6)\n"
T_"v[30:31],v2 offset:36360\n"
M_"v[192:195],v[8:11],v[48:51],v[192:195]\n"
T_"v[32:33],v1 offset:34560\n"
M_"v[196:199],v[8:11],v[52:55],v[196:199]\n"
T_"v[34:35],v2 offset:36608\n"
M_"v[200:203],v[12:15],v[48:51],v[200:203]\n"
T_"v[36:37],v1 offset:34568\n"
M_"v[204:207],v[12:15],v[52:55],v[204:207]\n"
W_"lgkmcnt(6)\n"
T_"v[38:39],v2 offset:36616\n"
M_"v[208:211],v[16:19],v[48:51],v[208:211]\n"
"s_barrier\n"
T_"v[8:9],v1 offset:42240\n"
M_"v[212:215],v[16:19],v[52:55],v[212:215]\n"
T_"v[10:11],v1 offset:50688\n"
M_"v[216:219],v[20:23],v[48:51],v[216:219]\n"
T_"v[12:13],v1 offset:42248\n"
M_"v[220:223],v[20:23],v[52:55],v[220:223]\n"
W_"lgkmcnt(6)\n"
T_"v[14:15],v1 offset:50696\n"
M_"v[224:227],v[24:27],v[48:51],v[224:227]\n"
T_"v[16:17],v1 offset:42496\n"
M_"v[228:231],v[24:27],v[52:55],v[228:231]\n"
T_"v[18:19],v1 offset:50944\n"
M_"v[232:235],v[28:31],v[48:51],v[232:235]\n"
T_"v[20:21],v1 offset:42504\n"
M_"v[236:239],v[28:31],v[52:55],v[236:239]\n"
W_"lgkmcnt(6)\n"
T_"v[22:23],v1 offset:50952\n"
M_"v[240:243],v[32:35],v[48:51],v[240:243]\n"
T_"v[24:25],v1 offset:42752\n"
M_"v[244:247],v[32:35],v[52:55],v[244:247]\n"
T_"v[26:27],v1 offset:51200\n"
M_"v[248:251],v[36:39],v[48:51],v[248:251]\n"
T_"v[28:29],v1 offset:42760\n"
M_"v[252:255],v[36:39],v[52:55],v[252:255]\n"
W_"lgkmcnt(6)\n"
T_"v[30:31],v1 offset:51208\n"
M_"v[128:131],v[8:11],v[40:43],v[128:131]\n"
T_"v[32:33],v1 offset:43008\n"
M_"v[132:135],v[8:11],v[44:47],v[132:135]\n"
T_"v[34:35],v1 offset:51456\n"
M_"v[136:139],v[12:15],v[40:43],v[136:139]\n"
T_"v[36:37],v1 offset:43016\n"
M_"v[140:143],v[12:15],v[44:47],v[140:143]\n"
W_"lgkmcnt(6)\n"
T_"v[38:39],v1 offset:51464\n"
M_"v[144:147],v[16:19],v[40:43],v[144:147]\n"
T_"v[8:9],v1 offset:59136\n"
R_"v[48:51],v4 offset:37888\n"
M_"v[148:151],v[16:19],v[44:47],v[148:151]\n"
T_"v[10:11],v2 offset:2048\n"
M_"v[152:155],v[20:23],v[40:43],v[152:155]\n"
T_"v[12:13],v1 offset:59144\n"
M_"v[156:159],v[20:23],v[44:47],v[156:159]\n"
W_"lgkmcnt(7)\n"
T_"v[14:15],v2 offset:2056\n"
M_"v[160:163],v[24:27],v[40:43],v[160:163]\n"
T_"v[16:17],v1 offset:59392\n"
R_"v[52:55],v4 offset:38912\n"
M_"v[164:167],v[24:27],v[44:47],v[164:167]\n"
T_"v[18:19],v2 offset:2304\n"
M_"v[168:171],v[28:31],v[40:43],v[168:171]\n"
T_"v[20:21],v1 offset:59400\n"
M_"v[172:175],v[28:31],v[44:47],v[172:175]\n"
W_"lgkmcnt(7)\n"
T_"v[22:23],v2 offset:2312\n"
M_"v[176:179],v[32:35],v[40:43],v[176:179]\n"
T_"v[24:25],v1 offset:59648\n"
M_"v[180:183],v[32:35],v[44:47],v[180:183]\n"
T_"v[26:27],v2 offset:2560\n"
M_"v[184:187],v[36:39],v[40:43],v[184:187]\n"
T_"v[28:29],v1 offset:59656\n"
M_"v[188:191],v[36:39],v[44:47],v[188:191]\n"
W_"lgkmcnt(6)\n"
T_"v[30:31],v2 offset:2568\n"
M_"v[192:195],v[8:11],v[40:43],v[192:195]\n"
T_"v[32:33],v1 offset:59904\n"
M_"v[196:199],v[8:11],v[44:47],v[196:199]\n"
T_"v[34:35],v2 offset:2816\n"
M_"v[200:203],v[12:15],v[40:43],v[200:203]\n"
T_"v[36:37],v1 offset:59912\n"
M_"v[204:207],v[12:15],v[44:47],v[204:207]\n"
W_"lgkmcnt(6)\n"
T_"v[38:39],v2 offset:2824\n"
M_"v[208:211],v[16:19],v[40:43],v[208:211]\n"
"s_barrier\n"
T_"v[8:9],v1 offset:0\n"
M_"v[212:215],v[16:19],v[44:47],v[212:215]\n"
T_"v[10:11],v1 offset:8448\n"
M_"v[216:219],v[20:23],v[40:43],v[216:219]\n"
T_"v[12:13],v1 offset:8\n"
M_"v[220:223],v[20:23],v[44:47],v[220:223]\n"
W_"lgkmcnt(6)\n"
T_"v[14:15],v1 offset:8456\n"
M_"v[224:227],v[24:27],v[40:43],v[224:227]\n"
T_"v[16:17],v1 offset:256\n"
M_"v[228:231],v[24:27],v[44:47],v[228:231]\n"
T_"v[18:19],v1 offset:8704\n"
M_"v[232:235],v[28:31],v[40:43],v[232:235]\n"
T_"v[20:21],v1 offset:264\n"
M_"v[236:239],v[28:31],v[44:47],v[236:239]\n"
W_"lgkmcnt(6)\n"
T_"v[22:23],v1 offset:8712\n"
M_"v[240:243],v[32:35],v[40:43],v[240:243]\n"
T_"v[24:25],v1 offset:512\n"
M_"v[244:247],v[32:35],v[44:47],v[244:247]\n"
T_"v[26:27],v1 offset:8960\n"
M_"v[248:251],v[36:39],v[40:43],v[248:251]\n"
T_"v[28:29],v1 offset:520\n"
M_"v[252:255],v[36:39],v[44:47],v[252:255]\n"
W_"lgkmcnt(6)\n"
T_"v[30:31],v1 offset:8968\n"
M_"v[128:131],v[8:11],v[48:51],v[128:131]\n"
T_"v[32:33],v1 offset:768\n"
M_"v[132:135],v[8:11],v[52:55],v[132:135]\n"
T_"v[34:35],v1 offset:9216\n"
M_"v[136:139],v[12:15],v[48:51],v[136:139]\n"
T_"v[36:37],v1 offset:776\n"
M_"v[140:143],v[12:15],v[52:55],v[140:143]\n"
W_"lgkmcnt(6)\n"
T_"v[38:39],v1 offset:9224\n"
M_"v[144:147],v[16:19],v[48:51],v[144:147]\n"
T_"v[8:9],v2 offset:10496\n"
R_"v[40:43],v4 offset:42240\n"
M_"v[148:151],v[16:19],v[52:55],v[148:151]\n"
T_"v[10:11],v2 offset:44288\n"
M_"v[152:155],v[20:23],v[48:51],v[152:155]\n"
T_"v[12:13],v2 offset:10504\n"
M_"v[156:159],v[20:23],v[52:55],v[156:159]\n"
W_"lgkmcnt(7)\n"
T_"v[14:15],v2 offset:44296\n"
M_"v[160:163],v[24:27],v[48:51],v[160:163]\n"
T_"v[16:17],v2 offset:10752\n"
R_"v[44:47],v4 offset:43264\n"
M_"v[164:167],v[24:27],v[52:55],v[164:167]\n"
T_"v[18:19],v2 offset:44544\n"
M_"v[168:171],v[28:31],v[48:51],v[168:171]\n"
T_"v[20:21],v2 offset:10760\n"
M_"v[172:175],v[28:31],v[52:55],v[172:175]\n"
W_"lgkmcnt(7)\n"
T_"v[22:23],v2 offset:44552\n"
M_"v[176:179],v[32:35],v[48:51],v[176:179]\n"
T_"v[24:25],v2 offset:11008\n"
M_"v[180:183],v[32:35],v[52:55],v[180:183]\n"
T_"v[26:27],v2 offset:44800\n"
M_"v[184:187],v[36:39],v[48:51],v[184:187]\n"
T_"v[28:29],v2 offset:11016\n"
M_"v[188:191],v[36:39],v[52:55],v[188:191]\n"
W_"lgkmcnt(6)\n"
T_"v[30:31],v2 offset:44808\n"
M_"v[192:195],v[8:11],v[48:51],v[192:195]\n"
T_"v[32:33],v2 offset:11264\n"
M_"v[196:199],v[8:11],v[52:55],v[196:199]\n"
T_"v[34:35],v2 offset:45056\n"
M_"v[200:203],v[12:15],v[48:51],v[200:203]\n"
T_"v[36:37],v2 offset:11272\n"
M_"v[204:207],v[12:15],v[52:55],v[204:207]\n"
W_"lgkmcnt(6)\n"
T_"v[38:39],v2 offset:45064\n"
M_"v[208:211],v[16:19],v[48:51],v[208:211]\n"
"s_barrier\n"
T_"v[8:9],v1 offset:16896\n"
M_"v[212:215],v[16:19],v[52:55],v[212:215]\n"
T_"v[10:11],v1 offset:25344\n"
M_"v[216:219],v[20:23],v[48:51],v[216:219]\n"
T_"v[12:13],v1 offset:16904\n"
M_"v[220:223],v[20:23],v[52:55],v[220:223]\n"
W_"lgkmcnt(6)\n"
T_"v[14:15],v1 offset:25352\n"
M_"v[224:227],v[24:27],v[48:51],v[224:227]\n"
T_"v[16:17],v1 offset:17152\n"
M_"v[228:231],v[24:27],v[52:55],v[228:231]\n"
T_"v[18:19],v1 offset:25600\n"
M_"v[232:235],v[28:31],v[48:51],v[232:235]\n"
T_"v[20:21],v1 offset:17160\n"
M_"v[236:239],v[28:31],v[52:55],v[236:239]\n"
W_"lgkmcnt(6)\n"
T_"v[22:23],v1 offset:25608\n"
M_"v[240:243],v[32:35],v[48:51],v[240:243]\n"
T_"v[24:25],v1 offset:17408\n"
M_"v[244:247],v[32:35],v[52:55],v[244:247]\n"
T_"v[26:27],v1 offset:25856\n"
M_"v[248:251],v[36:39],v[48:51],v[248:251]\n"
T_"v[28:29],v1 offset:17416\n"
M_"v[252:255],v[36:39],v[52:55],v[252:255]\n"
W_"lgkmcnt(6)\n"
T_"v[30:31],v1 offset:25864\n"
M_"v[128:131],v[8:11],v[40:43],v[128:131]\n"
T_"v[32:33],v1 offset:17664\n"
M_"v[132:135],v[8:11],v[44:47],v[132:135]\n"
T_"v[34:35],v1 offset:26112\n"
M_"v[136:139],v[12:15],v[40:43],v[136:139]\n"
T_"v[36:37],v1 offset:17672\n"
M_"v[140:143],v[12:15],v[44:47],v[140:143]\n"
W_"lgkmcnt(6)\n"
T_"v[38:39],v1 offset:26120\n"
M_"v[144:147],v[16:19],v[40:43],v[144:147]\n"
T_"v[8:9],v2 offset:18944\n"
R_"v[48:51],v4 offset:46336\n"
M_"v[148:151],v[16:19],v[44:47],v[148:151]\n"
T_"v[10:11],v2 offset:27392\n"
M_"v[152:155],v[20:23],v[40:43],v[152:155]\n"
T_"v[12:13],v2 offset:18952\n"
M_"v[156:159],v[20:23],v[44:47],v[156:159]\n"
W_"lgkmcnt(7)\n"
T_"v[14:15],v2 offset:27400\n"
M_"v[160:163],v[24:27],v[40:43],v[160:163]\n"
T_"v[16:17],v2 offset:19200\n"
R_"v[52:55],v4 offset:47360\n"
M_"v[164:167],v[24:27],v[44:47],v[164:167]\n"
T_"v[18:19],v2 offset:27648\n"
M_"v[168:171],v[28:31],v[40:43],v[168:171]\n"
T_"v[20:21],v2 offset:19208\n"
M_"v[172:175],v[28:31],v[44:47],v[172:175]\n"
W_"lgkmcnt(7)\n"
T_"v[22:23],v2 offset:27656\n"
M_"v[176:179],v[32:35],v[40:43],v[176:179]\n"
T_"v[24:25],v2 offset:19456\n"
M_"v[180:183],v[32:35],v[44:47],v[180:183]\n"
T_"v[26:27],v2 offset:27904\n"
M_"v[184:187],v[36:39],v[40:43],v[184:187]\n"
T_"v[28:29],v2 offset:19464\n"
M_"v[188:191],v[36:39],v[44:47],v[188:191]\n"
W_"lgkmcnt(6)\n"
T_"v[30:31],v2 offset:27912\n"
M_"v[192:195],v[8:11],v[40:43],v[192:195]\n"
T_"v[32:33],v2 offset:19712\n"
M_"v[196:199],v[8:11],v[44:47],v[196:199]\n"
T_"v[34:35],v2 offset:28160\n"
M_"v[200:203],v[12:15],v[40:43],v[200:203]\n"
T_"v[36:37],v2 offset:19720\n"
M_"v[204:207],v[12:15],v[44:47],v[204:207]\n"
W_"lgkmcnt(6)\n"
T_"v[38:39],v2 offset:28168\n"
M_"v[208:211],v[16:19],v[40:43],v[208:211]\n"
"s_barrier\n"
T_"v[8:9],v1 offset:33792\n"
M_"v[212:215],v[16:19],v[44:47],v[212:215]\n"
T_"v[10:11],v1 offset:42240\n"
M_"v[216:219],v[20:23],v[40:43],v[216:219]\n"
T_"v[12:13],v1 offset:33800\n"
M_"v[220:223],v[20:23],v[44:47],v[220:223]\n"
W_"lgkmcnt(6)\n"
T_"v[14:15],v1 offset:42248\n"
M_"v[224:227],v[24:27],v[40:43],v[224:227]\n"
T_"v[16:17],v1 offset:34048\n"
M_"v[228:231],v[24:27],v[44:47],v[228:231]\n"
T_"v[18:19],v1 offset:42496\n"
M_"v[232:235],v[28:31],v[40:43],v[232:235]\n"
T_"v[20:21],v1 offset:34056\n"
M_"v[236:239],v[28:31],v[44:47],v[236:239]\n"
W_"lgkmcnt(6)\n"
T_"v[22:23],v1 offset:42504\n"
M_"v[240:243],v[32:35],v[40:43],v[240:243]\n"
T_"v[24:25],v1 offset:34304\n"
M_"v[244:247],v[32:35],v[44:47],v[244:247]\n"
"PTH9:\n"
"s_cbranch_execz PTH17\n"
T_"v[26:27],v1 offset:42752\n"
M_"v[248:251],v[36:39],v[40:43],v[248:251]\n"
T_"v[28:29],v1 offset:34312\n"
M_"v[252:255],v[36:39],v[44:47],v[252:255]\n"
W_"lgkmcnt(6)\n"
T_"v[30:31],v1 offset:42760\n"
M_"v[128:131],v[8:11],v[48:51],v[128:131]\n"
T_"v[32:33],v1 offset:34560\n"
M_"v[132:135],v[8:11],v[52:55],v[132:135]\n"
T_"v[34:35],v1 offset:43008\n"
M_"v[136:139],v[12:15],v[48:51],v[136:139]\n"
T_"v[36:37],v1 offset:34568\n"
M_"v[140:143],v[12:15],v[52:55],v[140:143]\n"
W_"lgkmcnt(6)\n"
T_"v[38:39],v1 offset:43016\n"
M_"v[144:147],v[16:19],v[48:51],v[144:147]\n"
T_"v[8:9],v1 offset:50688\n"
R_"v[40:43],v4 offset:50688\n"
M_"v[148:151],v[16:19],v[52:55],v[148:151]\n"
T_"v[10:11],v2 offset:35840\n"
M_"v[152:155],v[20:23],v[48:51],v[152:155]\n"
T_"v[12:13],v1 offset:50696\n"
M_"v[156:159],v[20:23],v[52:55],v[156:159]\n"
W_"lgkmcnt(7)\n"
T_"v[14:15],v2 offset:35848\n"
M_"v[160:163],v[24:27],v[48:51],v[160:163]\n"
T_"v[16:17],v1 offset:50944\n"
R_"v[44:47],v4 offset:51712\n"
M_"v[164:167],v[24:27],v[52:55],v[164:167]\n"
T_"v[18:19],v2 offset:36096\n"
M_"v[168:171],v[28:31],v[48:51],v[168:171]\n"
T_"v[20:21],v1 offset:50952\n"
M_"v[172:175],v[28:31],v[52:55],v[172:175]\n"
W_"lgkmcnt(7)\n"
T_"v[22:23],v2 offset:36104\n"
M_"v[176:179],v[32:35],v[48:51],v[176:179]\n"
T_"v[24:25],v1 offset:51200\n"
M_"v[180:183],v[32:35],v[52:55],v[180:183]\n"
T_"v[26:27],v2 offset:36352\n"
M_"v[184:187],v[36:39],v[48:51],v[184:187]\n"
T_"v[28:29],v1 offset:51208\n"
M_"v[188:191],v[36:39],v[52:55],v[188:191]\n"
W_"lgkmcnt(6)\n"
T_"v[30:31],v2 offset:36360\n"
M_"v[192:195],v[8:11],v[48:51],v[192:195]\n"
T_"v[32:33],v1 offset:51456\n"
M_"v[196:199],v[8:11],v[52:55],v[196:199]\n"
T_"v[34:35],v2 offset:36608\n"
M_"v[200:203],v[12:15],v[48:51],v[200:203]\n"
T_"v[36:37],v1 offset:51464\n"
M_"v[204:207],v[12:15],v[52:55],v[204:207]\n"
W_"lgkmcnt(6)\n"
T_"v[38:39],v2 offset:36616\n"
M_"v[208:211],v[16:19],v[48:51],v[208:211]\n"
"s_barrier\n"
T_"v[8:9],v1 offset:0\n"
M_"v[212:215],v[16:19],v[52:55],v[212:215]\n"
T_"v[10:11],v1 offset:8448\n"
M_"v[216:219],v[20:23],v[48:51],v[216:219]\n"
T_"v[12:13],v1 offset:8\n"
M_"v[220:223],v[20:23],v[52:55],v[220:223]\n"
W_"lgkmcnt(6)\n"
T_"v[14:15],v1 offset:8456\n"
M_"v[224:227],v[24:27],v[48:51],v[224:227]\n"
T_"v[16:17],v1 offset:256\n"
M_"v[228:231],v[24:27],v[52:55],v[228:231]\n"
T_"v[18:19],v1 offset:8704\n"
M_"v[232:235],v[28:31],v[48:51],v[232:235]\n"
T_"v[20:21],v1 offset:264\n"
M_"v[236:239],v[28:31],v[52:55],v[236:239]\n"
W_"lgkmcnt(6)\n"
T_"v[22:23],v1 offset:8712\n"
M_"v[240:243],v[32:35],v[48:51],v[240:243]\n"
T_"v[24:25],v1 offset:512\n"
M_"v[244:247],v[32:35],v[52:55],v[244:247]\n"
T_"v[26:27],v1 offset:8960\n"
M_"v[248:251],v[36:39],v[48:51],v[248:251]\n"
T_"v[28:29],v1 offset:520\n"
M_"v[252:255],v[36:39],v[52:55],v[252:255]\n"
W_"lgkmcnt(6)\n"
T_"v[30:31],v1 offset:8968\n"
M_"v[128:131],v[8:11],v[40:43],v[128:131]\n"
T_"v[32:33],v1 offset:768\n"
M_"v[132:135],v[8:11],v[44:47],v[132:135]\n"
T_"v[34:35],v1 offset:9216\n"
M_"v[136:139],v[12:15],v[40:43],v[136:139]\n"
T_"v[36:37],v1 offset:776\n"
M_"v[140:143],v[12:15],v[44:47],v[140:143]\n"
W_"lgkmcnt(6)\n"
T_"v[38:39],v1 offset:9224\n"
M_"v[144:147],v[16:19],v[40:43],v[144:147]\n"
T_"v[8:9],v1 offset:59136\n"
R_"v[48:51],v4 offset:54784\n"
M_"v[148:151],v[16:19],v[44:47],v[148:151]\n"
T_"v[10:11],v2 offset:2048\n"
M_"v[152:155],v[20:23],v[40:43],v[152:155]\n"
T_"v[12:13],v1 offset:59144\n"
M_"v[156:159],v[20:23],v[44:47],v[156:159]\n"
W_"lgkmcnt(7)\n"
T_"v[14:15],v2 offset:2056\n"
M_"v[160:163],v[24:27],v[40:43],v[160:163]\n"
T_"v[16:17],v1 offset:59392\n"
R_"v[52:55],v4 offset:55808\n"
M_"v[164:167],v[24:27],v[44:47],v[164:167]\n"
T_"v[18:19],v2 offset:2304\n"
M_"v[168:171],v[28:31],v[40:43],v[168:171]\n"
T_"v[20:21],v1 offset:59400\n"
M_"v[172:175],v[28:31],v[44:47],v[172:175]\n"
W_"lgkmcnt(7)\n"
T_"v[22:23],v2 offset:2312\n"
M_"v[176:179],v[32:35],v[40:43],v[176:179]\n"
T_"v[24:25],v1 offset:59648\n"
M_"v[180:183],v[32:35],v[44:47],v[180:183]\n"
T_"v[26:27],v2 offset:2560\n"
M_"v[184:187],v[36:39],v[40:43],v[184:187]\n"
T_"v[28:29],v1 offset:59656\n"
M_"v[188:191],v[36:39],v[44:47],v[188:191]\n"
W_"lgkmcnt(6)\n"
T_"v[30:31],v2 offset:2568\n"
M_"v[192:195],v[8:11],v[40:43],v[192:195]\n"
T_"v[32:33],v1 offset:59904\n"
M_"v[196:199],v[8:11],v[44:47],v[196:199]\n"
T_"v[34:35],v2 offset:2816\n"
M_"v[200:203],v[12:15],v[40:43],v[200:203]\n"
T_"v[36:37],v1 offset:59912\n"
M_"v[204:207],v[12:15],v[44:47],v[204:207]\n"
W_"lgkmcnt(6)\n"
T_"v[38:39],v2 offset:2824\n"
M_"v[208:211],v[16:19],v[40:43],v[208:211]\n"
"s_barrier\n"
T_"v[8:9],v1 offset:16896\n"
M_"v[212:215],v[16:19],v[44:47],v[212:215]\n"
T_"v[10:11],v1 offset:25344\n"
M_"v[216:219],v[20:23],v[40:43],v[216:219]\n"
T_"v[12:13],v1 offset:16904\n"
M_"v[220:223],v[20:23],v[44:47],v[220:223]\n"
W_"lgkmcnt(6)\n"
T_"v[14:15],v1 offset:25352\n"
M_"v[224:227],v[24:27],v[40:43],v[224:227]\n"
T_"v[16:17],v1 offset:17152\n"
M_"v[228:231],v[24:27],v[44:47],v[228:231]\n"
T_"v[18:19],v1 offset:25600\n"
M_"v[232:235],v[28:31],v[40:43],v[232:235]\n"
T_"v[20:21],v1 offset:17160\n"
M_"v[236:239],v[28:31],v[44:47],v[236:239]\n"
W_"lgkmcnt(6)\n"
T_"v[22:23],v1 offset:25608\n"
M_"v[240:243],v[32:35],v[40:43],v[240:243]\n"
T_"v[24:25],v1 offset:17408\n"
M_"v[244:247],v[32:35],v[44:47],v[244:247]\n"
T_"v[26:27],v1 offset:25856\n"
M_"v[248:251],v[36:39],v[40:43],v[248:251]\n"
T_"v[28:29],v1 offset:17416\n"
M_"v[252:255],v[36:39],v[44:47],v[252:255]\n"
W_"lgkmcnt(6)\n"
T_"v[30:31],v1 offset:25864\n"
M_"v[128:131],v[8:11],v[48:51],v[128:131]\n"
T_"v[32:33],v1 offset:17664\n"
M_"v[132:135],v[8:11],v[52:55],v[132:135]\n"
T_"v[34:35],v1 offset:26112\n"
M_"v[136:139],v[12:15],v[48:51],v[136:139]\n"
T_"v[36:37],v1 offset:17672\n"
M_"v[140:143],v[12:15],v[52:55],v[140:143]\n"
W_"lgkmcnt(6)\n"
T_"v[38:39],v1 offset:26120\n"
M_"v[144:147],v[16:19],v[48:51],v[144:147]\n"
T_"v[8:9],v2 offset:10496\n"
M_"v[148:151],v[16:19],v[52:55],v[148:151]\n"
T_"v[10:11],v2 offset:44288\n"
M_"v[152:155],v[20:23],v[48:51],v[152:155]\n"
T_"v[12:13],v2 offset:10504\n"
C_"v64,v128,v129\n"
M_"v[156:159],v[20:23],v[52:55],v[156:159]\n"
W_"lgkmcnt(6)\n"
T_"v[14:15],v2 offset:44296\n"
C_"v65,v130,v131\n"
M_"v[160:163],v[24:27],v[48:51],v[160:163]\n"
T_"v[16:17],v2 offset:10752\n"
C_"v66,v136,v137\n"
M_"v[164:167],v[24:27],v[52:55],v[164:167]\n"
T_"v[18:19],v2 offset:44544\n"
C_"v67,v138,v139\n"
M_"v[168:171],v[28:31],v[48:51],v[168:171]\n"
T_"v[20:21],v2 offset:10760\n"
C_"v68,v132,v133\n"
M_"v[172:175],v[28:31],v[52:55],v[172:175]\n"
W_"lgkmcnt(6)\n"
T_"v[22:23],v2 offset:44552\n"
C_"v69,v134,v135\n"
M_"v[176:179],v[32:35],v[48:51],v[176:179]\n"
T_"v[24:25],v2 offset:11008\n"
C_"v70,v140,v141\n"
M_"v[180:183],v[32:35],v[52:55],v[180:183]\n"
T_"v[26:27],v2 offset:44800\n"
C_"v71,v142,v143\n"
M_"v[184:187],v[36:39],v[48:51],v[184:187]\n"
T_"v[28:29],v2 offset:11016\n"
C_"v72,v144,v145\n"
M_"v[188:191],v[36:39],v[52:55],v[188:191]\n"
W_"lgkmcnt(6)\n"
T_"v[30:31],v2 offset:44808\n"
C_"v73,v146,v147\n"
M_"v[192:195],v[8:11],v[48:51],v[192:195]\n"
T_"v[32:33],v2 offset:11264\n"
C_"v74,v152,v153\n"
M_"v[196:199],v[8:11],v[52:55],v[196:199]\n"
T_"v[34:35],v2 offset:45056\n"
C_"v75,v154,v155\n"
M_"v[200:203],v[12:15],v[48:51],v[200:203]\n"
T_"v[36:37],v2 offset:11272\n"
C_"v76,v148,v149\n"
C_"v77,v150,v151\n"
M_"v[204:207],v[12:15],v[52:55],v[204:207]\n"
W_"lgkmcnt(6)\n"
T_"v[38:39],v2 offset:45064\n"
C_"v78,v156,v157\n"
C_"v79,v158,v159\n"
M_"v[208:211],v[16:19],v[48:51],v[208:211]\n"
"s_barrier\n"
"v_add_u32 v1, 0, v7\n"
"v_add_u32 v2, 0x10000, v1\n"
"v_add_u32 v3, 0x20000, v1\n"
R_"v[8:11],v1 offset:33792\n"
C_"v80,v160,v161\n"
C_"v81,v162,v163\n"
M_"v[212:215],v[16:19],v[52:55],v[212:215]\n"
C_"v82,v168,v169\n"
C_"v83,v170,v171\n"
M_"v[216:219],v[20:23],v[48:51],v[216:219]\n"
R_"v[12:15],v1 offset:33856\n"
C_"v84,v164,v165\n"
C_"v85,v166,v167\n"
M_"v[220:223],v[20:23],v[52:55],v[220:223]\n"
W_"lgkmcnt(5)\n"
C_"v86,v172,v173\n"
C_"v87,v174,v175\n"
M_"v[224:227],v[24:27],v[48:51],v[224:227]\n"
R_"v[16:19],v1 offset:33920\n"
C_"v88,v176,v177\n"
C_"v89,v178,v179\n"
M_"v[228:231],v[24:27],v[52:55],v[228:231]\n"
C_"v90,v184,v185\n"
C_"v91,v186,v187\n"
M_"v[232:235],v[28:31],v[48:51],v[232:235]\n"
R_"v[20:23],v1 offset:33984\n"
C_"v92,v180,v181\n"
C_"v93,v182,v183\n"
M_"v[236:239],v[28:31],v[52:55],v[236:239]\n"
W_"lgkmcnt(3)\n"
C_"v94,v188,v189\n"
C_"v95,v190,v191\n"
M_"v[240:243],v[32:35],v[48:51],v[240:243]\n"
R_"v[24:27],v1 offset:34048\n"
C_"v96,v192,v193\n"
C_"v97,v194,v195\n"
M_"v[244:247],v[32:35],v[52:55],v[244:247]\n"
C_"v98,v200,v201\n"
C_"v99,v202,v203\n"
M_"v[248:251],v[36:39],v[48:51],v[248:251]\n"
R_"v[28:31],v1 offset:34112\n"
C_"v100,v196,v197\n"
M_"v[252:255],v[36:39],v[52:55],v[252:255]\n"
C_"v101,v198,v199\n"
C_"v102,v204,v205\n"
M_"v[40:43],v[8:11],v[64:67],0\n"
R_"v[32:35],v1 offset:34176\n"
C_"v103,v206,v207\n"
C_"v104,v208,v209\n"
M_"v[44:47],v[8:11],v[68:71],0\n"
W_"lgkmcnt(3)\n"
C_"v105,v210,v211\n"
C_"v106,v216,v217\n"
M_"v[40:43],v[12:15],v[72:75],v[40:43]\n"
R_"v[36:39],v1 offset:34240\n"
C_"v107,v218,v219\n"
C_"v108,v212,v213\n"
M_"v[44:47],v[12:15],v[76:79],v[44:47]\n"
C_"v109,v214,v215\n"
C_"v110,v220,v221\n"
M_"v[40:43],v[16:19],v[80:83],v[40:43]\n"
R_"v[8:11],v1 offset:42240\n"
C_"v111,v222,v223\n"
C_"v112,v224,v225\n"
M_"v[44:47],v[16:19],v[84:87],v[44:47]\n"
C_"v113,v226,v227\n"
C_"v114,v232,v233\n"
M_"v[40:43],v[20:23],v[88:91],v[40:43]\n"
R_"v[12:15],v1 offset:42304\n"
C_"v115,v234,v235\n"
C_"v116,v228,v229\n"
M_"v[44:47],v[20:23],v[92:95],v[44:47]\n"
W_"lgkmcnt(3)\n"
C_"v117,v230,v231\n"
C_"v118,v236,v237\n"
M_"v[40:43],v[24:27],v[96:99],v[40:43]\n"
R_"v[16:19],v1 offset:42368\n"
C_"v119,v238,v239\n"
C_"v120,v240,v241\n"
M_"v[44:47],v[24:27],v[100:103],v[44:47]\n"
C_"v121,v242,v243\n"
C_"v122,v248,v249\n"
M_"v[40:43],v[28:31],v[104:107],v[40:43]\n"
R_"v[20:23],v1 offset:42432\n"
C_"v123,v250,v251\n"
C_"v124,v244,v245\n"
M_"v[44:47],v[28:31],v[108:111],v[44:47]\n"
C_"v125,v246,v247\n"
C_"v126,v252,v253\n"
M_"v[40:43],v[32:35],v[112:115],v[40:43]\n"
R_"v[24:27],v1 offset:42496\n"
C_"v127,v254,v255\n"
M_"v[44:47],v[32:35],v[116:119],v[44:47]\n"
W_"lgkmcnt(3)\n"
M_"v[40:43],v[36:39],v[120:123],v[40:43]\n"
R_"v[28:31],v1 offset:42560\n"
M_"v[44:47],v[36:39],v[124:127],v[44:47]\n"
M_"v[48:51],v[8:11],v[64:67],0\n"
R_"v[32:35],v1 offset:42624\n"
M_"v[52:55],v[8:11],v[68:71],0\n"
"s_lshl_b32 s4, s34, 11\n"
M_"v[48:51],v[12:15],v[72:75],v[48:51]\n"
R_"v[36:39],v1 offset:42688\n"
M_"v[52:55],v[12:15],v[76:79],v[52:55]\n"
W_"lgkmcnt(3)\n"
"v_lshl_add_u32 v4, v63, 4, s4\n"
M_"v[48:51],v[16:19],v[80:83],v[48:51]\n"
"s_barrier\n"
R_"v[8:11],v2 offset:18944\n"
"v_cvt_pk_f16_f32 v192, v40, v41\n"
M_"v[52:55],v[16:19],v[84:87],v[52:55]\n"
"v_cvt_pk_f16_f32 v193, v42, v43\n"
"s_lshl_b32 s4, s35, 12\n"
M_"v[48:51],v[20:23],v[88:91],v[48:51]\n"
R_"v[12:15],v2 offset:19008\n"
M_"v[52:55],v[20:23],v[92:95],v[52:55]\n"
"v_cvt_pk_f16_f32 v196, v44, v45\n"
"v_add_u32 v5, s4, v4\n"
M_"v[48:51],v[24:27],v[96:99],v[48:51]\n"
R_"v[16:19],v2 offset:19072\n"
M_"v[52:55],v[24:27],v[100:103],v[52:55]\n"
W_"lgkmcnt(3)\n"
"v_cvt_pk_f16_f32 v197, v46, v47\n"
"s_lshl2_add_u32 s4, s34, s35\n"
M_"v[48:51],v[28:31],v[104:107],v[48:51]\n"
R_"v[20:23],v2 offset:19136\n"
"v_pk_maximum3_f16 v56, v192, v192, v193\n"
M_"v[52:55],v[28:31],v[108:111],v[52:55]\n"
"s_mul_i32 s4, s4, 114688\n"
M_"v[48:51],v[32:35],v[112:115],v[48:51]\n"
R_"v[24:27],v2 offset:19200\n"
"v_pk_maximum3_f16 v57, v196, v196, v197\n"
M_"v[52:55],v[32:35],v[116:119],v[52:55]\n"
"v_lshl_add_u32 v7, v63, 4, s4\n"
M_"v[48:51],v[36:39],v[120:123],v[48:51]\n"
R_"v[28:31],v2 offset:19264\n"
M_"v[52:55],v[36:39],v[124:127],v[52:55]\n"
W_"lgkmcnt(3)\n"
M_"v[40:43],v[8:11],v[64:67],0\n"
R_"v[32:35],v2 offset:19328\n"
"v_mov_b32 v60, 0x3f803f80\n"
M_"v[44:47],v[8:11],v[68:71],0\n"
M_"v[40:43],v[12:15],v[72:75],v[40:43]\n"
R_"v[36:39],v2 offset:19392\n"
M_"v[44:47],v[12:15],v[76:79],v[44:47]\n"
M_"v[40:43],v[16:19],v[80:83],v[40:43]\n"
R_"v[8:11],v2 offset:27392\n"
"v_cvt_pk_f16_f32 v194, v48, v49\n"
M_"v[44:47],v[16:19],v[84:87],v[44:47]\n"
W_"lgkmcnt(3)\n"
"v_mov_b32 v61, 0x3f803f80\n"
"v_cvt_pk_f16_f32 v195, v50, v51\n"
M_"v[40:43],v[20:23],v[88:91],v[40:43]\n"
R_"v[12:15],v2 offset:27456\n"
M_"v[44:47],v[20:23],v[92:95],v[44:47]\n"
"v_cvt_pk_f16_f32 v198, v52, v53\n"
M_"v[40:43],v[24:27],v[96:99],v[40:43]\n"
R_"v[16:19],v2 offset:27520\n"
"v_mov_b32 v62, 0x3f803f80\n"
M_"v[44:47],v[24:27],v[100:103],v[44:47]\n"
"v_cvt_pk_f16_f32 v199, v54, v55\n"
M_"v[40:43],v[28:31],v[104:107],v[40:43]\n"
R_"v[20:23],v2 offset:27584\n"
"v_pk_maximum3_f16 v56, v56, v194, v195\n"
M_"v[44:47],v[28:31],v[108:111],v[44:47]\n"
W_"lgkmcnt(3)\n"
M_"v[40:43],v[32:35],v[112:115],v[40:43]\n"
R_"v[24:27],v2 offset:27648\n"
"v_pk_maximum3_f16 v57, v57, v198, v199\n"
M_"v[44:47],v[32:35],v[116:119],v[44:47]\n"
"v_mov_b32 v63, 0x3f803f80\n"
M_"v[40:43],v[36:39],v[120:123],v[40:43]\n"
R_"v[28:31],v2 offset:27712\n"
M_"v[44:47],v[36:39],v[124:127],v[44:47]\n"
M_"v[48:51],v[8:11],v[64:67],0\n"
R_"v[32:35],v2 offset:27776\n"
M_"v[52:55],v[8:11],v[68:71],0\n"
W_"lgkmcnt(3)\n"
M_"v[48:51],v[12:15],v[72:75],v[48:51]\n"
R_"v[36:39],v2 offset:27840\n"
M_"v[52:55],v[12:15],v[76:79],v[52:55]\n"
M_"v[48:51],v[16:19],v[80:83],v[48:51]\n"
"s_barrier\n"
R_"v[8:11],v1 offset:0\n"
"v_cvt_pk_f16_f32 v200, v40, v41\n"
M_"v[52:55],v[16:19],v[84:87],v[52:55]\n"
"v_cvt_pk_f16_f32 v201, v42, v43\n"
M_"v[48:51],v[20:23],v[88:91],v[48:51]\n"
R_"v[12:15],v1 offset:64\n"
M_"v[52:55],v[20:23],v[92:95],v[52:55]\n"
W_"lgkmcnt(3)\n"
"v_cvt_pk_f16_f32 v204, v44, v45\n"
M_"v[48:51],v[24:27],v[96:99],v[48:51]\n"
R_"v[16:19],v1 offset:128\n"
M_"v[52:55],v[24:27],v[100:103],v[52:55]\n"
"v_cvt_pk_f16_f32 v205, v46, v47\n"
M_"v[48:51],v[28:31],v[104:107],v[48:51]\n"
R_"v[20:23],v1 offset:192\n"
"v_pk_maximum3_f16 v56, v56, v200, v201\n"
M_"v[52:55],v[28:31],v[108:111],v[52:55]\n"
M_"v[48:51],v[32:35],v[112:115],v[48:51]\n"
R_"v[24:27],v1 offset:256\n"
"v_pk_maximum3_f16 v57, v57, v204, v205\n"
M_"v[52:55],v[32:35],v[116:119],v[52:55]\n"
W_"lgkmcnt(3)\n"
M_"v[48:51],v[36:39],v[120:123],v[48:51]\n"
R_"v[28:31],v1 offset:320\n"
M_"v[52:55],v[36:39],v[124:127],v[52:55]\n"
M_"v[40:43],v[8:11],v[64:67],0\n"
R_"v[32:35],v1 offset:384\n"
M_"v[44:47],v[8:11],v[68:71],0\n"
"s_lshl_b32 s8, s30, 7\n"
M_"v[40:43],v[12:15],v[72:75],v[40:43]\n"
R_"v[36:39],v1 offset:448\n"
M_"v[44:47],v[12:15],v[76:79],v[44:47]\n"
W_"lgkmcnt(3)\n"
"s_lshl_b32 s4, s33, 20\n"
M_"v[40:43],v[16:19],v[80:83],v[40:43]\n"
R_"v[8:11],v1 offset:8448\n"
"v_cvt_pk_f16_f32 v202, v48, v49\n"
M_"v[44:47],v[16:19],v[84:87],v[44:47]\n"
"v_cvt_pk_f16_f32 v203, v50, v51\n"
"s_add_u32 s8, s8, s4\n"
M_"v[40:43],v[20:23],v[88:91],v[40:43]\n"
R_"v[12:15],v1 offset:8512\n"
"v_cvt_pk_f16_f32 v206, v52, v53\n"
M_"v[44:47],v[20:23],v[92:95],v[44:47]\n"
W_"vmcnt(0)\n"
"buffer_load_dword v6, off, s[80:83], s8 sc1\n"
M_"v[40:43],v[24:27],v[96:99],v[40:43]\n"
R_"v[16:19],v1 offset:8576\n"
M_"v[44:47],v[24:27],v[100:103],v[44:47]\n"
W_"lgkmcnt(3)\n"
"v_cvt_pk_f16_f32 v207, v54, v55\n"
M_"v[40:43],v[28:31],v[104:107],v[40:43]\n"
R_"v[20:23],v1 offset:8640\n"
"v_pk_maximum3_f16 v56, v56, v202, v203\n"
M_"v[44:47],v[28:31],v[108:111],v[44:47]\n"
M_"v[40:43],v[32:35],v[112:115],v[40:43]\n"
R_"v[24:27],v1 offset:8704\n"
"v_pk_maximum3_f16 v57, v57, v206, v207\n"
M_"v[44:47],v[32:35],v[116:119],v[44:47]\n"
M_"v[40:43],v[36:39],v[120:123],v[40:43]\n"
R_"v[28:31],v1 offset:8768\n"
M_"v[44:47],v[36:39],v[124:127],v[44:47]\n"
W_"lgkmcnt(3)\n"
M_"v[48:51],v[8:11],v[64:67],0\n"
R_"v[32:35],v1 offset:8832\n"
M_"v[52:55],v[8:11],v[68:71],0\n"
M_"v[48:51],v[12:15],v[72:75],v[48:51]\n"
R_"v[36:39],v1 offset:8896\n"
M_"v[52:55],v[12:15],v[76:79],v[52:55]\n"
M_"v[48:51],v[16:19],v[80:83],v[48:51]\n"
"s_barrier\n"
R_"v[8:11],v1 offset:50688\n"
"v_cvt_pk_f16_f32 v208, v40, v41\n"
M_"v[52:55],v[16:19],v[84:87],v[52:55]\n"
W_"lgkmcnt(3)\n"
"v_cvt_pk_f16_f32 v209, v42, v43\n"
M_"v[48:51],v[20:23],v[88:91],v[48:51]\n"
R_"v[12:15],v1 offset:50752\n"
M_"v[52:55],v[20:23],v[92:95],v[52:55]\n"
"v_cvt_pk_f16_f32 v212, v44, v45\n"
M_"v[48:51],v[24:27],v[96:99],v[48:51]\n"
R_"v[16:19],v1 offset:50816\n"
M_"v[52:55],v[24:27],v[100:103],v[52:55]\n"
"v_cvt_pk_f16_f32 v213, v46, v47\n"
M_"v[48:51],v[28:31],v[104:107],v[48:51]\n"
R_"v[20:23],v1 offset:50880\n"
"v_pk_maximum3_f16 v56, v56, v208, v209\n"
M_"v[52:55],v[28:31],v[108:111],v[52:55]\n"
W_"lgkmcnt(3)\n"
M_"v[48:51],v[32:35],v[112:115],v[48:51]\n"
R_"v[24:27],v1 offset:50944\n"
"v_pk_maximum3_f16 v57, v57, v212, v213\n"
M_"v[52:55],v[32:35],v[116:119],v[52:55]\n"
M_"v[48:51],v[36:39],v[120:123],v[48:51]\n"
R_"v[28:31],v1 offset:51008\n"
M_"v[52:55],v[36:39],v[124:127],v[52:55]\n"
M_"v[40:43],v[8:11],v[64:67],0\n"
R_"v[32:35],v1 offset:51072\n"
M_"v[44:47],v[8:11],v[68:71],0\n"
W_"lgkmcnt(3)\n"
M_"v[40:43],v[12:15],v[72:75],v[40:43]\n"
R_"v[36:39],v1 offset:51136\n"
M_"v[44:47],v[12:15],v[76:79],v[44:47]\n"
M_"v[40:43],v[16:19],v[80:83],v[40:43]\n"
R_"v[8:11],v2 offset:35840\n"
"v_cvt_pk_f16_f32 v210, v48, v49\n"
M_"v[44:47],v[16:19],v[84:87],v[44:47]\n"
"v_cvt_pk_f16_f32 v211, v50, v51\n"
M_"v[40:43],v[20:23],v[88:91],v[40:43]\n"
R_"v[12:15],v2 offset:35904\n"
M_"v[44:47],v[20:23],v[92:95],v[44:47]\n"
W_"lgkmcnt(3)\n"
"v_cvt_pk_f16_f32 v214, v52, v53\n"
M_"v[40:43],v[24:27],v[96:99],v[40:43]\n"
R_"v[16:19],v2 offset:35968\n"
M_"v[44:47],v[24:27],v[100:103],v[44:47]\n"
"v_cvt_pk_f16_f32 v215, v54, v55\n"
M_"v[40:43],v[28:31],v[104:107],v[40:43]\n"
R_"v[20:23],v2 offset:36032\n"
"v_pk_maximum3_f16 v56, v56, v210, v211\n"
M_"v[44:47],v[28:31],v[108:111],v[44:47]\n"
M_"v[40:43],v[32:35],v[112:115],v[40:43]\n"
R_"v[24:27],v2 offset:36096\n"
"v_pk_maximum3_f16 v57, v57, v214, v215\n"
M_"v[44:47],v[32:35],v[116:119],v[44:47]\n"
W_"lgkmcnt(3)\n"
M_"v[40:43],v[36:39],v[120:123],v[40:43]\n"
R_"v[28:31],v2 offset:36160\n"
M_"v[44:47],v[36:39],v[124:127],v[44:47]\n"
M_"v[48:51],v[8:11],v[64:67],0\n"
R_"v[32:35],v2 offset:36224\n"
M_"v[52:55],v[8:11],v[68:71],0\n"
M_"v[48:51],v[12:15],v[72:75],v[48:51]\n"
R_"v[36:39],v2 offset:36288\n"
M_"v[52:55],v[12:15],v[76:79],v[52:55]\n"
W_"lgkmcnt(3)\n"
M_"v[48:51],v[16:19],v[80:83],v[48:51]\n"
"s_barrier\n"
R_"v[8:11],v1 offset:16896\n"
"v_cvt_pk_f16_f32 v216, v40, v41\n"
M_"v[52:55],v[16:19],v[84:87],v[52:55]\n"
"PTH10:\n"
"s_cbranch_execz PTR\n"
"v_cvt_pk_f16_f32 v217, v42, v43\n"
M_"v[48:51],v[20:23],v[88:91],v[48:51]\n"
R_"v[12:15],v1 offset:16960\n"
M_"v[52:55],v[20:23],v[92:95],v[52:55]\n"
"v_cvt_pk_f16_f32 v220, v44, v45\n"
M_"v[48:51],v[24:27],v[96:99],v[48:51]\n"
R_"v[16:19],v1 offset:17024\n"
M_"v[52:55],v[24:27],v[100:103],v[52:55]\n"
W_"lgkmcnt(3)\n"
"v_cvt_pk_f16_f32 v221, v46, v47\n"
M_"v[48:51],v[28:31],v[104:107],v[48:51]\n"
R_"v[20:23],v1 offset:17088\n"
"v_pk_maximum3_f16 v56, v56, v216, v217\n"
M_"v[52:55],v[28:31],v[108:111],v[52:55]\n"
M_"v[48:51],v[32:35],v[112:115],v[48:51]\n"
R_"v[24:27],v1 offset:17152\n"
"v_pk_maximum3_f16 v57, v57, v220, v221\n"
M_"v[52:55],v[32:35],v[116:119],v[52:55]\n"
M_"v[48:51],v[36:39],v[120:123],v[48:51]\n"
R_"v[28:31],v1 offset:17216\n"
M_"v[52:55],v[36:39],v[124:127],v[52:55]\n"
W_"lgkmcnt(3)\n"
M_"v[40:43],v[8:11],v[64:67],0\n"
R_"v[32:35],v1 offset:17280\n"
M_"v[44:47],v[8:11],v[68:71],0\n"
M_"v[40:43],v[12:15],v[72:75],v[40:43]\n"
R_"v[36:39],v1 offset:17344\n"
M_"v[44:47],v[12:15],v[76:79],v[44:47]\n"
M_"v[40:43],v[16:19],v[80:83],v[40:43]\n"
R_"v[8:11],v1 offset:25344\n"
"v_cvt_pk_f16_f32 v218, v48, v49\n"
M_"v[44:47],v[16:19],v[84:87],v[44:47]\n"
W_"lgkmcnt(3)\n"
"v_cvt_pk_f16_f32 v219, v50, v51\n"
M_"v[40:43],v[20:23],v[88:91],v[40:43]\n"
R_"v[12:15],v1 offset:25408\n"
M_"v[44:47],v[20:23],v[92:95],v[44:47]\n"
"v_cvt_pk_f16_f32 v222, v52, v53\n"
M_"v[40:43],v[24:27],v[96:99],v[40:43]\n"
R_"v[16:19],v1 offset:25472\n"
M_"v[44:47],v[24:27],v[100:103],v[44:47]\n"
"v_cvt_pk_f16_f32 v223, v54, v55\n"
M_"v[40:43],v[28:31],v[104:107],v[40:43]\n"
R_"v[20:23],v1 offset:25536\n"
"v_pk_maximum3_f16 v56, v56, v218, v219\n"
M_"v[44:47],v[28:31],v[108:111],v[44:47]\n"
W_"lgkmcnt(3)\n"
M_"v[40:43],v[32:35],v[112:115],v[40:43]\n"
R_"v[24:27],v1 offset:25600\n"
"v_pk_maximum3_f16 v57, v57, v222, v223\n"
M_"v[44:47],v[32:35],v[116:119],v[44:47]\n"
M_"v[40:43],v[36:39],v[120:123],v[40:43]\n"
R_"v[28:31],v1 offset:25664\n"
M_"v[44:47],v[36:39],v[124:127],v[44:47]\n"
M_"v[48:51],v[8:11],v[64:67],0\n"
R_"v[32:35],v1 offset:25728\n"
M_"v[52:55],v[8:11],v[68:71],0\n"
W_"lgkmcnt(3)\n"
M_"v[48:51],v[12:15],v[72:75],v[48:51]\n"
R_"v[36:39],v1 offset:25792\n"
M_"v[52:55],v[12:15],v[76:79],v[52:55]\n"
M_"v[48:51],v[16:19],v[80:83],v[48:51]\n"
"v_cvt_pk_f16_f32 v224, v40, v41\n"
M_"v[52:55],v[16:19],v[84:87],v[52:55]\n"
"v_cvt_pk_f16_f32 v225, v42, v43\n"
M_"v[48:51],v[20:23],v[88:91],v[48:51]\n"
"s_barrier\n"
M_"v[52:55],v[20:23],v[92:95],v[52:55]\n"
W_"lgkmcnt(1)\n"
"v_cvt_pk_f16_f32 v228, v44, v45\n"
M_"v[48:51],v[24:27],v[96:99],v[48:51]\n"
M_"v[52:55],v[24:27],v[100:103],v[52:55]\n"
"v_cvt_pk_f16_f32 v229, v46, v47\n"
M_"v[48:51],v[28:31],v[104:107],v[48:51]\n"
"v_pk_maximum3_f16 v56, v56, v224, v225\n"
M_"v[52:55],v[28:31],v[108:111],v[52:55]\n"
M_"v[48:51],v[32:35],v[112:115],v[48:51]\n"
"v_pk_maximum3_f16 v57, v57, v228, v229\n"
M_"v[52:55],v[32:35],v[116:119],v[52:55]\n"
W_"lgkmcnt(0)\n"
M_"v[48:51],v[36:39],v[120:123],v[48:51]\n"
M_"v[52:55],v[36:39],v[124:127],v[52:55]\n"
"s_nop 6\n"
"v_cvt_pk_f16_f32 v226, v48, v49\n"
"v_cvt_pk_f16_f32 v227, v50, v51\n"
"v_cvt_pk_f16_f32 v230, v52, v53\n"
"v_cvt_pk_f16_f32 v231, v54, v55\n"
"v_pk_maximum3_f16 v56, v56, v226, v227\n"
"v_pk_maximum3_f16 v57, v57, v230, v231\n"
"v_pk_max_f16 v48, v56, v56 op_sel:[0,1] op_sel_hi:[1,0]\n"
"v_pk_max_f16 v49, v57, v57 op_sel:[0,1] op_sel_hi:[1,0]\n"
"v_bfi_b32 v48, s93, v49, v48\n"
"v_mov_b32 v49, v48\n"
"s_nop 1\n"
"v_permlane32_swap_b32 v48, v49\n"
"v_pk_max_f16 v48, v48, v49\n"
"v_mov_b32 v49, v48\n"
"s_nop 1\n"
"v_permlane16_swap_b32 v48, v49\n"
"v_pk_max_f16 v48, v48, v49\n"
X_"v50,v48,s94,0 op_sel_hi:[1,0,0]\n"
X_"v51,v48,s94,0 op_sel:[1,0,0] op_sel_hi:[1,0,0]\n"
"v_ceil_f32 v50, v50\n"
"v_ceil_f32 v51, v51\n"
"v_sub_f32 v58, 0xc2c80000, v50\n"
"v_sub_f32 v59, 0xc2c80000, v51\n"
X_"v40,v192,s94,v58 op_sel_hi:[1,0,0]\n"
"s_barrier\n"
X_"v41,v192,s94,v58 op_sel:[1,0,0] op_sel_hi:[1,0,0]\n"
X_"v42,v193,s94,v58 op_sel_hi:[1,0,0]\n"
X_"v43,v193,s94,v58 op_sel:[1,0,0] op_sel_hi:[1,0,0]\n"
E_"v40,v40\n"
E_"v41,v41\n"
X_"v44,v194,s94,v58 op_sel_hi:[1,0,0]\n"
X_"v45,v194,s94,v58 op_sel:[1,0,0] op_sel_hi:[1,0,0]\n"
E_"v42,v42\n"
E_"v43,v43\n"
C_"v192,v40,v41\n"
X_"v46,v195,s94,v58 op_sel_hi:[1,0,0]\n"
X_"v47,v195,s94,v58 op_sel:[1,0,0] op_sel_hi:[1,0,0]\n"
E_"v44,v44\n"
E_"v45,v45\n"
C_"v193,v42,v43\n"
E_"v46,v46\n"
E_"v47,v47\n"
C_"v194,v44,v45\n"
C_"v195,v46,v47\n"
X_"v40,v196,s94,v59 op_sel_hi:[1,0,0]\n"
X_"v41,v196,s94,v59 op_sel:[1,0,0] op_sel_hi:[1,0,0]\n"
X_"v42,v197,s94,v59 op_sel_hi:[1,0,0]\n"
X_"v43,v197,s94,v59 op_sel:[1,0,0] op_sel_hi:[1,0,0]\n"
E_"v40,v40\n"
E_"v41,v41\n"
X_"v44,v198,s94,v59 op_sel_hi:[1,0,0]\n"
X_"v45,v198,s94,v59 op_sel:[1,0,0] op_sel_hi:[1,0,0]\n"
E_"v42,v42\n"
E_"v43,v43\n"
C_"v196,v40,v41\n"
"s_barrier\n"
X_"v46,v199,s94,v59 op_sel_hi:[1,0,0]\n"
X_"v47,v199,s94,v59 op_sel:[1,0,0] op_sel_hi:[1,0,0]\n"
E_"v44,v44\n"
E_"v45,v45\n"
C_"v197,v42,v43\n"
E_"v46,v46\n"
E_"v47,v47\n"
C_"v198,v44,v45\n"
C_"v199,v46,v47\n"
X_"v40,v200,s94,v58 op_sel_hi:[1,0,0]\n"
X_"v41,v200,s94,v58 op_sel:[1,0,0] op_sel_hi:[1,0,0]\n"
X_"v42,v201,s94,v58 op_sel_hi:[1,0,0]\n"
X_"v43,v201,s94,v58 op_sel:[1,0,0] op_sel_hi:[1,0,0]\n"
E_"v40,v40\n"
E_"v41,v41\n"
X_"v44,v202,s94,v58 op_sel_hi:[1,0,0]\n"
X_"v45,v202,s94,v58 op_sel:[1,0,0] op_sel_hi:[1,0,0]\n"
E_"v42,v42\n"
E_"v43,v43\n"
C_"v200,v40,v41\n"
X_"v46,v203,s94,v58 op_sel_hi:[1,0,0]\n"
X_"v47,v203,s94,v58 op_sel:[1,0,0] op_sel_hi:[1,0,0]\n"
E_"v44,v44\n"
E_"v45,v45\n"
C_"v201,v42,v43\n"
E_"v46,v46\n"
E_"v47,v47\n"
C_"v202,v44,v45\n"
C_"v203,v46,v47\n"
X_"v40,v204,s94,v59 op_sel_hi:[1,0,0]\n"
"s_barrier\n"
X_"v41,v204,s94,v59 op_sel:[1,0,0] op_sel_hi:[1,0,0]\n"
X_"v42,v205,s94,v59 op_sel_hi:[1,0,0]\n"
X_"v43,v205,s94,v59 op_sel:[1,0,0] op_sel_hi:[1,0,0]\n"
E_"v40,v40\n"
E_"v41,v41\n"
X_"v44,v206,s94,v59 op_sel_hi:[1,0,0]\n"
X_"v45,v206,s94,v59 op_sel:[1,0,0] op_sel_hi:[1,0,0]\n"
E_"v42,v42\n"
E_"v43,v43\n"
C_"v204,v40,v41\n"
X_"v46,v207,s94,v59 op_sel_hi:[1,0,0]\n"
X_"v47,v207,s94,v59 op_sel:[1,0,0] op_sel_hi:[1,0,0]\n"
E_"v44,v44\n"
E_"v45,v45\n"
C_"v205,v42,v43\n"
E_"v46,v46\n"
E_"v47,v47\n"
C_"v206,v44,v45\n"
C_"v207,v46,v47\n"
X_"v40,v208,s94,v58 op_sel_hi:[1,0,0]\n"
X_"v41,v208,s94,v58 op_sel:[1,0,0] op_sel_hi:[1,0,0]\n"
X_"v42,v209,s94,v58 op_sel_hi:[1,0,0]\n"
X_"v43,v209,s94,v58 op_sel:[1,0,0] op_sel_hi:[1,0,0]\n"
E_"v40,v40\n"
E_"v41,v41\n"
X_"v44,v210,s94,v58 op_sel_hi:[1,0,0]\n"
X_"v45,v210,s94,v58 op_sel:[1,0,0] op_sel_hi:[1,0,0]\n"
E_"v42,v42\n"
E_"v43,v43\n"
C_"v208,v40,v41\n"
"s_barrier\n"
X_"v46,v211,s94,v58 op_sel_hi:[1,0,0]\n"
X_"v47,v211,s94,v58 op_sel:[1,0,0] op_sel_hi:[1,0,0]\n"
E_"v44,v44\n"
E_"v45,v45\n"
C_"v209,v42,v43\n"
E_"v46,v46\n"
E_"v47,v47\n"
C_"v210,v44,v45\n"
C_"v211,v46,v47\n"
X_"v40,v212,s94,v59 op_sel_hi:[1,0,0]\n"
X_"v41,v212,s94,v59 op_sel:[1,0,0] op_sel_hi:[1,0,0]\n"
X_"v42,v213,s94,v59 op_sel_hi:[1,0,0]\n"
X_"v43,v213,s94,v59 op_sel:[1,0,0] op_sel_hi:[1,0,0]\n"
E_"v40,v40\n"
E_"v41,v41\n"
X_"v44,v214,s94,v59 op_sel_hi:[1,0,0]\n"
X_"v45,v214,s94,v59 op_sel:[1,0,0] op_sel_hi:[1,0,0]\n"
E_"v42,v42\n"
E_"v43,v43\n"
C_"v212,v40,v41\n"
X_"v46,v215,s94,v59 op_sel_hi:[1,0,0]\n"
X_"v47,v215,s94,v59 op_sel:[1,0,0] op_sel_hi:[1,0,0]\n"
E_"v44,v44\n"
E_"v45,v45\n"
C_"v213,v42,v43\n"
E_"v46,v46\n"
E_"v47,v47\n"
C_"v214,v44,v45\n"
C_"v215,v46,v47\n"
X_"v40,v216,s94,v58 op_sel_hi:[1,0,0]\n"
"s_barrier\n"
X_"v41,v216,s94,v58 op_sel:[1,0,0] op_sel_hi:[1,0,0]\n"
X_"v42,v217,s94,v58 op_sel_hi:[1,0,0]\n"
X_"v43,v217,s94,v58 op_sel:[1,0,0] op_sel_hi:[1,0,0]\n"
E_"v40,v40\n"
E_"v41,v41\n"
X_"v44,v218,s94,v58 op_sel_hi:[1,0,0]\n"
X_"v45,v218,s94,v58 op_sel:[1,0,0] op_sel_hi:[1,0,0]\n"
E_"v42,v42\n"
E_"v43,v43\n"
C_"v216,v40,v41\n"
X_"v46,v219,s94,v58 op_sel_hi:[1,0,0]\n"
X_"v47,v219,s94,v58 op_sel:[1,0,0] op_sel_hi:[1,0,0]\n"
E_"v44,v44\n"
E_"v45,v45\n"
C_"v217,v42,v43\n"
E_"v46,v46\n"
E_"v47,v47\n"
C_"v218,v44,v45\n"
C_"v219,v46,v47\n"
X_"v40,v220,s94,v59 op_sel_hi:[1,0,0]\n"
X_"v41,v220,s94,v59 op_sel:[1,0,0] op_sel_hi:[1,0,0]\n"
X_"v42,v221,s94,v59 op_sel_hi:[1,0,0]\n"
X_"v43,v221,s94,v59 op_sel:[1,0,0] op_sel_hi:[1,0,0]\n"
E_"v40,v40\n"
E_"v41,v41\n"
X_"v44,v222,s94,v59 op_sel_hi:[1,0,0]\n"
X_"v45,v222,s94,v59 op_sel:[1,0,0] op_sel_hi:[1,0,0]\n"
E_"v42,v42\n"
E_"v43,v43\n"
C_"v220,v40,v41\n"
"s_barrier\n"
X_"v46,v223,s94,v59 op_sel_hi:[1,0,0]\n"
X_"v47,v223,s94,v59 op_sel:[1,0,0] op_sel_hi:[1,0,0]\n"
E_"v44,v44\n"
E_"v45,v45\n"
C_"v221,v42,v43\n"
E_"v46,v46\n"
E_"v47,v47\n"
C_"v222,v44,v45\n"
C_"v223,v46,v47\n"
X_"v40,v224,s94,v58 op_sel_hi:[1,0,0]\n"
X_"v41,v224,s94,v58 op_sel:[1,0,0] op_sel_hi:[1,0,0]\n"
X_"v42,v225,s94,v58 op_sel_hi:[1,0,0]\n"
X_"v43,v225,s94,v58 op_sel:[1,0,0] op_sel_hi:[1,0,0]\n"
E_"v40,v40\n"
E_"v41,v41\n"
X_"v44,v226,s94,v58 op_sel_hi:[1,0,0]\n"
X_"v45,v226,s94,v58 op_sel:[1,0,0] op_sel_hi:[1,0,0]\n"
E_"v42,v42\n"
E_"v43,v43\n"
C_"v224,v40,v41\n"
X_"v46,v227,s94,v58 op_sel_hi:[1,0,0]\n"
X_"v47,v227,s94,v58 op_sel:[1,0,0] op_sel_hi:[1,0,0]\n"
E_"v44,v44\n"
E_"v45,v45\n"
C_"v225,v42,v43\n"
E_"v46,v46\n"
E_"v47,v47\n"
C_"v226,v44,v45\n"
C_"v227,v46,v47\n"
"s_barrier\n"
R_"v[8:11],v2 offset:27392\n"
R_"v[12:15],v2 offset:27456\n"
R_"v[16:19],v2 offset:27520\n"
R_"v[20:23],v2 offset:27584\n"
R_"v[24:27],v2 offset:27648\n"
R_"v[28:31],v2 offset:27712\n"
X_"v40,v228,s94,v59 op_sel_hi:[1,0,0]\n"
X_"v41,v228,s94,v59 op_sel:[1,0,0] op_sel_hi:[1,0,0]\n"
X_"v42,v229,s94,v59 op_sel_hi:[1,0,0]\n"
X_"v43,v229,s94,v59 op_sel:[1,0,0] op_sel_hi:[1,0,0]\n"
E_"v40,v40\n"
E_"v41,v41\n"
X_"v44,v230,s94,v59 op_sel_hi:[1,0,0]\n"
X_"v45,v230,s94,v59 op_sel:[1,0,0] op_sel_hi:[1,0,0]\n"
E_"v42,v42\n"
E_"v43,v43\n"
C_"v228,v40,v41\n"
X_"v46,v231,s94,v59 op_sel_hi:[1,0,0]\n"
X_"v47,v231,s94,v59 op_sel:[1,0,0] op_sel_hi:[1,0,0]\n"
E_"v44,v44\n"
E_"v45,v45\n"
C_"v229,v42,v43\n"
E_"v46,v46\n"
E_"v47,v47\n"
C_"v230,v44,v45\n"
C_"v231,v46,v47\n"
W_"lgkmcnt(5)\n"
M_"v[40:43],v[8:11],v[64:67],0\n"
R_"v[32:35],v2 offset:27776\n"
M_"v[44:47],v[8:11],v[68:71],0\n"
W_"lgkmcnt(5)\n"
M_"v[40:43],v[12:15],v[72:75],v[40:43]\n"
R_"v[36:39],v2 offset:27840\n"
M_"v[44:47],v[12:15],v[76:79],v[44:47]\n"
W_"lgkmcnt(5)\n"
M_"v[40:43],v[16:19],v[80:83],v[40:43]\n"
R_"v[8:11],v2 offset:44288\n"
M_"v[44:47],v[16:19],v[84:87],v[44:47]\n"
W_"lgkmcnt(5)\n"
M_"v[40:43],v[20:23],v[88:91],v[40:43]\n"
R_"v[12:15],v2 offset:44352\n"
M_"v[44:47],v[20:23],v[92:95],v[44:47]\n"
W_"lgkmcnt(3)\n"
M_"v[40:43],v[24:27],v[96:99],v[40:43]\n"
R_"v[16:19],v2 offset:44416\n"
M_"v[44:47],v[24:27],v[100:103],v[44:47]\n"
M_"v[40:43],v[28:31],v[104:107],v[40:43]\n"
R_"v[20:23],v2 offset:44480\n"
M_"v[44:47],v[28:31],v[108:111],v[44:47]\n"
M_"v[40:43],v[32:35],v[112:115],v[40:43]\n"
R_"v[24:27],v2 offset:44544\n"
M_"v[44:47],v[32:35],v[116:119],v[44:47]\n"
W_"lgkmcnt(3)\n"
M_"v[40:43],v[36:39],v[120:123],v[40:43]\n"
R_"v[28:31],v2 offset:44608\n"
M_"v[44:47],v[36:39],v[124:127],v[44:47]\n"
M_"v[48:51],v[8:11],v[64:67],0\n"
R_"v[32:35],v2 offset:44672\n"
M_"v[52:55],v[8:11],v[68:71],0\n"
M_"v[48:51],v[12:15],v[72:75],v[48:51]\n"
R_"v[36:39],v2 offset:44736\n"
M_"v[52:55],v[12:15],v[76:79],v[52:55]\n"
W_"lgkmcnt(3)\n"
M_"v[48:51],v[16:19],v[80:83],v[48:51]\n"
"s_barrier\n"
R_"v[8:11],v1 offset:16896\n"
"v_cvt_pk_f16_f32 v232, v40, v41\n"
M_"v[52:55],v[16:19],v[84:87],v[52:55]\n"
"v_cvt_pk_f16_f32 v233, v42, v43\n"
M_"v[48:51],v[20:23],v[88:91],v[48:51]\n"
R_"v[12:15],v1 offset:16960\n"
M_"v[52:55],v[20:23],v[92:95],v[52:55]\n"
"v_cvt_pk_f16_f32 v236, v44, v45\n"
M_"v[48:51],v[24:27],v[96:99],v[48:51]\n"
R_"v[16:19],v1 offset:17024\n"
M_"v[52:55],v[24:27],v[100:103],v[52:55]\n"
W_"lgkmcnt(3)\n"
"v_cvt_pk_f16_f32 v237, v46, v47\n"
M_"v[48:51],v[28:31],v[104:107],v[48:51]\n"
R_"v[20:23],v1 offset:17088\n"
"v_pk_maximum3_f16 v56, v56, v232, v233\n"
M_"v[52:55],v[28:31],v[108:111],v[52:55]\n"
M_"v[48:51],v[32:35],v[112:115],v[48:51]\n"
R_"v[24:27],v1 offset:17152\n"
"v_pk_maximum3_f16 v57, v57, v236, v237\n"
M_"v[52:55],v[32:35],v[116:119],v[52:55]\n"
M_"v[48:51],v[36:39],v[120:123],v[48:51]\n"
R_"v[28:31],v1 offset:17216\n"
M_"v[52:55],v[36:39],v[124:127],v[52:55]\n"
W_"lgkmcnt(3)\n"
M_"v[40:43],v[8:11],v[64:67],0\n"
R_"v[32:35],v1 offset:17280\n"
M_"v[44:47],v[8:11],v[68:71],0\n"
M_"v[40:43],v[12:15],v[72:75],v[40:43]\n"
R_"v[36:39],v1 offset:17344\n"
M_"v[44:47],v[12:15],v[76:79],v[44:47]\n"
M_"v[40:43],v[16:19],v[80:83],v[40:43]\n"
R_"v[8:11],v2 offset:35840\n"
"v_cvt_pk_f16_f32 v234, v48, v49\n"
M_"v[44:47],v[16:19],v[84:87],v[44:47]\n"
W_"lgkmcnt(3)\n"
"v_cvt_pk_f16_f32 v235, v50, v51\n"
M_"v[40:43],v[20:23],v[88:91],v[40:43]\n"
R_"v[12:15],v2 offset:35904\n"
M_"v[44:47],v[20:23],v[92:95],v[44:47]\n"
"v_cvt_pk_f16_f32 v238, v52, v53\n"
M_"v[40:43],v[24:27],v[96:99],v[40:43]\n"
R_"v[16:19],v2 offset:35968\n"
M_"v[44:47],v[24:27],v[100:103],v[44:47]\n"
"v_cvt_pk_f16_f32 v239, v54, v55\n"
M_"v[40:43],v[28:31],v[104:107],v[40:43]\n"
R_"v[20:23],v2 offset:36032\n"
"v_pk_maximum3_f16 v56, v56, v234, v235\n"
M_"v[44:47],v[28:31],v[108:111],v[44:47]\n"
W_"lgkmcnt(3)\n"
M_"v[40:43],v[32:35],v[112:115],v[40:43]\n"
R_"v[24:27],v2 offset:36096\n"
"v_pk_maximum3_f16 v57, v57, v238, v239\n"
M_"v[44:47],v[32:35],v[116:119],v[44:47]\n"
M_"v[40:43],v[36:39],v[120:123],v[40:43]\n"
R_"v[28:31],v2 offset:36160\n"
M_"v[44:47],v[36:39],v[124:127],v[44:47]\n"
M_"v[48:51],v[8:11],v[64:67],0\n"
R_"v[32:35],v2 offset:36224\n"
M_"v[52:55],v[8:11],v[68:71],0\n"
W_"lgkmcnt(3)\n"
M_"v[48:51],v[12:15],v[72:75],v[48:51]\n"
R_"v[36:39],v2 offset:36288\n"
M_"v[52:55],v[12:15],v[76:79],v[52:55]\n"
M_"v[48:51],v[16:19],v[80:83],v[48:51]\n"
"s_barrier\n"
R_"v[8:11],v2 offset:52736\n"
"v_cvt_pk_f16_f32 v240, v40, v41\n"
M_"v[52:55],v[16:19],v[84:87],v[52:55]\n"
"v_cvt_pk_f16_f32 v241, v42, v43\n"
M_"v[48:51],v[20:23],v[88:91],v[48:51]\n"
R_"v[12:15],v2 offset:52800\n"
M_"v[52:55],v[20:23],v[92:95],v[52:55]\n"
W_"lgkmcnt(3)\n"
"v_cvt_pk_f16_f32 v244, v44, v45\n"
M_"v[48:51],v[24:27],v[96:99],v[48:51]\n"
R_"v[16:19],v2 offset:52864\n"
M_"v[52:55],v[24:27],v[100:103],v[52:55]\n"
"v_cvt_pk_f16_f32 v245, v46, v47\n"
M_"v[48:51],v[28:31],v[104:107],v[48:51]\n"
R_"v[20:23],v2 offset:52928\n"
"v_pk_maximum3_f16 v56, v56, v240, v241\n"
M_"v[52:55],v[28:31],v[108:111],v[52:55]\n"
M_"v[48:51],v[32:35],v[112:115],v[48:51]\n"
R_"v[24:27],v2 offset:52992\n"
"v_pk_maximum3_f16 v57, v57, v244, v245\n"
M_"v[52:55],v[32:35],v[116:119],v[52:55]\n"
W_"lgkmcnt(3)\n"
M_"v[48:51],v[36:39],v[120:123],v[48:51]\n"
R_"v[28:31],v2 offset:53056\n"
M_"v[52:55],v[36:39],v[124:127],v[52:55]\n"
M_"v[40:43],v[8:11],v[64:67],0\n"
R_"v[32:35],v2 offset:53120\n"
M_"v[44:47],v[8:11],v[68:71],0\n"
M_"v[40:43],v[12:15],v[72:75],v[40:43]\n"
R_"v[36:39],v2 offset:53184\n"
M_"v[44:47],v[12:15],v[76:79],v[44:47]\n"
W_"lgkmcnt(3)\n"
M_"v[40:43],v[16:19],v[80:83],v[40:43]\n"
R_"v[8:11],v2 offset:61184\n"
"v_cvt_pk_f16_f32 v242, v48, v49\n"
M_"v[44:47],v[16:19],v[84:87],v[44:47]\n"
"v_cvt_pk_f16_f32 v243, v50, v51\n"
M_"v[40:43],v[20:23],v[88:91],v[40:43]\n"
R_"v[12:15],v2 offset:61248\n"
M_"v[44:47],v[20:23],v[92:95],v[44:47]\n"
"v_cvt_pk_f16_f32 v246, v52, v53\n"
M_"v[40:43],v[24:27],v[96:99],v[40:43]\n"
R_"v[16:19],v2 offset:61312\n"
M_"v[44:47],v[24:27],v[100:103],v[44:47]\n"
W_"lgkmcnt(3)\n"
"v_cvt_pk_f16_f32 v247, v54, v55\n"
M_"v[40:43],v[28:31],v[104:107],v[40:43]\n"
R_"v[20:23],v2 offset:61376\n"
"v_pk_maximum3_f16 v56, v56, v242, v243\n"
M_"v[44:47],v[28:31],v[108:111],v[44:47]\n"
M_"v[40:43],v[32:35],v[112:115],v[40:43]\n"
R_"v[24:27],v2 offset:61440\n"
"v_pk_maximum3_f16 v57, v57, v246, v247\n"
M_"v[44:47],v[32:35],v[116:119],v[44:47]\n"
M_"v[40:43],v[36:39],v[120:123],v[40:43]\n"
R_"v[28:31],v2 offset:61504\n"
M_"v[44:47],v[36:39],v[124:127],v[44:47]\n"
W_"lgkmcnt(3)\n"
M_"v[48:51],v[8:11],v[64:67],0\n"
R_"v[32:35],v2 offset:61568\n"
M_"v[52:55],v[8:11],v[68:71],0\n"
M_"v[48:51],v[12:15],v[72:75],v[48:51]\n"
R_"v[36:39],v2 offset:61632\n"
M_"v[52:55],v[12:15],v[76:79],v[52:55]\n"
M_"v[48:51],v[16:19],v[80:83],v[48:51]\n"
"s_barrier\n"
R_"v[8:11],v1 offset:25344\n"
"v_cvt_pk_f16_f32 v248, v40, v41\n"
M_"v[52:55],v[16:19],v[84:87],v[52:55]\n"
W_"lgkmcnt(3)\n"
"v_cvt_pk_f16_f32 v249, v42, v43\n"
M_"v[48:51],v[20:23],v[88:91],v[48:51]\n"
R_"v[12:15],v1 offset:25408\n"
M_"v[52:55],v[20:23],v[92:95],v[52:55]\n"
"v_cvt_pk_f16_f32 v252, v44, v45\n"
M_"v[48:51],v[24:27],v[96:99],v[48:51]\n"
R_"v[16:19],v1 offset:25472\n"
M_"v[52:55],v[24:27],v[100:103],v[52:55]\n"
"v_cvt_pk_f16_f32 v253, v46, v47\n"
M_"v[48:51],v[28:31],v[104:107],v[48:51]\n"
R_"v[20:23],v1 offset:25536\n"
"v_pk_maximum3_f16 v56, v56, v248, v249\n"
M_"v[52:55],v[28:31],v[108:111],v[52:55]\n"
W_"lgkmcnt(3)\n"
M_"v[48:51],v[32:35],v[112:115],v[48:51]\n"
R_"v[24:27],v1 offset:25600\n"
"v_pk_maximum3_f16 v57, v57, v252, v253\n"
M_"v[52:55],v[32:35],v[116:119],v[52:55]\n"
M_"v[48:51],v[36:39],v[120:123],v[48:51]\n"
R_"v[28:31],v1 offset:25664\n"
M_"v[52:55],v[36:39],v[124:127],v[52:55]\n"
M_"v[40:43],v[8:11],v[64:67],0\n"
R_"v[32:35],v1 offset:25728\n"
M_"v[44:47],v[8:11],v[68:71],0\n"
W_"lgkmcnt(3)\n"
M_"v[40:43],v[12:15],v[72:75],v[40:43]\n"
R_"v[36:39],v1 offset:25792\n"
M_"v[44:47],v[12:15],v[76:79],v[44:47]\n"
M_"v[40:43],v[16:19],v[80:83],v[40:43]\n"
R_"v[8:11],v1 offset:33792\n"
"v_cvt_pk_f16_f32 v250, v48, v49\n"
M_"v[44:47],v[16:19],v[84:87],v[44:47]\n"
"v_cvt_pk_f16_f32 v251, v50, v51\n"
M_"v[40:43],v[20:23],v[88:91],v[40:43]\n"
R_"v[12:15],v1 offset:33856\n"
M_"v[44:47],v[20:23],v[92:95],v[44:47]\n"
W_"lgkmcnt(3)\n"
"v_cvt_pk_f16_f32 v254, v52, v53\n"
M_"v[40:43],v[24:27],v[96:99],v[40:43]\n"
R_"v[16:19],v1 offset:33920\n"
M_"v[44:47],v[24:27],v[100:103],v[44:47]\n"
"v_cvt_pk_f16_f32 v255, v54, v55\n"
M_"v[40:43],v[28:31],v[104:107],v[40:43]\n"
R_"v[20:23],v1 offset:33984\n"
"v_pk_maximum3_f16 v56, v56, v250, v251\n"
M_"v[44:47],v[28:31],v[108:111],v[44:47]\n"
M_"v[40:43],v[32:35],v[112:115],v[40:43]\n"
R_"v[24:27],v1 offset:34048\n"
"v_pk_maximum3_f16 v57, v57, v254, v255\n"
M_"v[44:47],v[32:35],v[116:119],v[44:47]\n"
W_"lgkmcnt(3)\n"
M_"v[40:43],v[36:39],v[120:123],v[40:43]\n"
R_"v[28:31],v1 offset:34112\n"
M_"v[44:47],v[36:39],v[124:127],v[44:47]\n"
M_"v[48:51],v[8:11],v[64:67],0\n"
R_"v[32:35],v1 offset:34176\n"
M_"v[52:55],v[8:11],v[68:71],0\n"
M_"v[48:51],v[12:15],v[72:75],v[48:51]\n"
R_"v[36:39],v1 offset:34240\n"
M_"v[52:55],v[12:15],v[76:79],v[52:55]\n"
W_"lgkmcnt(3)\n"
M_"v[48:51],v[16:19],v[80:83],v[48:51]\n"
"s_barrier\n"
R_"v[8:11],v3 offset:4096\n"
"v_cvt_pk_f16_f32 v128, v40, v41\n"
M_"v[52:55],v[16:19],v[84:87],v[52:55]\n"
"v_cvt_pk_f16_f32 v129, v42, v43\n"
M_"v[48:51],v[20:23],v[88:91],v[48:51]\n"
R_"v[12:15],v3 offset:4160\n"
M_"v[52:55],v[20:23],v[92:95],v[52:55]\n"
"v_cvt_pk_f16_f32 v132, v44, v45\n"
M_"v[48:51],v[24:27],v[96:99],v[48:51]\n"
R_"v[16:19],v3 offset:4224\n"
M_"v[52:55],v[24:27],v[100:103],v[52:55]\n"
W_"lgkmcnt(3)\n"
"v_cvt_pk_f16_f32 v133, v46, v47\n"
M_"v[48:51],v[28:31],v[104:107],v[48:51]\n"
R_"v[20:23],v3 offset:4288\n"
"v_pk_maximum3_f16 v56, v56, v128, v129\n"
M_"v[52:55],v[28:31],v[108:111],v[52:55]\n"
M_"v[48:51],v[32:35],v[112:115],v[48:51]\n"
"PTH11:\n"
"s_cbranch_execz PTR\n"
R_"v[24:27],v3 offset:4352\n"
"v_pk_maximum3_f16 v57, v57, v132, v133\n"
M_"v[52:55],v[32:35],v[116:119],v[52:55]\n"
M_"v[48:51],v[36:39],v[120:123],v[48:51]\n"
R_"v[28:31],v3 offset:4416\n"
M_"v[52:55],v[36:39],v[124:127],v[52:55]\n"
W_"lgkmcnt(3)\n"
M_"v[40:43],v[8:11],v[64:67],0\n"
R_"v[32:35],v3 offset:4480\n"
M_"v[44:47],v[8:11],v[68:71],0\n"
M_"v[40:43],v[12:15],v[72:75],v[40:43]\n"
R_"v[36:39],v3 offset:4544\n"
M_"v[44:47],v[12:15],v[76:79],v[44:47]\n"
M_"v[40:43],v[16:19],v[80:83],v[40:43]\n"
R_"v[8:11],v3 offset:12544\n"
"v_cvt_pk_f16_f32 v130, v48, v49\n"
M_"v[44:47],v[16:19],v[84:87],v[44:47]\n"
W_"lgkmcnt(3)\n"
"v_cvt_pk_f16_f32 v131, v50, v51\n"
M_"v[40:43],v[20:23],v[88:91],v[40:43]\n"
R_"v[12:15],v3 offset:12608\n"
M_"v[44:47],v[20:23],v[92:95],v[44:47]\n"
"v_cvt_pk_f16_f32 v134, v52, v53\n"
M_"v[40:43],v[24:27],v[96:99],v[40:43]\n"
R_"v[16:19],v3 offset:12672\n"
M_"v[44:47],v[24:27],v[100:103],v[44:47]\n"
"v_cvt_pk_f16_f32 v135, v54, v55\n"
M_"v[40:43],v[28:31],v[104:107],v[40:43]\n"
R_"v[20:23],v3 offset:12736\n"
"v_pk_maximum3_f16 v56, v56, v130, v131\n"
M_"v[44:47],v[28:31],v[108:111],v[44:47]\n"
W_"lgkmcnt(3)\n"
M_"v[40:43],v[32:35],v[112:115],v[40:43]\n"
R_"v[24:27],v3 offset:12800\n"
"v_pk_maximum3_f16 v57, v57, v134, v135\n"
M_"v[44:47],v[32:35],v[116:119],v[44:47]\n"
M_"v[40:43],v[36:39],v[120:123],v[40:43]\n"
R_"v[28:31],v3 offset:12864\n"
M_"v[44:47],v[36:39],v[124:127],v[44:47]\n"
M_"v[48:51],v[8:11],v[64:67],0\n"
R_"v[32:35],v3 offset:12928\n"
M_"v[52:55],v[8:11],v[68:71],0\n"
W_"lgkmcnt(3)\n"
M_"v[48:51],v[12:15],v[72:75],v[48:51]\n"
R_"v[36:39],v3 offset:12992\n"
M_"v[52:55],v[12:15],v[76:79],v[52:55]\n"
M_"v[48:51],v[16:19],v[80:83],v[48:51]\n"
"s_barrier\n"
R_"v[8:11],v1 offset:0\n"
"v_cvt_pk_f16_f32 v136, v40, v41\n"
M_"v[52:55],v[16:19],v[84:87],v[52:55]\n"
"v_cvt_pk_f16_f32 v137, v42, v43\n"
M_"v[48:51],v[20:23],v[88:91],v[48:51]\n"
R_"v[12:15],v1 offset:64\n"
M_"v[52:55],v[20:23],v[92:95],v[52:55]\n"
W_"lgkmcnt(3)\n"
"v_cvt_pk_f16_f32 v140, v44, v45\n"
M_"v[48:51],v[24:27],v[96:99],v[48:51]\n"
R_"v[16:19],v1 offset:128\n"
M_"v[52:55],v[24:27],v[100:103],v[52:55]\n"
"v_cvt_pk_f16_f32 v141, v46, v47\n"
M_"v[48:51],v[28:31],v[104:107],v[48:51]\n"
R_"v[20:23],v1 offset:192\n"
"v_pk_maximum3_f16 v56, v56, v136, v137\n"
M_"v[52:55],v[28:31],v[108:111],v[52:55]\n"
M_"v[48:51],v[32:35],v[112:115],v[48:51]\n"
R_"v[24:27],v1 offset:256\n"
"v_pk_maximum3_f16 v57, v57, v140, v141\n"
M_"v[52:55],v[32:35],v[116:119],v[52:55]\n"
W_"lgkmcnt(3)\n"
M_"v[48:51],v[36:39],v[120:123],v[48:51]\n"
R_"v[28:31],v1 offset:320\n"
M_"v[52:55],v[36:39],v[124:127],v[52:55]\n"
M_"v[40:43],v[8:11],v[64:67],0\n"
R_"v[32:35],v1 offset:384\n"
M_"v[44:47],v[8:11],v[68:71],0\n"
M_"v[40:43],v[12:15],v[72:75],v[40:43]\n"
R_"v[36:39],v1 offset:448\n"
M_"v[44:47],v[12:15],v[76:79],v[44:47]\n"
W_"lgkmcnt(3)\n"
M_"v[40:43],v[16:19],v[80:83],v[40:43]\n"
R_"v[8:11],v1 offset:42240\n"
"v_cvt_pk_f16_f32 v138, v48, v49\n"
M_"v[44:47],v[16:19],v[84:87],v[44:47]\n"
"v_cvt_pk_f16_f32 v139, v50, v51\n"
M_"v[40:43],v[20:23],v[88:91],v[40:43]\n"
R_"v[12:15],v1 offset:42304\n"
M_"v[44:47],v[20:23],v[92:95],v[44:47]\n"
"v_cvt_pk_f16_f32 v142, v52, v53\n"
M_"v[40:43],v[24:27],v[96:99],v[40:43]\n"
R_"v[16:19],v1 offset:42368\n"
M_"v[44:47],v[24:27],v[100:103],v[44:47]\n"
W_"lgkmcnt(3)\n"
"v_cvt_pk_f16_f32 v143, v54, v55\n"
M_"v[40:43],v[28:31],v[104:107],v[40:43]\n"
R_"v[20:23],v1 offset:42432\n"
"v_pk_maximum3_f16 v56, v56, v138, v139\n"
M_"v[44:47],v[28:31],v[108:111],v[44:47]\n"
M_"v[40:43],v[32:35],v[112:115],v[40:43]\n"
R_"v[24:27],v1 offset:42496\n"
"v_pk_maximum3_f16 v57, v57, v142, v143\n"
M_"v[44:47],v[32:35],v[116:119],v[44:47]\n"
M_"v[40:43],v[36:39],v[120:123],v[40:43]\n"
R_"v[28:31],v1 offset:42560\n"
M_"v[44:47],v[36:39],v[124:127],v[44:47]\n"
W_"lgkmcnt(3)\n"
M_"v[48:51],v[8:11],v[64:67],0\n"
R_"v[32:35],v1 offset:42624\n"
M_"v[52:55],v[8:11],v[68:71],0\n"
M_"v[48:51],v[12:15],v[72:75],v[48:51]\n"
R_"v[36:39],v1 offset:42688\n"
M_"v[52:55],v[12:15],v[76:79],v[52:55]\n"
M_"v[48:51],v[16:19],v[80:83],v[48:51]\n"
"s_barrier\n"
R_"v[8:11],v1 offset:59136\n"
"v_cvt_pk_f16_f32 v144, v40, v41\n"
M_"v[52:55],v[16:19],v[84:87],v[52:55]\n"
W_"lgkmcnt(3)\n"
"v_cvt_pk_f16_f32 v145, v42, v43\n"
M_"v[48:51],v[20:23],v[88:91],v[48:51]\n"
R_"v[12:15],v1 offset:59200\n"
M_"v[52:55],v[20:23],v[92:95],v[52:55]\n"
"v_cvt_pk_f16_f32 v148, v44, v45\n"
M_"v[48:51],v[24:27],v[96:99],v[48:51]\n"
R_"v[16:19],v1 offset:59264\n"
M_"v[52:55],v[24:27],v[100:103],v[52:55]\n"
"v_cvt_pk_f16_f32 v149, v46, v47\n"
M_"v[48:51],v[28:31],v[104:107],v[48:51]\n"
R_"v[20:23],v1 offset:59328\n"
"v_pk_maximum3_f16 v56, v56, v144, v145\n"
M_"v[52:55],v[28:31],v[108:111],v[52:55]\n"
W_"lgkmcnt(3)\n"
M_"v[48:51],v[32:35],v[112:115],v[48:51]\n"
R_"v[24:27],v1 offset:59392\n"
"v_pk_maximum3_f16 v57, v57, v148, v149\n"
M_"v[52:55],v[32:35],v[116:119],v[52:55]\n"
M_"v[48:51],v[36:39],v[120:123],v[48:51]\n"
R_"v[28:31],v1 offset:59456\n"
M_"v[52:55],v[36:39],v[124:127],v[52:55]\n"
M_"v[40:43],v[8:11],v[64:67],0\n"
R_"v[32:35],v1 offset:59520\n"
M_"v[44:47],v[8:11],v[68:71],0\n"
W_"lgkmcnt(3)\n"
M_"v[40:43],v[12:15],v[72:75],v[40:43]\n"
R_"v[36:39],v1 offset:59584\n"
M_"v[44:47],v[12:15],v[76:79],v[44:47]\n"
M_"v[40:43],v[16:19],v[80:83],v[40:43]\n"
R_"v[8:11],v3 offset:20992\n"
"v_cvt_pk_f16_f32 v146, v48, v49\n"
M_"v[44:47],v[16:19],v[84:87],v[44:47]\n"
"v_cvt_pk_f16_f32 v147, v50, v51\n"
M_"v[40:43],v[20:23],v[88:91],v[40:43]\n"
R_"v[12:15],v3 offset:21056\n"
M_"v[44:47],v[20:23],v[92:95],v[44:47]\n"
W_"lgkmcnt(3)\n"
"v_cvt_pk_f16_f32 v150, v52, v53\n"
M_"v[40:43],v[24:27],v[96:99],v[40:43]\n"
R_"v[16:19],v3 offset:21120\n"
M_"v[44:47],v[24:27],v[100:103],v[44:47]\n"
"v_cvt_pk_f16_f32 v151, v54, v55\n"
M_"v[40:43],v[28:31],v[104:107],v[40:43]\n"
R_"v[20:23],v3 offset:21184\n"
"v_pk_maximum3_f16 v56, v56, v146, v147\n"
M_"v[44:47],v[28:31],v[108:111],v[44:47]\n"
M_"v[40:43],v[32:35],v[112:115],v[40:43]\n"
R_"v[24:27],v3 offset:21248\n"
"v_pk_maximum3_f16 v57, v57, v150, v151\n"
M_"v[44:47],v[32:35],v[116:119],v[44:47]\n"
W_"lgkmcnt(3)\n"
M_"v[40:43],v[36:39],v[120:123],v[40:43]\n"
R_"v[28:31],v3 offset:21312\n"
M_"v[44:47],v[36:39],v[124:127],v[44:47]\n"
M_"v[48:51],v[8:11],v[64:67],0\n"
R_"v[32:35],v3 offset:21376\n"
M_"v[52:55],v[8:11],v[68:71],0\n"
M_"v[48:51],v[12:15],v[72:75],v[48:51]\n"
R_"v[36:39],v3 offset:21440\n"
M_"v[52:55],v[12:15],v[76:79],v[52:55]\n"
W_"lgkmcnt(3)\n"
M_"v[48:51],v[16:19],v[80:83],v[48:51]\n"
"s_barrier\n"
R_"v[8:11],v2 offset:2048\n"
"v_cvt_pk_f16_f32 v152, v40, v41\n"
M_"v[52:55],v[16:19],v[84:87],v[52:55]\n"
"v_cvt_pk_f16_f32 v153, v42, v43\n"
M_"v[48:51],v[20:23],v[88:91],v[48:51]\n"
R_"v[12:15],v2 offset:2112\n"
M_"v[52:55],v[20:23],v[92:95],v[52:55]\n"
"v_cvt_pk_f16_f32 v156, v44, v45\n"
M_"v[48:51],v[24:27],v[96:99],v[48:51]\n"
R_"v[16:19],v2 offset:2176\n"
M_"v[52:55],v[24:27],v[100:103],v[52:55]\n"
W_"lgkmcnt(3)\n"
"v_cvt_pk_f16_f32 v157, v46, v47\n"
M_"v[48:51],v[28:31],v[104:107],v[48:51]\n"
R_"v[20:23],v2 offset:2240\n"
"v_pk_maximum3_f16 v56, v56, v152, v153\n"
M_"v[52:55],v[28:31],v[108:111],v[52:55]\n"
M_"v[48:51],v[32:35],v[112:115],v[48:51]\n"
R_"v[24:27],v2 offset:2304\n"
"v_pk_maximum3_f16 v57, v57, v156, v157\n"
M_"v[52:55],v[32:35],v[116:119],v[52:55]\n"
M_"v[48:51],v[36:39],v[120:123],v[48:51]\n"
R_"v[28:31],v2 offset:2368\n"
M_"v[52:55],v[36:39],v[124:127],v[52:55]\n"
W_"lgkmcnt(3)\n"
M_"v[40:43],v[8:11],v[64:67],0\n"
R_"v[32:35],v2 offset:2432\n"
M_"v[44:47],v[8:11],v[68:71],0\n"
M_"v[40:43],v[12:15],v[72:75],v[40:43]\n"
R_"v[36:39],v2 offset:2496\n"
M_"v[44:47],v[12:15],v[76:79],v[44:47]\n"
M_"v[40:43],v[16:19],v[80:83],v[40:43]\n"
R_"v[8:11],v2 offset:10496\n"
"v_cvt_pk_f16_f32 v154, v48, v49\n"
M_"v[44:47],v[16:19],v[84:87],v[44:47]\n"
W_"lgkmcnt(3)\n"
"v_cvt_pk_f16_f32 v155, v50, v51\n"
M_"v[40:43],v[20:23],v[88:91],v[40:43]\n"
R_"v[12:15],v2 offset:10560\n"
M_"v[44:47],v[20:23],v[92:95],v[44:47]\n"
"v_cvt_pk_f16_f32 v158, v52, v53\n"
M_"v[40:43],v[24:27],v[96:99],v[40:43]\n"
R_"v[16:19],v2 offset:10624\n"
M_"v[44:47],v[24:27],v[100:103],v[44:47]\n"
"v_cvt_pk_f16_f32 v159, v54, v55\n"
M_"v[40:43],v[28:31],v[104:107],v[40:43]\n"
R_"v[20:23],v2 offset:10688\n"
"v_pk_maximum3_f16 v56, v56, v154, v155\n"
M_"v[44:47],v[28:31],v[108:111],v[44:47]\n"
W_"lgkmcnt(3)\n"
M_"v[40:43],v[32:35],v[112:115],v[40:43]\n"
R_"v[24:27],v2 offset:10752\n"
"v_pk_maximum3_f16 v57, v57, v158, v159\n"
M_"v[44:47],v[32:35],v[116:119],v[44:47]\n"
M_"v[40:43],v[36:39],v[120:123],v[40:43]\n"
R_"v[28:31],v2 offset:10816\n"
M_"v[44:47],v[36:39],v[124:127],v[44:47]\n"
M_"v[48:51],v[8:11],v[64:67],0\n"
R_"v[32:35],v2 offset:10880\n"
M_"v[52:55],v[8:11],v[68:71],0\n"
W_"lgkmcnt(3)\n"
M_"v[48:51],v[12:15],v[72:75],v[48:51]\n"
R_"v[36:39],v2 offset:10944\n"
M_"v[52:55],v[12:15],v[76:79],v[52:55]\n"
M_"v[48:51],v[16:19],v[80:83],v[48:51]\n"
"s_barrier\n"
R_"v[8:11],v2 offset:18944\n"
"v_cvt_pk_f16_f32 v160, v40, v41\n"
M_"v[52:55],v[16:19],v[84:87],v[52:55]\n"
"v_cvt_pk_f16_f32 v161, v42, v43\n"
M_"v[48:51],v[20:23],v[88:91],v[48:51]\n"
R_"v[12:15],v2 offset:19008\n"
M_"v[52:55],v[20:23],v[92:95],v[52:55]\n"
W_"lgkmcnt(3)\n"
"v_cvt_pk_f16_f32 v164, v44, v45\n"
M_"v[48:51],v[24:27],v[96:99],v[48:51]\n"
R_"v[16:19],v2 offset:19072\n"
M_"v[52:55],v[24:27],v[100:103],v[52:55]\n"
"v_cvt_pk_f16_f32 v165, v46, v47\n"
M_"v[48:51],v[28:31],v[104:107],v[48:51]\n"
R_"v[20:23],v2 offset:19136\n"
"v_pk_maximum3_f16 v56, v56, v160, v161\n"
M_"v[52:55],v[28:31],v[108:111],v[52:55]\n"
M_"v[48:51],v[32:35],v[112:115],v[48:51]\n"
R_"v[24:27],v2 offset:19200\n"
"v_pk_maximum3_f16 v57, v57, v164, v165\n"
M_"v[52:55],v[32:35],v[116:119],v[52:55]\n"
W_"lgkmcnt(3)\n"
M_"v[48:51],v[36:39],v[120:123],v[48:51]\n"
R_"v[28:31],v2 offset:19264\n"
M_"v[52:55],v[36:39],v[124:127],v[52:55]\n"
M_"v[40:43],v[8:11],v[64:67],0\n"
R_"v[32:35],v2 offset:19328\n"
M_"v[44:47],v[8:11],v[68:71],0\n"
M_"v[40:43],v[12:15],v[72:75],v[40:43]\n"
R_"v[36:39],v2 offset:19392\n"
M_"v[44:47],v[12:15],v[76:79],v[44:47]\n"
W_"lgkmcnt(3)\n"
M_"v[40:43],v[16:19],v[80:83],v[40:43]\n"
R_"v[8:11],v1 offset:16896\n"
"v_cvt_pk_f16_f32 v162, v48, v49\n"
M_"v[44:47],v[16:19],v[84:87],v[44:47]\n"
"v_cvt_pk_f16_f32 v163, v50, v51\n"
M_"v[40:43],v[20:23],v[88:91],v[40:43]\n"
R_"v[12:15],v1 offset:16960\n"
M_"v[44:47],v[20:23],v[92:95],v[44:47]\n"
"v_cvt_pk_f16_f32 v166, v52, v53\n"
M_"v[40:43],v[24:27],v[96:99],v[40:43]\n"
R_"v[16:19],v1 offset:17024\n"
M_"v[44:47],v[24:27],v[100:103],v[44:47]\n"
W_"lgkmcnt(3)\n"
"v_cvt_pk_f16_f32 v167, v54, v55\n"
M_"v[40:43],v[28:31],v[104:107],v[40:43]\n"
R_"v[20:23],v1 offset:17088\n"
"v_pk_maximum3_f16 v56, v56, v162, v163\n"
M_"v[44:47],v[28:31],v[108:111],v[44:47]\n"
M_"v[40:43],v[32:35],v[112:115],v[40:43]\n"
R_"v[24:27],v1 offset:17152\n"
"v_pk_maximum3_f16 v57, v57, v166, v167\n"
M_"v[44:47],v[32:35],v[116:119],v[44:47]\n"
M_"v[40:43],v[36:39],v[120:123],v[40:43]\n"
R_"v[28:31],v1 offset:17216\n"
M_"v[44:47],v[36:39],v[124:127],v[44:47]\n"
W_"lgkmcnt(3)\n"
M_"v[48:51],v[8:11],v[64:67],0\n"
R_"v[32:35],v1 offset:17280\n"
M_"v[52:55],v[8:11],v[68:71],0\n"
M_"v[48:51],v[12:15],v[72:75],v[48:51]\n"
R_"v[36:39],v1 offset:17344\n"
M_"v[52:55],v[12:15],v[76:79],v[52:55]\n"
M_"v[48:51],v[16:19],v[80:83],v[48:51]\n"
"s_barrier\n"
R_"v[8:11],v1 offset:50688\n"
"v_cvt_pk_f16_f32 v168, v40, v41\n"
M_"v[52:55],v[16:19],v[84:87],v[52:55]\n"
W_"lgkmcnt(3)\n"
"v_cvt_pk_f16_f32 v169, v42, v43\n"
M_"v[48:51],v[20:23],v[88:91],v[48:51]\n"
R_"v[12:15],v1 offset:50752\n"
M_"v[52:55],v[20:23],v[92:95],v[52:55]\n"
"v_cvt_pk_f16_f32 v172, v44, v45\n"
M_"v[48:51],v[24:27],v[96:99],v[48:51]\n"
R_"v[16:19],v1 offset:50816\n"
M_"v[52:55],v[24:27],v[100:103],v[52:55]\n"
"v_cvt_pk_f16_f32 v173, v46, v47\n"
M_"v[48:51],v[28:31],v[104:107],v[48:51]\n"
R_"v[20:23],v1 offset:50880\n"
"v_pk_maximum3_f16 v56, v56, v168, v169\n"
M_"v[52:55],v[28:31],v[108:111],v[52:55]\n"
W_"lgkmcnt(3)\n"
M_"v[48:51],v[32:35],v[112:115],v[48:51]\n"
R_"v[24:27],v1 offset:50944\n"
"v_pk_maximum3_f16 v57, v57, v172, v173\n"
M_"v[52:55],v[32:35],v[116:119],v[52:55]\n"
M_"v[48:51],v[36:39],v[120:123],v[48:51]\n"
R_"v[28:31],v1 offset:51008\n"
M_"v[52:55],v[36:39],v[124:127],v[52:55]\n"
M_"v[40:43],v[8:11],v[64:67],0\n"
R_"v[32:35],v1 offset:51072\n"
M_"v[44:47],v[8:11],v[68:71],0\n"
W_"lgkmcnt(3)\n"
M_"v[40:43],v[12:15],v[72:75],v[40:43]\n"
R_"v[36:39],v1 offset:51136\n"
M_"v[44:47],v[12:15],v[76:79],v[44:47]\n"
M_"v[40:43],v[16:19],v[80:83],v[40:43]\n"
R_"v[8:11],v2 offset:27392\n"
"v_cvt_pk_f16_f32 v170, v48, v49\n"
M_"v[44:47],v[16:19],v[84:87],v[44:47]\n"
"v_cvt_pk_f16_f32 v171, v50, v51\n"
M_"v[40:43],v[20:23],v[88:91],v[40:43]\n"
R_"v[12:15],v2 offset:27456\n"
M_"v[44:47],v[20:23],v[92:95],v[44:47]\n"
W_"lgkmcnt(3)\n"
"v_cvt_pk_f16_f32 v174, v52, v53\n"
M_"v[40:43],v[24:27],v[96:99],v[40:43]\n"
R_"v[16:19],v2 offset:27520\n"
M_"v[44:47],v[24:27],v[100:103],v[44:47]\n"
"v_cvt_pk_f16_f32 v175, v54, v55\n"
M_"v[40:43],v[28:31],v[104:107],v[40:43]\n"
R_"v[20:23],v2 offset:27584\n"
"v_pk_maximum3_f16 v56, v56, v170, v171\n"
M_"v[44:47],v[28:31],v[108:111],v[44:47]\n"
M_"v[40:43],v[32:35],v[112:115],v[40:43]\n"
R_"v[24:27],v2 offset:27648\n"
"v_pk_maximum3_f16 v57, v57, v174, v175\n"
M_"v[44:47],v[32:35],v[116:119],v[44:47]\n"
W_"lgkmcnt(3)\n"
M_"v[40:43],v[36:39],v[120:123],v[40:43]\n"
R_"v[28:31],v2 offset:27712\n"
M_"v[44:47],v[36:39],v[124:127],v[44:47]\n"
M_"v[48:51],v[8:11],v[64:67],0\n"
R_"v[32:35],v2 offset:27776\n"
M_"v[52:55],v[8:11],v[68:71],0\n"
M_"v[48:51],v[12:15],v[72:75],v[48:51]\n"
R_"v[36:39],v2 offset:27840\n"
M_"v[52:55],v[12:15],v[76:79],v[52:55]\n"
W_"lgkmcnt(3)\n"
M_"v[48:51],v[16:19],v[80:83],v[48:51]\n"
"s_barrier\n"
R_"v[8:11],v2 offset:35840\n"
"v_cvt_pk_f16_f32 v176, v40, v41\n"
M_"v[52:55],v[16:19],v[84:87],v[52:55]\n"
"v_cvt_pk_f16_f32 v177, v42, v43\n"
M_"v[48:51],v[20:23],v[88:91],v[48:51]\n"
R_"v[12:15],v2 offset:35904\n"
M_"v[52:55],v[20:23],v[92:95],v[52:55]\n"
"v_cvt_pk_f16_f32 v180, v44, v45\n"
M_"v[48:51],v[24:27],v[96:99],v[48:51]\n"
R_"v[16:19],v2 offset:35968\n"
M_"v[52:55],v[24:27],v[100:103],v[52:55]\n"
W_"lgkmcnt(3)\n"
"v_cvt_pk_f16_f32 v181, v46, v47\n"
M_"v[48:51],v[28:31],v[104:107],v[48:51]\n"
R_"v[20:23],v2 offset:36032\n"
"v_pk_maximum3_f16 v56, v56, v176, v177\n"
M_"v[52:55],v[28:31],v[108:111],v[52:55]\n"
M_"v[48:51],v[32:35],v[112:115],v[48:51]\n"
R_"v[24:27],v2 offset:36096\n"
"v_pk_maximum3_f16 v57, v57, v180, v181\n"
M_"v[52:55],v[32:35],v[116:119],v[52:55]\n"
M_"v[48:51],v[36:39],v[120:123],v[48:51]\n"
R_"v[28:31],v2 offset:36160\n"
M_"v[52:55],v[36:39],v[124:127],v[52:55]\n"
W_"lgkmcnt(3)\n"
M_"v[40:43],v[8:11],v[64:67],0\n"
R_"v[32:35],v2 offset:36224\n"
M_"v[44:47],v[8:11],v[68:71],0\n"
M_"v[40:43],v[12:15],v[72:75],v[40:43]\n"
R_"v[36:39],v2 offset:36288\n"
M_"v[44:47],v[12:15],v[76:79],v[44:47]\n"
M_"v[40:43],v[16:19],v[80:83],v[40:43]\n"
R_"v[8:11],v1 offset:25344\n"
"v_cvt_pk_f16_f32 v178, v48, v49\n"
M_"v[44:47],v[16:19],v[84:87],v[44:47]\n"
W_"lgkmcnt(3)\n"
"v_cvt_pk_f16_f32 v179, v50, v51\n"
M_"v[40:43],v[20:23],v[88:91],v[40:43]\n"
R_"v[12:15],v1 offset:25408\n"
M_"v[44:47],v[20:23],v[92:95],v[44:47]\n"
"v_cvt_pk_f16_f32 v182, v52, v53\n"
M_"v[40:43],v[24:27],v[96:99],v[40:43]\n"
R_"v[16:19],v1 offset:25472\n"
M_"v[44:47],v[24:27],v[100:103],v[44:47]\n"
"v_cvt_pk_f16_f32 v183, v54, v55\n"
M_"v[40:43],v[28:31],v[104:107],v[40:43]\n"
R_"v[20:23],v1 offset:25536\n"
"v_pk_maximum3_f16 v56, v56, v178, v179\n"
M_"v[44:47],v[28:31],v[108:111],v[44:47]\n"
W_"lgkmcnt(3)\n"
M_"v[40:43],v[32:35],v[112:115],v[40:43]\n"
R_"v[24:27],v1 offset:25600\n"
"v_pk_maximum3_f16 v57, v57, v182, v183\n"
M_"v[44:47],v[32:35],v[116:119],v[44:47]\n"
M_"v[40:43],v[36:39],v[120:123],v[40:43]\n"
R_"v[28:31],v1 offset:25664\n"
M_"v[44:47],v[36:39],v[124:127],v[44:47]\n"
M_"v[48:51],v[8:11],v[64:67],0\n"
R_"v[32:35],v1 offset:25728\n"
M_"v[52:55],v[8:11],v[68:71],0\n"
W_"lgkmcnt(3)\n"
M_"v[48:51],v[12:15],v[72:75],v[48:51]\n"
R_"v[36:39],v1 offset:25792\n"
M_"v[52:55],v[12:15],v[76:79],v[52:55]\n"
M_"v[48:51],v[16:19],v[80:83],v[48:51]\n"
"v_cvt_pk_f16_f32 v184, v40, v41\n"
M_"v[52:55],v[16:19],v[84:87],v[52:55]\n"
"v_cvt_pk_f16_f32 v185, v42, v43\n"
M_"v[48:51],v[20:23],v[88:91],v[48:51]\n"
"s_barrier\n"
M_"v[52:55],v[20:23],v[92:95],v[52:55]\n"
W_"lgkmcnt(1)\n"
"v_cvt_pk_f16_f32 v188, v44, v45\n"
M_"v[48:51],v[24:27],v[96:99],v[48:51]\n"
M_"v[52:55],v[24:27],v[100:103],v[52:55]\n"
"v_cvt_pk_f16_f32 v189, v46, v47\n"
M_"v[48:51],v[28:31],v[104:107],v[48:51]\n"
"v_pk_maximum3_f16 v56, v56, v184, v185\n"
M_"v[52:55],v[28:31],v[108:111],v[52:55]\n"
M_"v[48:51],v[32:35],v[112:115],v[48:51]\n"
"v_pk_maximum3_f16 v57, v57, v188, v189\n"
M_"v[52:55],v[32:35],v[116:119],v[52:55]\n"
W_"lgkmcnt(0)\n"
M_"v[48:51],v[36:39],v[120:123],v[48:51]\n"
M_"v[52:55],v[36:39],v[124:127],v[52:55]\n"
"s_nop 6\n"
"v_cvt_pk_f16_f32 v186, v48, v49\n"
"v_cvt_pk_f16_f32 v187, v50, v51\n"
"v_cvt_pk_f16_f32 v190, v52, v53\n"
"v_cvt_pk_f16_f32 v191, v54, v55\n"
"v_pk_maximum3_f16 v56, v56, v186, v187\n"
"v_pk_maximum3_f16 v57, v57, v190, v191\n"
"v_pk_max_f16 v48, v56, v56 op_sel:[0,1] op_sel_hi:[1,0]\n"
"v_pk_max_f16 v49, v57, v57 op_sel:[0,1] op_sel_hi:[1,0]\n"
"v_bfi_b32 v48, s93, v49, v48\n"
"v_mov_b32 v49, v48\n"
"s_nop 1\n"
"v_permlane32_swap_b32 v48, v49\n"
"v_pk_max_f16 v48, v48, v49\n"
"v_mov_b32 v49, v48\n"
"s_nop 1\n"
"v_permlane16_swap_b32 v48, v49\n"
"v_pk_max_f16 v48, v48, v49\n"
X_"v50,v48,s94,0 op_sel_hi:[1,0,0]\n"
X_"v51,v48,s94,0 op_sel:[1,0,0] op_sel_hi:[1,0,0]\n"
"v_ceil_f32 v50, v50\n"
"v_ceil_f32 v51, v51\n"
"v_mov_b32 v56, v58\n"
"v_mov_b32 v57, v59\n"
"v_sub_f32 v58, 0xc2c80000, v50\n"
"v_sub_f32 v59, 0xc2c80000, v51\n"
"v_sub_f32 v56, v56, v58\n"
"v_sub_f32 v57, v57, v59\n"
"v_cvt_u32_f32 v56, v56\n"
"v_cvt_u32_f32 v57, v57\n"
"v_min_u32 v56, 0xff, v56\n"
"v_min_u32 v57, 0xff, v57\n"
"v_mul_u32_u24 v56, 0x800080, v56\n"
"v_mul_u32_u24 v57, 0x800080, v57\n"
"v_pk_sub_u16 v192, v192, v56 clamp\n"
"v_pk_sub_u16 v193, v193, v56 clamp\n"
"v_pk_sub_u16 v194, v194, v56 clamp\n"
"v_pk_sub_u16 v195, v195, v56 clamp\n"
"v_pk_sub_u16 v196, v196, v57 clamp\n"
"v_pk_sub_u16 v197, v197, v57 clamp\n"
"v_pk_sub_u16 v198, v198, v57 clamp\n"
"v_pk_sub_u16 v199, v199, v57 clamp\n"
X_"v40,v232,s94,v58 op_sel_hi:[1,0,0]\n"
X_"v41,v232,s94,v58 op_sel:[1,0,0] op_sel_hi:[1,0,0]\n"
X_"v42,v233,s94,v58 op_sel_hi:[1,0,0]\n"
X_"v43,v233,s94,v58 op_sel:[1,0,0] op_sel_hi:[1,0,0]\n"
E_"v40,v40\n"
E_"v41,v41\n"
X_"v44,v234,s94,v58 op_sel_hi:[1,0,0]\n"
X_"v45,v234,s94,v58 op_sel:[1,0,0] op_sel_hi:[1,0,0]\n"
E_"v42,v42\n"
E_"v43,v43\n"
C_"v232,v40,v41\n"
X_"v46,v235,s94,v58 op_sel_hi:[1,0,0]\n"
X_"v47,v235,s94,v58 op_sel:[1,0,0] op_sel_hi:[1,0,0]\n"
E_"v44,v44\n"
"s_barrier\n"
E_"v45,v45\n"
C_"v233,v42,v43\n"
E_"v46,v46\n"
E_"v47,v47\n"
C_"v234,v44,v45\n"
C_"v235,v46,v47\n"
X_"v40,v236,s94,v59 op_sel_hi:[1,0,0]\n"
X_"v41,v236,s94,v59 op_sel:[1,0,0] op_sel_hi:[1,0,0]\n"
X_"v42,v237,s94,v59 op_sel_hi:[1,0,0]\n"
X_"v43,v237,s94,v59 op_sel:[1,0,0] op_sel_hi:[1,0,0]\n"
E_"v40,v40\n"
E_"v41,v41\n"
X_"v44,v238,s94,v59 op_sel_hi:[1,0,0]\n"
X_"v45,v238,s94,v59 op_sel:[1,0,0] op_sel_hi:[1,0,0]\n"
E_"v42,v42\n"
E_"v43,v43\n"
C_"v236,v40,v41\n"
X_"v46,v239,s94,v59 op_sel_hi:[1,0,0]\n"
X_"v47,v239,s94,v59 op_sel:[1,0,0] op_sel_hi:[1,0,0]\n"
E_"v44,v44\n"
E_"v45,v45\n"
C_"v237,v42,v43\n"
E_"v46,v46\n"
E_"v47,v47\n"
C_"v238,v44,v45\n"
C_"v239,v46,v47\n"
X_"v40,v240,s94,v58 op_sel_hi:[1,0,0]\n"
X_"v41,v240,s94,v58 op_sel:[1,0,0] op_sel_hi:[1,0,0]\n"
X_"v42,v241,s94,v58 op_sel_hi:[1,0,0]\n"
X_"v43,v241,s94,v58 op_sel:[1,0,0] op_sel_hi:[1,0,0]\n"
E_"v40,v40\n"
E_"v41,v41\n"
X_"v44,v242,s94,v58 op_sel_hi:[1,0,0]\n"
X_"v45,v242,s94,v58 op_sel:[1,0,0] op_sel_hi:[1,0,0]\n"
E_"v42,v42\n"
"PTH12:\n"
"s_cbranch_execz PTR\n"
E_"v43,v43\n"
C_"v240,v40,v41\n"
X_"v46,v243,s94,v58 op_sel_hi:[1,0,0]\n"
X_"v47,v243,s94,v58 op_sel:[1,0,0] op_sel_hi:[1,0,0]\n"
E_"v44,v44\n"
E_"v45,v45\n"
C_"v241,v42,v43\n"
E_"v46,v46\n"
E_"v47,v47\n"
C_"v242,v44,v45\n"
C_"v243,v46,v47\n"
X_"v40,v244,s94,v59 op_sel_hi:[1,0,0]\n"
X_"v41,v244,s94,v59 op_sel:[1,0,0] op_sel_hi:[1,0,0]\n"
X_"v42,v245,s94,v59 op_sel_hi:[1,0,0]\n"
X_"v43,v245,s94,v59 op_sel:[1,0,0] op_sel_hi:[1,0,0]\n"
E_"v40,v40\n"
E_"v41,v41\n"
X_"v44,v246,s94,v59 op_sel_hi:[1,0,0]\n"
X_"v45,v246,s94,v59 op_sel:[1,0,0] op_sel_hi:[1,0,0]\n"
"s_barrier\n"
E_"v42,v42\n"
E_"v43,v43\n"
C_"v244,v40,v41\n"
X_"v46,v247,s94,v59 op_sel_hi:[1,0,0]\n"
X_"v47,v247,s94,v59 op_sel:[1,0,0] op_sel_hi:[1,0,0]\n"
E_"v44,v44\n"
E_"v45,v45\n"
C_"v245,v42,v43\n"
E_"v46,v46\n"
E_"v47,v47\n"
C_"v246,v44,v45\n"
C_"v247,v46,v47\n"
X_"v40,v248,s94,v58 op_sel_hi:[1,0,0]\n"
X_"v41,v248,s94,v58 op_sel:[1,0,0] op_sel_hi:[1,0,0]\n"
X_"v42,v249,s94,v58 op_sel_hi:[1,0,0]\n"
X_"v43,v249,s94,v58 op_sel:[1,0,0] op_sel_hi:[1,0,0]\n"
E_"v40,v40\n"
E_"v41,v41\n"
X_"v44,v250,s94,v58 op_sel_hi:[1,0,0]\n"
X_"v45,v250,s94,v58 op_sel:[1,0,0] op_sel_hi:[1,0,0]\n"
E_"v42,v42\n"
E_"v43,v43\n"
C_"v248,v40,v41\n"
X_"v46,v251,s94,v58 op_sel_hi:[1,0,0]\n"
X_"v47,v251,s94,v58 op_sel:[1,0,0] op_sel_hi:[1,0,0]\n"
E_"v44,v44\n"
E_"v45,v45\n"
C_"v249,v42,v43\n"
E_"v46,v46\n"
E_"v47,v47\n"
C_"v250,v44,v45\n"
C_"v251,v46,v47\n"
X_"v40,v252,s94,v59 op_sel_hi:[1,0,0]\n"
X_"v41,v252,s94,v59 op_sel:[1,0,0] op_sel_hi:[1,0,0]\n"
X_"v42,v253,s94,v59 op_sel_hi:[1,0,0]\n"
X_"v43,v253,s94,v59 op_sel:[1,0,0] op_sel_hi:[1,0,0]\n"
E_"v40,v40\n"
E_"v41,v41\n"
X_"v44,v254,s94,v59 op_sel_hi:[1,0,0]\n"
X_"v45,v254,s94,v59 op_sel:[1,0,0] op_sel_hi:[1,0,0]\n"
E_"v42,v42\n"
E_"v43,v43\n"
C_"v252,v40,v41\n"
X_"v46,v255,s94,v59 op_sel_hi:[1,0,0]\n"
X_"v47,v255,s94,v59 op_sel:[1,0,0] op_sel_hi:[1,0,0]\n"
E_"v44,v44\n"
E_"v45,v45\n"
C_"v253,v42,v43\n"
E_"v46,v46\n"
E_"v47,v47\n"
C_"v254,v44,v45\n"
C_"v255,v46,v47\n"
X_"v40,v128,s94,v58 op_sel_hi:[1,0,0]\n"
X_"v41,v128,s94,v58 op_sel:[1,0,0] op_sel_hi:[1,0,0]\n"
X_"v42,v129,s94,v58 op_sel_hi:[1,0,0]\n"
"s_barrier\n"
X_"v43,v129,s94,v58 op_sel:[1,0,0] op_sel_hi:[1,0,0]\n"
E_"v40,v40\n"
E_"v41,v41\n"
X_"v44,v130,s94,v58 op_sel_hi:[1,0,0]\n"
X_"v45,v130,s94,v58 op_sel:[1,0,0] op_sel_hi:[1,0,0]\n"
E_"v42,v42\n"
E_"v43,v43\n"
C_"v128,v40,v41\n"
X_"v46,v131,s94,v58 op_sel_hi:[1,0,0]\n"
X_"v47,v131,s94,v58 op_sel:[1,0,0] op_sel_hi:[1,0,0]\n"
E_"v44,v44\n"
E_"v45,v45\n"
C_"v129,v42,v43\n"
E_"v46,v46\n"
E_"v47,v47\n"
C_"v130,v44,v45\n"
C_"v131,v46,v47\n"
X_"v40,v132,s94,v59 op_sel_hi:[1,0,0]\n"
X_"v41,v132,s94,v59 op_sel:[1,0,0] op_sel_hi:[1,0,0]\n"
X_"v42,v133,s94,v59 op_sel_hi:[1,0,0]\n"
X_"v43,v133,s94,v59 op_sel:[1,0,0] op_sel_hi:[1,0,0]\n"
E_"v40,v40\n"
E_"v41,v41\n"
X_"v44,v134,s94,v59 op_sel_hi:[1,0,0]\n"
X_"v45,v134,s94,v59 op_sel:[1,0,0] op_sel_hi:[1,0,0]\n"
E_"v42,v42\n"
E_"v43,v43\n"
C_"v132,v40,v41\n"
X_"v46,v135,s94,v59 op_sel_hi:[1,0,0]\n"
X_"v47,v135,s94,v59 op_sel:[1,0,0] op_sel_hi:[1,0,0]\n"
E_"v44,v44\n"
E_"v45,v45\n"
C_"v133,v42,v43\n"
E_"v46,v46\n"
E_"v47,v47\n"
C_"v134,v44,v45\n"
C_"v135,v46,v47\n"
X_"v40,v136,s94,v58 op_sel_hi:[1,0,0]\n"
X_"v41,v136,s94,v58 op_sel:[1,0,0] op_sel_hi:[1,0,0]\n"
X_"v42,v137,s94,v58 op_sel_hi:[1,0,0]\n"
X_"v43,v137,s94,v58 op_sel:[1,0,0] op_sel_hi:[1,0,0]\n"
E_"v40,v40\n"
E_"v41,v41\n"
X_"v44,v138,s94,v58 op_sel_hi:[1,0,0]\n"
X_"v45,v138,s94,v58 op_sel:[1,0,0] op_sel_hi:[1,0,0]\n"
E_"v42,v42\n"
E_"v43,v43\n"
C_"v136,v40,v41\n"
X_"v46,v139,s94,v58 op_sel_hi:[1,0,0]\n"
X_"v47,v139,s94,v58 op_sel:[1,0,0] op_sel_hi:[1,0,0]\n"
E_"v44,v44\n"
E_"v45,v45\n"
C_"v137,v42,v43\n"
E_"v46,v46\n"
"s_barrier\n"
E_"v47,v47\n"
C_"v138,v44,v45\n"
C_"v139,v46,v47\n"
X_"v40,v140,s94,v59 op_sel_hi:[1,0,0]\n"
X_"v41,v140,s94,v59 op_sel:[1,0,0] op_sel_hi:[1,0,0]\n"
X_"v42,v141,s94,v59 op_sel_hi:[1,0,0]\n"
X_"v43,v141,s94,v59 op_sel:[1,0,0] op_sel_hi:[1,0,0]\n"
E_"v40,v40\n"
E_"v41,v41\n"
X_"v44,v142,s94,v59 op_sel_hi:[1,0,0]\n"
X_"v45,v142,s94,v59 op_sel:[1,0,0] op_sel_hi:[1,0,0]\n"
E_"v42,v42\n"
E_"v43,v43\n"
C_"v140,v40,v41\n"
X_"v46,v143,s94,v59 op_sel_hi:[1,0,0]\n"
X_"v47,v143,s94,v59 op_sel:[1,0,0] op_sel_hi:[1,0,0]\n"
E_"v44,v44\n"
E_"v45,v45\n"
C_"v141,v42,v43\n"
E_"v46,v46\n"
E_"v47,v47\n"
C_"v142,v44,v45\n"
C_"v143,v46,v47\n"
X_"v40,v144,s94,v58 op_sel_hi:[1,0,0]\n"
X_"v41,v144,s94,v58 op_sel:[1,0,0] op_sel_hi:[1,0,0]\n"
X_"v42,v145,s94,v58 op_sel_hi:[1,0,0]\n"
X_"v43,v145,s94,v58 op_sel:[1,0,0] op_sel_hi:[1,0,0]\n"
E_"v40,v40\n"
E_"v41,v41\n"
X_"v44,v146,s94,v58 op_sel_hi:[1,0,0]\n"
X_"v45,v146,s94,v58 op_sel:[1,0,0] op_sel_hi:[1,0,0]\n"
E_"v42,v42\n"
E_"v43,v43\n"
C_"v144,v40,v41\n"
X_"v46,v147,s94,v58 op_sel_hi:[1,0,0]\n"
X_"v47,v147,s94,v58 op_sel:[1,0,0] op_sel_hi:[1,0,0]\n"
E_"v44,v44\n"
E_"v45,v45\n"
C_"v145,v42,v43\n"
E_"v46,v46\n"
E_"v47,v47\n"
C_"v146,v44,v45\n"
C_"v147,v46,v47\n"
X_"v40,v148,s94,v59 op_sel_hi:[1,0,0]\n"
X_"v41,v148,s94,v59 op_sel:[1,0,0] op_sel_hi:[1,0,0]\n"
X_"v42,v149,s94,v59 op_sel_hi:[1,0,0]\n"
X_"v43,v149,s94,v59 op_sel:[1,0,0] op_sel_hi:[1,0,0]\n"
E_"v40,v40\n"
E_"v41,v41\n"
X_"v44,v150,s94,v59 op_sel_hi:[1,0,0]\n"
X_"v45,v150,s94,v59 op_sel:[1,0,0] op_sel_hi:[1,0,0]\n"
E_"v42,v42\n"
E_"v43,v43\n"
C_"v148,v40,v41\n"
X_"v46,v151,s94,v59 op_sel_hi:[1,0,0]\n"
"s_barrier\n"
X_"v47,v151,s94,v59 op_sel:[1,0,0] op_sel_hi:[1,0,0]\n"
E_"v44,v44\n"
E_"v45,v45\n"
C_"v149,v42,v43\n"
E_"v46,v46\n"
E_"v47,v47\n"
C_"v150,v44,v45\n"
C_"v151,v46,v47\n"
X_"v40,v152,s94,v58 op_sel_hi:[1,0,0]\n"
X_"v41,v152,s94,v58 op_sel:[1,0,0] op_sel_hi:[1,0,0]\n"
X_"v42,v153,s94,v58 op_sel_hi:[1,0,0]\n"
X_"v43,v153,s94,v58 op_sel:[1,0,0] op_sel_hi:[1,0,0]\n"
E_"v40,v40\n"
E_"v41,v41\n"
X_"v44,v154,s94,v58 op_sel_hi:[1,0,0]\n"
X_"v45,v154,s94,v58 op_sel:[1,0,0] op_sel_hi:[1,0,0]\n"
E_"v42,v42\n"
E_"v43,v43\n"
C_"v152,v40,v41\n"
X_"v46,v155,s94,v58 op_sel_hi:[1,0,0]\n"
X_"v47,v155,s94,v58 op_sel:[1,0,0] op_sel_hi:[1,0,0]\n"
E_"v44,v44\n"
E_"v45,v45\n"
C_"v153,v42,v43\n"
E_"v46,v46\n"
E_"v47,v47\n"
C_"v154,v44,v45\n"
C_"v155,v46,v47\n"
X_"v40,v156,s94,v59 op_sel_hi:[1,0,0]\n"
X_"v41,v156,s94,v59 op_sel:[1,0,0] op_sel_hi:[1,0,0]\n"
X_"v42,v157,s94,v59 op_sel_hi:[1,0,0]\n"
X_"v43,v157,s94,v59 op_sel:[1,0,0] op_sel_hi:[1,0,0]\n"
E_"v40,v40\n"
E_"v41,v41\n"
X_"v44,v158,s94,v59 op_sel_hi:[1,0,0]\n"
X_"v45,v158,s94,v59 op_sel:[1,0,0] op_sel_hi:[1,0,0]\n"
E_"v42,v42\n"
E_"v43,v43\n"
C_"v156,v40,v41\n"
X_"v46,v159,s94,v59 op_sel_hi:[1,0,0]\n"
X_"v47,v159,s94,v59 op_sel:[1,0,0] op_sel_hi:[1,0,0]\n"
E_"v44,v44\n"
E_"v45,v45\n"
C_"v157,v42,v43\n"
E_"v46,v46\n"
E_"v47,v47\n"
C_"v158,v44,v45\n"
C_"v159,v46,v47\n"
X_"v40,v160,s94,v58 op_sel_hi:[1,0,0]\n"
X_"v41,v160,s94,v58 op_sel:[1,0,0] op_sel_hi:[1,0,0]\n"
X_"v42,v161,s94,v58 op_sel_hi:[1,0,0]\n"
X_"v43,v161,s94,v58 op_sel:[1,0,0] op_sel_hi:[1,0,0]\n"
E_"v40,v40\n"
E_"v41,v41\n"
"s_barrier\n"
X_"v44,v162,s94,v58 op_sel_hi:[1,0,0]\n"
X_"v45,v162,s94,v58 op_sel:[1,0,0] op_sel_hi:[1,0,0]\n"
E_"v42,v42\n"
E_"v43,v43\n"
C_"v160,v40,v41\n"
X_"v46,v163,s94,v58 op_sel_hi:[1,0,0]\n"
X_"v47,v163,s94,v58 op_sel:[1,0,0] op_sel_hi:[1,0,0]\n"
E_"v44,v44\n"
E_"v45,v45\n"
C_"v161,v42,v43\n"
E_"v46,v46\n"
E_"v47,v47\n"
C_"v162,v44,v45\n"
C_"v163,v46,v47\n"
X_"v40,v164,s94,v59 op_sel_hi:[1,0,0]\n"
X_"v41,v164,s94,v59 op_sel:[1,0,0] op_sel_hi:[1,0,0]\n"
X_"v42,v165,s94,v59 op_sel_hi:[1,0,0]\n"
X_"v43,v165,s94,v59 op_sel:[1,0,0] op_sel_hi:[1,0,0]\n"
E_"v40,v40\n"
E_"v41,v41\n"
X_"v44,v166,s94,v59 op_sel_hi:[1,0,0]\n"
X_"v45,v166,s94,v59 op_sel:[1,0,0] op_sel_hi:[1,0,0]\n"
E_"v42,v42\n"
E_"v43,v43\n"
C_"v164,v40,v41\n"
X_"v46,v167,s94,v59 op_sel_hi:[1,0,0]\n"
X_"v47,v167,s94,v59 op_sel:[1,0,0] op_sel_hi:[1,0,0]\n"
E_"v44,v44\n"
E_"v45,v45\n"
C_"v165,v42,v43\n"
E_"v46,v46\n"
E_"v47,v47\n"
C_"v166,v44,v45\n"
C_"v167,v46,v47\n"
X_"v40,v168,s94,v58 op_sel_hi:[1,0,0]\n"
X_"v41,v168,s94,v58 op_sel:[1,0,0] op_sel_hi:[1,0,0]\n"
X_"v42,v169,s94,v58 op_sel_hi:[1,0,0]\n"
X_"v43,v169,s94,v58 op_sel:[1,0,0] op_sel_hi:[1,0,0]\n"
E_"v40,v40\n"
E_"v41,v41\n"
X_"v44,v170,s94,v58 op_sel_hi:[1,0,0]\n"
X_"v45,v170,s94,v58 op_sel:[1,0,0] op_sel_hi:[1,0,0]\n"
E_"v42,v42\n"
E_"v43,v43\n"
C_"v168,v40,v41\n"
X_"v46,v171,s94,v58 op_sel_hi:[1,0,0]\n"
X_"v47,v171,s94,v58 op_sel:[1,0,0] op_sel_hi:[1,0,0]\n"
E_"v44,v44\n"
E_"v45,v45\n"
C_"v169,v42,v43\n"
E_"v46,v46\n"
E_"v47,v47\n"
C_"v170,v44,v45\n"
C_"v171,v46,v47\n"
"s_barrier\n"
"v_add_u32 v1, 0, v0\n"
"v_add_u32 v2, 0x10000, v1\n"
"v_add_u32 v3, 0x20000, v1\n"
T_"v[8:9],v1 offset:42240\n"
T_"v[10:11],v1 offset:43264\n"
T_"v[12:13],v1 offset:42248\n"
T_"v[14:15],v1 offset:43272\n"
T_"v[16:17],v1 offset:42496\n"
T_"v[18:19],v1 offset:43520\n"
T_"v[20:21],v1 offset:42504\n"
T_"v[22:23],v1 offset:43528\n"
T_"v[24:25],v1 offset:42752\n"
T_"v[26:27],v1 offset:43776\n"
T_"v[28:29],v1 offset:42760\n"
T_"v[30:31],v1 offset:43784\n"
X_"v40,v172,s94,v59 op_sel_hi:[1,0,0]\n"
X_"v41,v172,s94,v59 op_sel:[1,0,0] op_sel_hi:[1,0,0]\n"
X_"v42,v173,s94,v59 op_sel_hi:[1,0,0]\n"
X_"v43,v173,s94,v59 op_sel:[1,0,0] op_sel_hi:[1,0,0]\n"
E_"v40,v40\n"
E_"v41,v41\n"
X_"v44,v174,s94,v59 op_sel_hi:[1,0,0]\n"
X_"v45,v174,s94,v59 op_sel:[1,0,0] op_sel_hi:[1,0,0]\n"
E_"v42,v42\n"
E_"v43,v43\n"
C_"v172,v40,v41\n"
X_"v46,v175,s94,v59 op_sel_hi:[1,0,0]\n"
X_"v47,v175,s94,v59 op_sel:[1,0,0] op_sel_hi:[1,0,0]\n"
E_"v44,v44\n"
E_"v45,v45\n"
C_"v173,v42,v43\n"
E_"v46,v46\n"
E_"v47,v47\n"
C_"v174,v44,v45\n"
C_"v175,v46,v47\n"
M_"v[48:51],v[60:63],v[192:195],0\n"
"v_pk_sub_u16 v200, v200, v56 clamp\n"
M_"v[52:55],v[60:63],v[196:199],0\n"
W_"lgkmcnt(10)\n"
"v_pk_sub_u16 v201, v201, v56 clamp\n"
M_"v[64:67],v[8:11],v[192:195],0\n"
T_"v[32:33],v1 offset:43008\n"
"v_pk_sub_u16 v202, v202, v56 clamp\n"
M_"v[68:71],v[8:11],v[196:199],0\n"
W_"lgkmcnt(9)\n"
T_"v[34:35],v1 offset:44032\n"
"v_pk_sub_u16 v203, v203, v56 clamp\n"
M_"v[72:75],v[12:15],v[192:195],0\n"
T_"v[36:37],v1 offset:43016\n"
"v_pk_sub_u16 v204, v204, v57 clamp\n"
M_"v[76:79],v[12:15],v[196:199],0\n"
W_"lgkmcnt(9)\n"
T_"v[38:39],v1 offset:44040\n"
"v_pk_sub_u16 v205, v205, v57 clamp\n"
M_"v[80:83],v[16:19],v[192:195],0\n"
T_"v[8:9],v1 offset:59136\n"
"v_pk_sub_u16 v206, v206, v57 clamp\n"
M_"v[84:87],v[16:19],v[196:199],0\n"
W_"lgkmcnt(5)\n"
T_"v[10:11],v1 offset:60160\n"
"v_pk_sub_u16 v207, v207, v57 clamp\n"
M_"v[88:91],v[20:23],v[192:195],0\n"
T_"v[12:13],v1 offset:59144\n"
"v_pk_sub_u16 v208, v208, v56 clamp\n"
M_"v[92:95],v[20:23],v[196:199],0\n"
T_"v[14:15],v1 offset:60168\n"
"v_pk_sub_u16 v209, v209, v56 clamp\n"
M_"v[96:99],v[24:27],v[192:195],0\n"
T_"v[16:17],v1 offset:59392\n"
"v_pk_sub_u16 v210, v210, v56 clamp\n"
M_"v[100:103],v[24:27],v[196:199],0\n"
T_"v[18:19],v1 offset:60416\n"
"v_pk_sub_u16 v211, v211, v56 clamp\n"
M_"v[104:107],v[28:31],v[192:195],0\n"
T_"v[20:21],v1 offset:59400\n"
"v_pk_sub_u16 v212, v212, v57 clamp\n"
M_"v[108:111],v[28:31],v[196:199],0\n"
W_"lgkmcnt(6)\n"
T_"v[22:23],v1 offset:60424\n"
"v_pk_sub_u16 v213, v213, v57 clamp\n"
M_"v[112:115],v[32:35],v[192:195],0\n"
T_"v[24:25],v1 offset:59648\n"
"v_pk_sub_u16 v214, v214, v57 clamp\n"
M_"v[116:119],v[32:35],v[196:199],0\n"
T_"v[26:27],v1 offset:60672\n"
"v_pk_sub_u16 v215, v215, v57 clamp\n"
M_"v[120:123],v[36:39],v[192:195],0\n"
T_"v[28:29],v1 offset:59656\n"
"v_pk_sub_u16 v216, v216, v56 clamp\n"
M_"v[124:127],v[36:39],v[196:199],0\n"
T_"v[30:31],v1 offset:60680\n"
"v_pk_sub_u16 v217, v217, v56 clamp\n"
M_"v[48:51],v[60:63],v[200:203],v[48:51]\n"
"v_pk_sub_u16 v218, v218, v56 clamp\n"
M_"v[52:55],v[60:63],v[204:207],v[52:55]\n"
W_"lgkmcnt(5)\n"
"v_pk_sub_u16 v219, v219, v56 clamp\n"
M_"v[64:67],v[8:11],v[200:203],v[64:67]\n"
T_"v[32:33],v1 offset:59904\n"
"v_pk_sub_u16 v220, v220, v57 clamp\n"
M_"v[68:71],v[8:11],v[204:207],v[68:71]\n"
T_"v[34:35],v1 offset:60928\n"
"v_pk_sub_u16 v221, v221, v57 clamp\n"
M_"v[72:75],v[12:15],v[200:203],v[72:75]\n"
T_"v[36:37],v1 offset:59912\n"
"v_pk_sub_u16 v222, v222, v57 clamp\n"
M_"v[76:79],v[12:15],v[204:207],v[76:79]\n"
T_"v[38:39],v1 offset:60936\n"
"v_pk_sub_u16 v223, v223, v57 clamp\n"
M_"v[80:83],v[16:19],v[200:203],v[80:83]\n"
"s_barrier\n"
T_"v[8:9],v1 offset:50688\n"
"v_pk_sub_u16 v224, v224, v56 clamp\n"
M_"v[84:87],v[16:19],v[204:207],v[84:87]\n"
W_"lgkmcnt(5)\n"
T_"v[10:11],v1 offset:51712\n"
"v_pk_sub_u16 v225, v225, v56 clamp\n"
M_"v[88:91],v[20:23],v[200:203],v[88:91]\n"
T_"v[12:13],v1 offset:50696\n"
"v_pk_sub_u16 v226, v226, v56 clamp\n"
M_"v[92:95],v[20:23],v[204:207],v[92:95]\n"
T_"v[14:15],v1 offset:51720\n"
"v_pk_sub_u16 v227, v227, v56 clamp\n"
M_"v[96:99],v[24:27],v[200:203],v[96:99]\n"
T_"v[16:17],v1 offset:50944\n"
"v_pk_sub_u16 v228, v228, v57 clamp\n"
M_"v[100:103],v[24:27],v[204:207],v[100:103]\n"
T_"v[18:19],v1 offset:51968\n"
"v_pk_sub_u16 v229, v229, v57 clamp\n"
M_"v[104:107],v[28:31],v[200:203],v[104:107]\n"
T_"v[20:21],v1 offset:50952\n"
"v_pk_sub_u16 v230, v230, v57 clamp\n"
M_"v[108:111],v[28:31],v[204:207],v[108:111]\n"
W_"lgkmcnt(6)\n"
T_"v[22:23],v1 offset:51976\n"
"v_pk_sub_u16 v231, v231, v57 clamp\n"
M_"v[112:115],v[32:35],v[200:203],v[112:115]\n"
T_"v[24:25],v1 offset:51200\n"
X_"v40,v176,s94,v58 op_sel_hi:[1,0,0]\n"
M_"v[116:119],v[32:35],v[204:207],v[116:119]\n"
T_"v[26:27],v1 offset:52224\n"
M_"v[120:123],v[36:39],v[200:203],v[120:123]\n"
T_"v[28:29],v1 offset:51208\n"
X_"v41,v176,s94,v58 op_sel:[1,0,0] op_sel_hi:[1,0,0]\n"
M_"v[124:127],v[36:39],v[204:207],v[124:127]\n"
T_"v[30:31],v1 offset:52232\n"
M_"v[48:51],v[60:63],v[208:211],v[48:51]\n"
M_"v[52:55],v[60:63],v[212:215],v[52:55]\n"
W_"lgkmcnt(5)\n"
X_"v42,v177,s94,v58 op_sel_hi:[1,0,0]\n"
M_"v[64:67],v[8:11],v[208:211],v[64:67]\n"
T_"v[32:33],v1 offset:51456\n"
M_"v[68:71],v[8:11],v[212:215],v[68:71]\n"
T_"v[34:35],v1 offset:52480\n"
M_"v[72:75],v[12:15],v[208:211],v[72:75]\n"
T_"v[36:37],v1 offset:51464\n"
X_"v43,v177,s94,v58 op_sel:[1,0,0] op_sel_hi:[1,0,0]\n"
M_"v[76:79],v[12:15],v[212:215],v[76:79]\n"
T_"v[38:39],v1 offset:52488\n"
M_"v[80:83],v[16:19],v[208:211],v[80:83]\n"
T_"v[8:9],v2 offset:2048\n"
T_"v[10:11],v2 offset:3072\n"
M_"v[84:87],v[16:19],v[212:215],v[84:87]\n"
W_"lgkmcnt(6)\n"
E_"v40,v40\n"
M_"v[88:91],v[20:23],v[208:211],v[88:91]\n"
T_"v[12:13],v2 offset:2056\n"
M_"v[92:95],v[20:23],v[212:215],v[92:95]\n"
T_"v[14:15],v2 offset:3080\n"
M_"v[96:99],v[24:27],v[208:211],v[96:99]\n"
T_"v[16:17],v2 offset:2304\n"
T_"v[18:19],v2 offset:3328\n"
M_"v[100:103],v[24:27],v[212:215],v[100:103]\n"
E_"v41,v41\n"
M_"v[104:107],v[28:31],v[208:211],v[104:107]\n"
T_"v[20:21],v2 offset:2312\n"
M_"v[108:111],v[28:31],v[212:215],v[108:111]\n"
W_"lgkmcnt(5)\n"
T_"v[22:23],v2 offset:3336\n"
X_"v44,v178,s94,v58 op_sel_hi:[1,0,0]\n"
M_"v[112:115],v[32:35],v[208:211],v[112:115]\n"
T_"v[24:25],v2 offset:2560\n"
M_"v[116:119],v[32:35],v[212:215],v[116:119]\n"
T_"v[26:27],v2 offset:3584\n"
M_"v[120:123],v[36:39],v[208:211],v[120:123]\n"
T_"v[28:29],v2 offset:2568\n"
X_"v45,v178,s94,v58 op_sel:[1,0,0] op_sel_hi:[1,0,0]\n"
M_"v[124:127],v[36:39],v[212:215],v[124:127]\n"
T_"v[30:31],v2 offset:3592\n"
M_"v[48:51],v[60:63],v[216:219],v[48:51]\n"
M_"v[52:55],v[60:63],v[220:223],v[52:55]\n"
E_"v42,v42\n"
M_"v[64:67],v[8:11],v[216:219],v[64:67]\n"
T_"v[32:33],v2 offset:2816\n"
M_"v[68:71],v[8:11],v[220:223],v[68:71]\n"
W_"lgkmcnt(4)\n"
T_"v[34:35],v2 offset:3840\n"
M_"v[72:75],v[12:15],v[216:219],v[72:75]\n"
T_"v[36:37],v2 offset:2824\n"
T_"v[38:39],v2 offset:3848\n"
M_"v[76:79],v[12:15],v[220:223],v[76:79]\n"
E_"v43,v43\n"
M_"v[80:83],v[16:19],v[216:219],v[80:83]\n"
"s_barrier\n"
T_"v[8:9],v2 offset:10496\n"
M_"v[84:87],v[16:19],v[220:223],v[84:87]\n"
T_"v[10:11],v2 offset:11520\n"
C_"v176,v40,v41\n"
M_"v[88:91],v[20:23],v[216:219],v[88:91]\n"
T_"v[12:13],v2 offset:10504\n"
M_"v[92:95],v[20:23],v[220:223],v[92:95]\n"
W_"lgkmcnt(6)\n"
T_"v[14:15],v2 offset:11528\n"
M_"v[96:99],v[24:27],v[216:219],v[96:99]\n"
T_"v[16:17],v2 offset:10752\n"
X_"v46,v179,s94,v58 op_sel_hi:[1,0,0]\n"
M_"v[100:103],v[24:27],v[220:223],v[100:103]\n"
T_"v[18:19],v2 offset:11776\n"
M_"v[104:107],v[28:31],v[216:219],v[104:107]\n"
T_"v[20:21],v2 offset:10760\n"
M_"v[108:111],v[28:31],v[220:223],v[108:111]\n"
W_"lgkmcnt(6)\n"
T_"v[22:23],v2 offset:11784\n"
X_"v47,v179,s94,v58 op_sel:[1,0,0] op_sel_hi:[1,0,0]\n"
M_"v[112:115],v[32:35],v[216:219],v[112:115]\n"
T_"v[24:25],v2 offset:11008\n"
M_"v[116:119],v[32:35],v[220:223],v[116:119]\n"
T_"v[26:27],v2 offset:12032\n"
M_"v[120:123],v[36:39],v[216:219],v[120:123]\n"
T_"v[28:29],v2 offset:11016\n"
T_"v[30:31],v2 offset:12040\n"
M_"v[124:127],v[36:39],v[220:223],v[124:127]\n"
E_"v44,v44\n"
M_"v[48:51],v[60:63],v[224:227],v[48:51]\n"
M_"v[52:55],v[60:63],v[228:231],v[52:55]\n"
W_"lgkmcnt(5)\n"
E_"v45,v45\n"
M_"v[64:67],v[8:11],v[224:227],v[64:67]\n"
T_"v[32:33],v2 offset:11264\n"
M_"v[68:71],v[8:11],v[228:231],v[68:71]\n"
T_"v[34:35],v2 offset:12288\n"
M_"v[72:75],v[12:15],v[224:227],v[72:75]\n"
T_"v[36:37],v2 offset:11272\n"
C_"v177,v42,v43\n"
M_"v[76:79],v[12:15],v[228:231],v[76:79]\n"
T_"v[38:39],v2 offset:12296\n"
M_"v[80:83],v[16:19],v[224:227],v[80:83]\n"
T_"v[8:9],v2 offset:18944\n"
T_"v[10:11],v2 offset:19968\n"
M_"v[84:87],v[16:19],v[228:231],v[84:87]\n"
W_"lgkmcnt(6)\n"
E_"v46,v46\n"
M_"v[88:91],v[20:23],v[224:227],v[88:91]\n"
T_"v[12:13],v2 offset:18952\n"
M_"v[92:95],v[20:23],v[228:231],v[92:95]\n"
T_"v[14:15],v2 offset:19976\n"
M_"v[96:99],v[24:27],v[224:227],v[96:99]\n"
T_"v[16:17],v2 offset:19200\n"
T_"v[18:19],v2 offset:20224\n"
M_"v[100:103],v[24:27],v[228:231],v[100:103]\n"
E_"v47,v47\n"
M_"v[104:107],v[28:31],v[224:227],v[104:107]\n"
T_"v[20:21],v2 offset:19208\n"
M_"v[108:111],v[28:31],v[228:231],v[108:111]\n"
W_"lgkmcnt(5)\n"
T_"v[22:23],v2 offset:20232\n"
C_"v178,v44,v45\n"
M_"v[112:115],v[32:35],v[224:227],v[112:115]\n"
T_"v[24:25],v2 offset:19456\n"
M_"v[116:119],v[32:35],v[228:231],v[116:119]\n"
T_"v[26:27],v2 offset:20480\n"
M_"v[120:123],v[36:39],v[224:227],v[120:123]\n"
T_"v[28:29],v2 offset:19464\n"
C_"v179,v46,v47\n"
M_"v[124:127],v[36:39],v[228:231],v[124:127]\n"
T_"v[30:31],v2 offset:20488\n"
M_"v[48:51],v[60:63],v[232:235],v[48:51]\n"
M_"v[52:55],v[60:63],v[236:239],v[52:55]\n"
X_"v40,v180,s94,v59 op_sel_hi:[1,0,0]\n"
M_"v[64:67],v[8:11],v[232:235],v[64:67]\n"
T_"v[32:33],v2 offset:19712\n"
M_"v[68:71],v[8:11],v[236:239],v[68:71]\n"
W_"lgkmcnt(4)\n"
T_"v[34:35],v2 offset:20736\n"
M_"v[72:75],v[12:15],v[232:235],v[72:75]\n"
T_"v[36:37],v2 offset:19720\n"
X_"v41,v180,s94,v59 op_sel:[1,0,0] op_sel_hi:[1,0,0]\n"
M_"v[76:79],v[12:15],v[236:239],v[76:79]\n"
T_"v[38:39],v2 offset:20744\n"
M_"v[80:83],v[16:19],v[232:235],v[80:83]\n"
"s_barrier\n"
T_"v[8:9],v1 offset:25344\n"
M_"v[84:87],v[16:19],v[236:239],v[84:87]\n"
"PTH13:\n"
"s_cbranch_execz PTR\n"
T_"v[10:11],v1 offset:26368\n"
X_"v42,v181,s94,v59 op_sel_hi:[1,0,0]\n"
M_"v[88:91],v[20:23],v[232:235],v[88:91]\n"
T_"v[12:13],v1 offset:25352\n"
M_"v[92:95],v[20:23],v[236:239],v[92:95]\n"
W_"lgkmcnt(6)\n"
T_"v[14:15],v1 offset:26376\n"
M_"v[96:99],v[24:27],v[232:235],v[96:99]\n"
T_"v[16:17],v1 offset:25600\n"
X_"v43,v181,s94,v59 op_sel:[1,0,0] op_sel_hi:[1,0,0]\n"
M_"v[100:103],v[24:27],v[236:239],v[100:103]\n"
T_"v[18:19],v1 offset:26624\n"
M_"v[104:107],v[28:31],v[232:235],v[104:107]\n"
T_"v[20:21],v1 offset:25608\n"
T_"v[22:23],v1 offset:26632\n"
M_"v[108:111],v[28:31],v[236:239],v[108:111]\n"
W_"lgkmcnt(7)\n"
E_"v40,v40\n"
M_"v[112:115],v[32:35],v[232:235],v[112:115]\n"
T_"v[24:25],v1 offset:25856\n"
M_"v[116:119],v[32:35],v[236:239],v[116:119]\n"
T_"v[26:27],v1 offset:26880\n"
M_"v[120:123],v[36:39],v[232:235],v[120:123]\n"
T_"v[28:29],v1 offset:25864\n"
T_"v[30:31],v1 offset:26888\n"
M_"v[124:127],v[36:39],v[236:239],v[124:127]\n"
E_"v41,v41\n"
M_"v[48:51],v[60:63],v[240:243],v[48:51]\n"
M_"v[52:55],v[60:63],v[244:247],v[52:55]\n"
W_"lgkmcnt(4)\n"
X_"v44,v182,s94,v59 op_sel_hi:[1,0,0]\n"
M_"v[64:67],v[8:11],v[240:243],v[64:67]\n"
T_"v[32:33],v1 offset:26112\n"
M_"v[68:71],v[8:11],v[244:247],v[68:71]\n"
T_"v[34:35],v1 offset:27136\n"
X_"v45,v182,s94,v59 op_sel:[1,0,0] op_sel_hi:[1,0,0]\n"
M_"v[72:75],v[12:15],v[240:243],v[72:75]\n"
T_"v[36:37],v1 offset:26120\n"
M_"v[76:79],v[12:15],v[244:247],v[76:79]\n"
T_"v[38:39],v1 offset:27144\n"
M_"v[80:83],v[16:19],v[240:243],v[80:83]\n"
T_"v[8:9],v2 offset:27392\n"
T_"v[10:11],v2 offset:28416\n"
M_"v[84:87],v[16:19],v[244:247],v[84:87]\n"
E_"v42,v42\n"
M_"v[88:91],v[20:23],v[240:243],v[88:91]\n"
T_"v[12:13],v2 offset:27400\n"
T_"v[14:15],v2 offset:28424\n"
M_"v[92:95],v[20:23],v[244:247],v[92:95]\n"
W_"lgkmcnt(7)\n"
E_"v43,v43\n"
M_"v[96:99],v[24:27],v[240:243],v[96:99]\n"
T_"v[16:17],v2 offset:27648\n"
M_"v[100:103],v[24:27],v[244:247],v[100:103]\n"
T_"v[18:19],v2 offset:28672\n"
M_"v[104:107],v[28:31],v[240:243],v[104:107]\n"
T_"v[20:21],v2 offset:27656\n"
C_"v180,v40,v41\n"
M_"v[108:111],v[28:31],v[244:247],v[108:111]\n"
W_"lgkmcnt(5)\n"
T_"v[22:23],v2 offset:28680\n"
M_"v[112:115],v[32:35],v[240:243],v[112:115]\n"
T_"v[24:25],v2 offset:27904\n"
M_"v[116:119],v[32:35],v[244:247],v[116:119]\n"
T_"v[26:27],v2 offset:28928\n"
X_"v46,v183,s94,v59 op_sel_hi:[1,0,0]\n"
M_"v[120:123],v[36:39],v[240:243],v[120:123]\n"
T_"v[28:29],v2 offset:27912\n"
M_"v[124:127],v[36:39],v[244:247],v[124:127]\n"
T_"v[30:31],v2 offset:28936\n"
M_"v[48:51],v[60:63],v[248:251],v[48:51]\n"
X_"v47,v183,s94,v59 op_sel:[1,0,0] op_sel_hi:[1,0,0]\n"
M_"v[52:55],v[60:63],v[252:255],v[52:55]\n"
M_"v[64:67],v[8:11],v[248:251],v[64:67]\n"
T_"v[32:33],v2 offset:28160\n"
T_"v[34:35],v2 offset:29184\n"
M_"v[68:71],v[8:11],v[252:255],v[68:71]\n"
W_"lgkmcnt(5)\n"
E_"v44,v44\n"
M_"v[72:75],v[12:15],v[248:251],v[72:75]\n"
T_"v[36:37],v2 offset:28168\n"
M_"v[76:79],v[12:15],v[252:255],v[76:79]\n"
T_"v[38:39],v2 offset:29192\n"
M_"v[80:83],v[16:19],v[248:251],v[80:83]\n"
"s_barrier\n"
T_"v[8:9],v2 offset:35840\n"
T_"v[10:11],v2 offset:36864\n"
M_"v[84:87],v[16:19],v[252:255],v[84:87]\n"
E_"v45,v45\n"
M_"v[88:91],v[20:23],v[248:251],v[88:91]\n"
T_"v[12:13],v2 offset:35848\n"
M_"v[92:95],v[20:23],v[252:255],v[92:95]\n"
W_"lgkmcnt(5)\n"
T_"v[14:15],v2 offset:36872\n"
C_"v181,v42,v43\n"
M_"v[96:99],v[24:27],v[248:251],v[96:99]\n"
T_"v[16:17],v2 offset:36096\n"
M_"v[100:103],v[24:27],v[252:255],v[100:103]\n"
T_"v[18:19],v2 offset:37120\n"
M_"v[104:107],v[28:31],v[248:251],v[104:107]\n"
T_"v[20:21],v2 offset:36104\n"
T_"v[22:23],v2 offset:37128\n"
M_"v[108:111],v[28:31],v[252:255],v[108:111]\n"
E_"v46,v46\n"
M_"v[112:115],v[32:35],v[248:251],v[112:115]\n"
T_"v[24:25],v2 offset:36352\n"
T_"v[26:27],v2 offset:37376\n"
M_"v[116:119],v[32:35],v[252:255],v[116:119]\n"
W_"lgkmcnt(7)\n"
E_"v47,v47\n"
M_"v[120:123],v[36:39],v[248:251],v[120:123]\n"
T_"v[28:29],v2 offset:36360\n"
M_"v[124:127],v[36:39],v[252:255],v[124:127]\n"
T_"v[30:31],v2 offset:37384\n"
M_"v[48:51],v[60:63],v[128:131],v[48:51]\n"
C_"v182,v44,v45\n"
M_"v[52:55],v[60:63],v[132:135],v[52:55]\n"
M_"v[64:67],v[8:11],v[128:131],v[64:67]\n"
T_"v[32:33],v2 offset:36608\n"
M_"v[68:71],v[8:11],v[132:135],v[68:71]\n"
W_"lgkmcnt(3)\n"
T_"v[34:35],v2 offset:37632\n"
C_"v183,v46,v47\n"
M_"v[72:75],v[12:15],v[128:131],v[72:75]\n"
T_"v[36:37],v2 offset:36616\n"
M_"v[76:79],v[12:15],v[132:135],v[76:79]\n"
T_"v[38:39],v2 offset:37640\n"
M_"v[80:83],v[16:19],v[128:131],v[80:83]\n"
T_"v[8:9],v3 offset:20992\n"
X_"v40,v184,s94,v58 op_sel_hi:[1,0,0]\n"
M_"v[84:87],v[16:19],v[132:135],v[84:87]\n"
T_"v[10:11],v3 offset:22016\n"
M_"v[88:91],v[20:23],v[128:131],v[88:91]\n"
T_"v[12:13],v3 offset:21000\n"
M_"v[92:95],v[20:23],v[132:135],v[92:95]\n"
T_"v[14:15],v3 offset:22024\n"
X_"v41,v184,s94,v58 op_sel:[1,0,0] op_sel_hi:[1,0,0]\n"
M_"v[96:99],v[24:27],v[128:131],v[96:99]\n"
T_"v[16:17],v3 offset:21248\n"
M_"v[100:103],v[24:27],v[132:135],v[100:103]\n"
W_"lgkmcnt(6)\n"
T_"v[18:19],v3 offset:22272\n"
M_"v[104:107],v[28:31],v[128:131],v[104:107]\n"
T_"v[20:21],v3 offset:21256\n"
X_"v42,v185,s94,v58 op_sel_hi:[1,0,0]\n"
M_"v[108:111],v[28:31],v[132:135],v[108:111]\n"
T_"v[22:23],v3 offset:22280\n"
M_"v[112:115],v[32:35],v[128:131],v[112:115]\n"
T_"v[24:25],v3 offset:21504\n"
M_"v[116:119],v[32:35],v[132:135],v[116:119]\n"
W_"lgkmcnt(6)\n"
T_"v[26:27],v3 offset:22528\n"
X_"v43,v185,s94,v58 op_sel:[1,0,0] op_sel_hi:[1,0,0]\n"
M_"v[120:123],v[36:39],v[128:131],v[120:123]\n"
T_"v[28:29],v3 offset:21512\n"
M_"v[124:127],v[36:39],v[132:135],v[124:127]\n"
T_"v[30:31],v3 offset:22536\n"
M_"v[48:51],v[60:63],v[136:139],v[48:51]\n"
E_"v40,v40\n"
M_"v[52:55],v[60:63],v[140:143],v[52:55]\n"
M_"v[64:67],v[8:11],v[136:139],v[64:67]\n"
T_"v[32:33],v3 offset:21760\n"
T_"v[34:35],v3 offset:22784\n"
M_"v[68:71],v[8:11],v[140:143],v[68:71]\n"
W_"lgkmcnt(5)\n"
E_"v41,v41\n"
M_"v[72:75],v[12:15],v[136:139],v[72:75]\n"
T_"v[36:37],v3 offset:21768\n"
M_"v[76:79],v[12:15],v[140:143],v[76:79]\n"
T_"v[38:39],v3 offset:22792\n"
M_"v[80:83],v[16:19],v[136:139],v[80:83]\n"
"s_barrier\n"
T_"v[8:9],v1 offset:0\n"
X_"v44,v186,s94,v58 op_sel_hi:[1,0,0]\n"
M_"v[84:87],v[16:19],v[140:143],v[84:87]\n"
T_"v[10:11],v1 offset:1024\n"
M_"v[88:91],v[20:23],v[136:139],v[88:91]\n"
T_"v[12:13],v1 offset:8\n"
M_"v[92:95],v[20:23],v[140:143],v[92:95]\n"
W_"lgkmcnt(5)\n"
T_"v[14:15],v1 offset:1032\n"
X_"v45,v186,s94,v58 op_sel:[1,0,0] op_sel_hi:[1,0,0]\n"
M_"v[96:99],v[24:27],v[136:139],v[96:99]\n"
T_"v[16:17],v1 offset:256\n"
M_"v[100:103],v[24:27],v[140:143],v[100:103]\n"
T_"v[18:19],v1 offset:1280\n"
M_"v[104:107],v[28:31],v[136:139],v[104:107]\n"
T_"v[20:21],v1 offset:264\n"
T_"v[22:23],v1 offset:1288\n"
M_"v[108:111],v[28:31],v[140:143],v[108:111]\n"
E_"v42,v42\n"
M_"v[112:115],v[32:35],v[136:139],v[112:115]\n"
T_"v[24:25],v1 offset:512\n"
T_"v[26:27],v1 offset:1536\n"
M_"v[116:119],v[32:35],v[140:143],v[116:119]\n"
W_"lgkmcnt(7)\n"
E_"v43,v43\n"
M_"v[120:123],v[36:39],v[136:139],v[120:123]\n"
T_"v[28:29],v1 offset:520\n"
M_"v[124:127],v[36:39],v[140:143],v[124:127]\n"
T_"v[30:31],v1 offset:1544\n"
M_"v[48:51],v[60:63],v[144:147],v[48:51]\n"
C_"v184,v40,v41\n"
M_"v[52:55],v[60:63],v[148:151],v[52:55]\n"
M_"v[64:67],v[8:11],v[144:147],v[64:67]\n"
T_"v[32:33],v1 offset:768\n"
M_"v[68:71],v[8:11],v[148:151],v[68:71]\n"
W_"lgkmcnt(3)\n"
T_"v[34:35],v1 offset:1792\n"
X_"v46,v187,s94,v58 op_sel_hi:[1,0,0]\n"
M_"v[72:75],v[12:15],v[144:147],v[72:75]\n"
T_"v[36:37],v1 offset:776\n"
M_"v[76:79],v[12:15],v[148:151],v[76:79]\n"
T_"v[38:39],v1 offset:1800\n"
M_"v[80:83],v[16:19],v[144:147],v[80:83]\n"
T_"v[8:9],v2 offset:44288\n"
X_"v47,v187,s94,v58 op_sel:[1,0,0] op_sel_hi:[1,0,0]\n"
M_"v[84:87],v[16:19],v[148:151],v[84:87]\n"
T_"v[10:11],v2 offset:45312\n"
M_"v[88:91],v[20:23],v[144:147],v[88:91]\n"
T_"v[12:13],v2 offset:44296\n"
T_"v[14:15],v2 offset:45320\n"
M_"v[92:95],v[20:23],v[148:151],v[92:95]\n"
E_"v44,v44\n"
M_"v[96:99],v[24:27],v[144:147],v[96:99]\n"
T_"v[16:17],v2 offset:44544\n"
T_"v[18:19],v2 offset:45568\n"
M_"v[100:103],v[24:27],v[148:151],v[100:103]\n"
W_"lgkmcnt(7)\n"
E_"v45,v45\n"
M_"v[104:107],v[28:31],v[144:147],v[104:107]\n"
T_"v[20:21],v2 offset:44552\n"
M_"v[108:111],v[28:31],v[148:151],v[108:111]\n"
T_"v[22:23],v2 offset:45576\n"
M_"v[112:115],v[32:35],v[144:147],v[112:115]\n"
T_"v[24:25],v2 offset:44800\n"
C_"v185,v42,v43\n"
M_"v[116:119],v[32:35],v[148:151],v[116:119]\n"
W_"lgkmcnt(5)\n"
T_"v[26:27],v2 offset:45824\n"
M_"v[120:123],v[36:39],v[144:147],v[120:123]\n"
T_"v[28:29],v2 offset:44808\n"
T_"v[30:31],v2 offset:45832\n"
M_"v[124:127],v[36:39],v[148:151],v[124:127]\n"
E_"v46,v46\n"
M_"v[48:51],v[60:63],v[152:155],v[48:51]\n"
M_"v[52:55],v[60:63],v[156:159],v[52:55]\n"
M_"v[64:67],v[8:11],v[152:155],v[64:67]\n"
T_"v[32:33],v2 offset:45056\n"
T_"v[34:35],v2 offset:46080\n"
M_"v[68:71],v[8:11],v[156:159],v[68:71]\n"
E_"v47,v47\n"
M_"v[72:75],v[12:15],v[152:155],v[72:75]\n"
T_"v[36:37],v2 offset:45064\n"
M_"v[76:79],v[12:15],v[156:159],v[76:79]\n"
W_"lgkmcnt(3)\n"
T_"v[38:39],v2 offset:46088\n"
C_"v186,v44,v45\n"
M_"v[80:83],v[16:19],v[152:155],v[80:83]\n"
"s_barrier\n"
T_"v[8:9],v2 offset:52736\n"
M_"v[84:87],v[16:19],v[156:159],v[84:87]\n"
T_"v[10:11],v2 offset:53760\n"
M_"v[88:91],v[20:23],v[152:155],v[88:91]\n"
T_"v[12:13],v2 offset:52744\n"
C_"v187,v46,v47\n"
M_"v[92:95],v[20:23],v[156:159],v[92:95]\n"
T_"v[14:15],v2 offset:53768\n"
M_"v[96:99],v[24:27],v[152:155],v[96:99]\n"
T_"v[16:17],v2 offset:52992\n"
M_"v[100:103],v[24:27],v[156:159],v[100:103]\n"
T_"v[18:19],v2 offset:54016\n"
X_"v40,v188,s94,v59 op_sel_hi:[1,0,0]\n"
M_"v[104:107],v[28:31],v[152:155],v[104:107]\n"
T_"v[20:21],v2 offset:53000\n"
M_"v[108:111],v[28:31],v[156:159],v[108:111]\n"
W_"lgkmcnt(6)\n"
T_"v[22:23],v2 offset:54024\n"
M_"v[112:115],v[32:35],v[152:155],v[112:115]\n"
T_"v[24:25],v2 offset:53248\n"
X_"v41,v188,s94,v59 op_sel:[1,0,0] op_sel_hi:[1,0,0]\n"
M_"v[116:119],v[32:35],v[156:159],v[116:119]\n"
T_"v[26:27],v2 offset:54272\n"
M_"v[120:123],v[36:39],v[152:155],v[120:123]\n"
T_"v[28:29],v2 offset:53256\n"
M_"v[124:127],v[36:39],v[156:159],v[124:127]\n"
T_"v[30:31],v2 offset:54280\n"
X_"v42,v189,s94,v59 op_sel_hi:[1,0,0]\n"
M_"v[48:51],v[60:63],v[160:163],v[48:51]\n"
M_"v[52:55],v[60:63],v[164:167],v[52:55]\n"
W_"lgkmcnt(5)\n"
M_"v[64:67],v[8:11],v[160:163],v[64:67]\n"
T_"v[32:33],v2 offset:53504\n"
X_"v43,v189,s94,v59 op_sel:[1,0,0] op_sel_hi:[1,0,0]\n"
M_"v[68:71],v[8:11],v[164:167],v[68:71]\n"
T_"v[34:35],v2 offset:54528\n"
M_"v[72:75],v[12:15],v[160:163],v[72:75]\n"
T_"v[36:37],v2 offset:53512\n"
T_"v[38:39],v2 offset:54536\n"
M_"v[76:79],v[12:15],v[164:167],v[76:79]\n"
E_"v40,v40\n"
M_"v[80:83],v[16:19],v[160:163],v[80:83]\n"
T_"v[8:9],v2 offset:61184\n"
M_"v[84:87],v[16:19],v[164:167],v[84:87]\n"
W_"lgkmcnt(5)\n"
T_"v[10:11],v2 offset:62208\n"
M_"v[88:91],v[20:23],v[160:163],v[88:91]\n"
T_"v[12:13],v2 offset:61192\n"
T_"v[14:15],v2 offset:62216\n"
M_"v[92:95],v[20:23],v[164:167],v[92:95]\n"
E_"v41,v41\n"
M_"v[96:99],v[24:27],v[160:163],v[96:99]\n"
T_"v[16:17],v2 offset:61440\n"
M_"v[100:103],v[24:27],v[164:167],v[100:103]\n"
T_"v[18:19],v2 offset:62464\n"
X_"v44,v190,s94,v59 op_sel_hi:[1,0,0]\n"
M_"v[104:107],v[28:31],v[160:163],v[104:107]\n"
T_"v[20:21],v2 offset:61448\n"
M_"v[108:111],v[28:31],v[164:167],v[108:111]\n"
W_"lgkmcnt(6)\n"
T_"v[22:23],v2 offset:62472\n"
M_"v[112:115],v[32:35],v[160:163],v[112:115]\n"
T_"v[24:25],v2 offset:61696\n"
X_"v45,v190,s94,v59 op_sel:[1,0,0] op_sel_hi:[1,0,0]\n"
M_"v[116:119],v[32:35],v[164:167],v[116:119]\n"
T_"v[26:27],v2 offset:62720\n"
M_"v[120:123],v[36:39],v[160:163],v[120:123]\n"
T_"v[28:29],v2 offset:61704\n"
T_"v[30:31],v2 offset:62728\n"
M_"v[124:127],v[36:39],v[164:167],v[124:127]\n"
E_"v42,v42\n"
M_"v[48:51],v[60:63],v[168:171],v[48:51]\n"
M_"v[52:55],v[60:63],v[172:175],v[52:55]\n"
W_"lgkmcnt(5)\n"
M_"v[64:67],v[8:11],v[168:171],v[64:67]\n"
T_"v[32:33],v2 offset:61952\n"
T_"v[34:35],v2 offset:62976\n"
M_"v[68:71],v[8:11],v[172:175],v[68:71]\n"
E_"v43,v43\n"
M_"v[72:75],v[12:15],v[168:171],v[72:75]\n"
T_"v[36:37],v2 offset:61960\n"
M_"v[76:79],v[12:15],v[172:175],v[76:79]\n"
T_"v[38:39],v2 offset:62984\n"
C_"v188,v40,v41\n"
M_"v[80:83],v[16:19],v[168:171],v[80:83]\n"
"s_barrier\n"
T_"v[8:9],v1 offset:8448\n"
M_"v[84:87],v[16:19],v[172:175],v[84:87]\n"
W_"lgkmcnt(5)\n"
T_"v[10:11],v1 offset:9472\n"
M_"v[88:91],v[20:23],v[168:171],v[88:91]\n"
T_"v[12:13],v1 offset:8456\n"
X_"v46,v191,s94,v59 op_sel_hi:[1,0,0]\n"
M_"v[92:95],v[20:23],v[172:175],v[92:95]\n"
T_"v[14:15],v1 offset:9480\n"
M_"v[96:99],v[24:27],v[168:171],v[96:99]\n"
T_"v[16:17],v1 offset:8704\n"
M_"v[100:103],v[24:27],v[172:175],v[100:103]\n"
T_"v[18:19],v1 offset:9728\n"
X_"v47,v191,s94,v59 op_sel:[1,0,0] op_sel_hi:[1,0,0]\n"
M_"v[104:107],v[28:31],v[168:171],v[104:107]\n"
T_"v[20:21],v1 offset:8712\n"
M_"v[108:111],v[28:31],v[172:175],v[108:111]\n"
W_"lgkmcnt(6)\n"
T_"v[22:23],v1 offset:9736\n"
M_"v[112:115],v[32:35],v[168:171],v[112:115]\n"
T_"v[24:25],v1 offset:8960\n"
T_"v[26:27],v1 offset:9984\n"
M_"v[116:119],v[32:35],v[172:175],v[116:119]\n"
E_"v44,v44\n"
M_"v[120:123],v[36:39],v[168:171],v[120:123]\n"
T_"v[28:29],v1 offset:8968\n"
T_"v[30:31],v1 offset:9992\n"
M_"v[124:127],v[36:39],v[172:175],v[124:127]\n"
E_"v45,v45\n"
M_"v[48:51],v[60:63],v[176:179],v[48:51]\n"
M_"v[52:55],v[60:63],v[180:183],v[52:55]\n"
W_"lgkmcnt(5)\n"
M_"v[64:67],v[8:11],v[176:179],v[64:67]\n"
T_"v[32:33],v1 offset:9216\n"
C_"v189,v42,v43\n"
M_"v[68:71],v[8:11],v[180:183],v[68:71]\n"
T_"v[34:35],v1 offset:10240\n"
M_"v[72:75],v[12:15],v[176:179],v[72:75]\n"
T_"v[36:37],v1 offset:9224\n"
T_"v[38:39],v1 offset:10248\n"
M_"v[76:79],v[12:15],v[180:183],v[76:79]\n"
E_"v46,v46\n"
M_"v[80:83],v[16:19],v[176:179],v[80:83]\n"
T_"v[8:9],v1 offset:16896\n"
M_"v[84:87],v[16:19],v[180:183],v[84:87]\n"
W_"lgkmcnt(5)\n"
T_"v[10:11],v1 offset:17920\n"
M_"v[88:91],v[20:23],v[176:179],v[88:91]\n"
T_"v[12:13],v1 offset:16904\n"
T_"v[14:15],v1 offset:17928\n"
M_"v[92:95],v[20:23],v[180:183],v[92:95]\n"
E_"v47,v47\n"
M_"v[96:99],v[24:27],v[176:179],v[96:99]\n"
T_"v[16:17],v1 offset:17152\n"
M_"v[100:103],v[24:27],v[180:183],v[100:103]\n"
T_"v[18:19],v1 offset:18176\n"
C_"v190,v44,v45\n"
M_"v[104:107],v[28:31],v[176:179],v[104:107]\n"
T_"v[20:21],v1 offset:17160\n"
M_"v[108:111],v[28:31],v[180:183],v[108:111]\n"
W_"lgkmcnt(6)\n"
T_"v[22:23],v1 offset:18184\n"
M_"v[112:115],v[32:35],v[176:179],v[112:115]\n"
T_"v[24:25],v1 offset:17408\n"
C_"v191,v46,v47\n"
M_"v[116:119],v[32:35],v[180:183],v[116:119]\n"
T_"v[26:27],v1 offset:18432\n"
M_"v[120:123],v[36:39],v[176:179],v[120:123]\n"
T_"v[28:29],v1 offset:17416\n"
M_"v[124:127],v[36:39],v[180:183],v[124:127]\n"
T_"v[30:31],v1 offset:18440\n"
M_"v[48:51],v[60:63],v[184:187],v[48:51]\n"
M_"v[52:55],v[60:63],v[188:191],v[52:55]\n"
W_"lgkmcnt(5)\n"
M_"v[64:67],v[8:11],v[184:187],v[64:67]\n"
T_"v[32:33],v1 offset:17664\n"
M_"v[68:71],v[8:11],v[188:191],v[68:71]\n"
T_"v[34:35],v1 offset:18688\n"
M_"v[72:75],v[12:15],v[184:187],v[72:75]\n"
T_"v[36:37],v1 offset:17672\n"
T_"v[38:39],v1 offset:18696\n"
M_"v[76:79],v[12:15],v[188:191],v[76:79]\n"
"v_rcp_f32 v58, v48\n"
M_"v[80:83],v[16:19],v[184:187],v[80:83]\n"
"s_barrier\n"
T_"v[8:9],v3 offset:4096\n"
T_"v[10:11],v3 offset:5120\n"
W_"vmcnt(0)\n"
"v_add_u32 v6, 1, v6\n"
M_"v[84:87],v[16:19],v[188:191],v[84:87]\n"
W_"lgkmcnt(6)\n"
"v_rcp_f32 v59, v52\n"
M_"v[88:91],v[20:23],v[184:187],v[88:91]\n"
T_"v[12:13],v3 offset:4104\n"
M_"v[92:95],v[20:23],v[188:191],v[92:95]\n"
T_"v[14:15],v3 offset:5128\n"
M_"v[96:99],v[24:27],v[184:187],v[96:99]\n"
T_"v[16:17],v3 offset:4352\n"
M_"v[100:103],v[24:27],v[188:191],v[100:103]\n"
T_"v[18:19],v3 offset:5376\n"
M_"v[104:107],v[28:31],v[184:187],v[104:107]\n"
T_"v[20:21],v3 offset:4360\n"
M_"v[108:111],v[28:31],v[188:191],v[108:111]\n"
W_"lgkmcnt(5)\n"
T_"v[22:23],v3 offset:5384\n"
M_"v[112:115],v[32:35],v[184:187],v[112:115]\n"
T_"v[24:25],v3 offset:4608\n"
M_"v[116:119],v[32:35],v[188:191],v[116:119]\n"
T_"v[26:27],v3 offset:5632\n"
M_"v[120:123],v[36:39],v[184:187],v[120:123]\n"
T_"v[28:29],v3 offset:4616\n"
T_"v[30:31],v3 offset:5640\n"
M_"v[124:127],v[36:39],v[188:191],v[124:127]\n"
"v_pk_mul_f32 v[64:65], v[64:65], v[58:59] op_sel_hi:[1,0]\n"
"v_pk_mul_f32 v[66:67], v[66:67], v[58:59] op_sel_hi:[1,0]\n"
"v_pk_mul_f32 v[72:73], v[72:73], v[58:59] op_sel_hi:[1,0]\n"
"v_pk_mul_f32 v[74:75], v[74:75], v[58:59] op_sel_hi:[1,0]\n"
C_"v64,v64,v65\n"
C_"v65,v66,v67\n"
C_"v66,v72,v73\n"
C_"v67,v74,v75\n"
"v_pk_mul_f32 v[68:69], v[68:69], v[58:59] op_sel:[0,1] op_sel_hi:[1,1]\n"
"v_pk_mul_f32 v[70:71], v[70:71], v[58:59] op_sel:[0,1] op_sel_hi:[1,1]\n"
"v_pk_mul_f32 v[76:77], v[76:77], v[58:59] op_sel:[0,1] op_sel_hi:[1,1]\n"
"v_pk_mul_f32 v[78:79], v[78:79], v[58:59] op_sel:[0,1] op_sel_hi:[1,1]\n"
"ds_write_b128 v5, v[64:67] offset:8448\n"
C_"v68,v68,v69\n"
C_"v69,v70,v71\n"
C_"v70,v76,v77\n"
C_"v71,v78,v79\n"
"v_pk_mul_f32 v[80:81], v[80:81], v[58:59] op_sel_hi:[1,0]\n"
"v_pk_mul_f32 v[82:83], v[82:83], v[58:59] op_sel_hi:[1,0]\n"
"v_pk_mul_f32 v[88:89], v[88:89], v[58:59] op_sel_hi:[1,0]\n"
"v_pk_mul_f32 v[90:91], v[90:91], v[58:59] op_sel_hi:[1,0]\n"
"ds_write_b128 v5, v[68:71] offset:9472\n"
C_"v80,v80,v81\n"
C_"v81,v82,v83\n"
C_"v82,v88,v89\n"
C_"v83,v90,v91\n"
"v_pk_mul_f32 v[84:85], v[84:85], v[58:59] op_sel:[0,1] op_sel_hi:[1,1]\n"
"v_pk_mul_f32 v[86:87], v[86:87], v[58:59] op_sel:[0,1] op_sel_hi:[1,1]\n"
"v_pk_mul_f32 v[92:93], v[92:93], v[58:59] op_sel:[0,1] op_sel_hi:[1,1]\n"
"v_pk_mul_f32 v[94:95], v[94:95], v[58:59] op_sel:[0,1] op_sel_hi:[1,1]\n"
"v_add_u32 v5, 0x10000, v5\n"
"ds_write_b128 v5, v[80:83] offset:52736\n"
C_"v84,v84,v85\n"
C_"v85,v86,v87\n"
C_"v86,v92,v93\n"
C_"v87,v94,v95\n"
"v_pk_mul_f32 v[96:97], v[96:97], v[58:59] op_sel_hi:[1,0]\n"
"v_pk_mul_f32 v[98:99], v[98:99], v[58:59] op_sel_hi:[1,0]\n"
"v_pk_mul_f32 v[104:105], v[104:105], v[58:59] op_sel_hi:[1,0]\n"
"v_pk_mul_f32 v[106:107], v[106:107], v[58:59] op_sel_hi:[1,0]\n"
"ds_write_b128 v5, v[84:87] offset:53760\n"
C_"v96,v96,v97\n"
C_"v97,v98,v99\n"
C_"v98,v104,v105\n"
C_"v99,v106,v107\n"
"v_pk_mul_f32 v[100:101], v[100:101], v[58:59] op_sel:[0,1] op_sel_hi:[1,1]\n"
"v_pk_mul_f32 v[102:103], v[102:103], v[58:59] op_sel:[0,1] op_sel_hi:[1,1]\n"
"v_pk_mul_f32 v[108:109], v[108:109], v[58:59] op_sel:[0,1] op_sel_hi:[1,1]\n"
"v_pk_mul_f32 v[110:111], v[110:111], v[58:59] op_sel:[0,1] op_sel_hi:[1,1]\n"
"ds_write_b128 v5, v[96:99] offset:61184\n"
C_"v100,v100,v101\n"
C_"v101,v102,v103\n"
C_"v102,v108,v109\n"
C_"v103,v110,v111\n"
"v_pk_mul_f32 v[112:113], v[112:113], v[58:59] op_sel_hi:[1,0]\n"
"v_pk_mul_f32 v[114:115], v[114:115], v[58:59] op_sel_hi:[1,0]\n"
"v_pk_mul_f32 v[120:121], v[120:121], v[58:59] op_sel_hi:[1,0]\n"
"v_pk_mul_f32 v[122:123], v[122:123], v[58:59] op_sel_hi:[1,0]\n"
"ds_write_b128 v5, v[100:103] offset:62208\n"
C_"v112,v112,v113\n"
C_"v113,v114,v115\n"
C_"v114,v120,v121\n"
C_"v115,v122,v123\n"
"v_pk_mul_f32 v[116:117], v[116:117], v[58:59] op_sel:[0,1] op_sel_hi:[1,1]\n"
"v_pk_mul_f32 v[118:119], v[118:119], v[58:59] op_sel:[0,1] op_sel_hi:[1,1]\n"
"v_pk_mul_f32 v[124:125], v[124:125], v[58:59] op_sel:[0,1] op_sel_hi:[1,1]\n"
"v_pk_mul_f32 v[126:127], v[126:127], v[58:59] op_sel:[0,1] op_sel_hi:[1,1]\n"
"v_add_u32 v5, 0x10000, v5\n"
"ds_write_b128 v5, v[112:115] offset:20992\n"
C_"v116,v116,v117\n"
C_"v117,v118,v119\n"
C_"v118,v124,v125\n"
C_"v119,v126,v127\n"
"ds_write_b128 v5, v[116:119] offset:22016\n"
M_"v[64:67],v[8:11],v[192:195],0\n"
T_"v[32:33],v3 offset:4864\n"
M_"v[68:71],v[8:11],v[196:199],0\n"
W_"lgkmcnt(14)\n"
T_"v[34:35],v3 offset:5888\n"
M_"v[72:75],v[12:15],v[192:195],0\n"
T_"v[36:37],v3 offset:4872\n"
M_"v[76:79],v[12:15],v[196:199],0\n"
"PTH14:\n"
"s_cbranch_execz PTR\n"
T_"v[38:39],v3 offset:5896\n"
M_"v[80:83],v[16:19],v[192:195],0\n"
T_"v[8:9],v3 offset:12544\n"
M_"v[84:87],v[16:19],v[196:199],0\n"
W_"lgkmcnt(13)\n"
T_"v[10:11],v3 offset:13568\n"
M_"v[88:91],v[20:23],v[192:195],0\n"
T_"v[12:13],v3 offset:12552\n"
M_"v[92:95],v[20:23],v[196:199],0\n"
T_"v[14:15],v3 offset:13576\n"
M_"v[96:99],v[24:27],v[192:195],0\n"
T_"v[16:17],v3 offset:12800\n"
M_"v[100:103],v[24:27],v[196:199],0\n"
T_"v[18:19],v3 offset:13824\n"
M_"v[104:107],v[28:31],v[192:195],0\n"
T_"v[20:21],v3 offset:12808\n"
M_"v[108:111],v[28:31],v[196:199],0\n"
W_"lgkmcnt(6)\n"
T_"v[22:23],v3 offset:13832\n"
M_"v[112:115],v[32:35],v[192:195],0\n"
T_"v[24:25],v3 offset:13056\n"
M_"v[116:119],v[32:35],v[196:199],0\n"
T_"v[26:27],v3 offset:14080\n"
M_"v[120:123],v[36:39],v[192:195],0\n"
T_"v[28:29],v3 offset:13064\n"
M_"v[124:127],v[36:39],v[196:199],0\n"
W_"lgkmcnt(6)\n"
T_"v[30:31],v3 offset:14088\n"
M_"v[64:67],v[8:11],v[200:203],v[64:67]\n"
T_"v[32:33],v3 offset:13312\n"
M_"v[68:71],v[8:11],v[204:207],v[68:71]\n"
T_"v[34:35],v3 offset:14336\n"
M_"v[72:75],v[12:15],v[200:203],v[72:75]\n"
T_"v[36:37],v3 offset:13320\n"
M_"v[76:79],v[12:15],v[204:207],v[76:79]\n"
W_"lgkmcnt(6)\n"
T_"v[38:39],v3 offset:14344\n"
M_"v[80:83],v[16:19],v[200:203],v[80:83]\n"
"s_barrier\n"
"s_mov_b64 exec, 1\n"
"buffer_store_dword v6, off, s[80:83], s8 sc1\n"
"s_mov_b64 exec, -1\n"
T_"v[8:9],v1 offset:33792\n"
M_"v[84:87],v[16:19],v[204:207],v[84:87]\n"
T_"v[10:11],v1 offset:34816\n"
M_"v[88:91],v[20:23],v[200:203],v[88:91]\n"
T_"v[12:13],v1 offset:33800\n"
M_"v[92:95],v[20:23],v[204:207],v[92:95]\n"
W_"lgkmcnt(6)\n"
T_"v[14:15],v1 offset:34824\n"
M_"v[96:99],v[24:27],v[200:203],v[96:99]\n"
T_"v[16:17],v1 offset:34048\n"
M_"v[100:103],v[24:27],v[204:207],v[100:103]\n"
T_"v[18:19],v1 offset:35072\n"
M_"v[104:107],v[28:31],v[200:203],v[104:107]\n"
T_"v[20:21],v1 offset:34056\n"
M_"v[108:111],v[28:31],v[204:207],v[108:111]\n"
W_"lgkmcnt(6)\n"
T_"v[22:23],v1 offset:35080\n"
M_"v[112:115],v[32:35],v[200:203],v[112:115]\n"
T_"v[24:25],v1 offset:34304\n"
M_"v[116:119],v[32:35],v[204:207],v[116:119]\n"
T_"v[26:27],v1 offset:35328\n"
M_"v[120:123],v[36:39],v[200:203],v[120:123]\n"
T_"v[28:29],v1 offset:34312\n"
M_"v[124:127],v[36:39],v[204:207],v[124:127]\n"
W_"lgkmcnt(6)\n"
T_"v[30:31],v1 offset:35336\n"
M_"v[64:67],v[8:11],v[208:211],v[64:67]\n"
T_"v[32:33],v1 offset:34560\n"
M_"v[68:71],v[8:11],v[212:215],v[68:71]\n"
T_"v[34:35],v1 offset:35584\n"
M_"v[72:75],v[12:15],v[208:211],v[72:75]\n"
T_"v[36:37],v1 offset:34568\n"
M_"v[76:79],v[12:15],v[212:215],v[76:79]\n"
W_"lgkmcnt(6)\n"
T_"v[38:39],v1 offset:35592\n"
M_"v[80:83],v[16:19],v[208:211],v[80:83]\n"
T_"v[8:9],v1 offset:42240\n"
M_"v[84:87],v[16:19],v[212:215],v[84:87]\n"
T_"v[10:11],v1 offset:43264\n"
M_"v[88:91],v[20:23],v[208:211],v[88:91]\n"
T_"v[12:13],v1 offset:42248\n"
M_"v[92:95],v[20:23],v[212:215],v[92:95]\n"
W_"lgkmcnt(6)\n"
T_"v[14:15],v1 offset:43272\n"
M_"v[96:99],v[24:27],v[208:211],v[96:99]\n"
T_"v[16:17],v1 offset:42496\n"
M_"v[100:103],v[24:27],v[212:215],v[100:103]\n"
T_"v[18:19],v1 offset:43520\n"
M_"v[104:107],v[28:31],v[208:211],v[104:107]\n"
T_"v[20:21],v1 offset:42504\n"
M_"v[108:111],v[28:31],v[212:215],v[108:111]\n"
W_"lgkmcnt(6)\n"
T_"v[22:23],v1 offset:43528\n"
M_"v[112:115],v[32:35],v[208:211],v[112:115]\n"
T_"v[24:25],v1 offset:42752\n"
M_"v[116:119],v[32:35],v[212:215],v[116:119]\n"
T_"v[26:27],v1 offset:43776\n"
M_"v[120:123],v[36:39],v[208:211],v[120:123]\n"
T_"v[28:29],v1 offset:42760\n"
M_"v[124:127],v[36:39],v[212:215],v[124:127]\n"
W_"lgkmcnt(6)\n"
T_"v[30:31],v1 offset:43784\n"
M_"v[64:67],v[8:11],v[216:219],v[64:67]\n"
T_"v[32:33],v1 offset:43008\n"
M_"v[68:71],v[8:11],v[220:223],v[68:71]\n"
T_"v[34:35],v1 offset:44032\n"
M_"v[72:75],v[12:15],v[216:219],v[72:75]\n"
T_"v[36:37],v1 offset:43016\n"
R_"v[192:195],v4 offset:8448\n"
M_"v[76:79],v[12:15],v[220:223],v[76:79]\n"
W_"lgkmcnt(7)\n"
T_"v[38:39],v1 offset:44040\n"
M_"v[80:83],v[16:19],v[216:219],v[80:83]\n"
"s_barrier\n"
T_"v[8:9],v1 offset:50688\n"
M_"v[84:87],v[16:19],v[220:223],v[84:87]\n"
T_"v[10:11],v1 offset:51712\n"
M_"v[88:91],v[20:23],v[216:219],v[88:91]\n"
T_"v[12:13],v1 offset:50696\n"
M_"v[92:95],v[20:23],v[220:223],v[92:95]\n"
W_"lgkmcnt(7)\n"
T_"v[14:15],v1 offset:51720\n"
M_"v[96:99],v[24:27],v[216:219],v[96:99]\n"
T_"v[16:17],v1 offset:50944\n"
R_"v[196:199],v4 offset:9472\n"
M_"v[100:103],v[24:27],v[220:223],v[100:103]\n"
T_"v[18:19],v1 offset:51968\n"
M_"v[104:107],v[28:31],v[216:219],v[104:107]\n"
T_"v[20:21],v1 offset:50952\n"
M_"v[108:111],v[28:31],v[220:223],v[108:111]\n"
W_"lgkmcnt(7)\n"
T_"v[22:23],v1 offset:51976\n"
M_"v[112:115],v[32:35],v[216:219],v[112:115]\n"
T_"v[24:25],v1 offset:51200\n"
M_"v[116:119],v[32:35],v[220:223],v[116:119]\n"
T_"v[26:27],v1 offset:52224\n"
M_"v[120:123],v[36:39],v[216:219],v[120:123]\n"
T_"v[28:29],v1 offset:51208\n"
M_"v[124:127],v[36:39],v[220:223],v[124:127]\n"
W_"lgkmcnt(6)\n"
T_"v[30:31],v1 offset:52232\n"
M_"v[64:67],v[8:11],v[224:227],v[64:67]\n"
T_"v[32:33],v1 offset:51456\n"
M_"v[68:71],v[8:11],v[228:231],v[68:71]\n"
T_"v[34:35],v1 offset:52480\n"
M_"v[72:75],v[12:15],v[224:227],v[72:75]\n"
T_"v[36:37],v1 offset:51464\n"
"v_add_u32 v4, 0x10000, v4\n"
R_"v[200:203],v4 offset:52736\n"
M_"v[76:79],v[12:15],v[228:231],v[76:79]\n"
W_"lgkmcnt(7)\n"
T_"v[38:39],v1 offset:52488\n"
M_"v[80:83],v[16:19],v[224:227],v[80:83]\n"
T_"v[8:9],v1 offset:59136\n"
M_"v[84:87],v[16:19],v[228:231],v[84:87]\n"
T_"v[10:11],v1 offset:60160\n"
M_"v[88:91],v[20:23],v[224:227],v[88:91]\n"
T_"v[12:13],v1 offset:59144\n"
M_"v[92:95],v[20:23],v[228:231],v[92:95]\n"
W_"lgkmcnt(7)\n"
T_"v[14:15],v1 offset:60168\n"
M_"v[96:99],v[24:27],v[224:227],v[96:99]\n"
T_"v[16:17],v1 offset:59392\n"
R_"v[204:207],v4 offset:53760\n"
M_"v[100:103],v[24:27],v[228:231],v[100:103]\n"
T_"v[18:19],v1 offset:60416\n"
M_"v[104:107],v[28:31],v[224:227],v[104:107]\n"
T_"v[20:21],v1 offset:59400\n"
M_"v[108:111],v[28:31],v[228:231],v[108:111]\n"
W_"lgkmcnt(7)\n"
T_"v[22:23],v1 offset:60424\n"
M_"v[112:115],v[32:35],v[224:227],v[112:115]\n"
T_"v[24:25],v1 offset:59648\n"
M_"v[116:119],v[32:35],v[228:231],v[116:119]\n"
T_"v[26:27],v1 offset:60672\n"
M_"v[120:123],v[36:39],v[224:227],v[120:123]\n"
T_"v[28:29],v1 offset:59656\n"
M_"v[124:127],v[36:39],v[228:231],v[124:127]\n"
W_"lgkmcnt(6)\n"
T_"v[30:31],v1 offset:60680\n"
M_"v[64:67],v[8:11],v[232:235],v[64:67]\n"
T_"v[32:33],v1 offset:59904\n"
M_"v[68:71],v[8:11],v[236:239],v[68:71]\n"
T_"v[34:35],v1 offset:60928\n"
M_"v[72:75],v[12:15],v[232:235],v[72:75]\n"
T_"v[36:37],v1 offset:59912\n"
R_"v[208:211],v4 offset:61184\n"
M_"v[76:79],v[12:15],v[236:239],v[76:79]\n"
W_"lgkmcnt(7)\n"
T_"v[38:39],v1 offset:60936\n"
M_"v[80:83],v[16:19],v[232:235],v[80:83]\n"
"s_barrier\n"
T_"v[8:9],v2 offset:2048\n"
M_"v[84:87],v[16:19],v[236:239],v[84:87]\n"
T_"v[10:11],v2 offset:3072\n"
M_"v[88:91],v[20:23],v[232:235],v[88:91]\n"
T_"v[12:13],v2 offset:2056\n"
M_"v[92:95],v[20:23],v[236:239],v[92:95]\n"
W_"lgkmcnt(7)\n"
T_"v[14:15],v2 offset:3080\n"
M_"v[96:99],v[24:27],v[232:235],v[96:99]\n"
T_"v[16:17],v2 offset:2304\n"
R_"v[212:215],v4 offset:62208\n"
M_"v[100:103],v[24:27],v[236:239],v[100:103]\n"
T_"v[18:19],v2 offset:3328\n"
M_"v[104:107],v[28:31],v[232:235],v[104:107]\n"
T_"v[20:21],v2 offset:2312\n"
M_"v[108:111],v[28:31],v[236:239],v[108:111]\n"
W_"lgkmcnt(7)\n"
T_"v[22:23],v2 offset:3336\n"
M_"v[112:115],v[32:35],v[232:235],v[112:115]\n"
T_"v[24:25],v2 offset:2560\n"
M_"v[116:119],v[32:35],v[236:239],v[116:119]\n"
T_"v[26:27],v2 offset:3584\n"
M_"v[120:123],v[36:39],v[232:235],v[120:123]\n"
T_"v[28:29],v2 offset:2568\n"
M_"v[124:127],v[36:39],v[236:239],v[124:127]\n"
W_"lgkmcnt(6)\n"
T_"v[30:31],v2 offset:3592\n"
M_"v[64:67],v[8:11],v[240:243],v[64:67]\n"
T_"v[32:33],v2 offset:2816\n"
M_"v[68:71],v[8:11],v[244:247],v[68:71]\n"
T_"v[34:35],v2 offset:3840\n"
M_"v[72:75],v[12:15],v[240:243],v[72:75]\n"
T_"v[36:37],v2 offset:2824\n"
"v_add_u32 v4, 0x10000, v4\n"
R_"v[216:219],v4 offset:20992\n"
M_"v[76:79],v[12:15],v[244:247],v[76:79]\n"
W_"lgkmcnt(7)\n"
T_"v[38:39],v2 offset:3848\n"
M_"v[80:83],v[16:19],v[240:243],v[80:83]\n"
T_"v[8:9],v2 offset:10496\n"
M_"v[84:87],v[16:19],v[244:247],v[84:87]\n"
T_"v[10:11],v2 offset:11520\n"
M_"v[88:91],v[20:23],v[240:243],v[88:91]\n"
T_"v[12:13],v2 offset:10504\n"
M_"v[92:95],v[20:23],v[244:247],v[92:95]\n"
W_"lgkmcnt(7)\n"
T_"v[14:15],v2 offset:11528\n"
M_"v[96:99],v[24:27],v[240:243],v[96:99]\n"
T_"v[16:17],v2 offset:10752\n"
R_"v[220:223],v4 offset:22016\n"
M_"v[100:103],v[24:27],v[244:247],v[100:103]\n"
T_"v[18:19],v2 offset:11776\n"
M_"v[104:107],v[28:31],v[240:243],v[104:107]\n"
T_"v[20:21],v2 offset:10760\n"
M_"v[108:111],v[28:31],v[244:247],v[108:111]\n"
W_"lgkmcnt(7)\n"
T_"v[22:23],v2 offset:11784\n"
M_"v[112:115],v[32:35],v[240:243],v[112:115]\n"
T_"v[24:25],v2 offset:11008\n"
M_"v[116:119],v[32:35],v[244:247],v[116:119]\n"
T_"v[26:27],v2 offset:12032\n"
M_"v[120:123],v[36:39],v[240:243],v[120:123]\n"
T_"v[28:29],v2 offset:11016\n"
M_"v[124:127],v[36:39],v[244:247],v[124:127]\n"
W_"lgkmcnt(6)\n"
T_"v[30:31],v2 offset:12040\n"
M_"v[64:67],v[8:11],v[248:251],v[64:67]\n"
T_"v[32:33],v2 offset:11264\n"
M_"v[68:71],v[8:11],v[252:255],v[68:71]\n"
T_"v[34:35],v2 offset:12288\n"
M_"v[72:75],v[12:15],v[248:251],v[72:75]\n"
T_"v[36:37],v2 offset:11272\n"
"v_add_u32 v4, 0xfffe0000, v4\n"
R_"v[224:227],v4 offset:12544\n"
M_"v[76:79],v[12:15],v[252:255],v[76:79]\n"
W_"lgkmcnt(7)\n"
T_"v[38:39],v2 offset:12296\n"
M_"v[80:83],v[16:19],v[248:251],v[80:83]\n"
"s_barrier\n"
T_"v[8:9],v1 offset:25344\n"
M_"v[84:87],v[16:19],v[252:255],v[84:87]\n"
T_"v[10:11],v1 offset:26368\n"
M_"v[88:91],v[20:23],v[248:251],v[88:91]\n"
T_"v[12:13],v1 offset:25352\n"
M_"v[92:95],v[20:23],v[252:255],v[92:95]\n"
W_"lgkmcnt(7)\n"
T_"v[14:15],v1 offset:26376\n"
M_"v[96:99],v[24:27],v[248:251],v[96:99]\n"
T_"v[16:17],v1 offset:25600\n"
R_"v[228:231],v4 offset:13568\n"
M_"v[100:103],v[24:27],v[252:255],v[100:103]\n"
T_"v[18:19],v1 offset:26624\n"
M_"v[104:107],v[28:31],v[248:251],v[104:107]\n"
T_"v[20:21],v1 offset:25608\n"
M_"v[108:111],v[28:31],v[252:255],v[108:111]\n"
W_"lgkmcnt(7)\n"
T_"v[22:23],v1 offset:26632\n"
M_"v[112:115],v[32:35],v[248:251],v[112:115]\n"
T_"v[24:25],v1 offset:25856\n"
M_"v[116:119],v[32:35],v[252:255],v[116:119]\n"
T_"v[26:27],v1 offset:26880\n"
M_"v[120:123],v[36:39],v[248:251],v[120:123]\n"
T_"v[28:29],v1 offset:25864\n"
M_"v[124:127],v[36:39],v[252:255],v[124:127]\n"
W_"lgkmcnt(6)\n"
T_"v[30:31],v1 offset:26888\n"
M_"v[64:67],v[8:11],v[128:131],v[64:67]\n"
T_"v[32:33],v1 offset:26112\n"
M_"v[68:71],v[8:11],v[132:135],v[68:71]\n"
T_"v[34:35],v1 offset:27136\n"
M_"v[72:75],v[12:15],v[128:131],v[72:75]\n"
T_"v[36:37],v1 offset:26120\n"
"v_add_u32 v4, 0x10000, v4\n"
R_"v[232:235],v4 offset:56832\n"
M_"v[76:79],v[12:15],v[132:135],v[76:79]\n"
W_"lgkmcnt(7)\n"
T_"v[38:39],v1 offset:27144\n"
M_"v[80:83],v[16:19],v[128:131],v[80:83]\n"
T_"v[8:9],v2 offset:18944\n"
M_"v[84:87],v[16:19],v[132:135],v[84:87]\n"
T_"v[10:11],v2 offset:19968\n"
M_"v[88:91],v[20:23],v[128:131],v[88:91]\n"
T_"v[12:13],v2 offset:18952\n"
M_"v[92:95],v[20:23],v[132:135],v[92:95]\n"
W_"lgkmcnt(7)\n"
T_"v[14:15],v2 offset:19976\n"
M_"v[96:99],v[24:27],v[128:131],v[96:99]\n"
T_"v[16:17],v2 offset:19200\n"
R_"v[236:239],v4 offset:57856\n"
M_"v[100:103],v[24:27],v[132:135],v[100:103]\n"
T_"v[18:19],v2 offset:20224\n"
M_"v[104:107],v[28:31],v[128:131],v[104:107]\n"
T_"v[20:21],v2 offset:19208\n"
M_"v[108:111],v[28:31],v[132:135],v[108:111]\n"
W_"lgkmcnt(7)\n"
T_"v[22:23],v2 offset:20232\n"
M_"v[112:115],v[32:35],v[128:131],v[112:115]\n"
T_"v[24:25],v2 offset:19456\n"
M_"v[116:119],v[32:35],v[132:135],v[116:119]\n"
T_"v[26:27],v2 offset:20480\n"
M_"v[120:123],v[36:39],v[128:131],v[120:123]\n"
T_"v[28:29],v2 offset:19464\n"
M_"v[124:127],v[36:39],v[132:135],v[124:127]\n"
W_"lgkmcnt(6)\n"
T_"v[30:31],v2 offset:20488\n"
M_"v[64:67],v[8:11],v[136:139],v[64:67]\n"
T_"v[32:33],v2 offset:19712\n"
M_"v[68:71],v[8:11],v[140:143],v[68:71]\n"
T_"v[34:35],v2 offset:20736\n"
M_"v[72:75],v[12:15],v[136:139],v[72:75]\n"
T_"v[36:37],v2 offset:19720\n"
R_"v[240:243],v4 offset:65280\n"
M_"v[76:79],v[12:15],v[140:143],v[76:79]\n"
W_"lgkmcnt(7)\n"
T_"v[38:39],v2 offset:20744\n"
M_"v[80:83],v[16:19],v[136:139],v[80:83]\n"
"s_barrier\n"
T_"v[8:9],v2 offset:27392\n"
M_"v[84:87],v[16:19],v[140:143],v[84:87]\n"
T_"v[10:11],v2 offset:28416\n"
M_"v[88:91],v[20:23],v[136:139],v[88:91]\n"
T_"v[12:13],v2 offset:27400\n"
M_"v[92:95],v[20:23],v[140:143],v[92:95]\n"
W_"lgkmcnt(7)\n"
T_"v[14:15],v2 offset:28424\n"
M_"v[96:99],v[24:27],v[136:139],v[96:99]\n"
T_"v[16:17],v2 offset:27648\n"
"v_add_u32 v4, 0x10000, v4\n"
R_"v[244:247],v4 offset:768\n"
M_"v[100:103],v[24:27],v[140:143],v[100:103]\n"
T_"v[18:19],v2 offset:28672\n"
M_"v[104:107],v[28:31],v[136:139],v[104:107]\n"
T_"v[20:21],v2 offset:27656\n"
M_"v[108:111],v[28:31],v[140:143],v[108:111]\n"
W_"lgkmcnt(7)\n"
T_"v[22:23],v2 offset:28680\n"
M_"v[112:115],v[32:35],v[136:139],v[112:115]\n"
T_"v[24:25],v2 offset:27904\n"
M_"v[116:119],v[32:35],v[140:143],v[116:119]\n"
T_"v[26:27],v2 offset:28928\n"
M_"v[120:123],v[36:39],v[136:139],v[120:123]\n"
T_"v[28:29],v2 offset:27912\n"
M_"v[124:127],v[36:39],v[140:143],v[124:127]\n"
W_"lgkmcnt(6)\n"
T_"v[30:31],v2 offset:28936\n"
M_"v[64:67],v[8:11],v[144:147],v[64:67]\n"
T_"v[32:33],v2 offset:28160\n"
M_"v[68:71],v[8:11],v[148:151],v[68:71]\n"
T_"v[34:35],v2 offset:29184\n"
M_"v[72:75],v[12:15],v[144:147],v[72:75]\n"
T_"v[36:37],v2 offset:28168\n"
R_"v[248:251],v4 offset:25088\n"
M_"v[76:79],v[12:15],v[148:151],v[76:79]\n"
W_"lgkmcnt(7)\n"
T_"v[38:39],v2 offset:29192\n"
M_"v[80:83],v[16:19],v[144:147],v[80:83]\n"
T_"v[8:9],v2 offset:35840\n"
M_"v[84:87],v[16:19],v[148:151],v[84:87]\n"
T_"v[10:11],v2 offset:36864\n"
M_"v[88:91],v[20:23],v[144:147],v[88:91]\n"
T_"v[12:13],v2 offset:35848\n"
M_"v[92:95],v[20:23],v[148:151],v[92:95]\n"
W_"lgkmcnt(7)\n"
T_"v[14:15],v2 offset:36872\n"
M_"v[96:99],v[24:27],v[144:147],v[96:99]\n"
T_"v[16:17],v2 offset:36096\n"
R_"v[252:255],v4 offset:26112\n"
M_"v[100:103],v[24:27],v[148:151],v[100:103]\n"
T_"v[18:19],v2 offset:37120\n"
M_"v[104:107],v[28:31],v[144:147],v[104:107]\n"
T_"v[20:21],v2 offset:36104\n"
M_"v[108:111],v[28:31],v[148:151],v[108:111]\n"
W_"lgkmcnt(7)\n"
T_"v[22:23],v2 offset:37128\n"
M_"v[112:115],v[32:35],v[144:147],v[112:115]\n"
T_"v[24:25],v2 offset:36352\n"
M_"v[116:119],v[32:35],v[148:151],v[116:119]\n"
T_"v[26:27],v2 offset:37376\n"
M_"v[120:123],v[36:39],v[144:147],v[120:123]\n"
T_"v[28:29],v2 offset:36360\n"
M_"v[124:127],v[36:39],v[148:151],v[124:127]\n"
W_"lgkmcnt(6)\n"
T_"v[30:31],v2 offset:37384\n"
M_"v[64:67],v[8:11],v[152:155],v[64:67]\n"
T_"v[32:33],v2 offset:36608\n"
M_"v[68:71],v[8:11],v[156:159],v[68:71]\n"
T_"v[34:35],v2 offset:37632\n"
M_"v[72:75],v[12:15],v[152:155],v[72:75]\n"
T_"v[36:37],v2 offset:36616\n"
M_"v[76:79],v[12:15],v[156:159],v[76:79]\n"
W_"lgkmcnt(6)\n"
T_"v[38:39],v2 offset:37640\n"
M_"v[80:83],v[16:19],v[152:155],v[80:83]\n"
"s_barrier\n"
T_"v[8:9],v1 offset:0\n"
M_"v[84:87],v[16:19],v[156:159],v[84:87]\n"
T_"v[10:11],v1 offset:1024\n"
M_"v[88:91],v[20:23],v[152:155],v[88:91]\n"
T_"v[12:13],v1 offset:8\n"
M_"v[92:95],v[20:23],v[156:159],v[92:95]\n"
W_"lgkmcnt(6)\n"
T_"v[14:15],v1 offset:1032\n"
M_"v[96:99],v[24:27],v[152:155],v[96:99]\n"
T_"v[16:17],v1 offset:256\n"
M_"v[100:103],v[24:27],v[156:159],v[100:103]\n"
T_"v[18:19],v1 offset:1280\n"
M_"v[104:107],v[28:31],v[152:155],v[104:107]\n"
T_"v[20:21],v1 offset:264\n"
M_"v[108:111],v[28:31],v[156:159],v[108:111]\n"
W_"lgkmcnt(6)\n"
T_"v[22:23],v1 offset:1288\n"
M_"v[112:115],v[32:35],v[152:155],v[112:115]\n"
T_"v[24:25],v1 offset:512\n"
M_"v[116:119],v[32:35],v[156:159],v[116:119]\n"
T_"v[26:27],v1 offset:1536\n"
M_"v[120:123],v[36:39],v[152:155],v[120:123]\n"
T_"v[28:29],v1 offset:520\n"
M_"v[124:127],v[36:39],v[156:159],v[124:127]\n"
W_"lgkmcnt(6)\n"
T_"v[30:31],v1 offset:1544\n"
M_"v[64:67],v[8:11],v[160:163],v[64:67]\n"
T_"v[32:33],v1 offset:768\n"
M_"v[68:71],v[8:11],v[164:167],v[68:71]\n"
T_"v[34:35],v1 offset:1792\n"
M_"v[72:75],v[12:15],v[160:163],v[72:75]\n"
T_"v[36:37],v1 offset:776\n"
M_"v[76:79],v[12:15],v[164:167],v[76:79]\n"
W_"lgkmcnt(6)\n"
T_"v[38:39],v1 offset:1800\n"
M_"v[80:83],v[16:19],v[160:163],v[80:83]\n"
T_"v[8:9],v1 offset:16896\n"
M_"v[84:87],v[16:19],v[164:167],v[84:87]\n"
T_"v[10:11],v1 offset:17920\n"
M_"v[88:91],v[20:23],v[160:163],v[88:91]\n"
T_"v[12:13],v1 offset:16904\n"
M_"v[92:95],v[20:23],v[164:167],v[92:95]\n"
W_"lgkmcnt(6)\n"
T_"v[14:15],v1 offset:17928\n"
M_"v[96:99],v[24:27],v[160:163],v[96:99]\n"
T_"v[16:17],v1 offset:17152\n"
M_"v[100:103],v[24:27],v[164:167],v[100:103]\n"
T_"v[18:19],v1 offset:18176\n"
M_"v[104:107],v[28:31],v[160:163],v[104:107]\n"
T_"v[20:21],v1 offset:17160\n"
M_"v[108:111],v[28:31],v[164:167],v[108:111]\n"
W_"lgkmcnt(6)\n"
T_"v[22:23],v1 offset:18184\n"
M_"v[112:115],v[32:35],v[160:163],v[112:115]\n"
T_"v[24:25],v1 offset:17408\n"
M_"v[116:119],v[32:35],v[164:167],v[116:119]\n"
T_"v[26:27],v1 offset:18432\n"
M_"v[120:123],v[36:39],v[160:163],v[120:123]\n"
T_"v[28:29],v1 offset:17416\n"
M_"v[124:127],v[36:39],v[164:167],v[124:127]\n"
W_"lgkmcnt(6)\n"
T_"v[30:31],v1 offset:18440\n"
M_"v[64:67],v[8:11],v[168:171],v[64:67]\n"
T_"v[32:33],v1 offset:17664\n"
M_"v[68:71],v[8:11],v[172:175],v[68:71]\n"
T_"v[34:35],v1 offset:18688\n"
M_"v[72:75],v[12:15],v[168:171],v[72:75]\n"
T_"v[36:37],v1 offset:17672\n"
M_"v[76:79],v[12:15],v[172:175],v[76:79]\n"
W_"lgkmcnt(6)\n"
T_"v[38:39],v1 offset:18696\n"
M_"v[80:83],v[16:19],v[168:171],v[80:83]\n"
"s_barrier\n"
T_"v[8:9],v2 offset:44288\n"
M_"v[84:87],v[16:19],v[172:175],v[84:87]\n"
T_"v[10:11],v2 offset:45312\n"
M_"v[88:91],v[20:23],v[168:171],v[88:91]\n"
T_"v[12:13],v2 offset:44296\n"
M_"v[92:95],v[20:23],v[172:175],v[92:95]\n"
W_"lgkmcnt(6)\n"
T_"v[14:15],v2 offset:45320\n"
M_"v[96:99],v[24:27],v[168:171],v[96:99]\n"
T_"v[16:17],v2 offset:44544\n"
M_"v[100:103],v[24:27],v[172:175],v[100:103]\n"
T_"v[18:19],v2 offset:45568\n"
M_"v[104:107],v[28:31],v[168:171],v[104:107]\n"
T_"v[20:21],v2 offset:44552\n"
M_"v[108:111],v[28:31],v[172:175],v[108:111]\n"
W_"lgkmcnt(6)\n"
T_"v[22:23],v2 offset:45576\n"
M_"v[112:115],v[32:35],v[168:171],v[112:115]\n"
T_"v[24:25],v2 offset:44800\n"
M_"v[116:119],v[32:35],v[172:175],v[116:119]\n"
T_"v[26:27],v2 offset:45824\n"
M_"v[120:123],v[36:39],v[168:171],v[120:123]\n"
T_"v[28:29],v2 offset:44808\n"
M_"v[124:127],v[36:39],v[172:175],v[124:127]\n"
W_"lgkmcnt(6)\n"
T_"v[30:31],v2 offset:45832\n"
M_"v[64:67],v[8:11],v[176:179],v[64:67]\n"
T_"v[32:33],v2 offset:45056\n"
M_"v[68:71],v[8:11],v[180:183],v[68:71]\n"
T_"v[34:35],v2 offset:46080\n"
M_"v[72:75],v[12:15],v[176:179],v[72:75]\n"
T_"v[36:37],v2 offset:45064\n"
M_"v[76:79],v[12:15],v[180:183],v[76:79]\n"
W_"lgkmcnt(6)\n"
T_"v[38:39],v2 offset:46088\n"
M_"v[80:83],v[16:19],v[176:179],v[80:83]\n"
T_"v[8:9],v3 offset:4096\n"
M_"v[84:87],v[16:19],v[180:183],v[84:87]\n"
T_"v[10:11],v3 offset:5120\n"
M_"v[88:91],v[20:23],v[176:179],v[88:91]\n"
T_"v[12:13],v3 offset:4104\n"
M_"v[92:95],v[20:23],v[180:183],v[92:95]\n"
W_"lgkmcnt(6)\n"
T_"v[14:15],v3 offset:5128\n"
M_"v[96:99],v[24:27],v[176:179],v[96:99]\n"
"PTH15:\n"
"s_cbranch_execz PTR\n"
T_"v[16:17],v3 offset:4352\n"
M_"v[100:103],v[24:27],v[180:183],v[100:103]\n"
T_"v[18:19],v3 offset:5376\n"
M_"v[104:107],v[28:31],v[176:179],v[104:107]\n"
T_"v[20:21],v3 offset:4360\n"
M_"v[108:111],v[28:31],v[180:183],v[108:111]\n"
W_"lgkmcnt(6)\n"
T_"v[22:23],v3 offset:5384\n"
M_"v[112:115],v[32:35],v[176:179],v[112:115]\n"
T_"v[24:25],v3 offset:4608\n"
M_"v[116:119],v[32:35],v[180:183],v[116:119]\n"
T_"v[26:27],v3 offset:5632\n"
M_"v[120:123],v[36:39],v[176:179],v[120:123]\n"
T_"v[28:29],v3 offset:4616\n"
M_"v[124:127],v[36:39],v[180:183],v[124:127]\n"
W_"lgkmcnt(6)\n"
T_"v[30:31],v3 offset:5640\n"
M_"v[64:67],v[8:11],v[184:187],v[64:67]\n"
T_"v[32:33],v3 offset:4864\n"
M_"v[68:71],v[8:11],v[188:191],v[68:71]\n"
T_"v[34:35],v3 offset:5888\n"
M_"v[72:75],v[12:15],v[184:187],v[72:75]\n"
T_"v[36:37],v3 offset:4872\n"
M_"v[76:79],v[12:15],v[188:191],v[76:79]\n"
W_"lgkmcnt(6)\n"
T_"v[38:39],v3 offset:5896\n"
M_"v[80:83],v[16:19],v[184:187],v[80:83]\n"
"s_barrier\n"
"s_lshl_b32 s4, s35, 9\n"
"v_add_u32 v1, s4, v0\n"
"v_add_u32 v2, 0x10000, v1\n"
"v_add_u32 v3, 0x20000, v1\n"
T_"v[8:9],v1 offset:33792\n"
M_"v[84:87],v[16:19],v[188:191],v[84:87]\n"
T_"v[10:11],v1 offset:34048\n"
M_"v[88:91],v[20:23],v[184:187],v[88:91]\n"
T_"v[12:13],v1 offset:33800\n"
M_"v[92:95],v[20:23],v[188:191],v[92:95]\n"
W_"lgkmcnt(6)\n"
T_"v[14:15],v1 offset:34056\n"
M_"v[96:99],v[24:27],v[184:187],v[96:99]\n"
T_"v[16:17],v1 offset:34816\n"
M_"v[100:103],v[24:27],v[188:191],v[100:103]\n"
T_"v[18:19],v1 offset:35072\n"
M_"v[104:107],v[28:31],v[184:187],v[104:107]\n"
T_"v[20:21],v1 offset:34824\n"
M_"v[108:111],v[28:31],v[188:191],v[108:111]\n"
W_"lgkmcnt(6)\n"
T_"v[22:23],v1 offset:35080\n"
M_"v[112:115],v[32:35],v[184:187],v[112:115]\n"
T_"v[24:25],v3 offset:12544\n"
M_"v[116:119],v[32:35],v[188:191],v[116:119]\n"
T_"v[26:27],v3 offset:12800\n"
M_"v[120:123],v[36:39],v[184:187],v[120:123]\n"
T_"v[28:29],v3 offset:12552\n"
T_"v[30:31],v3 offset:12808\n"
M_"v[124:127],v[36:39],v[188:191],v[124:127]\n"
W_"lgkmcnt(7)\n"
"v_pk_mul_f32 v[64:65], v[64:65], v[58:59] op_sel_hi:[1,0]\n"
"v_pk_mul_f32 v[66:67], v[66:67], v[58:59] op_sel_hi:[1,0]\n"
"v_pk_mul_f32 v[72:73], v[72:73], v[58:59] op_sel_hi:[1,0]\n"
"v_pk_mul_f32 v[74:75], v[74:75], v[58:59] op_sel_hi:[1,0]\n"
C_"v64,v64,v65\n"
C_"v65,v66,v67\n"
C_"v66,v72,v73\n"
C_"v67,v74,v75\n"
"v_pk_mul_f32 v[68:69], v[68:69], v[58:59] op_sel:[0,1] op_sel_hi:[1,1]\n"
"v_pk_mul_f32 v[70:71], v[70:71], v[58:59] op_sel:[0,1] op_sel_hi:[1,1]\n"
"v_pk_mul_f32 v[76:77], v[76:77], v[58:59] op_sel:[0,1] op_sel_hi:[1,1]\n"
"v_pk_mul_f32 v[78:79], v[78:79], v[58:59] op_sel:[0,1] op_sel_hi:[1,1]\n"
"v_add_u32 v5, 0xfffe0000, v5\n"
"ds_write_b128 v5, v[64:67] offset:0\n"
C_"v68,v68,v69\n"
C_"v69,v70,v71\n"
C_"v70,v76,v77\n"
C_"v71,v78,v79\n"
"v_pk_mul_f32 v[80:81], v[80:81], v[58:59] op_sel_hi:[1,0]\n"
"v_pk_mul_f32 v[82:83], v[82:83], v[58:59] op_sel_hi:[1,0]\n"
"v_pk_mul_f32 v[88:89], v[88:89], v[58:59] op_sel_hi:[1,0]\n"
"v_pk_mul_f32 v[90:91], v[90:91], v[58:59] op_sel_hi:[1,0]\n"
"ds_write_b128 v5, v[68:71] offset:1024\n"
C_"v80,v80,v81\n"
C_"v81,v82,v83\n"
C_"v82,v88,v89\n"
C_"v83,v90,v91\n"
"v_pk_mul_f32 v[84:85], v[84:85], v[58:59] op_sel:[0,1] op_sel_hi:[1,1]\n"
"v_pk_mul_f32 v[86:87], v[86:87], v[58:59] op_sel:[0,1] op_sel_hi:[1,1]\n"
"v_pk_mul_f32 v[92:93], v[92:93], v[58:59] op_sel:[0,1] op_sel_hi:[1,1]\n"
"v_pk_mul_f32 v[94:95], v[94:95], v[58:59] op_sel:[0,1] op_sel_hi:[1,1]\n"
"ds_write_b128 v5, v[80:83] offset:16896\n"
C_"v84,v84,v85\n"
C_"v85,v86,v87\n"
C_"v86,v92,v93\n"
C_"v87,v94,v95\n"
"v_pk_mul_f32 v[96:97], v[96:97], v[58:59] op_sel_hi:[1,0]\n"
"v_pk_mul_f32 v[98:99], v[98:99], v[58:59] op_sel_hi:[1,0]\n"
"v_pk_mul_f32 v[104:105], v[104:105], v[58:59] op_sel_hi:[1,0]\n"
"v_pk_mul_f32 v[106:107], v[106:107], v[58:59] op_sel_hi:[1,0]\n"
"ds_write_b128 v5, v[84:87] offset:17920\n"
C_"v96,v96,v97\n"
C_"v97,v98,v99\n"
C_"v98,v104,v105\n"
C_"v99,v106,v107\n"
"v_pk_mul_f32 v[100:101], v[100:101], v[58:59] op_sel:[0,1] op_sel_hi:[1,1]\n"
"v_pk_mul_f32 v[102:103], v[102:103], v[58:59] op_sel:[0,1] op_sel_hi:[1,1]\n"
"v_pk_mul_f32 v[108:109], v[108:109], v[58:59] op_sel:[0,1] op_sel_hi:[1,1]\n"
"v_pk_mul_f32 v[110:111], v[110:111], v[58:59] op_sel:[0,1] op_sel_hi:[1,1]\n"
"v_add_u32 v5, 0x10000, v5\n"
"ds_write_b128 v5, v[96:99] offset:35840\n"
C_"v100,v100,v101\n"
C_"v101,v102,v103\n"
C_"v102,v108,v109\n"
C_"v103,v110,v111\n"
"v_pk_mul_f32 v[112:113], v[112:113], v[58:59] op_sel_hi:[1,0]\n"
"v_pk_mul_f32 v[114:115], v[114:115], v[58:59] op_sel_hi:[1,0]\n"
"v_pk_mul_f32 v[120:121], v[120:121], v[58:59] op_sel_hi:[1,0]\n"
"v_pk_mul_f32 v[122:123], v[122:123], v[58:59] op_sel_hi:[1,0]\n"
"ds_write_b128 v5, v[100:103] offset:36864\n"
C_"v112,v112,v113\n"
C_"v113,v114,v115\n"
C_"v114,v120,v121\n"
C_"v115,v122,v123\n"
"v_pk_mul_f32 v[116:117], v[116:117], v[58:59] op_sel:[0,1] op_sel_hi:[1,1]\n"
"v_pk_mul_f32 v[118:119], v[118:119], v[58:59] op_sel:[0,1] op_sel_hi:[1,1]\n"
"v_pk_mul_f32 v[124:125], v[124:125], v[58:59] op_sel:[0,1] op_sel_hi:[1,1]\n"
"v_pk_mul_f32 v[126:127], v[126:127], v[58:59] op_sel:[0,1] op_sel_hi:[1,1]\n"
"ds_write_b128 v5, v[112:115] offset:44288\n"
C_"v116,v116,v117\n"
C_"v117,v118,v119\n"
C_"v118,v124,v125\n"
C_"v119,v126,v127\n"
"ds_write_b128 v5, v[116:119] offset:45312\n"
M_"v[40:43],v[8:11],v[192:195],0\n"
T_"v[32:33],v3 offset:13568\n"
M_"v[44:47],v[8:11],v[196:199],0\n"
T_"v[34:35],v3 offset:13824\n"
M_"v[48:51],v[12:15],v[192:195],0\n"
T_"v[36:37],v3 offset:13576\n"
M_"v[52:55],v[12:15],v[196:199],0\n"
W_"lgkmcnt(14)\n"
T_"v[38:39],v3 offset:13832\n"
M_"v[40:43],v[16:19],v[200:203],v[40:43]\n"
T_"v[8:9],v1 offset:42240\n"
M_"v[44:47],v[16:19],v[204:207],v[44:47]\n"
T_"v[10:11],v1 offset:42496\n"
M_"v[48:51],v[20:23],v[200:203],v[48:51]\n"
T_"v[12:13],v1 offset:42248\n"
M_"v[52:55],v[20:23],v[204:207],v[52:55]\n"
W_"lgkmcnt(6)\n"
T_"v[14:15],v1 offset:42504\n"
M_"v[40:43],v[24:27],v[208:211],v[40:43]\n"
T_"v[16:17],v1 offset:43264\n"
M_"v[44:47],v[24:27],v[212:215],v[44:47]\n"
T_"v[18:19],v1 offset:43520\n"
M_"v[48:51],v[28:31],v[208:211],v[48:51]\n"
T_"v[20:21],v1 offset:43272\n"
M_"v[52:55],v[28:31],v[212:215],v[52:55]\n"
W_"lgkmcnt(6)\n"
T_"v[22:23],v1 offset:43528\n"
M_"v[40:43],v[32:35],v[216:219],v[40:43]\n"
T_"v[24:25],v1 offset:50688\n"
M_"v[44:47],v[32:35],v[220:223],v[44:47]\n"
T_"v[26:27],v1 offset:50944\n"
M_"v[48:51],v[36:39],v[216:219],v[48:51]\n"
T_"v[28:29],v1 offset:50696\n"
M_"v[52:55],v[36:39],v[220:223],v[52:55]\n"
W_"lgkmcnt(6)\n"
T_"v[30:31],v1 offset:50952\n"
M_"v[40:43],v[8:11],v[224:227],v[40:43]\n"
T_"v[32:33],v1 offset:51712\n"
M_"v[44:47],v[8:11],v[228:231],v[44:47]\n"
T_"v[34:35],v1 offset:51968\n"
M_"v[48:51],v[12:15],v[224:227],v[48:51]\n"
T_"v[36:37],v1 offset:51720\n"
M_"v[52:55],v[12:15],v[228:231],v[52:55]\n"
W_"lgkmcnt(6)\n"
T_"v[38:39],v1 offset:51976\n"
M_"v[40:43],v[16:19],v[232:235],v[40:43]\n"
"s_barrier\n"
"v_add_u32 v4, 0xfffe0000, v4\n"
R_"v[128:131],v4 offset:0\n"
R_"v[132:135],v4 offset:1024\n"
M_"v[44:47],v[16:19],v[236:239],v[44:47]\n"
T_"v[8:9],v1 offset:59136\n"
T_"v[10:11],v1 offset:59392\n"
M_"v[48:51],v[20:23],v[232:235],v[48:51]\n"
T_"v[12:13],v1 offset:59144\n"
M_"v[52:55],v[20:23],v[236:239],v[52:55]\n"
W_"lgkmcnt(8)\n"
T_"v[14:15],v1 offset:59400\n"
M_"v[40:43],v[24:27],v[240:243],v[40:43]\n"
R_"v[136:139],v4 offset:16896\n"
R_"v[140:143],v4 offset:17920\n"
M_"v[44:47],v[24:27],v[244:247],v[44:47]\n"
T_"v[16:17],v1 offset:60160\n"
T_"v[18:19],v1 offset:60416\n"
M_"v[48:51],v[28:31],v[240:243],v[48:51]\n"
T_"v[20:21],v1 offset:60168\n"
M_"v[52:55],v[28:31],v[244:247],v[52:55]\n"
W_"lgkmcnt(9)\n"
T_"v[22:23],v1 offset:60424\n"
M_"v[40:43],v[32:35],v[248:251],v[40:43]\n"
"v_add_u32 v4, 0x10000, v4\n"
R_"v[144:147],v4 offset:35840\n"
R_"v[148:151],v4 offset:36864\n"
M_"v[44:47],v[32:35],v[252:255],v[44:47]\n"
T_"v[24:25],v2 offset:2048\n"
T_"v[26:27],v2 offset:2304\n"
M_"v[48:51],v[36:39],v[248:251],v[48:51]\n"
T_"v[28:29],v2 offset:2056\n"
M_"v[52:55],v[36:39],v[252:255],v[52:55]\n"
W_"lgkmcnt(9)\n"
T_"v[30:31],v2 offset:2312\n"
M_"v[40:43],v[8:11],v[128:131],v[40:43]\n"
R_"v[152:155],v4 offset:44288\n"
R_"v[156:159],v4 offset:45312\n"
M_"v[44:47],v[8:11],v[132:135],v[44:47]\n"
T_"v[32:33],v2 offset:3072\n"
T_"v[34:35],v2 offset:3328\n"
M_"v[48:51],v[12:15],v[128:131],v[48:51]\n"
T_"v[36:37],v2 offset:3080\n"
M_"v[52:55],v[12:15],v[132:135],v[52:55]\n"
W_"lgkmcnt(9)\n"
T_"v[38:39],v2 offset:3336\n"
M_"v[40:43],v[16:19],v[136:139],v[40:43]\n"
"v_add_u32 v4, 0xffff0000, v4\n"
R_"v[160:163],v4 offset:4096\n"
R_"v[164:167],v4 offset:5120\n"
M_"v[44:47],v[16:19],v[140:143],v[44:47]\n"
T_"v[8:9],v1 offset:8448\n"
T_"v[10:11],v1 offset:8704\n"
M_"v[48:51],v[20:23],v[136:139],v[48:51]\n"
T_"v[12:13],v1 offset:8456\n"
M_"v[52:55],v[20:23],v[140:143],v[52:55]\n"
W_"lgkmcnt(9)\n"
T_"v[14:15],v1 offset:8712\n"
M_"v[40:43],v[24:27],v[144:147],v[40:43]\n"
R_"v[168:171],v4 offset:20992\n"
R_"v[172:175],v4 offset:22016\n"
M_"v[44:47],v[24:27],v[148:151],v[44:47]\n"
T_"v[16:17],v1 offset:9472\n"
T_"v[18:19],v1 offset:9728\n"
M_"v[48:51],v[28:31],v[144:147],v[48:51]\n"
T_"v[20:21],v1 offset:9480\n"
M_"v[52:55],v[28:31],v[148:151],v[52:55]\n"
W_"lgkmcnt(9)\n"
T_"v[22:23],v1 offset:9736\n"
M_"v[40:43],v[32:35],v[152:155],v[40:43]\n"
"v_add_u32 v4, 0x10000, v4\n"
R_"v[176:179],v4 offset:39936\n"
R_"v[180:183],v4 offset:40960\n"
M_"v[44:47],v[32:35],v[156:159],v[44:47]\n"
T_"v[24:25],v1 offset:25344\n"
T_"v[26:27],v1 offset:25600\n"
M_"v[48:51],v[36:39],v[152:155],v[48:51]\n"
T_"v[28:29],v1 offset:25352\n"
M_"v[52:55],v[36:39],v[156:159],v[52:55]\n"
W_"lgkmcnt(9)\n"
T_"v[30:31],v1 offset:25608\n"
M_"v[40:43],v[8:11],v[160:163],v[40:43]\n"
R_"v[184:187],v4 offset:48384\n"
R_"v[188:191],v4 offset:49408\n"
M_"v[44:47],v[8:11],v[164:167],v[44:47]\n"
T_"v[32:33],v1 offset:26368\n"
T_"v[34:35],v1 offset:26624\n"
M_"v[48:51],v[12:15],v[160:163],v[48:51]\n"
T_"v[36:37],v1 offset:26376\n"
M_"v[52:55],v[12:15],v[164:167],v[52:55]\n"
W_"lgkmcnt(9)\n"
T_"v[38:39],v1 offset:26632\n"
M_"v[40:43],v[16:19],v[168:171],v[40:43]\n"
"s_barrier\n"
T_"v[8:9],v2 offset:10496\n"
M_"v[44:47],v[16:19],v[172:175],v[44:47]\n"
T_"v[10:11],v2 offset:10752\n"
M_"v[48:51],v[20:23],v[168:171],v[48:51]\n"
T_"v[12:13],v2 offset:10504\n"
M_"v[52:55],v[20:23],v[172:175],v[52:55]\n"
W_"lgkmcnt(7)\n"
T_"v[14:15],v2 offset:10760\n"
M_"v[40:43],v[24:27],v[176:179],v[40:43]\n"
T_"v[16:17],v2 offset:11520\n"
M_"v[44:47],v[24:27],v[180:183],v[44:47]\n"
T_"v[18:19],v2 offset:11776\n"
M_"v[48:51],v[28:31],v[176:179],v[48:51]\n"
T_"v[20:21],v2 offset:11528\n"
M_"v[52:55],v[28:31],v[180:183],v[52:55]\n"
W_"lgkmcnt(6)\n"
T_"v[22:23],v2 offset:11784\n"
M_"v[40:43],v[32:35],v[184:187],v[40:43]\n"
T_"v[24:25],v2 offset:52736\n"
M_"v[44:47],v[32:35],v[188:191],v[44:47]\n"
T_"v[26:27],v2 offset:52992\n"
M_"v[48:51],v[36:39],v[184:187],v[48:51]\n"
T_"v[28:29],v2 offset:52744\n"
M_"v[52:55],v[36:39],v[188:191],v[52:55]\n"
W_"lgkmcnt(6)\n"
T_"v[30:31],v2 offset:53000\n"
M_"v[64:67],v[8:11],v[192:195],0\n"
T_"v[32:33],v2 offset:53760\n"
M_"v[68:71],v[8:11],v[196:199],0\n"
T_"v[34:35],v2 offset:54016\n"
M_"v[72:75],v[12:15],v[192:195],0\n"
T_"v[36:37],v2 offset:53768\n"
M_"v[76:79],v[12:15],v[196:199],0\n"
W_"lgkmcnt(6)\n"
T_"v[38:39],v2 offset:54024\n"
M_"v[64:67],v[16:19],v[200:203],v[64:67]\n"
T_"v[8:9],v2 offset:18944\n"
C_"v80,v40,v41\n"
M_"v[68:71],v[16:19],v[204:207],v[68:71]\n"
T_"v[10:11],v2 offset:19200\n"
C_"v81,v42,v43\n"
M_"v[72:75],v[20:23],v[200:203],v[72:75]\n"
T_"v[12:13],v2 offset:18952\n"
C_"v82,v48,v49\n"
M_"v[76:79],v[20:23],v[204:207],v[76:79]\n"
W_"lgkmcnt(6)\n"
T_"v[14:15],v2 offset:19208\n"
C_"v83,v50,v51\n"
M_"v[64:67],v[24:27],v[208:211],v[64:67]\n"
T_"v[16:17],v2 offset:19968\n"
T_"v[18:19],v2 offset:20224\n"
M_"v[68:71],v[24:27],v[212:215],v[68:71]\n"
"s_add_u32 s87, s90, 57344\n"
"buffer_store_dwordx4 v[80:83], v7, s[60:63], s87 offen sc1\n"
M_"v[72:75],v[28:31],v[208:211],v[72:75]\n"
T_"v[20:21],v2 offset:19976\n"
M_"v[76:79],v[28:31],v[212:215],v[76:79]\n"
W_"lgkmcnt(6)\n"
T_"v[22:23],v2 offset:20232\n"
M_"v[64:67],v[32:35],v[216:219],v[64:67]\n"
T_"v[24:25],v2 offset:27392\n"
M_"v[68:71],v[32:35],v[220:223],v[68:71]\n"
T_"v[26:27],v2 offset:27648\n"
M_"v[72:75],v[36:39],v[216:219],v[72:75]\n"
T_"v[28:29],v2 offset:27400\n"
M_"v[76:79],v[36:39],v[220:223],v[76:79]\n"
W_"lgkmcnt(5)\n"
T_"v[30:31],v2 offset:27656\n"
M_"v[64:67],v[8:11],v[224:227],v[64:67]\n"
T_"v[32:33],v2 offset:28416\n"
M_"v[68:71],v[8:11],v[228:231],v[68:71]\n"
T_"v[34:35],v2 offset:28672\n"
M_"v[72:75],v[12:15],v[224:227],v[72:75]\n"
T_"v[36:37],v2 offset:28424\n"
M_"v[76:79],v[12:15],v[228:231],v[76:79]\n"
T_"v[38:39],v2 offset:28680\n"
M_"v[64:67],v[16:19],v[232:235],v[64:67]\n"
"s_barrier\n"
T_"v[8:9],v2 offset:61184\n"
M_"v[68:71],v[16:19],v[236:239],v[68:71]\n"
W_"lgkmcnt(6)\n"
T_"v[10:11],v2 offset:61440\n"
M_"v[72:75],v[20:23],v[232:235],v[72:75]\n"
T_"v[12:13],v2 offset:61192\n"
M_"v[76:79],v[20:23],v[236:239],v[76:79]\n"
T_"v[14:15],v2 offset:61448\n"
M_"v[64:67],v[24:27],v[240:243],v[64:67]\n"
T_"v[16:17],v2 offset:62208\n"
M_"v[68:71],v[24:27],v[244:247],v[68:71]\n"
W_"lgkmcnt(6)\n"
T_"v[18:19],v2 offset:62464\n"
M_"v[72:75],v[28:31],v[240:243],v[72:75]\n"
T_"v[20:21],v2 offset:62216\n"
M_"v[76:79],v[28:31],v[244:247],v[76:79]\n"
T_"v[22:23],v2 offset:62472\n"
M_"v[64:67],v[32:35],v[248:251],v[64:67]\n"
T_"v[24:25],v3 offset:20992\n"
M_"v[68:71],v[32:35],v[252:255],v[68:71]\n"
W_"lgkmcnt(6)\n"
T_"v[26:27],v3 offset:21248\n"
M_"v[72:75],v[36:39],v[248:251],v[72:75]\n"
T_"v[28:29],v3 offset:21000\n"
M_"v[76:79],v[36:39],v[252:255],v[76:79]\n"
T_"v[30:31],v3 offset:21256\n"
M_"v[64:67],v[8:11],v[128:131],v[64:67]\n"
T_"v[32:33],v3 offset:22016\n"
M_"v[68:71],v[8:11],v[132:135],v[68:71]\n"
W_"lgkmcnt(6)\n"
T_"v[34:35],v3 offset:22272\n"
M_"v[72:75],v[12:15],v[128:131],v[72:75]\n"
T_"v[36:37],v3 offset:22024\n"
M_"v[76:79],v[12:15],v[132:135],v[76:79]\n"
T_"v[38:39],v3 offset:22280\n"
M_"v[64:67],v[16:19],v[136:139],v[64:67]\n"
T_"v[8:9],v1 offset:33792\n"
C_"v84,v44,v45\n"
M_"v[68:71],v[16:19],v[140:143],v[68:71]\n"
W_"lgkmcnt(6)\n"
T_"v[10:11],v1 offset:34048\n"
C_"v85,v46,v47\n"
M_"v[72:75],v[20:23],v[136:139],v[72:75]\n"
T_"v[12:13],v1 offset:33800\n"
C_"v86,v52,v53\n"
M_"v[76:79],v[20:23],v[140:143],v[76:79]\n"
T_"v[14:15],v1 offset:34056\n"
C_"v87,v54,v55\n"
M_"v[64:67],v[24:27],v[144:147],v[64:67]\n"
T_"v[16:17],v1 offset:34816\n"
T_"v[18:19],v1 offset:35072\n"
M_"v[68:71],v[24:27],v[148:151],v[68:71]\n"
W_"lgkmcnt(7)\n"
"s_add_u32 s87, s91, 286720\n"
"buffer_store_dwordx4 v[84:87], v7, s[60:63], s87 offen sc1\n"
M_"v[72:75],v[28:31],v[144:147],v[72:75]\n"
T_"v[20:21],v1 offset:34824\n"
M_"v[76:79],v[28:31],v[148:151],v[76:79]\n"
T_"v[22:23],v1 offset:35080\n"
M_"v[64:67],v[32:35],v[152:155],v[64:67]\n"
T_"v[24:25],v1 offset:42240\n"
M_"v[68:71],v[32:35],v[156:159],v[68:71]\n"
W_"lgkmcnt(6)\n"
T_"v[26:27],v1 offset:42496\n"
M_"v[72:75],v[36:39],v[152:155],v[72:75]\n"
T_"v[28:29],v1 offset:42248\n"
M_"v[76:79],v[36:39],v[156:159],v[76:79]\n"
T_"v[30:31],v1 offset:42504\n"
M_"v[64:67],v[8:11],v[160:163],v[64:67]\n"
T_"v[32:33],v1 offset:43264\n"
M_"v[68:71],v[8:11],v[164:167],v[68:71]\n"
W_"lgkmcnt(6)\n"
T_"v[34:35],v1 offset:43520\n"
M_"v[72:75],v[12:15],v[160:163],v[72:75]\n"
T_"v[36:37],v1 offset:43272\n"
M_"v[76:79],v[12:15],v[164:167],v[76:79]\n"
T_"v[38:39],v1 offset:43528\n"
M_"v[64:67],v[16:19],v[168:171],v[64:67]\n"
"s_barrier\n"
T_"v[8:9],v3 offset:4096\n"
M_"v[68:71],v[16:19],v[172:175],v[68:71]\n"
W_"lgkmcnt(6)\n"
T_"v[10:11],v3 offset:4352\n"
M_"v[72:75],v[20:23],v[168:171],v[72:75]\n"
T_"v[12:13],v3 offset:4104\n"
M_"v[76:79],v[20:23],v[172:175],v[76:79]\n"
T_"v[14:15],v3 offset:4360\n"
M_"v[64:67],v[24:27],v[176:179],v[64:67]\n"
T_"v[16:17],v3 offset:5120\n"
M_"v[68:71],v[24:27],v[180:183],v[68:71]\n"
W_"lgkmcnt(6)\n"
T_"v[18:19],v3 offset:5376\n"
M_"v[72:75],v[28:31],v[176:179],v[72:75]\n"
T_"v[20:21],v3 offset:5128\n"
M_"v[76:79],v[28:31],v[180:183],v[76:79]\n"
T_"v[22:23],v3 offset:5384\n"
M_"v[64:67],v[32:35],v[184:187],v[64:67]\n"
T_"v[24:25],v3 offset:12544\n"
M_"v[68:71],v[32:35],v[188:191],v[68:71]\n"
W_"lgkmcnt(6)\n"
T_"v[26:27],v3 offset:12800\n"
M_"v[72:75],v[36:39],v[184:187],v[72:75]\n"
T_"v[28:29],v3 offset:12552\n"
M_"v[76:79],v[36:39],v[188:191],v[76:79]\n"
T_"v[30:31],v3 offset:12808\n"
M_"v[40:43],v[8:11],v[192:195],0\n"
T_"v[32:33],v3 offset:13568\n"
M_"v[44:47],v[8:11],v[196:199],0\n"
W_"lgkmcnt(6)\n"
T_"v[34:35],v3 offset:13824\n"
M_"v[48:51],v[12:15],v[192:195],0\n"
T_"v[36:37],v3 offset:13576\n"
M_"v[52:55],v[12:15],v[196:199],0\n"
T_"v[38:39],v3 offset:13832\n"
M_"v[40:43],v[16:19],v[200:203],v[40:43]\n"
T_"v[8:9],v1 offset:0\n"
C_"v88,v64,v65\n"
M_"v[44:47],v[16:19],v[204:207],v[44:47]\n"
W_"lgkmcnt(6)\n"
T_"v[10:11],v1 offset:256\n"
C_"v89,v66,v67\n"
M_"v[48:51],v[20:23],v[200:203],v[48:51]\n"
T_"v[12:13],v1 offset:8\n"
C_"v90,v72,v73\n"
M_"v[52:55],v[20:23],v[204:207],v[52:55]\n"
T_"v[14:15],v1 offset:264\n"
C_"v91,v74,v75\n"
M_"v[40:43],v[24:27],v[208:211],v[40:43]\n"
T_"v[16:17],v1 offset:1024\n"
T_"v[18:19],v1 offset:1280\n"
M_"v[44:47],v[24:27],v[212:215],v[44:47]\n"
W_"lgkmcnt(7)\n"
"s_add_u32 s87, s90, 65536\n"
"buffer_store_dwordx4 v[88:91], v7, s[60:63], s87 offen sc1\n"
M_"v[48:51],v[28:31],v[208:211],v[48:51]\n"
T_"v[20:21],v1 offset:1032\n"
M_"v[52:55],v[28:31],v[212:215],v[52:55]\n"
T_"v[22:23],v1 offset:1288\n"
M_"v[40:43],v[32:35],v[216:219],v[40:43]\n"
T_"v[24:25],v1 offset:8448\n"
M_"v[44:47],v[32:35],v[220:223],v[44:47]\n"
W_"lgkmcnt(6)\n"
T_"v[26:27],v1 offset:8704\n"
M_"v[48:51],v[36:39],v[216:219],v[48:51]\n"
T_"v[28:29],v1 offset:8456\n"
M_"v[52:55],v[36:39],v[220:223],v[52:55]\n"
T_"v[30:31],v1 offset:8712\n"
M_"v[40:43],v[8:11],v[224:227],v[40:43]\n"
T_"v[32:33],v1 offset:9472\n"
M_"v[44:47],v[8:11],v[228:231],v[44:47]\n"
W_"lgkmcnt(6)\n"
T_"v[34:35],v1 offset:9728\n"
M_"v[48:51],v[12:15],v[224:227],v[48:51]\n"
T_"v[36:37],v1 offset:9480\n"
M_"v[52:55],v[12:15],v[228:231],v[52:55]\n"
T_"v[38:39],v1 offset:9736\n"
M_"v[40:43],v[16:19],v[232:235],v[40:43]\n"
"s_barrier\n"
T_"v[8:9],v1 offset:16896\n"
M_"v[44:47],v[16:19],v[236:239],v[44:47]\n"
W_"lgkmcnt(6)\n"
T_"v[10:11],v1 offset:17152\n"
M_"v[48:51],v[20:23],v[232:235],v[48:51]\n"
T_"v[12:13],v1 offset:16904\n"
M_"v[52:55],v[20:23],v[236:239],v[52:55]\n"
T_"v[14:15],v1 offset:17160\n"
M_"v[40:43],v[24:27],v[240:243],v[40:43]\n"
T_"v[16:17],v1 offset:17920\n"
M_"v[44:47],v[24:27],v[244:247],v[44:47]\n"
W_"lgkmcnt(6)\n"
T_"v[18:19],v1 offset:18176\n"
M_"v[48:51],v[28:31],v[240:243],v[48:51]\n"
T_"v[20:21],v1 offset:17928\n"
M_"v[52:55],v[28:31],v[244:247],v[52:55]\n"
T_"v[22:23],v1 offset:18184\n"
M_"v[40:43],v[32:35],v[248:251],v[40:43]\n"
T_"v[24:25],v1 offset:50688\n"
M_"v[44:47],v[32:35],v[252:255],v[44:47]\n"
W_"lgkmcnt(6)\n"
T_"v[26:27],v1 offset:50944\n"
M_"v[48:51],v[36:39],v[248:251],v[48:51]\n"
T_"v[28:29],v1 offset:50696\n"
M_"v[52:55],v[36:39],v[252:255],v[52:55]\n"
T_"v[30:31],v1 offset:50952\n"
M_"v[40:43],v[8:11],v[128:131],v[40:43]\n"
T_"v[32:33],v1 offset:51712\n"
M_"v[44:47],v[8:11],v[132:135],v[44:47]\n"
W_"lgkmcnt(6)\n"
T_"v[34:35],v1 offset:51968\n"
M_"v[48:51],v[12:15],v[128:131],v[48:51]\n"
"PTH16:\n"
"s_cbranch_execz PTR\n"
T_"v[36:37],v1 offset:51720\n"
M_"v[52:55],v[12:15],v[132:135],v[52:55]\n"
T_"v[38:39],v1 offset:51976\n"
M_"v[40:43],v[16:19],v[136:139],v[40:43]\n"
T_"v[8:9],v1 offset:25344\n"
C_"v92,v68,v69\n"
M_"v[44:47],v[16:19],v[140:143],v[44:47]\n"
W_"lgkmcnt(6)\n"
T_"v[10:11],v1 offset:25600\n"
C_"v93,v70,v71\n"
M_"v[48:51],v[20:23],v[136:139],v[48:51]\n"
T_"v[12:13],v1 offset:25352\n"
C_"v94,v76,v77\n"
M_"v[52:55],v[20:23],v[140:143],v[52:55]\n"
T_"v[14:15],v1 offset:25608\n"
C_"v95,v78,v79\n"
M_"v[40:43],v[24:27],v[144:147],v[40:43]\n"
T_"v[16:17],v1 offset:26368\n"
T_"v[18:19],v1 offset:26624\n"
M_"v[44:47],v[24:27],v[148:151],v[44:47]\n"
W_"lgkmcnt(7)\n"
"s_add_u32 s87, s91, 294912\n"
"buffer_store_dwordx4 v[92:95], v7, s[60:63], s87 offen sc1\n"
M_"v[48:51],v[28:31],v[144:147],v[48:51]\n"
T_"v[20:21],v1 offset:26376\n"
M_"v[52:55],v[28:31],v[148:151],v[52:55]\n"
T_"v[22:23],v1 offset:26632\n"
M_"v[40:43],v[32:35],v[152:155],v[40:43]\n"
T_"v[24:25],v1 offset:59136\n"
M_"v[44:47],v[32:35],v[156:159],v[44:47]\n"
W_"lgkmcnt(6)\n"
T_"v[26:27],v1 offset:59392\n"
M_"v[48:51],v[36:39],v[152:155],v[48:51]\n"
T_"v[28:29],v1 offset:59144\n"
M_"v[52:55],v[36:39],v[156:159],v[52:55]\n"
T_"v[30:31],v1 offset:59400\n"
M_"v[40:43],v[8:11],v[160:163],v[40:43]\n"
T_"v[32:33],v1 offset:60160\n"
M_"v[44:47],v[8:11],v[164:167],v[44:47]\n"
W_"lgkmcnt(6)\n"
T_"v[34:35],v1 offset:60416\n"
M_"v[48:51],v[12:15],v[160:163],v[48:51]\n"
T_"v[36:37],v1 offset:60168\n"
M_"v[52:55],v[12:15],v[164:167],v[52:55]\n"
T_"v[38:39],v1 offset:60424\n"
M_"v[40:43],v[16:19],v[168:171],v[40:43]\n"
"s_barrier\n"
T_"v[8:9],v2 offset:2048\n"
M_"v[44:47],v[16:19],v[172:175],v[44:47]\n"
W_"lgkmcnt(6)\n"
T_"v[10:11],v2 offset:2304\n"
M_"v[48:51],v[20:23],v[168:171],v[48:51]\n"
T_"v[12:13],v2 offset:2056\n"
M_"v[52:55],v[20:23],v[172:175],v[52:55]\n"
T_"v[14:15],v2 offset:2312\n"
M_"v[40:43],v[24:27],v[176:179],v[40:43]\n"
T_"v[16:17],v2 offset:3072\n"
M_"v[44:47],v[24:27],v[180:183],v[44:47]\n"
W_"lgkmcnt(6)\n"
T_"v[18:19],v2 offset:3328\n"
M_"v[48:51],v[28:31],v[176:179],v[48:51]\n"
T_"v[20:21],v2 offset:3080\n"
M_"v[52:55],v[28:31],v[180:183],v[52:55]\n"
T_"v[22:23],v2 offset:3336\n"
M_"v[40:43],v[32:35],v[184:187],v[40:43]\n"
T_"v[24:25],v2 offset:10496\n"
M_"v[44:47],v[32:35],v[188:191],v[44:47]\n"
W_"lgkmcnt(6)\n"
T_"v[26:27],v2 offset:10752\n"
M_"v[48:51],v[36:39],v[184:187],v[48:51]\n"
T_"v[28:29],v2 offset:10504\n"
M_"v[52:55],v[36:39],v[188:191],v[52:55]\n"
T_"v[30:31],v2 offset:10760\n"
M_"v[64:67],v[8:11],v[192:195],0\n"
T_"v[32:33],v2 offset:11520\n"
M_"v[68:71],v[8:11],v[196:199],0\n"
W_"lgkmcnt(6)\n"
T_"v[34:35],v2 offset:11776\n"
M_"v[72:75],v[12:15],v[192:195],0\n"
T_"v[36:37],v2 offset:11528\n"
M_"v[76:79],v[12:15],v[196:199],0\n"
T_"v[38:39],v2 offset:11784\n"
M_"v[64:67],v[16:19],v[200:203],v[64:67]\n"
T_"v[8:9],v2 offset:18944\n"
C_"v96,v40,v41\n"
M_"v[68:71],v[16:19],v[204:207],v[68:71]\n"
W_"lgkmcnt(6)\n"
T_"v[10:11],v2 offset:19200\n"
C_"v97,v42,v43\n"
M_"v[72:75],v[20:23],v[200:203],v[72:75]\n"
T_"v[12:13],v2 offset:18952\n"
C_"v98,v48,v49\n"
M_"v[76:79],v[20:23],v[204:207],v[76:79]\n"
T_"v[14:15],v2 offset:19208\n"
C_"v99,v50,v51\n"
M_"v[64:67],v[24:27],v[208:211],v[64:67]\n"
T_"v[16:17],v2 offset:19968\n"
T_"v[18:19],v2 offset:20224\n"
M_"v[68:71],v[24:27],v[212:215],v[68:71]\n"
W_"lgkmcnt(7)\n"
"s_add_u32 s87, s90, 73728\n"
"buffer_store_dwordx4 v[96:99], v7, s[60:63], s87 offen sc1\n"
M_"v[72:75],v[28:31],v[208:211],v[72:75]\n"
T_"v[20:21],v2 offset:19976\n"
M_"v[76:79],v[28:31],v[212:215],v[76:79]\n"
T_"v[22:23],v2 offset:20232\n"
M_"v[64:67],v[32:35],v[216:219],v[64:67]\n"
T_"v[24:25],v2 offset:27392\n"
M_"v[68:71],v[32:35],v[220:223],v[68:71]\n"
W_"lgkmcnt(6)\n"
T_"v[26:27],v2 offset:27648\n"
M_"v[72:75],v[36:39],v[216:219],v[72:75]\n"
T_"v[28:29],v2 offset:27400\n"
M_"v[76:79],v[36:39],v[220:223],v[76:79]\n"
T_"v[30:31],v2 offset:27656\n"
M_"v[64:67],v[8:11],v[224:227],v[64:67]\n"
T_"v[32:33],v2 offset:28416\n"
M_"v[68:71],v[8:11],v[228:231],v[68:71]\n"
W_"lgkmcnt(6)\n"
T_"v[34:35],v2 offset:28672\n"
M_"v[72:75],v[12:15],v[224:227],v[72:75]\n"
T_"v[36:37],v2 offset:28424\n"
M_"v[76:79],v[12:15],v[228:231],v[76:79]\n"
T_"v[38:39],v2 offset:28680\n"
M_"v[64:67],v[16:19],v[232:235],v[64:67]\n"
"s_barrier\n"
T_"v[8:9],v2 offset:35840\n"
M_"v[68:71],v[16:19],v[236:239],v[68:71]\n"
W_"lgkmcnt(6)\n"
T_"v[10:11],v2 offset:36096\n"
M_"v[72:75],v[20:23],v[232:235],v[72:75]\n"
T_"v[12:13],v2 offset:35848\n"
M_"v[76:79],v[20:23],v[236:239],v[76:79]\n"
T_"v[14:15],v2 offset:36104\n"
M_"v[64:67],v[24:27],v[240:243],v[64:67]\n"
T_"v[16:17],v2 offset:36864\n"
M_"v[68:71],v[24:27],v[244:247],v[68:71]\n"
W_"lgkmcnt(6)\n"
T_"v[18:19],v2 offset:37120\n"
M_"v[72:75],v[28:31],v[240:243],v[72:75]\n"
T_"v[20:21],v2 offset:36872\n"
M_"v[76:79],v[28:31],v[244:247],v[76:79]\n"
T_"v[22:23],v2 offset:37128\n"
M_"v[64:67],v[32:35],v[248:251],v[64:67]\n"
T_"v[24:25],v1 offset:33792\n"
M_"v[68:71],v[32:35],v[252:255],v[68:71]\n"
W_"lgkmcnt(6)\n"
T_"v[26:27],v1 offset:34048\n"
M_"v[72:75],v[36:39],v[248:251],v[72:75]\n"
T_"v[28:29],v1 offset:33800\n"
M_"v[76:79],v[36:39],v[252:255],v[76:79]\n"
T_"v[30:31],v1 offset:34056\n"
M_"v[64:67],v[8:11],v[128:131],v[64:67]\n"
T_"v[32:33],v1 offset:34816\n"
M_"v[68:71],v[8:11],v[132:135],v[68:71]\n"
W_"lgkmcnt(6)\n"
T_"v[34:35],v1 offset:35072\n"
M_"v[72:75],v[12:15],v[128:131],v[72:75]\n"
T_"v[36:37],v1 offset:34824\n"
M_"v[76:79],v[12:15],v[132:135],v[76:79]\n"
T_"v[38:39],v1 offset:35080\n"
M_"v[64:67],v[16:19],v[136:139],v[64:67]\n"
T_"v[8:9],v1 offset:42240\n"
C_"v100,v44,v45\n"
M_"v[68:71],v[16:19],v[140:143],v[68:71]\n"
W_"lgkmcnt(6)\n"
T_"v[10:11],v1 offset:42496\n"
C_"v101,v46,v47\n"
M_"v[72:75],v[20:23],v[136:139],v[72:75]\n"
T_"v[12:13],v1 offset:42248\n"
C_"v102,v52,v53\n"
M_"v[76:79],v[20:23],v[140:143],v[76:79]\n"
T_"v[14:15],v1 offset:42504\n"
C_"v103,v54,v55\n"
M_"v[64:67],v[24:27],v[144:147],v[64:67]\n"
T_"v[16:17],v1 offset:43264\n"
T_"v[18:19],v1 offset:43520\n"
M_"v[68:71],v[24:27],v[148:151],v[68:71]\n"
W_"lgkmcnt(7)\n"
"s_add_u32 s87, s91, 303104\n"
"buffer_store_dwordx4 v[100:103], v7, s[60:63], s87 offen sc1\n"
M_"v[72:75],v[28:31],v[144:147],v[72:75]\n"
T_"v[20:21],v1 offset:43272\n"
M_"v[76:79],v[28:31],v[148:151],v[76:79]\n"
T_"v[22:23],v1 offset:43528\n"
M_"v[64:67],v[32:35],v[152:155],v[64:67]\n"
T_"v[24:25],v2 offset:44288\n"
M_"v[68:71],v[32:35],v[156:159],v[68:71]\n"
W_"lgkmcnt(6)\n"
T_"v[26:27],v2 offset:44544\n"
M_"v[72:75],v[36:39],v[152:155],v[72:75]\n"
T_"v[28:29],v2 offset:44296\n"
M_"v[76:79],v[36:39],v[156:159],v[76:79]\n"
T_"v[30:31],v2 offset:44552\n"
M_"v[64:67],v[8:11],v[160:163],v[64:67]\n"
T_"v[32:33],v2 offset:45312\n"
M_"v[68:71],v[8:11],v[164:167],v[68:71]\n"
W_"lgkmcnt(6)\n"
T_"v[34:35],v2 offset:45568\n"
M_"v[72:75],v[12:15],v[160:163],v[72:75]\n"
T_"v[36:37],v2 offset:45320\n"
M_"v[76:79],v[12:15],v[164:167],v[76:79]\n"
T_"v[38:39],v2 offset:45576\n"
M_"v[64:67],v[16:19],v[168:171],v[64:67]\n"
"s_barrier\n"
T_"v[8:9],v2 offset:52736\n"
"v_add_u32 v6, 1, v6\n"
M_"v[68:71],v[16:19],v[172:175],v[68:71]\n"
W_"lgkmcnt(6)\n"
T_"v[10:11],v2 offset:52992\n"
M_"v[72:75],v[20:23],v[168:171],v[72:75]\n"
T_"v[12:13],v2 offset:52744\n"
M_"v[76:79],v[20:23],v[172:175],v[76:79]\n"
T_"v[14:15],v2 offset:53000\n"
M_"v[64:67],v[24:27],v[176:179],v[64:67]\n"
T_"v[16:17],v2 offset:53760\n"
M_"v[68:71],v[24:27],v[180:183],v[68:71]\n"
W_"lgkmcnt(6)\n"
T_"v[18:19],v2 offset:54016\n"
M_"v[72:75],v[28:31],v[176:179],v[72:75]\n"
T_"v[20:21],v2 offset:53768\n"
M_"v[76:79],v[28:31],v[180:183],v[76:79]\n"
T_"v[22:23],v2 offset:54024\n"
M_"v[64:67],v[32:35],v[184:187],v[64:67]\n"
T_"v[24:25],v1 offset:0\n"
M_"v[68:71],v[32:35],v[188:191],v[68:71]\n"
W_"lgkmcnt(6)\n"
T_"v[26:27],v1 offset:256\n"
M_"v[72:75],v[36:39],v[184:187],v[72:75]\n"
T_"v[28:29],v1 offset:8\n"
M_"v[76:79],v[36:39],v[188:191],v[76:79]\n"
T_"v[30:31],v1 offset:264\n"
M_"v[40:43],v[8:11],v[192:195],0\n"
T_"v[32:33],v1 offset:1024\n"
M_"v[44:47],v[8:11],v[196:199],0\n"
W_"lgkmcnt(6)\n"
T_"v[34:35],v1 offset:1280\n"
M_"v[48:51],v[12:15],v[192:195],0\n"
T_"v[36:37],v1 offset:1032\n"
M_"v[52:55],v[12:15],v[196:199],0\n"
T_"v[38:39],v1 offset:1288\n"
M_"v[40:43],v[16:19],v[200:203],v[40:43]\n"
T_"v[8:9],v1 offset:8448\n"
C_"v104,v64,v65\n"
M_"v[44:47],v[16:19],v[204:207],v[44:47]\n"
W_"lgkmcnt(6)\n"
T_"v[10:11],v1 offset:8704\n"
C_"v105,v66,v67\n"
M_"v[48:51],v[20:23],v[200:203],v[48:51]\n"
T_"v[12:13],v1 offset:8456\n"
C_"v106,v72,v73\n"
M_"v[52:55],v[20:23],v[204:207],v[52:55]\n"
T_"v[14:15],v1 offset:8712\n"
C_"v107,v74,v75\n"
M_"v[40:43],v[24:27],v[208:211],v[40:43]\n"
T_"v[16:17],v1 offset:9472\n"
T_"v[18:19],v1 offset:9728\n"
M_"v[44:47],v[24:27],v[212:215],v[44:47]\n"
W_"lgkmcnt(7)\n"
"s_add_u32 s87, s90, 81920\n"
"buffer_store_dwordx4 v[104:107], v7, s[60:63], s87 offen sc1\n"
M_"v[48:51],v[28:31],v[208:211],v[48:51]\n"
T_"v[20:21],v1 offset:9480\n"
M_"v[52:55],v[28:31],v[212:215],v[52:55]\n"
T_"v[22:23],v1 offset:9736\n"
M_"v[40:43],v[32:35],v[216:219],v[40:43]\n"
T_"v[24:25],v1 offset:16896\n"
M_"v[44:47],v[32:35],v[220:223],v[44:47]\n"
W_"lgkmcnt(6)\n"
T_"v[26:27],v1 offset:17152\n"
M_"v[48:51],v[36:39],v[216:219],v[48:51]\n"
T_"v[28:29],v1 offset:16904\n"
M_"v[52:55],v[36:39],v[220:223],v[52:55]\n"
T_"v[30:31],v1 offset:17160\n"
M_"v[40:43],v[8:11],v[224:227],v[40:43]\n"
T_"v[32:33],v1 offset:17920\n"
M_"v[44:47],v[8:11],v[228:231],v[44:47]\n"
W_"lgkmcnt(6)\n"
T_"v[34:35],v1 offset:18176\n"
M_"v[48:51],v[12:15],v[224:227],v[48:51]\n"
T_"v[36:37],v1 offset:17928\n"
M_"v[52:55],v[12:15],v[228:231],v[52:55]\n"
T_"v[38:39],v1 offset:18184\n"
M_"v[40:43],v[16:19],v[232:235],v[40:43]\n"
W_"vmcnt(1)\n"
"s_barrier\n"
"s_mov_b64 exec, 1\n"
"buffer_store_dword v6, off, s[80:83], s8 sc1\n"
"s_mov_b64 exec, -1\n"
T_"v[8:9],v1 offset:25344\n"
M_"v[44:47],v[16:19],v[236:239],v[44:47]\n"
W_"lgkmcnt(6)\n"
T_"v[10:11],v1 offset:25600\n"
M_"v[48:51],v[20:23],v[232:235],v[48:51]\n"
T_"v[12:13],v1 offset:25352\n"
M_"v[52:55],v[20:23],v[236:239],v[52:55]\n"
T_"v[14:15],v1 offset:25608\n"
M_"v[40:43],v[24:27],v[240:243],v[40:43]\n"
T_"v[16:17],v1 offset:26368\n"
M_"v[44:47],v[24:27],v[244:247],v[44:47]\n"
W_"lgkmcnt(6)\n"
T_"v[18:19],v1 offset:26624\n"
M_"v[48:51],v[28:31],v[240:243],v[48:51]\n"
T_"v[20:21],v1 offset:26376\n"
M_"v[52:55],v[28:31],v[244:247],v[52:55]\n"
T_"v[22:23],v1 offset:26632\n"
M_"v[40:43],v[32:35],v[248:251],v[40:43]\n"
T_"v[24:25],v1 offset:50688\n"
M_"v[44:47],v[32:35],v[252:255],v[44:47]\n"
W_"lgkmcnt(6)\n"
T_"v[26:27],v1 offset:50944\n"
M_"v[48:51],v[36:39],v[248:251],v[48:51]\n"
T_"v[28:29],v1 offset:50696\n"
M_"v[52:55],v[36:39],v[252:255],v[52:55]\n"
T_"v[30:31],v1 offset:50952\n"
M_"v[40:43],v[8:11],v[128:131],v[40:43]\n"
T_"v[32:33],v1 offset:51712\n"
M_"v[44:47],v[8:11],v[132:135],v[44:47]\n"
W_"lgkmcnt(6)\n"
T_"v[34:35],v1 offset:51968\n"
M_"v[48:51],v[12:15],v[128:131],v[48:51]\n"
T_"v[36:37],v1 offset:51720\n"
M_"v[52:55],v[12:15],v[132:135],v[52:55]\n"
T_"v[38:39],v1 offset:51976\n"
M_"v[40:43],v[16:19],v[136:139],v[40:43]\n"
T_"v[8:9],v1 offset:59136\n"
C_"v108,v68,v69\n"
M_"v[44:47],v[16:19],v[140:143],v[44:47]\n"
W_"lgkmcnt(6)\n"
T_"v[10:11],v1 offset:59392\n"
C_"v109,v70,v71\n"
M_"v[48:51],v[20:23],v[136:139],v[48:51]\n"
T_"v[12:13],v1 offset:59144\n"
C_"v110,v76,v77\n"
M_"v[52:55],v[20:23],v[140:143],v[52:55]\n"
T_"v[14:15],v1 offset:59400\n"
C_"v111,v78,v79\n"
M_"v[40:43],v[24:27],v[144:147],v[40:43]\n"
T_"v[16:17],v1 offset:60160\n"
T_"v[18:19],v1 offset:60416\n"
M_"v[44:47],v[24:27],v[148:151],v[44:47]\n"
W_"lgkmcnt(7)\n"
"s_add_u32 s87, s91, 311296\n"
"buffer_store_dwordx4 v[108:111], v7, s[60:63], s87 offen sc1\n"
M_"v[48:51],v[28:31],v[144:147],v[48:51]\n"
T_"v[20:21],v1 offset:60168\n"
M_"v[52:55],v[28:31],v[148:151],v[52:55]\n"
T_"v[22:23],v1 offset:60424\n"
M_"v[40:43],v[32:35],v[152:155],v[40:43]\n"
T_"v[24:25],v2 offset:2048\n"
M_"v[44:47],v[32:35],v[156:159],v[44:47]\n"
W_"lgkmcnt(6)\n"
T_"v[26:27],v2 offset:2304\n"
M_"v[48:51],v[36:39],v[152:155],v[48:51]\n"
T_"v[28:29],v2 offset:2056\n"
M_"v[52:55],v[36:39],v[156:159],v[52:55]\n"
T_"v[30:31],v2 offset:2312\n"
M_"v[40:43],v[8:11],v[160:163],v[40:43]\n"
T_"v[32:33],v2 offset:3072\n"
M_"v[44:47],v[8:11],v[164:167],v[44:47]\n"
W_"lgkmcnt(6)\n"
T_"v[34:35],v2 offset:3328\n"
M_"v[48:51],v[12:15],v[160:163],v[48:51]\n"
T_"v[36:37],v2 offset:3080\n"
M_"v[52:55],v[12:15],v[164:167],v[52:55]\n"
T_"v[38:39],v2 offset:3336\n"
M_"v[40:43],v[16:19],v[168:171],v[40:43]\n"
"s_barrier\n"
T_"v[8:9],v1 offset:33792\n"
M_"v[44:47],v[16:19],v[172:175],v[44:47]\n"
W_"lgkmcnt(6)\n"
T_"v[10:11],v1 offset:34048\n"
M_"v[48:51],v[20:23],v[168:171],v[48:51]\n"
T_"v[12:13],v1 offset:33800\n"
M_"v[52:55],v[20:23],v[172:175],v[52:55]\n"
T_"v[14:15],v1 offset:34056\n"
M_"v[40:43],v[24:27],v[176:179],v[40:43]\n"
T_"v[16:17],v1 offset:34816\n"
M_"v[44:47],v[24:27],v[180:183],v[44:47]\n"
W_"lgkmcnt(6)\n"
T_"v[18:19],v1 offset:35072\n"
M_"v[48:51],v[28:31],v[176:179],v[48:51]\n"
T_"v[20:21],v1 offset:34824\n"
M_"v[52:55],v[28:31],v[180:183],v[52:55]\n"
T_"v[22:23],v1 offset:35080\n"
M_"v[40:43],v[32:35],v[184:187],v[40:43]\n"
T_"v[24:25],v2 offset:10496\n"
M_"v[44:47],v[32:35],v[188:191],v[44:47]\n"
W_"lgkmcnt(6)\n"
T_"v[26:27],v2 offset:10752\n"
M_"v[48:51],v[36:39],v[184:187],v[48:51]\n"
T_"v[28:29],v2 offset:10504\n"
M_"v[52:55],v[36:39],v[188:191],v[52:55]\n"
T_"v[30:31],v2 offset:10760\n"
M_"v[64:67],v[8:11],v[192:195],0\n"
T_"v[32:33],v2 offset:11520\n"
M_"v[68:71],v[8:11],v[196:199],0\n"
W_"lgkmcnt(6)\n"
T_"v[34:35],v2 offset:11776\n"
M_"v[72:75],v[12:15],v[192:195],0\n"
T_"v[36:37],v2 offset:11528\n"
M_"v[76:79],v[12:15],v[196:199],0\n"
T_"v[38:39],v2 offset:11784\n"
M_"v[64:67],v[16:19],v[200:203],v[64:67]\n"
T_"v[8:9],v2 offset:18944\n"
C_"v112,v40,v41\n"
M_"v[68:71],v[16:19],v[204:207],v[68:71]\n"
W_"lgkmcnt(6)\n"
T_"v[10:11],v2 offset:19200\n"
C_"v113,v42,v43\n"
M_"v[72:75],v[20:23],v[200:203],v[72:75]\n"
T_"v[12:13],v2 offset:18952\n"
C_"v114,v48,v49\n"
M_"v[76:79],v[20:23],v[204:207],v[76:79]\n"
T_"v[14:15],v2 offset:19208\n"
C_"v115,v50,v51\n"
M_"v[64:67],v[24:27],v[208:211],v[64:67]\n"
T_"v[16:17],v2 offset:19968\n"
T_"v[18:19],v2 offset:20224\n"
M_"v[68:71],v[24:27],v[212:215],v[68:71]\n"
W_"lgkmcnt(7)\n"
"s_add_u32 s87, s90, 90112\n"
"buffer_store_dwordx4 v[112:115], v7, s[60:63], s87 offen sc1\n"
M_"v[72:75],v[28:31],v[208:211],v[72:75]\n"
T_"v[20:21],v2 offset:19976\n"
M_"v[76:79],v[28:31],v[212:215],v[76:79]\n"
T_"v[22:23],v2 offset:20232\n"
M_"v[64:67],v[32:35],v[216:219],v[64:67]\n"
T_"v[24:25],v2 offset:27392\n"
C_"v116,v44,v45\n"
M_"v[68:71],v[32:35],v[220:223],v[68:71]\n"
W_"lgkmcnt(6)\n"
T_"v[26:27],v2 offset:27648\n"
C_"v117,v46,v47\n"
M_"v[72:75],v[36:39],v[216:219],v[72:75]\n"
T_"v[28:29],v2 offset:27400\n"
C_"v118,v52,v53\n"
M_"v[76:79],v[36:39],v[220:223],v[76:79]\n"
T_"v[30:31],v2 offset:27656\n"
C_"v119,v54,v55\n"
M_"v[64:67],v[8:11],v[224:227],v[64:67]\n"
T_"v[32:33],v2 offset:28416\n"
T_"v[34:35],v2 offset:28672\n"
M_"v[68:71],v[8:11],v[228:231],v[68:71]\n"
W_"lgkmcnt(7)\n"
"s_add_u32 s87, s91, 319488\n"
"buffer_store_dwordx4 v[116:119], v7, s[60:63], s87 offen sc1\n"
M_"v[72:75],v[12:15],v[224:227],v[72:75]\n"
T_"v[36:37],v2 offset:28424\n"
M_"v[76:79],v[12:15],v[228:231],v[76:79]\n"
T_"v[38:39],v2 offset:28680\n"
M_"v[64:67],v[16:19],v[232:235],v[64:67]\n"
"s_barrier\n"
T_"v[8:9],v1 offset:0\n"
M_"v[68:71],v[16:19],v[236:239],v[68:71]\n"
W_"lgkmcnt(6)\n"
T_"v[10:11],v1 offset:256\n"
M_"v[72:75],v[20:23],v[232:235],v[72:75]\n"
T_"v[12:13],v1 offset:8\n"
M_"v[76:79],v[20:23],v[236:239],v[76:79]\n"
T_"v[14:15],v1 offset:264\n"
M_"v[64:67],v[24:27],v[240:243],v[64:67]\n"
T_"v[16:17],v1 offset:1024\n"
M_"v[68:71],v[24:27],v[244:247],v[68:71]\n"
W_"lgkmcnt(6)\n"
T_"v[18:19],v1 offset:1280\n"
M_"v[72:75],v[28:31],v[240:243],v[72:75]\n"
T_"v[20:21],v1 offset:1032\n"
M_"v[76:79],v[28:31],v[244:247],v[76:79]\n"
T_"v[22:23],v1 offset:1288\n"
M_"v[64:67],v[32:35],v[248:251],v[64:67]\n"
T_"v[24:25],v1 offset:42240\n"
M_"v[68:71],v[32:35],v[252:255],v[68:71]\n"
W_"lgkmcnt(6)\n"
T_"v[26:27],v1 offset:42496\n"
M_"v[72:75],v[36:39],v[248:251],v[72:75]\n"
T_"v[28:29],v1 offset:42248\n"
M_"v[76:79],v[36:39],v[252:255],v[76:79]\n"
T_"v[30:31],v1 offset:42504\n"
M_"v[64:67],v[8:11],v[128:131],v[64:67]\n"
T_"v[32:33],v1 offset:43264\n"
M_"v[68:71],v[8:11],v[132:135],v[68:71]\n"
W_"lgkmcnt(6)\n"
T_"v[34:35],v1 offset:43520\n"
M_"v[72:75],v[12:15],v[128:131],v[72:75]\n"
T_"v[36:37],v1 offset:43272\n"
M_"v[76:79],v[12:15],v[132:135],v[76:79]\n"
T_"v[38:39],v1 offset:43528\n"
M_"v[64:67],v[16:19],v[136:139],v[64:67]\n"
T_"v[8:9],v2 offset:35840\n"
M_"v[68:71],v[16:19],v[140:143],v[68:71]\n"
W_"lgkmcnt(6)\n"
T_"v[10:11],v2 offset:36096\n"
M_"v[72:75],v[20:23],v[136:139],v[72:75]\n"
T_"v[12:13],v2 offset:35848\n"
M_"v[76:79],v[20:23],v[140:143],v[76:79]\n"
T_"v[14:15],v2 offset:36104\n"
M_"v[64:67],v[24:27],v[144:147],v[64:67]\n"
T_"v[16:17],v2 offset:36864\n"
M_"v[68:71],v[24:27],v[148:151],v[68:71]\n"
W_"lgkmcnt(6)\n"
T_"v[18:19],v2 offset:37120\n"
M_"v[72:75],v[28:31],v[144:147],v[72:75]\n"
T_"v[20:21],v2 offset:36872\n"
M_"v[76:79],v[28:31],v[148:151],v[76:79]\n"
T_"v[22:23],v2 offset:37128\n"
M_"v[64:67],v[32:35],v[152:155],v[64:67]\n"
T_"v[24:25],v2 offset:44288\n"
M_"v[68:71],v[32:35],v[156:159],v[68:71]\n"
W_"lgkmcnt(6)\n"
T_"v[26:27],v2 offset:44544\n"
M_"v[72:75],v[36:39],v[152:155],v[72:75]\n"
T_"v[28:29],v2 offset:44296\n"
M_"v[76:79],v[36:39],v[156:159],v[76:79]\n"
T_"v[30:31],v2 offset:44552\n"
M_"v[64:67],v[8:11],v[160:163],v[64:67]\n"
T_"v[32:33],v2 offset:45312\n"
M_"v[68:71],v[8:11],v[164:167],v[68:71]\n"
W_"lgkmcnt(6)\n"
T_"v[34:35],v2 offset:45568\n"
M_"v[72:75],v[12:15],v[160:163],v[72:75]\n"
T_"v[36:37],v2 offset:45320\n"
M_"v[76:79],v[12:15],v[164:167],v[76:79]\n"
T_"v[38:39],v2 offset:45576\n"
M_"v[64:67],v[16:19],v[168:171],v[64:67]\n"
"s_barrier\n"
T_"v[8:9],v1 offset:8448\n"
"v_add_u32 v6, 1, v6\n"
M_"v[68:71],v[16:19],v[172:175],v[68:71]\n"
W_"lgkmcnt(6)\n"
T_"v[10:11],v1 offset:8704\n"
M_"v[72:75],v[20:23],v[168:171],v[72:75]\n"
T_"v[12:13],v1 offset:8456\n"
M_"v[76:79],v[20:23],v[172:175],v[76:79]\n"
T_"v[14:15],v1 offset:8712\n"
M_"v[64:67],v[24:27],v[176:179],v[64:67]\n"
T_"v[16:17],v1 offset:9472\n"
M_"v[68:71],v[24:27],v[180:183],v[68:71]\n"
W_"lgkmcnt(6)\n"
T_"v[18:19],v1 offset:9728\n"
M_"v[72:75],v[28:31],v[176:179],v[72:75]\n"
"PTH17:\n"
"s_cbranch_execz PTR\n"
T_"v[20:21],v1 offset:9480\n"
M_"v[76:79],v[28:31],v[180:183],v[76:79]\n"
T_"v[22:23],v1 offset:9736\n"
M_"v[64:67],v[32:35],v[184:187],v[64:67]\n"
T_"v[24:25],v1 offset:16896\n"
M_"v[68:71],v[32:35],v[188:191],v[68:71]\n"
W_"lgkmcnt(6)\n"
T_"v[26:27],v1 offset:17152\n"
M_"v[72:75],v[36:39],v[184:187],v[72:75]\n"
T_"v[28:29],v1 offset:16904\n"
M_"v[76:79],v[36:39],v[188:191],v[76:79]\n"
T_"v[30:31],v1 offset:17160\n"
M_"v[40:43],v[8:11],v[192:195],0\n"
T_"v[32:33],v1 offset:17920\n"
M_"v[44:47],v[8:11],v[196:199],0\n"
W_"lgkmcnt(6)\n"
T_"v[34:35],v1 offset:18176\n"
M_"v[48:51],v[12:15],v[192:195],0\n"
T_"v[36:37],v1 offset:17928\n"
M_"v[52:55],v[12:15],v[196:199],0\n"
T_"v[38:39],v1 offset:18184\n"
M_"v[40:43],v[16:19],v[200:203],v[40:43]\n"
T_"v[8:9],v1 offset:25344\n"
C_"v120,v64,v65\n"
M_"v[44:47],v[16:19],v[204:207],v[44:47]\n"
W_"lgkmcnt(6)\n"
T_"v[10:11],v1 offset:25600\n"
C_"v121,v66,v67\n"
M_"v[48:51],v[20:23],v[200:203],v[48:51]\n"
T_"v[12:13],v1 offset:25352\n"
C_"v122,v72,v73\n"
M_"v[52:55],v[20:23],v[204:207],v[52:55]\n"
T_"v[14:15],v1 offset:25608\n"
C_"v123,v74,v75\n"
M_"v[40:43],v[24:27],v[208:211],v[40:43]\n"
T_"v[16:17],v1 offset:26368\n"
T_"v[18:19],v1 offset:26624\n"
M_"v[44:47],v[24:27],v[212:215],v[44:47]\n"
W_"lgkmcnt(7)\n"
"s_add_u32 s87, s90, 98304\n"
"buffer_store_dwordx4 v[120:123], v7, s[60:63], s87 offen sc1\n"
M_"v[48:51],v[28:31],v[208:211],v[48:51]\n"
T_"v[20:21],v1 offset:26376\n"
M_"v[52:55],v[28:31],v[212:215],v[52:55]\n"
T_"v[22:23],v1 offset:26632\n"
M_"v[40:43],v[32:35],v[216:219],v[40:43]\n"
T_"v[24:25],v1 offset:50688\n"
C_"v124,v68,v69\n"
M_"v[44:47],v[32:35],v[220:223],v[44:47]\n"
W_"lgkmcnt(6)\n"
T_"v[26:27],v1 offset:50944\n"
C_"v125,v70,v71\n"
M_"v[48:51],v[36:39],v[216:219],v[48:51]\n"
T_"v[28:29],v1 offset:50696\n"
C_"v126,v76,v77\n"
M_"v[52:55],v[36:39],v[220:223],v[52:55]\n"
T_"v[30:31],v1 offset:50952\n"
C_"v127,v78,v79\n"
M_"v[40:43],v[8:11],v[224:227],v[40:43]\n"
T_"v[32:33],v1 offset:51712\n"
T_"v[34:35],v1 offset:51968\n"
M_"v[44:47],v[8:11],v[228:231],v[44:47]\n"
W_"lgkmcnt(7)\n"
"s_add_u32 s87, s91, 327680\n"
"buffer_store_dwordx4 v[124:127], v7, s[60:63], s87 offen sc1\n"
M_"v[48:51],v[12:15],v[224:227],v[48:51]\n"
T_"v[36:37],v1 offset:51720\n"
M_"v[52:55],v[12:15],v[228:231],v[52:55]\n"
T_"v[38:39],v1 offset:51976\n"
M_"v[40:43],v[16:19],v[232:235],v[40:43]\n"
W_"vmcnt(2)\n"
"s_barrier\n"
"s_mov_b64 exec, 1\n"
"buffer_store_dword v6, off, s[80:83], s8 sc1\n"
"s_mov_b64 exec, -1\n"
T_"v[8:9],v1 offset:33792\n"
M_"v[44:47],v[16:19],v[236:239],v[44:47]\n"
W_"lgkmcnt(6)\n"
T_"v[10:11],v1 offset:34048\n"
M_"v[48:51],v[20:23],v[232:235],v[48:51]\n"
T_"v[12:13],v1 offset:33800\n"
M_"v[52:55],v[20:23],v[236:239],v[52:55]\n"
T_"v[14:15],v1 offset:34056\n"
M_"v[40:43],v[24:27],v[240:243],v[40:43]\n"
T_"v[16:17],v1 offset:34816\n"
M_"v[44:47],v[24:27],v[244:247],v[44:47]\n"
W_"lgkmcnt(6)\n"
T_"v[18:19],v1 offset:35072\n"
M_"v[48:51],v[28:31],v[240:243],v[48:51]\n"
T_"v[20:21],v1 offset:34824\n"
M_"v[52:55],v[28:31],v[244:247],v[52:55]\n"
T_"v[22:23],v1 offset:35080\n"
M_"v[40:43],v[32:35],v[248:251],v[40:43]\n"
T_"v[24:25],v1 offset:59136\n"
M_"v[44:47],v[32:35],v[252:255],v[44:47]\n"
W_"lgkmcnt(6)\n"
T_"v[26:27],v1 offset:59392\n"
M_"v[48:51],v[36:39],v[248:251],v[48:51]\n"
T_"v[28:29],v1 offset:59144\n"
M_"v[52:55],v[36:39],v[252:255],v[52:55]\n"
T_"v[30:31],v1 offset:59400\n"
M_"v[40:43],v[8:11],v[128:131],v[40:43]\n"
T_"v[32:33],v1 offset:60160\n"
M_"v[44:47],v[8:11],v[132:135],v[44:47]\n"
W_"lgkmcnt(6)\n"
T_"v[34:35],v1 offset:60416\n"
M_"v[48:51],v[12:15],v[128:131],v[48:51]\n"
T_"v[36:37],v1 offset:60168\n"
M_"v[52:55],v[12:15],v[132:135],v[52:55]\n"
T_"v[38:39],v1 offset:60424\n"
M_"v[40:43],v[16:19],v[136:139],v[40:43]\n"
T_"v[8:9],v2 offset:2048\n"
M_"v[44:47],v[16:19],v[140:143],v[44:47]\n"
W_"lgkmcnt(6)\n"
T_"v[10:11],v2 offset:2304\n"
M_"v[48:51],v[20:23],v[136:139],v[48:51]\n"
T_"v[12:13],v2 offset:2056\n"
M_"v[52:55],v[20:23],v[140:143],v[52:55]\n"
T_"v[14:15],v2 offset:2312\n"
M_"v[40:43],v[24:27],v[144:147],v[40:43]\n"
T_"v[16:17],v2 offset:3072\n"
M_"v[44:47],v[24:27],v[148:151],v[44:47]\n"
W_"lgkmcnt(6)\n"
T_"v[18:19],v2 offset:3328\n"
M_"v[48:51],v[28:31],v[144:147],v[48:51]\n"
T_"v[20:21],v2 offset:3080\n"
M_"v[52:55],v[28:31],v[148:151],v[52:55]\n"
T_"v[22:23],v2 offset:3336\n"
M_"v[40:43],v[32:35],v[152:155],v[40:43]\n"
T_"v[24:25],v2 offset:10496\n"
M_"v[44:47],v[32:35],v[156:159],v[44:47]\n"
W_"lgkmcnt(6)\n"
T_"v[26:27],v2 offset:10752\n"
M_"v[48:51],v[36:39],v[152:155],v[48:51]\n"
T_"v[28:29],v2 offset:10504\n"
M_"v[52:55],v[36:39],v[156:159],v[52:55]\n"
T_"v[30:31],v2 offset:10760\n"
M_"v[40:43],v[8:11],v[160:163],v[40:43]\n"
T_"v[32:33],v2 offset:11520\n"
M_"v[44:47],v[8:11],v[164:167],v[44:47]\n"
W_"lgkmcnt(6)\n"
T_"v[34:35],v2 offset:11776\n"
M_"v[48:51],v[12:15],v[160:163],v[48:51]\n"
T_"v[36:37],v2 offset:11528\n"
M_"v[52:55],v[12:15],v[164:167],v[52:55]\n"
T_"v[38:39],v2 offset:11784\n"
M_"v[40:43],v[16:19],v[168:171],v[40:43]\n"
M_"v[44:47],v[16:19],v[172:175],v[44:47]\n"
W_"lgkmcnt(5)\n"
M_"v[48:51],v[20:23],v[168:171],v[48:51]\n"
M_"v[52:55],v[20:23],v[172:175],v[52:55]\n"
M_"v[40:43],v[24:27],v[176:179],v[40:43]\n"
M_"v[44:47],v[24:27],v[180:183],v[44:47]\n"
W_"lgkmcnt(1)\n"
M_"v[48:51],v[28:31],v[176:179],v[48:51]\n"
M_"v[52:55],v[28:31],v[180:183],v[52:55]\n"
M_"v[40:43],v[32:35],v[184:187],v[40:43]\n"
M_"v[44:47],v[32:35],v[188:191],v[44:47]\n"
W_"lgkmcnt(0)\n"
M_"v[48:51],v[36:39],v[184:187],v[48:51]\n"
M_"v[52:55],v[36:39],v[188:191],v[52:55]\n"
"s_nop 3\n"
C_"v56,v40,v41\n"
C_"v57,v42,v43\n"
"s_nop 0\n"
C_"v58,v48,v49\n"
C_"v59,v50,v51\n"
"s_add_u32 s87, s90, 106496\n"
"buffer_store_dwordx4 v[56:59], v7, s[60:63], s87 offen sc1\n"
C_"v60,v44,v45\n"
C_"v61,v46,v47\n"
C_"v62,v52,v53\n"
C_"v63,v54,v55\n"
"s_add_u32 s87, s91, 335872\n"
"buffer_store_dwordx4 v[60:63], v7, s[60:63], s87 offen sc1\n"
"v_mbcnt_lo_u32_b32 v5, -1, 0\n"
"v_mbcnt_hi_u32_b32 v5, -1, v5\n"
"s_mov_b32 s4, 0\n"
"v_lshl_add_u32 v5, v5, 4, s4\n"
"s_bitcmp1_b32 s90, 30\n"
"s_cbranch_scc0 OWS0_H\n"
"ds_write_b128 v5, v[80:83] offset:0\n"
"ds_write_b128 v5, v[88:91] offset:1024\n"
"ds_write_b128 v5, v[96:99] offset:2048\n"
"ds_write_b128 v5, v[104:107] offset:3072\n"
"ds_write_b128 v5, v[112:115] offset:4096\n"
"ds_write_b128 v5, v[120:123] offset:5120\n"
"ds_write_b128 v5, v[56:59] offset:6144\n"
"OWS0_H:\n"
"s_bitcmp1_b32 s91, 30\n"
"s_cbranch_scc0 OWS1_H\n"
"ds_write_b128 v5, v[84:87] offset:0\n"
"ds_write_b128 v5, v[92:95] offset:1024\n"
"ds_write_b128 v5, v[100:103] offset:2048\n"
"ds_write_b128 v5, v[108:111] offset:3072\n"
"ds_write_b128 v5, v[116:119] offset:4096\n"
"ds_write_b128 v5, v[124:127] offset:5120\n"
"ds_write_b128 v5, v[60:63] offset:6144\n"
"OWS1_H:\n"
W_"lgkmcnt(0)\n"
"s_barrier\n"
"s_endpgm\n"
".p2align 6\n"
"VARB:\n"
"PTL0:\n"
"s_cbranch_execz PTL8\n"
"s_cmp_ge_u32 s7, 4\n"
"s_cbranch_scc0 AROLE_L\n"
W_"lgkmcnt(6)\n"
"s_barrier\n"
"s_add_u32 m0, s84, 67584\n"
"s_mov_b32 s86, 524288\n"
L_"v8,s[36:39],s86 offen lds\n"
L_"v9,s[36:39],s86 offen offset:1024 lds\n"
"s_setprio 2\n"
R_"v[128:131],v6 offset:0\n"
"s_add_u32 m0, s84, 76032\n"
"s_mov_b32 s86, 557056\n"
"s_setprio 0\n"
L_"v8,s[36:39],s86 offen lds\n"
L_"v9,s[36:39],s86 offen offset:1024 lds\n"
"s_setprio 2\n"
R_"v[132:135],v6 offset:1024\n"
"s_add_u32 m0, s84, 84480\n"
"s_mov_b32 s86, 524544\n"
"s_setprio 0\n"
L_"v8,s[36:39],s86 offen lds\n"
"s_setprio 2\n"
R_"v[136:139],v6 offset:4096\n"
"s_setprio 0\n"
L_"v9,s[36:39],s86 offen offset:1024 lds\n"
"s_add_u32 m0, s84, 92928\n"
"s_mov_b32 s86, 557312\n"
L_"v8,s[36:39],s86 offen lds\n"
"s_setprio 2\n"
R_"v[140:143],v6 offset:5120\n"
"s_setprio 0\n"
L_"v9,s[36:39],s86 offen offset:1024 lds\n"
W_"lgkmcnt(0)\n"
"s_barrier\n"
"s_add_u32 m0, s84, 0\n"
"s_mov_b32 s86, 786432\n"
L_"v8,s[36:39],s86 offen lds\n"
"s_setprio 2\n"
R_"v[144:147],v6 offset:8448\n"
R_"v[148:151],v6 offset:9472\n"
"s_setprio 0\n"
L_"v9,s[36:39],s86 offen offset:1024 lds\n"
"s_setprio 2\n"
R_"v[152:155],v6 offset:12544\n"
R_"v[156:159],v6 offset:13568\n"
"s_mov_b32 s94, 0xd2d2d3e8\n"
"s_add_u32 m0, s84, 8448\n"
"s_mov_b32 s86, 819200\n"
"s_setprio 0\n"
L_"v8,s[36:39],s86 offen lds\n"
"v_mov_b32 v241, 0xc7135761\n"
"s_mov_b32 s95, 0x38800000\n"
"s_mov_b32 s96, 0x46800000\n"
L_"v9,s[36:39],s86 offen offset:1024 lds\n"
"s_mov_b32 s97, 0xbfb8aa3b\n"
"s_lshl3_add_u32 s4, s31, s30\n"
"s_add_u32 m0, s84, 101376\n"
"s_mov_b32 s86, 786688\n"
L_"v8,s[36:39],s86 offen lds\n"
"s_mul_i32 s5, s4, 114688\n"
"s_add_u32 s5, s5, 65536\n"
"s_add_u32 s64, s28, s5\n"
"s_addc_u32 s65, s29, 0\n"
L_"v9,s[36:39],s86 offen offset:1024 lds\n"
"s_mov_b32 s66, 114688\n"
"s_mov_b32 s67, 0x20000\n"
W_"vmcnt(6) lgkmcnt(0)\n"
"s_barrier\n"
"s_add_u32 m0, s84, 16896\n"
"s_mov_b32 s86, 819456\n"
L_"v8,s[36:39],s86 offen lds\n"
"s_setprio 2\n"
R_"v[160:163],v6 offset:16896\n"
"s_setprio 0\n"
L_"v9,s[36:39],s86 offen offset:1024 lds\n"
"s_setprio 2\n"
R_"v[164:167],v6 offset:17920\n"
R_"v[168:171],v6 offset:20992\n"
"s_add_u32 m0, s84, 25344\n"
"s_mov_b32 s86, 1048576\n"
"s_setprio 0\n"
L_"v8,s[36:39],s86 offen lds\n"
"s_setprio 2\n"
R_"v[172:175],v6 offset:22016\n"
"s_setprio 0\n"
L_"v9,s[36:39],s86 offen offset:1024 lds\n"
"s_and_b32 s4, s4, -2\n"
"s_mul_i32 s5, s4, 7168\n"
"s_add_u32 m0, s84, 33792\n"
"s_mov_b32 s86, 1081344\n"
L_"v8,s[36:39],s86 offen lds\n"
"s_add_u32 s68, s8, s5\n"
"s_addc_u32 s69, s9, 0\n"
"s_mov_b32 s70, 14336\n"
L_"v9,s[36:39],s86 offen offset:1024 lds\n"
"s_mov_b32 s71, 0x20000\n"
"s_add_u32 s72, s26, s5\n"
"s_addc_u32 s73, s27, 0\n"
"s_add_u32 m0, s84, 42240\n"
"s_mov_b32 s86, 1048832\n"
L_"v8,s[36:39],s86 offen lds\n"
"s_mov_b32 s74, 14336\n"
"s_mov_b32 s75, 0x20000\n"
L_"v9,s[36:39],s86 offen offset:1024 lds\n"
"v_lshrrev_b32 v16, 4, v240\n"
W_"vmcnt(6) lgkmcnt(0)\n"
"s_barrier\n"
"s_add_u32 m0, s84, 50688\n"
"s_mov_b32 s86, 1081600\n"
L_"v8,s[36:39],s86 offen lds\n"
"s_setprio 2\n"
R_"v[176:179],v6 offset:25344\n"
"s_setprio 0\n"
L_"v9,s[36:39],s86 offen offset:1024 lds\n"
"s_setprio 2\n"
R_"v[180:183],v6 offset:26368\n"
R_"v[184:187],v6 offset:29440\n"
"s_add_u32 m0, s84, 59136\n"
"s_mov_b32 s86, 1310720\n"
"s_setprio 0\n"
L_"v8,s[36:39],s86 offen lds\n"
"s_setprio 2\n"
R_"v[188:191],v6 offset:30464\n"
"s_setprio 0\n"
L_"v9,s[36:39],s86 offen offset:1024 lds\n"
"v_and_b32 v17, 15, v240\n"
"v_lshlrev_b32 v18, 4, v17\n"
"s_add_u32 m0, s84, 67584\n"
"s_mov_b32 s86, 1343488\n"
L_"v8,s[36:39],s86 offen lds\n"
"v_lshl_add_u32 v18, v16, 10, v18\n"
"s_lshl_b32 s4, s34, 9\n"
L_"v9,s[36:39],s86 offen offset:1024 lds\n"
"s_lshl_b32 s5, s35, 13\n"
"s_add_u32 s4, s4, s5\n"
"s_add_u32 m0, s84, 76032\n"
"s_mov_b32 s86, 1310976\n"
L_"v8,s[36:39],s86 offen lds\n"
"v_add_u32 v255, s4, v18\n"
L_"v9,s[36:39],s86 offen offset:1024 lds\n"
"s_and_b32 s4, s30, 1\n"
"s_lshl1_add_u32 s4, s35, s4\n"
"s_add_u32 m0, s84, 109824\n"
"s_mov_b32 s86, 1343744\n"
L_"v8,s[36:39],s86 offen lds\n"
"s_lshl1_add_u32 s4, s4, s34\n"
"s_lshl_b32 s4, s4, 5\n"
L_"v9,s[36:39],s86 offen offset:1024 lds\n"
"v_lshl_add_u32 v244, v16, 3, s4\n"
W_"vmcnt(8) lgkmcnt(0)\n"
"s_barrier\n"
"s_add_u32 m0, s84, 0\n"
"s_mov_b32 s86, 1572864\n"
L_"v8,s[36:39],s86 offen lds\n"
"s_setprio 2\n"
R_"v[192:195],v6 offset:33792\n"
R_"v[196:199],v6 offset:34816\n"
"s_setprio 0\n"
L_"v9,s[36:39],s86 offen offset:1024 lds\n"
"s_setprio 2\n"
R_"v[200:203],v6 offset:37888\n"
R_"v[204:207],v6 offset:38912\n"
"s_add_u32 m0, s84, 8448\n"
"s_mov_b32 s86, 1605632\n"
"s_setprio 0\n"
L_"v8,s[36:39],s86 offen lds\n"
"v_mul_u32_u24 v20, 896, v17\n"
"v_add_u32 v245, v20, v244\n"
L_"v9,s[36:39],s86 offen offset:1024 lds\n"
"v_and_b32 v19, 1, v17\n"
"v_lshlrev_b32 v19, 4, v19\n"
"s_add_u32 m0, s84, 84480\n"
"s_mov_b32 s86, 1573120\n"
L_"v8,s[36:39],s86 offen lds\n"
"v_mov_b32 v20, 0x3f80\n"
"v_lshlrev_b32 v19, v19, v20\n"
L_"v9,s[36:39],s86 offen offset:1024 lds\n"
"v_lshrrev_b32 v20, 1, v17\n"
"v_cmp_eq_u32 vcc, 0, v20\n"
"s_add_u32 m0, s84, 92928\n"
"s_mov_b32 s86, 1605888\n"
L_"v8,s[36:39],s86 offen lds\n"
"v_cndmask_b32 v112, 0, v19, vcc\n"
"v_cmp_eq_u32 vcc, 1, v20\n"
L_"v9,s[36:39],s86 offen offset:1024 lds\n"
"s_nop 0\n"
"v_cndmask_b32 v113, 0, v19, vcc\n"
W_"vmcnt(8) lgkmcnt(0)\n"
"s_barrier\n"
"s_add_u32 m0, s84, 16896\n"
"s_mov_b32 s86, 1835008\n"
L_"v8,s[36:39],s86 offen lds\n"
"s_setprio 2\n"
R_"v[208:211],v6 offset:42240\n"
R_"v[212:215],v6 offset:43264\n"
"s_setprio 0\n"
L_"v9,s[36:39],s86 offen offset:1024 lds\n"
"s_setprio 2\n"
R_"v[216:219],v6 offset:46336\n"
R_"v[220:223],v6 offset:47360\n"
"s_add_u32 m0, s84, 25344\n"
"s_mov_b32 s86, 1867776\n"
"s_setprio 0\n"
L_"v8,s[36:39],s86 offen lds\n"
"v_cmp_eq_u32 vcc, 2, v20\n"
"s_nop 1\n"
"v_cndmask_b32 v114, 0, v19, vcc\n"
L_"v9,s[36:39],s86 offen offset:1024 lds\n"
"v_cmp_eq_u32 vcc, 3, v20\n"
"s_nop 1\n"
"v_cndmask_b32 v115, 0, v19, vcc\n"
"s_add_u32 m0, s84, 33792\n"
"s_mov_b32 s86, 1835264\n"
L_"v8,s[36:39],s86 offen lds\n"
"v_cmp_eq_u32 vcc, 4, v20\n"
"s_nop 1\n"
"v_cndmask_b32 v116, 0, v19, vcc\n"
L_"v9,s[36:39],s86 offen offset:1024 lds\n"
"v_cmp_eq_u32 vcc, 5, v20\n"
"s_nop 1\n"
"v_cndmask_b32 v117, 0, v19, vcc\n"
"s_add_u32 m0, s84, 101376\n"
"s_mov_b32 s86, 1868032\n"
L_"v8,s[36:39],s86 offen lds\n"
"v_cmp_eq_u32 vcc, 6, v20\n"
"s_nop 1\n"
"v_cndmask_b32 v118, 0, v19, vcc\n"
L_"v9,s[36:39],s86 offen offset:1024 lds\n"
"v_cmp_eq_u32 vcc, 7, v20\n"
"s_nop 1\n"
"v_cndmask_b32 v119, 0, v19, vcc\n"
W_"vmcnt(8) lgkmcnt(0)\n"
"s_barrier\n"
"s_add_u32 m0, s84, 42240\n"
"s_mov_b32 s86, 2097152\n"
L_"v8,s[36:39],s86 offen lds\n"
L_"v9,s[36:39],s86 offen offset:1024 lds\n"
"s_setprio 2\n"
R_"v[224:227],v6 offset:50688\n"
"s_add_u32 m0, s84, 50688\n"
"s_mov_b32 s86, 2129920\n"
"s_setprio 0\n"
L_"v8,s[36:39],s86 offen lds\n"
L_"v9,s[36:39],s86 offen offset:1024 lds\n"
"s_setprio 2\n"
R_"v[228:231],v6 offset:51712\n"
"s_add_u32 m0, s84, 59136\n"
"s_mov_b32 s86, 2097408\n"
"s_setprio 0\n"
L_"v8,s[36:39],s86 offen lds\n"
"s_setprio 2\n"
R_"v[232:235],v6 offset:54784\n"
"s_setprio 0\n"
L_"v9,s[36:39],s86 offen offset:1024 lds\n"
"s_add_u32 m0, s84, 67584\n"
"s_mov_b32 s86, 2130176\n"
L_"v8,s[36:39],s86 offen lds\n"
"s_setprio 2\n"
R_"v[236:239],v6 offset:55808\n"
"s_setprio 0\n"
L_"v9,s[36:39],s86 offen offset:1024 lds\n"
W_"vmcnt(8) lgkmcnt(0)\n"
"s_barrier\n"
"s_add_u32 m0, s84, 0\n"
"s_mov_b32 s86, 2359296\n"
L_"v8,s[36:39],s86 offen lds\n"
L_"v9,s[36:39],s86 offen offset:1024 lds\n"
"s_lshl_b32 s4, s34, 11\n"
"s_add_u32 m0, s84, 8448\n"
"s_mov_b32 s86, 2392064\n"
L_"v8,s[36:39],s86 offen lds\n"
L_"v9,s[36:39],s86 offen offset:1024 lds\n"
"v_lshl_add_u32 v6, v240, 4, s4\n"
"s_add_u32 m0, s84, 76032\n"
"s_mov_b32 s86, 2359552\n"
L_"v8,s[36:39],s86 offen lds\n"
"s_lshl_b32 s4, s35, 12\n"
L_"v9,s[36:39],s86 offen offset:1024 lds\n"
"s_add_u32 m0, s84, 109824\n"
"s_mov_b32 s86, 2392320\n"
L_"v8,s[36:39],s86 offen lds\n"
"v_add_u32 v5, s4, v6\n"
L_"v9,s[36:39],s86 offen offset:1024 lds\n"
W_"vmcnt(8)\n"
"s_barrier\n"
"s_add_u32 m0, s84, 16896\n"
"s_mov_b32 s86, 2621440\n"
L_"v8,s[36:39],s86 offen lds\n"
L_"v9,s[36:39],s86 offen offset:1024 lds\n"
"s_add_u32 m0, s84, 25344\n"
"s_mov_b32 s86, 2654208\n"
L_"v8,s[36:39],s86 offen lds\n"
L_"v9,s[36:39],s86 offen offset:1024 lds\n"
"s_add_u32 m0, s84, 84480\n"
"s_mov_b32 s86, 2621696\n"
L_"v8,s[36:39],s86 offen lds\n"
L_"v9,s[36:39],s86 offen offset:1024 lds\n"
"s_add_u32 m0, s84, 92928\n"
"s_mov_b32 s86, 2654464\n"
L_"v8,s[36:39],s86 offen lds\n"
L_"v9,s[36:39],s86 offen offset:1024 lds\n"
W_"vmcnt(8)\n"
"s_barrier\n"
"s_add_u32 m0, s84, 33792\n"
"s_mov_b32 s86, 2883584\n"
L_"v8,s[36:39],s86 offen lds\n"
L_"v9,s[36:39],s86 offen offset:1024 lds\n"
"s_add_u32 m0, s84, 42240\n"
"s_mov_b32 s86, 2916352\n"
L_"v8,s[36:39],s86 offen lds\n"
L_"v9,s[36:39],s86 offen offset:1024 lds\n"
"s_add_u32 m0, s84, 50688\n"
"s_mov_b32 s86, 2883840\n"
L_"v8,s[36:39],s86 offen lds\n"
L_"v9,s[36:39],s86 offen offset:1024 lds\n"
"s_add_u32 m0, s84, 101376\n"
"s_mov_b32 s86, 2916608\n"
L_"v8,s[36:39],s86 offen lds\n"
L_"v9,s[36:39],s86 offen offset:1024 lds\n"
W_"vmcnt(8)\n"
"s_barrier\n"
"s_add_u32 m0, s84, 0\n"
"s_mov_b32 s86, 3145728\n"
L_"v8,s[36:39],s86 offen lds\n"
L_"v9,s[36:39],s86 offen offset:1024 lds\n"
"s_add_u32 m0, s84, 8448\n"
"s_mov_b32 s86, 3178496\n"
L_"v8,s[36:39],s86 offen lds\n"
L_"v9,s[36:39],s86 offen offset:1024 lds\n"
"s_add_u32 m0, s84, 59136\n"
"s_mov_b32 s86, 3145984\n"
L_"v8,s[36:39],s86 offen lds\n"
L_"v9,s[36:39],s86 offen offset:1024 lds\n"
"s_add_u32 m0, s84, 67584\n"
"s_mov_b32 s86, 3178752\n"
L_"v8,s[36:39],s86 offen lds\n"
L_"v9,s[36:39],s86 offen offset:1024 lds\n"
W_"vmcnt(8)\n"
"s_barrier\n"
"s_add_u32 m0, s84, 16896\n"
"s_mov_b32 s86, 3407872\n"
L_"v8,s[36:39],s86 offen lds\n"
L_"v9,s[36:39],s86 offen offset:1024 lds\n"
"s_add_u32 m0, s84, 25344\n"
"s_mov_b32 s86, 3440640\n"
L_"v8,s[36:39],s86 offen lds\n"
L_"v9,s[36:39],s86 offen offset:1024 lds\n"
"s_add_u32 m0, s84, 76032\n"
"s_mov_b32 s86, 3408128\n"
L_"v8,s[36:39],s86 offen lds\n"
L_"v9,s[36:39],s86 offen offset:1024 lds\n"
"s_add_u32 m0, s84, 109824\n"
"s_mov_b32 s86, 3440896\n"
L_"v8,s[36:39],s86 offen lds\n"
L_"v9,s[36:39],s86 offen offset:1024 lds\n"
W_"vmcnt(8)\n"
"s_barrier\n"
"s_add_u32 m0, s85, 33792\n"
"s_mov_b32 s86, 0\n"
L_"v10,s[40:43],s86 offen lds\n"
L_"v11,s[40:43],s86 offen offset:1056 lds\n"
"s_add_u32 m0, s85, 42240\n"
"s_mov_b32 s86, 2048\n"
L_"v10,s[40:43],s86 offen lds\n"
L_"v11,s[40:43],s86 offen offset:1056 lds\n"
"s_add_u32 m0, s85, 84480\n"
"s_mov_b32 s86, 16384\n"
L_"v10,s[40:43],s86 offen lds\n"
L_"v11,s[40:43],s86 offen offset:1056 lds\n"
"s_add_u32 m0, s85, 92928\n"
"s_mov_b32 s86, 18432\n"
L_"v10,s[40:43],s86 offen lds\n"
L_"v11,s[40:43],s86 offen offset:1056 lds\n"
W_"vmcnt(8)\n"
"s_barrier\n"
"s_add_u32 m0, s85, 0\n"
"s_mov_b32 s86, 32768\n"
L_"v10,s[40:43],s86 offen lds\n"
"s_mul_i32 s87, s32, 28\n"
L_"v11,s[40:43],s86 offen offset:1056 lds\n"
"v_add_u32 v24, s87, v240\n"
"v_lshrrev_b32 v26, 2, v24\n"
"s_add_u32 m0, s85, 8448\n"
"s_mov_b32 s86, 34816\n"
L_"v10,s[40:43],s86 offen lds\n"
"v_and_b32 v24, 3, v24\n"
L_"v11,s[40:43],s86 offen offset:1056 lds\n"
"v_mul_u32_u24 v26, 3584, v26\n"
"v_lshl_add_u32 v24, v24, 7, v26\n"
"s_add_u32 m0, s85, 50688\n"
"PTL1:\n"
"s_cbranch_execz PTL9\n"
"s_mov_b32 s86, 49152\n"
L_"v10,s[40:43],s86 offen lds\n"
"s_mul_i32 s87, s32, 24\n"
L_"v11,s[40:43],s86 offen offset:1056 lds\n"
"v_add_u32 v25, s87, v240\n"
"v_lshlrev_b32 v25, 7, v25\n"
"s_add_u32 m0, s85, 101376\n"
"s_mov_b32 s86, 51200\n"
L_"v10,s[40:43],s86 offen lds\n"
"v_add_u32 v25, 65600, v25\n"
L_"v11,s[40:43],s86 offen offset:1056 lds\n"
"s_bfm_b64 exec, 28, 0\n"
"buffer_load_dword v7, v24, s[52:55], 0 offen\n"
"s_mov_b64 exec, -1\n"
"s_bfm_b64 exec, 24, 0\n"
"buffer_load_dword v7, v25, s[40:43], 0 offen\n"
"s_mov_b64 exec, -1\n"
"buffer_load_dword v243, v242, s[80:83], 0 offen sc1\n"
L_"v[48:51],v15,s[76:79],0 offen\n"
L_"v[64:67],v15,s[76:79],0 offen offset:128\n"
L_"v[80:83],v15,s[76:79],0 offen offset:256\n"
W_"vmcnt(18)\n"
"s_barrier\n"
"s_add_u32 m0, s85, 16896\n"
"s_mov_b32 s86, 65536\n"
L_"v10,s[40:43],s86 offen lds\n"
"s_add_u32 s76, s24, 0\n"
"s_addc_u32 s77, s25, 0\n"
"s_mov_b32 s78, 896\n"
L_"v11,s[40:43],s86 offen offset:1056 lds\n"
"s_mov_b32 s79, 0x20000\n"
W_"vmcnt(16)\n"
"s_barrier\n"
"s_add_u32 m0, s85, 25344\n"
"s_mov_b32 s86, 67584\n"
L_"v10,s[40:43],s86 offen lds\n"
L_"v11,s[40:43],s86 offen offset:1056 lds\n"
W_"vmcnt(14)\n"
"s_barrier\n"
"s_add_u32 m0, s84, 33792\n"
"s_mov_b32 s86, 0\n"
L_"v12,s[52:55],s86 offen lds\n"
W_"vmcnt(8)\n"
"v_mov_b32 v243, v243\n"
W_"vmcnt(7)\n"
"v_lshlrev_b32 v52, 16, v48\n"
L_"v13,s[52:55],s86 offen offset:1024 lds\n"
"v_and_b32 v53, s93, v48\n"
"v_lshlrev_b32 v54, 16, v49\n"
"s_add_u32 m0, s84, 42240\n"
"s_mov_b32 s86, 229376\n"
L_"v12,s[52:55],s86 offen lds\n"
"v_and_b32 v55, s93, v49\n"
"v_lshlrev_b32 v60, 16, v50\n"
L_"v13,s[52:55],s86 offen offset:1024 lds\n"
"v_and_b32 v61, s93, v50\n"
"v_lshlrev_b32 v62, 16, v51\n"
"v_and_b32 v63, s93, v51\n"
"s_add_u32 m0, s84, 59136\n"
"s_mov_b32 s86, 458752\n"
L_"v12,s[52:55],s86 offen lds\n"
"v_mul_f32 v52, s95, v52\n"
"v_mul_f32 v53, s95, v53\n"
L_"v13,s[52:55],s86 offen offset:1024 lds\n"
"v_mul_f32 v54, s95, v54\n"
"v_mul_f32 v55, s95, v55\n"
"s_add_u32 m0, s84, 67584\n"
"s_mov_b32 s86, 688128\n"
L_"v12,s[52:55],s86 offen lds\n"
"v_mul_f32 v60, s95, v60\n"
"v_mul_f32 v61, s95, v61\n"
L_"v13,s[52:55],s86 offen offset:1024 lds\n"
"v_mul_f32 v62, s95, v62\n"
"v_mul_f32 v63, s95, v63\n"
"s_barrier\n"
"s_add_u32 m0, s84, 0\n"
"s_mov_b32 s86, 917504\n"
L_"v12,s[52:55],s86 offen lds\n"
W_"vmcnt(14)\n"
"v_lshlrev_b32 v68, 16, v64\n"
"v_and_b32 v69, s93, v64\n"
L_"v13,s[52:55],s86 offen offset:1024 lds\n"
"v_lshlrev_b32 v70, 16, v65\n"
"v_and_b32 v71, s93, v65\n"
"v_lshlrev_b32 v76, 16, v66\n"
"s_add_u32 m0, s84, 76032\n"
"s_mov_b32 s86, 1146880\n"
L_"v12,s[52:55],s86 offen lds\n"
"v_and_b32 v77, s93, v66\n"
"v_lshlrev_b32 v78, 16, v67\n"
L_"v13,s[52:55],s86 offen offset:1024 lds\n"
"v_and_b32 v79, s93, v67\n"
"v_mul_f32 v68, s95, v68\n"
"v_mul_f32 v69, s95, v69\n"
"s_add_u32 m0, s84, 84480\n"
"s_mov_b32 s86, 1376256\n"
L_"v12,s[52:55],s86 offen lds\n"
"v_mul_f32 v70, s95, v70\n"
"v_mul_f32 v71, s95, v71\n"
"v_mul_f32 v76, s95, v76\n"
L_"v13,s[52:55],s86 offen offset:1024 lds\n"
"v_mul_f32 v77, s95, v77\n"
"v_mul_f32 v78, s95, v78\n"
"s_add_u32 m0, s84, 92928\n"
"s_mov_b32 s86, 128\n"
L_"v12,s[52:55],s86 offen lds\n"
"v_mul_f32 v79, s95, v79\n"
"v_not_b32 v16, v243\n"
"s_lshl2_add_u32 s4, s30, 32768\n"
L_"v13,s[52:55],s86 offen offset:1024 lds\n"
"s_lshl_b32 s5, s33, 20\n"
"s_add_u32 s4, s4, s5\n"
"s_mov_b64 exec, 0xff\n"
"buffer_store_dword v16, v242, s[80:83], s4 offen sc1\n"
"s_mov_b64 exec, -1\n"
W_"vmcnt(17)\n"
"s_barrier\n"
"s_add_u32 m0, s84, 8448\n"
"s_mov_b32 s86, 229504\n"
L_"v12,s[52:55],s86 offen lds\n"
"v_lshlrev_b32 v84, 16, v80\n"
"v_and_b32 v85, s93, v80\n"
L_"v13,s[52:55],s86 offen offset:1024 lds\n"
"v_lshlrev_b32 v86, 16, v81\n"
"v_and_b32 v87, s93, v81\n"
"s_add_u32 m0, s84, 50688\n"
"s_mov_b32 s86, 458880\n"
L_"v12,s[52:55],s86 offen lds\n"
"v_lshlrev_b32 v92, 16, v82\n"
"v_and_b32 v93, s93, v82\n"
L_"v13,s[52:55],s86 offen offset:1024 lds\n"
"v_lshlrev_b32 v94, 16, v83\n"
"v_and_b32 v95, s93, v83\n"
"s_add_u32 m0, s84, 109824\n"
"s_mov_b32 s86, 688256\n"
L_"v12,s[52:55],s86 offen lds\n"
"v_mul_f32 v84, s95, v84\n"
"v_mul_f32 v85, s95, v85\n"
L_"v13,s[52:55],s86 offen offset:1024 lds\n"
"v_mul_f32 v86, s95, v86\n"
"v_mul_f32 v87, s95, v87\n"
"s_add_u32 m0, s84, 118272\n"
"s_mov_b32 s86, 917632\n"
L_"v12,s[52:55],s86 offen lds\n"
"v_mul_f32 v92, s95, v92\n"
"v_mul_f32 v93, s95, v93\n"
L_"v13,s[52:55],s86 offen offset:1024 lds\n"
"v_mul_f32 v94, s95, v94\n"
"v_mul_f32 v95, s95, v95\n"
"s_setprio 2\n"
W_"vmcnt(19)\n"
"s_barrier\n"
T_"v[16:17],v1 offset:33792\n"
T_"v[18:19],v1 offset:34048\n"
T_"v[20:21],v1 offset:33800\n"
T_"v[22:23],v1 offset:34056\n"
T_"v[24:25],v1 offset:34816\n"
T_"v[26:27],v1 offset:35072\n"
T_"v[28:29],v1 offset:34824\n"
T_"v[30:31],v1 offset:35080\n"
T_"v[32:33],v1 offset:42240\n"
T_"v[34:35],v1 offset:42496\n"
W_"lgkmcnt(8)\n"
M_"v[48:51],v[16:19],v[128:131],v[52:55]\n"
"s_add_u32 m0, s84, 16896\n"
"s_mov_b32 s86, 1147008\n"
T_"v[36:37],v1 offset:42248\n"
L_"v12,s[52:55],s86 offen lds\n"
M_"v[52:55],v[16:19],v[132:135],v[52:55]\n"
W_"lgkmcnt(7)\n"
T_"v[38:39],v1 offset:42504\n"
L_"v13,s[52:55],s86 offen offset:1024 lds\n"
M_"v[56:59],v[20:23],v[128:131],v[60:63]\n"
"s_add_u32 m0, s84, 101376\n"
"s_mov_b32 s86, 1376384\n"
T_"v[40:41],v1 offset:43264\n"
M_"v[60:63],v[20:23],v[132:135],v[60:63]\n"
W_"lgkmcnt(7)\n"
T_"v[42:43],v1 offset:43520\n"
L_"v12,s[52:55],s86 offen lds\n"
M_"v[48:51],v[24:27],v[136:139],v[48:51]\n"
T_"v[44:45],v1 offset:43272\n"
L_"v13,s[52:55],s86 offen offset:1024 lds\n"
M_"v[52:55],v[24:27],v[140:143],v[52:55]\n"
W_"lgkmcnt(4)\n"
T_"v[46:47],v1 offset:43528\n"
M_"v[56:59],v[28:31],v[136:139],v[56:59]\n"
"s_add_u32 m0, s84, 126720\n"
"s_mov_b32 s86, 256\n"
T_"v[16:17],v1 offset:59136\n"
L_"v12,s[52:55],s86 offen lds\n"
M_"v[60:63],v[28:31],v[140:143],v[60:63]\n"
T_"v[18:19],v1 offset:59392\n"
L_"v13,s[52:55],s86 offen offset:1024 lds\n"
M_"v[48:51],v[32:35],v[144:147],v[48:51]\n"
"s_add_u32 m0, s84, 135168\n"
"s_mov_b32 s86, 229632\n"
T_"v[20:21],v1 offset:59144\n"
L_"v12,s[52:55],s86 offen lds\n"
M_"v[52:55],v[32:35],v[148:151],v[52:55]\n"
W_"lgkmcnt(4)\n"
T_"v[22:23],v1 offset:59400\n"
M_"v[56:59],v[36:39],v[144:147],v[56:59]\n"
T_"v[24:25],v1 offset:60160\n"
L_"v13,s[52:55],s86 offen offset:1024 lds\n"
M_"v[60:63],v[36:39],v[148:151],v[60:63]\n"
"s_add_u32 m0, s84, 143616\n"
"s_mov_b32 s86, 459008\n"
T_"v[26:27],v1 offset:60416\n"
L_"v12,s[52:55],s86 offen lds\n"
M_"v[48:51],v[40:43],v[152:155],v[48:51]\n"
T_"v[28:29],v1 offset:60168\n"
M_"v[52:55],v[40:43],v[156:159],v[52:55]\n"
W_"lgkmcnt(4)\n"
T_"v[30:31],v1 offset:60424\n"
L_"v13,s[52:55],s86 offen offset:1024 lds\n"
M_"v[56:59],v[44:47],v[152:155],v[56:59]\n"
W_"vmcnt(23)\n"
"s_barrier\n"
T_"v[32:33],v2 offset:2048\n"
M_"v[60:63],v[44:47],v[156:159],v[60:63]\n"
"s_add_u32 m0, s84, 25344\n"
T_"v[34:35],v2 offset:2304\n"
M_"v[48:51],v[16:19],v[160:163],v[48:51]\n"
"s_mov_b32 s86, 688384\n"
T_"v[36:37],v2 offset:2056\n"
L_"v12,s[52:55],s86 offen lds\n"
M_"v[52:55],v[16:19],v[164:167],v[52:55]\n"
W_"lgkmcnt(4)\n"
T_"v[38:39],v2 offset:2312\n"
M_"v[56:59],v[20:23],v[160:163],v[56:59]\n"
T_"v[40:41],v2 offset:3072\n"
L_"v13,s[52:55],s86 offen offset:1024 lds\n"
M_"v[60:63],v[20:23],v[164:167],v[60:63]\n"
T_"v[42:43],v2 offset:3328\n"
M_"v[48:51],v[24:27],v[168:171],v[48:51]\n"
"s_add_u32 m0, s84, 33792\n"
"s_mov_b32 s86, 917760\n"
T_"v[44:45],v2 offset:3080\n"
M_"v[52:55],v[24:27],v[172:175],v[52:55]\n"
W_"lgkmcnt(4)\n"
T_"v[46:47],v2 offset:3336\n"
L_"v12,s[52:55],s86 offen lds\n"
M_"v[56:59],v[28:31],v[168:171],v[56:59]\n"
T_"v[16:17],v1 offset:0\n"
M_"v[60:63],v[28:31],v[172:175],v[60:63]\n"
T_"v[18:19],v1 offset:256\n"
M_"v[48:51],v[32:35],v[176:179],v[48:51]\n"
T_"v[20:21],v1 offset:8\n"
L_"v13,s[52:55],s86 offen offset:1024 lds\n"
M_"v[52:55],v[32:35],v[180:183],v[52:55]\n"
W_"lgkmcnt(4)\n"
T_"v[22:23],v1 offset:264\n"
M_"v[56:59],v[36:39],v[176:179],v[56:59]\n"
"s_add_u32 m0, s84, 42240\n"
T_"v[24:25],v1 offset:1024\n"
M_"v[60:63],v[36:39],v[180:183],v[60:63]\n"
"s_mov_b32 s86, 1147136\n"
T_"v[26:27],v1 offset:1280\n"
L_"v12,s[52:55],s86 offen lds\n"
M_"v[48:51],v[40:43],v[184:187],v[48:51]\n"
T_"v[28:29],v1 offset:1032\n"
M_"v[52:55],v[40:43],v[188:191],v[52:55]\n"
W_"lgkmcnt(4)\n"
T_"v[30:31],v1 offset:1288\n"
M_"v[56:59],v[44:47],v[184:187],v[56:59]\n"
T_"v[32:33],v2 offset:10496\n"
L_"v13,s[52:55],s86 offen offset:1024 lds\n"
M_"v[60:63],v[44:47],v[188:191],v[60:63]\n"
T_"v[34:35],v2 offset:10752\n"
M_"v[48:51],v[16:19],v[192:195],v[48:51]\n"
"s_add_u32 m0, s84, 152064\n"
"s_mov_b32 s86, 1376512\n"
T_"v[36:37],v2 offset:10504\n"
M_"v[52:55],v[16:19],v[196:199],v[52:55]\n"
W_"lgkmcnt(4)\n"
T_"v[38:39],v2 offset:10760\n"
L_"v12,s[52:55],s86 offen lds\n"
M_"v[56:59],v[20:23],v[192:195],v[56:59]\n"
T_"v[40:41],v2 offset:11520\n"
M_"v[60:63],v[20:23],v[196:199],v[60:63]\n"
T_"v[42:43],v2 offset:11776\n"
M_"v[48:51],v[24:27],v[200:203],v[48:51]\n"
T_"v[44:45],v2 offset:11528\n"
L_"v13,s[52:55],s86 offen offset:1024 lds\n"
M_"v[52:55],v[24:27],v[204:207],v[52:55]\n"
W_"lgkmcnt(4)\n"
T_"v[46:47],v2 offset:11784\n"
M_"v[56:59],v[28:31],v[200:203],v[56:59]\n"
W_"vmcnt(24)\n"
"s_barrier\n"
T_"v[16:17],v2 offset:18944\n"
M_"v[60:63],v[28:31],v[204:207],v[60:63]\n"
"s_add_u32 m0, s85, 0\n"
T_"v[18:19],v2 offset:19200\n"
M_"v[48:51],v[32:35],v[208:211],v[48:51]\n"
"s_mov_b32 s86, 81920\n"
T_"v[20:21],v2 offset:18952\n"
L_"v10,s[40:43],s86 offen lds\n"
M_"v[52:55],v[32:35],v[212:215],v[52:55]\n"
W_"lgkmcnt(4)\n"
T_"v[22:23],v2 offset:19208\n"
M_"v[56:59],v[36:39],v[208:211],v[56:59]\n"
T_"v[24:25],v2 offset:19968\n"
M_"v[60:63],v[36:39],v[212:215],v[60:63]\n"
T_"v[26:27],v2 offset:20224\n"
L_"v11,s[40:43],s86 offen offset:1056 lds\n"
M_"v[48:51],v[40:43],v[216:219],v[48:51]\n"
T_"v[28:29],v2 offset:19976\n"
M_"v[52:55],v[40:43],v[220:223],v[52:55]\n"
W_"lgkmcnt(4)\n"
T_"v[30:31],v2 offset:20232\n"
M_"v[56:59],v[44:47],v[216:219],v[56:59]\n"
"s_add_u32 m0, s85, 59136\n"
T_"v[32:33],v2 offset:27392\n"
M_"v[60:63],v[44:47],v[220:223],v[60:63]\n"
"s_mov_b32 s86, 83968\n"
T_"v[34:35],v2 offset:27648\n"
L_"v10,s[40:43],s86 offen lds\n"
M_"v[48:51],v[16:19],v[224:227],v[48:51]\n"
T_"v[36:37],v2 offset:27400\n"
M_"v[52:55],v[16:19],v[228:231],v[52:55]\n"
W_"lgkmcnt(4)\n"
T_"v[38:39],v2 offset:27656\n"
M_"v[56:59],v[20:23],v[224:227],v[56:59]\n"
T_"v[40:41],v2 offset:28416\n"
M_"v[60:63],v[20:23],v[228:231],v[60:63]\n"
T_"v[42:43],v2 offset:28672\n"
L_"v11,s[40:43],s86 offen offset:1056 lds\n"
M_"v[48:51],v[24:27],v[232:235],v[48:51]\n"
T_"v[44:45],v2 offset:28424\n"
M_"v[52:55],v[24:27],v[236:239],v[52:55]\n"
W_"lgkmcnt(4)\n"
T_"v[46:47],v2 offset:28680\n"
M_"v[56:59],v[28:31],v[232:235],v[56:59]\n"
"s_add_u32 m0, s85, 67584\n"
T_"v[16:17],v1 offset:8448\n"
M_"v[60:63],v[28:31],v[236:239],v[60:63]\n"
"s_mov_b32 s86, 98304\n"
T_"v[18:19],v1 offset:8704\n"
L_"v10,s[40:43],s86 offen lds\n"
M_"v[64:67],v[32:35],v[128:131],v[68:71]\n"
T_"v[20:21],v1 offset:8456\n"
M_"v[68:71],v[32:35],v[132:135],v[68:71]\n"
W_"lgkmcnt(4)\n"
T_"v[22:23],v1 offset:8712\n"
M_"v[72:75],v[36:39],v[128:131],v[76:79]\n"
T_"v[24:25],v1 offset:9472\n"
M_"v[76:79],v[36:39],v[132:135],v[76:79]\n"
T_"v[26:27],v1 offset:9728\n"
L_"v11,s[40:43],s86 offen offset:1056 lds\n"
M_"v[64:67],v[40:43],v[136:139],v[64:67]\n"
T_"v[28:29],v1 offset:9480\n"
M_"v[68:71],v[40:43],v[140:143],v[68:71]\n"
W_"lgkmcnt(4)\n"
T_"v[30:31],v1 offset:9736\n"
M_"v[72:75],v[44:47],v[136:139],v[72:75]\n"
W_"vmcnt(24)\n"
"s_barrier\n"
T_"v[32:33],v1 offset:50688\n"
M_"v[76:79],v[44:47],v[140:143],v[76:79]\n"
"s_add_u32 m0, s85, 76032\n"
T_"v[34:35],v1 offset:50944\n"
M_"v[64:67],v[16:19],v[144:147],v[64:67]\n"
"s_mov_b32 s86, 100352\n"
T_"v[36:37],v1 offset:50696\n"
L_"v10,s[40:43],s86 offen lds\n"
"v_mul_f32 v120, v48, v48\n"
M_"v[68:71],v[16:19],v[148:151],v[68:71]\n"
W_"lgkmcnt(4)\n"
T_"v[38:39],v1 offset:50952\n"
"v_mul_f32 v121, v49, v49\n"
M_"v[72:75],v[20:23],v[144:147],v[72:75]\n"
T_"v[40:41],v1 offset:51712\n"
M_"v[76:79],v[20:23],v[148:151],v[76:79]\n"
T_"v[42:43],v1 offset:51968\n"
"v_mul_f32 v122, v50, v50\n"
M_"v[64:67],v[24:27],v[152:155],v[64:67]\n"
T_"v[44:45],v1 offset:51720\n"
"v_mul_f32 v123, v51, v51\n"
M_"v[68:71],v[24:27],v[156:159],v[68:71]\n"
W_"lgkmcnt(4)\n"
T_"v[46:47],v1 offset:51976\n"
L_"v11,s[40:43],s86 offen offset:1056 lds\n"
M_"v[72:75],v[28:31],v[152:155],v[72:75]\n"
"s_add_u32 m0, s85, 84480\n"
T_"v[16:17],v2 offset:44288\n"
"v_fma_f32 v120, v120, s94, v241\n"
M_"v[76:79],v[28:31],v[156:159],v[76:79]\n"
T_"v[18:19],v2 offset:44544\n"
"v_fma_f32 v121, v121, s94, v241\n"
M_"v[64:67],v[32:35],v[160:163],v[64:67]\n"
"s_mov_b32 s86, 114688\n"
T_"v[20:21],v2 offset:44296\n"
L_"v10,s[40:43],s86 offen lds\n"
M_"v[68:71],v[32:35],v[164:167],v[68:71]\n"
W_"lgkmcnt(4)\n"
T_"v[22:23],v2 offset:44552\n"
"v_fma_f32 v122, v122, s94, v241\n"
M_"v[72:75],v[36:39],v[160:163],v[72:75]\n"
T_"v[24:25],v2 offset:45312\n"
M_"v[76:79],v[36:39],v[164:167],v[76:79]\n"
T_"v[26:27],v2 offset:45568\n"
"v_fma_f32 v123, v123, s94, v241\n"
M_"v[64:67],v[40:43],v[168:171],v[64:67]\n"
T_"v[28:29],v2 offset:45320\n"
"v_mul_f32 v120, v120, v48\n"
M_"v[68:71],v[40:43],v[172:175],v[68:71]\n"
W_"lgkmcnt(4)\n"
T_"v[30:31],v2 offset:45576\n"
L_"v11,s[40:43],s86 offen offset:1056 lds\n"
M_"v[72:75],v[44:47],v[168:171],v[72:75]\n"
"s_add_u32 m0, s85, 92928\n"
T_"v[32:33],v2 offset:52736\n"
"v_mul_f32 v121, v121, v49\n"
M_"v[76:79],v[44:47],v[172:175],v[76:79]\n"
T_"v[34:35],v2 offset:52992\n"
"v_mul_f32 v122, v122, v50\n"
M_"v[64:67],v[16:19],v[176:179],v[64:67]\n"
"s_mov_b32 s86, 116736\n"
T_"v[36:37],v2 offset:52744\n"
L_"v10,s[40:43],s86 offen lds\n"
M_"v[68:71],v[16:19],v[180:183],v[68:71]\n"
W_"lgkmcnt(4)\n"
T_"v[38:39],v2 offset:53000\n"
"v_mul_f32 v123, v123, v51\n"
M_"v[72:75],v[20:23],v[176:179],v[72:75]\n"
T_"v[40:41],v2 offset:53760\n"
T_"v[42:43],v2 offset:54016\n"
M_"v[76:79],v[20:23],v[180:183],v[76:79]\n"
L_"v11,s[40:43],s86 offen offset:1056 lds\n"
E_"v120,v120\n"
M_"v[64:67],v[24:27],v[184:187],v[64:67]\n"
T_"v[44:45],v2 offset:53768\n"
T_"v[46:47],v2 offset:54024\n"
M_"v[68:71],v[24:27],v[188:191],v[68:71]\n"
W_"lgkmcnt(5)\n"
E_"v121,v121\n"
M_"v[72:75],v[28:31],v[184:187],v[72:75]\n"
W_"vmcnt(24)\n"
"s_barrier\n"
T_"v[16:17],v1 offset:16896\n"
E_"v122,v122\n"
M_"v[76:79],v[28:31],v[188:191],v[76:79]\n"
"s_add_u32 m0, s85, 8448\n"
T_"v[18:19],v1 offset:17152\n"
E_"v123,v123\n"
M_"v[64:67],v[32:35],v[192:195],v[64:67]\n"
"s_mov_b32 s86, 131072\n"
T_"v[20:21],v1 offset:16904\n"
L_"v10,s[40:43],s86 offen lds\n"
M_"v[68:71],v[32:35],v[196:199],v[68:71]\n"
W_"lgkmcnt(3)\n"
T_"v[22:23],v1 offset:17160\n"
"v_add_f32 v120, 1.0, v120\n"
M_"v[72:75],v[36:39],v[192:195],v[72:75]\n"
T_"v[24:25],v1 offset:17920\n"
"v_add_f32 v121, 1.0, v121\n"
M_"v[76:79],v[36:39],v[196:199],v[76:79]\n"
T_"v[26:27],v1 offset:18176\n"
L_"v11,s[40:43],s86 offen offset:1056 lds\n"
M_"v[64:67],v[40:43],v[200:203],v[64:67]\n"
T_"v[28:29],v1 offset:17928\n"
"v_add_f32 v122, 1.0, v122\n"
M_"v[68:71],v[40:43],v[204:207],v[68:71]\n"
T_"v[30:31],v1 offset:18184\n"
M_"v[72:75],v[44:47],v[200:203],v[72:75]\n"
"s_add_u32 m0, s85, 50688\n"
T_"v[32:33],v2 offset:35840\n"
"v_add_f32 v123, 1.0, v123\n"
M_"v[76:79],v[44:47],v[204:207],v[76:79]\n"
W_"lgkmcnt(4)\n"
T_"v[34:35],v2 offset:36096\n"
"v_rcp_f32 v120, v120\n"
M_"v[64:67],v[16:19],v[208:211],v[64:67]\n"
T_"v[36:37],v2 offset:35848\n"
T_"v[38:39],v2 offset:36104\n"
M_"v[68:71],v[16:19],v[212:215],v[68:71]\n"
"s_mov_b32 s86, 133120\n"
L_"v10,s[40:43],s86 offen lds\n"
"v_rcp_f32 v121, v121\n"
M_"v[72:75],v[20:23],v[208:211],v[72:75]\n"
T_"v[40:41],v2 offset:36864\n"
T_"v[42:43],v2 offset:37120\n"
M_"v[76:79],v[20:23],v[212:215],v[76:79]\n"
W_"lgkmcnt(5)\n"
L_"v11,s[40:43],s86 offen offset:1056 lds\n"
"v_rcp_f32 v122, v122\n"
M_"v[64:67],v[24:27],v[216:219],v[64:67]\n"
T_"v[44:45],v2 offset:36872\n"
T_"v[46:47],v2 offset:37128\n"
M_"v[68:71],v[24:27],v[220:223],v[68:71]\n"
"v_rcp_f32 v123, v123\n"
M_"v[72:75],v[28:31],v[216:219],v[72:75]\n"
"s_add_u32 m0, s85, 109824\n"
T_"v[16:17],v2 offset:61184\n"
"v_mul_f32 v48, v48, v120\n"
M_"v[76:79],v[28:31],v[220:223],v[76:79]\n"
W_"lgkmcnt(3)\n"
T_"v[18:19],v2 offset:61440\n"
"v_mul_f32 v49, v49, v121\n"
M_"v[64:67],v[32:35],v[224:227],v[64:67]\n"
"s_mov_b32 s86, 147456\n"
T_"v[20:21],v2 offset:61192\n"
L_"v10,s[40:43],s86 offen lds\n"
M_"v[68:71],v[32:35],v[228:231],v[68:71]\n"
T_"v[22:23],v2 offset:61448\n"
"v_mul_f32 v50, v50, v122\n"
M_"v[72:75],v[36:39],v[224:227],v[72:75]\n"
T_"v[24:25],v2 offset:62208\n"
"v_mul_f32 v51, v51, v123\n"
M_"v[76:79],v[36:39],v[228:231],v[76:79]\n"
T_"v[26:27],v2 offset:62464\n"
L_"v11,s[40:43],s86 offen offset:1056 lds\n"
M_"v[64:67],v[40:43],v[232:235],v[64:67]\n"
T_"v[28:29],v2 offset:62216\n"
"v_mul_f32 v120, v56, v56\n"
M_"v[68:71],v[40:43],v[236:239],v[68:71]\n"
W_"lgkmcnt(4)\n"
T_"v[30:31],v2 offset:62472\n"
M_"v[72:75],v[44:47],v[232:235],v[72:75]\n"
W_"vmcnt(24)\n"
"s_barrier\n"
T_"v[32:33],v3 offset:4096\n"
"v_mul_f32 v121, v57, v57\n"
M_"v[76:79],v[44:47],v[236:239],v[76:79]\n"
"s_add_u32 m0, s85, 16896\n"
T_"v[34:35],v3 offset:4352\n"
"v_mul_f32 v122, v58, v58\n"
M_"v[80:83],v[16:19],v[128:131],v[84:87]\n"
"s_mov_b32 s86, 149504\n"
T_"v[36:37],v3 offset:4104\n"
L_"v10,s[40:43],s86 offen lds\n"
M_"v[84:87],v[16:19],v[132:135],v[84:87]\n"
W_"lgkmcnt(4)\n"
T_"v[38:39],v3 offset:4360\n"
"v_mul_f32 v123, v59, v59\n"
M_"v[88:91],v[20:23],v[128:131],v[92:95]\n"
T_"v[40:41],v3 offset:5120\n"
"v_fma_f32 v120, v120, s94, v241\n"
M_"v[92:95],v[20:23],v[132:135],v[92:95]\n"
T_"v[42:43],v3 offset:5376\n"
L_"v11,s[40:43],s86 offen offset:1056 lds\n"
M_"v[80:83],v[24:27],v[136:139],v[80:83]\n"
T_"v[44:45],v3 offset:5128\n"
"v_fma_f32 v121, v121, s94, v241\n"
M_"v[84:87],v[24:27],v[140:143],v[84:87]\n"
W_"lgkmcnt(4)\n"
T_"v[46:47],v3 offset:5384\n"
"v_fma_f32 v122, v122, s94, v241\n"
M_"v[88:91],v[28:31],v[136:139],v[88:91]\n"
"s_add_u32 m0, s85, 101376\n"
T_"v[16:17],v3 offset:12544\n"
M_"v[92:95],v[28:31],v[140:143],v[92:95]\n"
T_"v[18:19],v3 offset:12800\n"
"v_fma_f32 v123, v123, s94, v241\n"
M_"v[80:83],v[32:35],v[144:147],v[80:83]\n"
"s_mov_b32 s86, 163840\n"
T_"v[20:21],v3 offset:12552\n"
L_"v10,s[40:43],s86 offen lds\n"
M_"v[84:87],v[32:35],v[148:151],v[84:87]\n"
W_"lgkmcnt(4)\n"
T_"v[22:23],v3 offset:12808\n"
"v_mul_f32 v120, v120, v56\n"
M_"v[88:91],v[36:39],v[144:147],v[88:91]\n"
T_"v[24:25],v3 offset:13568\n"
"v_mul_f32 v121, v121, v57\n"
M_"v[92:95],v[36:39],v[148:151],v[92:95]\n"
T_"v[26:27],v3 offset:13824\n"
L_"v11,s[40:43],s86 offen offset:1056 lds\n"
M_"v[80:83],v[40:43],v[152:155],v[80:83]\n"
T_"v[28:29],v3 offset:13576\n"
"v_mul_f32 v122, v122, v58\n"
M_"v[84:87],v[40:43],v[156:159],v[84:87]\n"
W_"lgkmcnt(4)\n"
T_"v[30:31],v3 offset:13832\n"
"v_mul_f32 v123, v123, v59\n"
M_"v[88:91],v[44:47],v[152:155],v[88:91]\n"
"s_add_u32 m0, s85, 118272\n"
T_"v[32:33],v1 offset:25344\n"
T_"v[34:35],v1 offset:25600\n"
M_"v[92:95],v[44:47],v[156:159],v[92:95]\n"
"s_mov_b32 s86, 165888\n"
L_"v10,s[40:43],s86 offen lds\n"
E_"v120,v120\n"
M_"v[80:83],v[16:19],v[160:163],v[80:83]\n"
"PTL2:\n"
"s_cbranch_execz PTL10\n"
T_"v[36:37],v1 offset:25352\n"
T_"v[38:39],v1 offset:25608\n"
M_"v[84:87],v[16:19],v[164:167],v[84:87]\n"
W_"lgkmcnt(5)\n"
E_"v121,v121\n"
M_"v[88:91],v[20:23],v[160:163],v[88:91]\n"
T_"v[40:41],v1 offset:26368\n"
T_"v[42:43],v1 offset:26624\n"
M_"v[92:95],v[20:23],v[164:167],v[92:95]\n"
L_"v11,s[40:43],s86 offen offset:1056 lds\n"
E_"v122,v122\n"
M_"v[80:83],v[24:27],v[168:171],v[80:83]\n"
T_"v[44:45],v1 offset:26376\n"
E_"v123,v123\n"
M_"v[84:87],v[24:27],v[172:175],v[84:87]\n"
W_"lgkmcnt(3)\n"
T_"v[46:47],v1 offset:26632\n"
"v_add_f32 v120, 1.0, v120\n"
M_"v[88:91],v[28:31],v[168:171],v[88:91]\n"
W_"vmcnt(24)\n"
"s_barrier\n"
T_"v[16:17],v1 offset:33792\n"
M_"v[92:95],v[28:31],v[172:175],v[92:95]\n"
"s_add_u32 m0, s85, 126720\n"
T_"v[18:19],v1 offset:34048\n"
"v_add_f32 v121, 1.0, v121\n"
M_"v[80:83],v[32:35],v[176:179],v[80:83]\n"
T_"v[20:21],v1 offset:33800\n"
"v_add_f32 v122, 1.0, v122\n"
M_"v[84:87],v[32:35],v[180:183],v[84:87]\n"
"s_mov_b32 s86, 180224\n"
T_"v[22:23],v1 offset:34056\n"
L_"v10,s[40:43],s86 offen lds\n"
M_"v[88:91],v[36:39],v[176:179],v[88:91]\n"
T_"v[24:25],v1 offset:34816\n"
"v_add_f32 v123, 1.0, v123\n"
M_"v[92:95],v[36:39],v[180:183],v[92:95]\n"
W_"lgkmcnt(4)\n"
T_"v[26:27],v1 offset:35072\n"
L_"v11,s[40:43],s86 offen offset:1056 lds\n"
M_"v[80:83],v[40:43],v[184:187],v[80:83]\n"
T_"v[28:29],v1 offset:34824\n"
"v_rcp_f32 v120, v120\n"
M_"v[84:87],v[40:43],v[188:191],v[84:87]\n"
T_"v[30:31],v1 offset:35080\n"
"v_rcp_f32 v121, v121\n"
M_"v[88:91],v[44:47],v[184:187],v[88:91]\n"
"s_add_u32 m0, s85, 135168\n"
"s_mov_b32 s86, 182272\n"
T_"v[32:33],v1 offset:42240\n"
T_"v[34:35],v1 offset:42496\n"
M_"v[92:95],v[44:47],v[188:191],v[92:95]\n"
W_"lgkmcnt(5)\n"
L_"v10,s[40:43],s86 offen lds\n"
"v_rcp_f32 v122, v122\n"
M_"v[80:83],v[16:19],v[192:195],v[80:83]\n"
T_"v[36:37],v1 offset:42248\n"
T_"v[38:39],v1 offset:42504\n"
M_"v[84:87],v[16:19],v[196:199],v[84:87]\n"
"v_rcp_f32 v123, v123\n"
M_"v[88:91],v[20:23],v[192:195],v[88:91]\n"
T_"v[40:41],v1 offset:43264\n"
"v_mul_f32 v56, v56, v120\n"
M_"v[92:95],v[20:23],v[196:199],v[92:95]\n"
W_"lgkmcnt(3)\n"
T_"v[42:43],v1 offset:43520\n"
L_"v11,s[40:43],s86 offen offset:1056 lds\n"
M_"v[80:83],v[24:27],v[200:203],v[80:83]\n"
T_"v[44:45],v1 offset:43272\n"
"v_mul_f32 v57, v57, v121\n"
M_"v[84:87],v[24:27],v[204:207],v[84:87]\n"
T_"v[46:47],v1 offset:43528\n"
"v_mul_f32 v58, v58, v122\n"
M_"v[88:91],v[28:31],v[200:203],v[88:91]\n"
"s_add_u32 m0, s85, 143616\n"
T_"v[16:17],v3 offset:20992\n"
M_"v[92:95],v[28:31],v[204:207],v[92:95]\n"
T_"v[18:19],v3 offset:21248\n"
"v_mul_f32 v59, v59, v123\n"
M_"v[80:83],v[32:35],v[208:211],v[80:83]\n"
"s_mov_b32 s86, 196608\n"
T_"v[20:21],v3 offset:21000\n"
C_"v48,v48,v49\n"
M_"v[84:87],v[32:35],v[212:215],v[84:87]\n"
W_"lgkmcnt(4)\n"
T_"v[22:23],v3 offset:21256\n"
L_"v10,s[40:43],s86 offen lds\n"
M_"v[88:91],v[36:39],v[208:211],v[88:91]\n"
T_"v[24:25],v3 offset:22016\n"
C_"v49,v50,v51\n"
M_"v[92:95],v[36:39],v[212:215],v[92:95]\n"
T_"v[26:27],v3 offset:22272\n"
L_"v11,s[40:43],s86 offen offset:1056 lds\n"
M_"v[80:83],v[40:43],v[216:219],v[80:83]\n"
T_"v[28:29],v3 offset:22024\n"
M_"v[84:87],v[40:43],v[220:223],v[84:87]\n"
W_"lgkmcnt(4)\n"
T_"v[30:31],v3 offset:22280\n"
M_"v[88:91],v[44:47],v[216:219],v[88:91]\n"
W_"vmcnt(26)\n"
"s_barrier\n"
M_"v[92:95],v[44:47],v[220:223],v[92:95]\n"
M_"v[80:83],v[16:19],v[224:227],v[80:83]\n"
M_"v[84:87],v[16:19],v[228:231],v[84:87]\n"
W_"lgkmcnt(1)\n"
M_"v[88:91],v[20:23],v[224:227],v[88:91]\n"
M_"v[92:95],v[20:23],v[228:231],v[92:95]\n"
M_"v[80:83],v[24:27],v[232:235],v[80:83]\n"
M_"v[84:87],v[24:27],v[236:239],v[84:87]\n"
W_"lgkmcnt(0)\n"
M_"v[88:91],v[28:31],v[232:235],v[88:91]\n"
M_"v[92:95],v[28:31],v[236:239],v[92:95]\n"
"s_add_u32 m0, s85, 33792\n"
"s_mov_b32 s86, 198656\n"
"s_setprio 0\n"
L_"v10,s[40:43],s86 offen lds\n"
C_"v50,v56,v57\n"
C_"v51,v58,v59\n"
"s_setprio 2\n"
"ds_write_b128 v5, v[48:51] offset:25344\n"
"s_setprio 0\n"
"v_mul_f32 v120, v52, v52\n"
"v_mul_f32 v121, v53, v53\n"
"v_mul_f32 v122, v54, v54\n"
"v_mul_f32 v123, v55, v55\n"
"v_fma_f32 v120, v120, s94, v241\n"
L_"v11,s[40:43],s86 offen offset:1056 lds\n"
"v_fma_f32 v121, v121, s94, v241\n"
"v_fma_f32 v122, v122, s94, v241\n"
"v_fma_f32 v123, v123, s94, v241\n"
"v_mul_f32 v120, v120, v52\n"
"v_mul_f32 v121, v121, v53\n"
"v_mul_f32 v122, v122, v54\n"
"v_mul_f32 v123, v123, v55\n"
E_"v120,v120\n"
"s_add_u32 m0, s85, 42240\n"
"s_mov_b32 s86, 212992\n"
L_"v10,s[40:43],s86 offen lds\n"
E_"v121,v121\n"
E_"v122,v122\n"
E_"v123,v123\n"
"v_add_f32 v120, 1.0, v120\n"
"v_add_f32 v121, 1.0, v121\n"
"v_add_f32 v122, 1.0, v122\n"
"s_mul_i32 s87, s32, 21\n"
"v_add_u32 v24, s87, v240\n"
L_"v11,s[40:43],s86 offen offset:1056 lds\n"
"v_lshlrev_b32 v24, 7, v24\n"
"s_mul_i32 s87, s32, 32\n"
"v_add_u32 v25, s87, v240\n"
"v_lshlrev_b32 v25, 7, v25\n"
"s_bfm_b64 exec, 21, 0\n"
"buffer_load_dword v7, v24, s[56:59], 0 offen\n"
"s_mov_b64 exec, -1\n"
"s_bfm_b64 exec, 32, 0\n"
"buffer_load_dword v7, v25, s[44:47], 0 offen\n"
"s_mov_b64 exec, -1\n"
"v_readlane_b32 s8, v243, s30\n"
"v_lshrrev_b32 v126, 5, v242\n"
"s_lshl_b32 s9, s30, 7\n"
"s_add_u32 s9, s9, 32768\n"
"buffer_load_dword v127, v126, s[80:83], s9 offen sc1\n"
"buffer_load_dword v7, v242, s[76:79], 0 offen\n"
W_"vmcnt(30)\n"
"s_barrier\n"
"v_add_f32 v123, 1.0, v123\n"
"v_rcp_f32 v120, v120\n"
"v_rcp_f32 v121, v121\n"
"v_rcp_f32 v122, v122\n"
"v_rcp_f32 v123, v123\n"
"v_mul_f32 v52, v52, v120\n"
"v_mul_f32 v53, v53, v121\n"
"v_mul_f32 v54, v54, v122\n"
"v_mul_f32 v55, v55, v123\n"
"v_mul_f32 v120, v60, v60\n"
"v_mul_f32 v121, v61, v61\n"
"v_mul_f32 v122, v62, v62\n"
"v_mul_f32 v123, v63, v63\n"
"v_fma_f32 v120, v120, s94, v241\n"
"v_fma_f32 v121, v121, s94, v241\n"
"v_fma_f32 v122, v122, s94, v241\n"
"v_fma_f32 v123, v123, s94, v241\n"
"v_mul_f32 v120, v120, v60\n"
"v_mul_f32 v121, v121, v61\n"
"v_mul_f32 v122, v122, v62\n"
"v_mul_f32 v123, v123, v63\n"
E_"v120,v120\n"
E_"v121,v121\n"
E_"v122,v122\n"
E_"v123,v123\n"
"v_add_f32 v120, 1.0, v120\n"
"v_add_f32 v121, 1.0, v121\n"
"v_add_f32 v122, 1.0, v122\n"
"v_add_f32 v123, 1.0, v123\n"
"v_rcp_f32 v120, v120\n"
"v_rcp_f32 v121, v121\n"
"v_rcp_f32 v122, v122\n"
W_"vmcnt(26)\n"
"s_barrier\n"
"v_rcp_f32 v123, v123\n"
"v_mul_f32 v60, v60, v120\n"
"v_mul_f32 v61, v61, v121\n"
"v_mul_f32 v62, v62, v122\n"
"v_mul_f32 v63, v63, v123\n"
C_"v52,v52,v53\n"
C_"v53,v54,v55\n"
C_"v54,v60,v61\n"
C_"v55,v62,v63\n"
"s_setprio 2\n"
"ds_write_b128 v5, v[52:55] offset:26368\n"
"s_setprio 0\n"
"v_mul_f32 v120, v64, v64\n"
"v_mul_f32 v121, v65, v65\n"
"v_mul_f32 v122, v66, v66\n"
"v_mul_f32 v123, v67, v67\n"
"v_fma_f32 v120, v120, s94, v241\n"
"v_fma_f32 v121, v121, s94, v241\n"
"v_fma_f32 v122, v122, s94, v241\n"
"v_fma_f32 v123, v123, s94, v241\n"
"v_mul_f32 v120, v120, v64\n"
"v_mul_f32 v121, v121, v65\n"
"v_mul_f32 v122, v122, v66\n"
"v_mul_f32 v123, v123, v67\n"
E_"v120,v120\n"
E_"v121,v121\n"
E_"v122,v122\n"
E_"v123,v123\n"
"v_add_f32 v120, 1.0, v120\n"
"v_add_f32 v121, 1.0, v121\n"
"v_add_f32 v122, 1.0, v122\n"
"v_add_f32 v123, 1.0, v123\n"
"v_rcp_f32 v120, v120\n"
"v_rcp_f32 v121, v121\n"
W_"vmcnt(22) lgkmcnt(0)\n"
"s_barrier\n"
"s_add_u32 m0, s85, 0\n"
"s_mov_b32 s86, 215040\n"
L_"v10,s[40:43],s86 offen lds\n"
"s_setprio 2\n"
R_"v[136:139],v6 offset:29440\n"
R_"v[140:143],v6 offset:30464\n"
R_"v[128:131],v6 offset:25344\n"
R_"v[132:135],v6 offset:26368\n"
"s_setprio 0\n"
"v_rcp_f32 v122, v122\n"
L_"v11,s[40:43],s86 offen offset:1056 lds\n"
"v_rcp_f32 v123, v123\n"
"v_mul_f32 v64, v64, v120\n"
"v_mul_f32 v65, v65, v121\n"
"v_mul_f32 v66, v66, v122\n"
"v_mul_f32 v67, v67, v123\n"
"s_add_u32 m0, s85, 59136\n"
"s_mov_b32 s86, 229376\n"
L_"v10,s[40:43],s86 offen lds\n"
"v_mul_f32 v120, v72, v72\n"
"v_mul_f32 v121, v73, v73\n"
"v_mul_f32 v122, v74, v74\n"
"v_mul_f32 v123, v75, v75\n"
"v_fma_f32 v120, v120, s94, v241\n"
L_"v11,s[40:43],s86 offen offset:1056 lds\n"
"v_fma_f32 v121, v121, s94, v241\n"
"v_fma_f32 v122, v122, s94, v241\n"
"v_fma_f32 v123, v123, s94, v241\n"
"v_mul_f32 v120, v120, v72\n"
"v_mul_f32 v121, v121, v73\n"
"s_add_u32 m0, s85, 67584\n"
"s_mov_b32 s86, 231424\n"
L_"v10,s[40:43],s86 offen lds\n"
"v_mul_f32 v122, v122, v74\n"
"v_mul_f32 v123, v123, v75\n"
E_"v120,v120\n"
E_"v121,v121\n"
E_"v122,v122\n"
L_"v11,s[40:43],s86 offen offset:1056 lds\n"
E_"v123,v123\n"
"v_add_f32 v120, 1.0, v120\n"
"v_add_f32 v121, 1.0, v121\n"
"v_add_f32 v122, 1.0, v122\n"
"v_add_f32 v123, 1.0, v123\n"
"s_add_u32 m0, s85, 76032\n"
"s_mov_b32 s86, 245760\n"
L_"v10,s[40:43],s86 offen lds\n"
"v_rcp_f32 v120, v120\n"
"v_rcp_f32 v121, v121\n"
"v_rcp_f32 v122, v122\n"
"v_rcp_f32 v123, v123\n"
"v_mul_f32 v72, v72, v120\n"
L_"v11,s[40:43],s86 offen offset:1056 lds\n"
"v_mov_b32 v44, -1\n"
"v_mov_b32 v45, -1\n"
"v_mov_b32 v46, -1\n"
"v_mov_b32 v47, -1\n"
W_"vmcnt(26) lgkmcnt(0)\n"
"s_barrier\n"
"s_add_u32 m0, s85, 25344\n"
"s_mov_b32 s86, 247808\n"
L_"v10,s[40:43],s86 offen lds\n"
"v_mul_f32 v73, v73, v121\n"
"v_mul_f32 v74, v74, v122\n"
"v_mul_f32 v75, v75, v123\n"
C_"v64,v64,v65\n"
L_"v11,s[40:43],s86 offen offset:1056 lds\n"
C_"v65,v66,v67\n"
C_"v66,v72,v73\n"
C_"v67,v74,v75\n"
"s_setprio 2\n"
"ds_write_b128 v5, v[64:67] offset:8448\n"
"s_setprio 0\n"
"v_mul_f32 v120, v68, v68\n"
"s_add_u32 m0, s84, 84480\n"
"s_mov_b32 s86, 0\n"
L_"v124,s[56:59],s86 offen lds\n"
"v_mul_f32 v121, v69, v69\n"
"v_mul_f32 v122, v70, v70\n"
"v_mul_f32 v123, v71, v71\n"
"v_fma_f32 v120, v120, s94, v241\n"
L_"v125,s[56:59],s86 offen offset:1024 lds\n"
"v_fma_f32 v121, v121, s94, v241\n"
"v_fma_f32 v122, v122, s94, v241\n"
"v_fma_f32 v123, v123, s94, v241\n"
"v_mul_f32 v120, v120, v68\n"
"v_mul_f32 v121, v121, v69\n"
"s_add_u32 m0, s84, 92928\n"
"s_mov_b32 s86, 57344\n"
L_"v124,s[56:59],s86 offen lds\n"
"v_mul_f32 v122, v122, v70\n"
"v_mul_f32 v123, v123, v71\n"
E_"v120,v120\n"
E_"v121,v121\n"
L_"v125,s[56:59],s86 offen offset:1024 lds\n"
E_"v122,v122\n"
E_"v123,v123\n"
"v_add_f32 v120, 1.0, v120\n"
"v_add_f32 v121, 1.0, v121\n"
"v_add_f32 v122, 1.0, v122\n"
"s_add_u32 m0, s84, 152064\n"
"s_mov_b32 s86, 114688\n"
L_"v124,s[56:59],s86 offen lds\n"
"v_add_f32 v123, 1.0, v123\n"
"v_rcp_f32 v120, v120\n"
"v_rcp_f32 v121, v121\n"
"v_rcp_f32 v122, v122\n"
L_"v125,s[56:59],s86 offen offset:1024 lds\n"
"v_rcp_f32 v123, v123\n"
"s_add_u32 s87, s90, 98304\n"
"buffer_store_dwordx4 v[44:47], v248, s[60:63], s87 offen sc1\n"
"buffer_load_dwordx2 v[8:9], v245, s[68:71], 0 offen\n"
W_"vmcnt(32)\n"
"s_barrier\n"
"s_add_u32 m0, s84, 50688\n"
"s_mov_b32 s86, 128\n"
L_"v124,s[56:59],s86 offen lds\n"
W_"vmcnt(20)\n"
"v_not_b32 v127, v127\n"
"s_mov_b32 s4, 0\n"
"ACKL_L:\n"
"v_cmp_ne_u32 vcc, s8, v127\n"
"s_add_u32 s4, s4, 1\n"
"s_nop 3\n"
"s_cbranch_vccz ACKED_L\n"
"s_cmp_lt_u32 s4, 0x40000\n"
"s_cbranch_scc0 ACKED_L\n"
"s_sleep 2\n"
"buffer_load_dword v127, v126, s[80:83], s9 offen sc1\n"
W_"vmcnt(0)\n"
"v_not_b32 v127, v127\n"
"s_branch ACKL_L\n"
"ACKED_L:\n"
"v_mul_f32 v68, v68, v120\n"
"v_mul_f32 v69, v69, v121\n"
"v_mul_f32 v70, v70, v122\n"
"v_mul_f32 v71, v71, v123\n"
"v_mul_f32 v120, v76, v76\n"
"v_mul_f32 v121, v77, v77\n"
"v_mul_f32 v122, v78, v78\n"
"v_mul_f32 v123, v79, v79\n"
L_"v125,s[56:59],s86 offen offset:1024 lds\n"
"v_fma_f32 v120, v120, s94, v241\n"
"v_fma_f32 v121, v121, s94, v241\n"
"v_fma_f32 v122, v122, s94, v241\n"
"v_fma_f32 v123, v123, s94, v241\n"
"v_mul_f32 v120, v120, v76\n"
"v_mul_f32 v121, v121, v77\n"
"v_mul_f32 v122, v122, v78\n"
"v_mul_f32 v123, v123, v79\n"
E_"v120,v120\n"
"s_add_u32 m0, s84, 109824\n"
"s_mov_b32 s86, 57472\n"
L_"v124,s[56:59],s86 offen lds\n"
E_"v121,v121\n"
E_"v122,v122\n"
E_"v123,v123\n"
"v_add_f32 v120, 1.0, v120\n"
"v_add_f32 v121, 1.0, v121\n"
"v_add_f32 v122, 1.0, v122\n"
"v_add_f32 v123, 1.0, v123\n"
"v_rcp_f32 v120, v120\n"
"v_rcp_f32 v121, v121\n"
L_"v125,s[56:59],s86 offen offset:1024 lds\n"
"v_rcp_f32 v122, v122\n"
"v_rcp_f32 v123, v123\n"
"v_mul_f32 v76, v76, v120\n"
"v_mul_f32 v77, v77, v121\n"
"v_mul_f32 v78, v78, v122\n"
"v_mul_f32 v79, v79, v123\n"
"s_add_u32 s87, s91, 327680\n"
"buffer_store_dwordx4 v[44:47], v248, s[60:63], s87 offen sc1\n"
"buffer_load_dwordx2 v[12:13], v245, s[68:71], 0 offen offset:256\n"
"s_barrier\n"
"s_add_u32 m0, s84, 16896\n"
"s_mov_b32 s86, 114816\n"
L_"v124,s[56:59],s86 offen lds\n"
C_"v68,v68,v69\n"
C_"v69,v70,v71\n"
C_"v70,v76,v77\n"
C_"v71,v78,v79\n"
"s_setprio 2\n"
"ds_write_b128 v5, v[68:71] offset:9472\n"
"s_setprio 0\n"
"v_mul_f32 v120, v80, v80\n"
"v_mul_f32 v121, v81, v81\n"
"v_mul_f32 v122, v82, v82\n"
"v_mul_f32 v123, v83, v83\n"
L_"v125,s[56:59],s86 offen offset:1024 lds\n"
"v_fma_f32 v120, v120, s94, v241\n"
"v_fma_f32 v121, v121, s94, v241\n"
"v_fma_f32 v122, v122, s94, v241\n"
"v_fma_f32 v123, v123, s94, v241\n"
"v_mul_f32 v120, v120, v80\n"
"v_mul_f32 v121, v121, v81\n"
"v_mul_f32 v122, v122, v82\n"
"v_mul_f32 v123, v123, v83\n"
E_"v120,v120\n"
"s_add_u32 m0, s84, 101376\n"
"s_mov_b32 s86, 256\n"
L_"v124,s[56:59],s86 offen lds\n"
E_"v121,v121\n"
E_"v122,v122\n"
E_"v123,v123\n"
"v_add_f32 v120, 1.0, v120\n"
"v_add_f32 v121, 1.0, v121\n"
"v_add_f32 v122, 1.0, v122\n"
"v_add_f32 v123, 1.0, v123\n"
"v_rcp_f32 v120, v120\n"
"v_rcp_f32 v121, v121\n"
L_"v125,s[56:59],s86 offen offset:1024 lds\n"
"v_rcp_f32 v122, v122\n"
"v_rcp_f32 v123, v123\n"
"v_mul_f32 v80, v80, v120\n"
"v_mul_f32 v81, v81, v121\n"
"v_mul_f32 v82, v82, v122\n"
"s_add_u32 s87, s90, 106496\n"
"buffer_store_dwordx4 v[44:47], v248, s[60:63], s87 offen sc1\n"
"buffer_load_dwordx2 v[14:15], v245, s[68:71], 0 offen offset:512\n"
W_"lgkmcnt(0)\n"
"s_barrier\n"
"s_add_u32 m0, s84, 118272\n"
"s_mov_b32 s86, 57600\n"
L_"v124,s[56:59],s86 offen lds\n"
"s_setprio 2\n"
R_"v[152:155],v6 offset:12544\n"
R_"v[156:159],v6 offset:13568\n"
R_"v[144:147],v6 offset:8448\n"
R_"v[148:151],v6 offset:9472\n"
"s_setprio 0\n"
"v_mul_f32 v83, v83, v123\n"
"v_mul_f32 v120, v88, v88\n"
"v_mul_f32 v121, v89, v89\n"
"v_mul_f32 v122, v90, v90\n"
"v_mul_f32 v123, v91, v91\n"
"v_fma_f32 v120, v120, s94, v241\n"
L_"v125,s[56:59],s86 offen offset:1024 lds\n"
"v_fma_f32 v121, v121, s94, v241\n"
"v_fma_f32 v122, v122, s94, v241\n"
"v_fma_f32 v123, v123, s94, v241\n"
"v_mul_f32 v120, v120, v88\n"
"v_mul_f32 v121, v121, v89\n"
"v_mul_f32 v122, v122, v90\n"
"v_mul_f32 v123, v123, v91\n"
E_"v120,v120\n"
E_"v121,v121\n"
E_"v122,v122\n"
E_"v123,v123\n"
"s_add_u32 m0, s84, 126720\n"
"s_mov_b32 s86, 114944\n"
L_"v124,s[56:59],s86 offen lds\n"
"v_add_f32 v120, 1.0, v120\n"
"v_add_f32 v121, 1.0, v121\n"
"v_add_f32 v122, 1.0, v122\n"
"v_add_f32 v123, 1.0, v123\n"
"v_rcp_f32 v120, v120\n"
"v_rcp_f32 v121, v121\n"
"v_rcp_f32 v122, v122\n"
"v_rcp_f32 v123, v123\n"
"v_mul_f32 v88, v88, v120\n"
"v_mul_f32 v89, v89, v121\n"
"v_mul_f32 v90, v90, v122\n"
L_"v125,s[56:59],s86 offen offset:1024 lds\n"
"v_mul_f32 v91, v91, v123\n"
C_"v80,v80,v81\n"
C_"v81,v82,v83\n"
"s_add_u32 s87, s91, 335872\n"
"buffer_store_dwordx4 v[44:47], v248, s[60:63], s87 offen sc1\n"
"buffer_load_dwordx2 v[126:127], v245, s[68:71], 0 offen offset:768\n"
"s_mul_i32 s87, s32, 56\n"
"v_add_u32 v24, s87, v240\n"
"v_lshlrev_b32 v24, 7, v24\n"
"s_bfm_b64 exec, 56, 0\n"
"buffer_load_dword v7, v24, s[48:51], 0 offen\n"
"s_mov_b64 exec, -1\n"
W_"vmcnt(35) lgkmcnt(0)\n"
"s_barrier\n"
C_"v82,v88,v89\n"
C_"v83,v90,v91\n"
"s_setprio 2\n"
"ds_write_b128 v5, v[80:83] offset:8448\n"
"s_setprio 0\n"
"v_mul_f32 v120, v84, v84\n"
"v_mul_f32 v121, v85, v85\n"
"v_mul_f32 v122, v86, v86\n"
"v_mul_f32 v123, v87, v87\n"
"v_fma_f32 v120, v120, s94, v241\n"
"v_fma_f32 v121, v121, s94, v241\n"
"v_fma_f32 v122, v122, s94, v241\n"
"v_fma_f32 v123, v123, s94, v241\n"
"v_mul_f32 v120, v120, v84\n"
"v_mul_f32 v121, v121, v85\n"
"v_mul_f32 v122, v122, v86\n"
"v_mul_f32 v123, v123, v87\n"
E_"v120,v120\n"
E_"v121,v121\n"
E_"v122,v122\n"
E_"v123,v123\n"
"v_add_f32 v120, 1.0, v120\n"
"v_add_f32 v121, 1.0, v121\n"
"v_add_f32 v122, 1.0, v122\n"
"v_add_f32 v123, 1.0, v123\n"
"v_rcp_f32 v120, v120\n"
"v_rcp_f32 v121, v121\n"
"v_rcp_f32 v122, v122\n"
"v_rcp_f32 v123, v123\n"
"v_mul_f32 v84, v84, v120\n"
"v_mul_f32 v85, v85, v121\n"
"v_mul_f32 v86, v86, v122\n"
"v_mul_f32 v87, v87, v123\n"
"v_mul_f32 v120, v92, v92\n"
W_"vmcnt(31)\n"
"s_barrier\n"
"v_mul_f32 v121, v93, v93\n"
"v_mul_f32 v122, v94, v94\n"
"v_mul_f32 v123, v95, v95\n"
"v_fma_f32 v120, v120, s94, v241\n"
"v_fma_f32 v121, v121, s94, v241\n"
"v_fma_f32 v122, v122, s94, v241\n"
"v_fma_f32 v123, v123, s94, v241\n"
"v_mul_f32 v120, v120, v92\n"
"v_mul_f32 v121, v121, v93\n"
"v_mul_f32 v122, v122, v94\n"
"v_mul_f32 v123, v123, v95\n"
E_"v120,v120\n"
E_"v121,v121\n"
E_"v122,v122\n"
E_"v123,v123\n"
"v_add_f32 v120, 1.0, v120\n"
"v_add_f32 v121, 1.0, v121\n"
"v_add_f32 v122, 1.0, v122\n"
"v_add_f32 v123, 1.0, v123\n"
"v_rcp_f32 v120, v120\n"
"v_rcp_f32 v121, v121\n"
"v_rcp_f32 v122, v122\n"
"v_rcp_f32 v123, v123\n"
"v_mul_f32 v92, v92, v120\n"
"v_mul_f32 v93, v93, v121\n"
"v_mul_f32 v94, v94, v122\n"
"v_mul_f32 v95, v95, v123\n"
C_"v84,v84,v85\n"
C_"v85,v86,v87\n"
C_"v86,v92,v93\n"
C_"v87,v94,v95\n"
"s_setprio 2\n"
"ds_write_b128 v5, v[84:87] offset:9472\n"
W_"vmcnt(27) lgkmcnt(0)\n"
"s_barrier\n"
"s_add_u32 m0, s84, 0\n"
"s_mov_b32 s86, 384\n"
"s_setprio 0\n"
L_"v124,s[56:59],s86 offen lds\n"
L_"v125,s[56:59],s86 offen offset:1024 lds\n"
"s_setprio 2\n"
R_"v[168:171],v6 offset:12544\n"
"s_add_u32 m0, s84, 33792\n"
"s_mov_b32 s86, 57728\n"
"s_setprio 0\n"
L_"v124,s[56:59],s86 offen lds\n"
L_"v125,s[56:59],s86 offen offset:1024 lds\n"
"s_setprio 2\n"
R_"v[172:175],v6 offset:13568\n"
"s_add_u32 m0, s84, 42240\n"
"s_mov_b32 s86, 115072\n"
"s_setprio 0\n"
L_"v124,s[56:59],s86 offen lds\n"
"s_setprio 2\n"
R_"v[160:163],v6 offset:8448\n"
"s_setprio 0\n"
L_"v125,s[56:59],s86 offen offset:1024 lds\n"
"s_add_u32 m0, s84, 59136\n"
"s_mov_b32 s86, 512\n"
L_"v124,s[56:59],s86 offen lds\n"
"s_setprio 2\n"
R_"v[164:167],v6 offset:9472\n"
"s_setprio 0\n"
L_"v125,s[56:59],s86 offen offset:1024 lds\n"
W_"lgkmcnt(0)\n"
"s_setprio 2\n"
W_"vmcnt(29)\n"
"s_barrier\n"
T_"v[16:17],v2 offset:18944\n"
T_"v[18:19],v2 offset:19200\n"
T_"v[20:21],v2 offset:18952\n"
T_"v[22:23],v2 offset:19208\n"
T_"v[24:25],v2 offset:19968\n"
T_"v[26:27],v2 offset:20224\n"
T_"v[28:29],v2 offset:19976\n"
T_"v[30:31],v2 offset:20232\n"
T_"v[32:33],v2 offset:27392\n"
T_"v[34:35],v2 offset:27648\n"
W_"lgkmcnt(8)\n"
M_"v[48:51],v[16:19],v[128:131],0\n"
"s_add_u32 m0, s84, 8448\n"
"s_mov_b32 s86, 57856\n"
T_"v[36:37],v2 offset:27400\n"
L_"v124,s[56:59],s86 offen lds\n"
M_"v[52:55],v[16:19],v[132:135],0\n"
W_"lgkmcnt(7)\n"
T_"v[38:39],v2 offset:27656\n"
L_"v125,s[56:59],s86 offen offset:1024 lds\n"
M_"v[56:59],v[20:23],v[128:131],0\n"
"s_add_u32 m0, s84, 67584\n"
"s_mov_b32 s86, 115200\n"
T_"v[40:41],v2 offset:28416\n"
M_"v[60:63],v[20:23],v[132:135],0\n"
W_"lgkmcnt(7)\n"
T_"v[42:43],v2 offset:28672\n"
L_"v124,s[56:59],s86 offen lds\n"
M_"v[48:51],v[24:27],v[136:139],v[48:51]\n"
T_"v[44:45],v2 offset:28424\n"
L_"v125,s[56:59],s86 offen offset:1024 lds\n"
M_"v[52:55],v[24:27],v[140:143],v[52:55]\n"
W_"lgkmcnt(4)\n"
T_"v[46:47],v2 offset:28680\n"
M_"v[56:59],v[28:31],v[136:139],v[56:59]\n"
"s_add_u32 m0, s84, 76032\n"
"s_mov_b32 s86, 640\n"
T_"v[16:17],v3 offset:20992\n"
L_"v124,s[56:59],s86 offen lds\n"
M_"v[60:63],v[28:31],v[140:143],v[60:63]\n"
T_"v[18:19],v3 offset:21248\n"
L_"v125,s[56:59],s86 offen offset:1024 lds\n"
M_"v[48:51],v[32:35],v[144:147],v[48:51]\n"
"s_add_u32 m0, s84, 135168\n"
"s_mov_b32 s86, 57984\n"
T_"v[20:21],v3 offset:21000\n"
L_"v124,s[56:59],s86 offen lds\n"
M_"v[52:55],v[32:35],v[148:151],v[52:55]\n"
W_"lgkmcnt(4)\n"
T_"v[22:23],v3 offset:21256\n"
M_"v[56:59],v[36:39],v[144:147],v[56:59]\n"
T_"v[24:25],v3 offset:22016\n"
L_"v125,s[56:59],s86 offen offset:1024 lds\n"
M_"v[60:63],v[36:39],v[148:151],v[60:63]\n"
"s_add_u32 m0, s84, 143616\n"
"s_mov_b32 s86, 115328\n"
T_"v[26:27],v3 offset:22272\n"
L_"v124,s[56:59],s86 offen lds\n"
M_"v[48:51],v[40:43],v[152:155],v[48:51]\n"
T_"v[28:29],v3 offset:22024\n"
M_"v[52:55],v[40:43],v[156:159],v[52:55]\n"
W_"lgkmcnt(4)\n"
T_"v[30:31],v3 offset:22280\n"
L_"v125,s[56:59],s86 offen offset:1024 lds\n"
M_"v[56:59],v[44:47],v[152:155],v[56:59]\n"
W_"vmcnt(29)\n"
"s_barrier\n"
T_"v[32:33],v1 offset:50688\n"
M_"v[60:63],v[44:47],v[156:159],v[60:63]\n"
"s_add_u32 m0, s84, 25344\n"
T_"v[34:35],v1 offset:50944\n"
M_"v[48:51],v[16:19],v[160:163],v[48:51]\n"
"s_mov_b32 s86, 768\n"
T_"v[36:37],v1 offset:50696\n"
L_"v124,s[56:59],s86 offen lds\n"
M_"v[52:55],v[16:19],v[164:167],v[52:55]\n"
W_"lgkmcnt(4)\n"
T_"v[38:39],v1 offset:50952\n"
M_"v[56:59],v[20:23],v[160:163],v[56:59]\n"
"PTL3:\n"
"s_cbranch_execz PTL11\n"
T_"v[40:41],v1 offset:51712\n"
M_"v[60:63],v[20:23],v[164:167],v[60:63]\n"
T_"v[42:43],v1 offset:51968\n"
L_"v125,s[56:59],s86 offen offset:1024 lds\n"
M_"v[48:51],v[24:27],v[168:171],v[48:51]\n"
T_"v[44:45],v1 offset:51720\n"
M_"v[52:55],v[24:27],v[172:175],v[52:55]\n"
W_"lgkmcnt(4)\n"
T_"v[46:47],v1 offset:51976\n"
M_"v[56:59],v[28:31],v[168:171],v[56:59]\n"
"s_add_u32 m0, s84, 84480\n"
T_"v[16:17],v2 offset:44288\n"
M_"v[60:63],v[28:31],v[172:175],v[60:63]\n"
"s_mov_b32 s86, 58112\n"
T_"v[18:19],v2 offset:44544\n"
L_"v124,s[56:59],s86 offen lds\n"
M_"v[64:67],v[32:35],v[128:131],0\n"
T_"v[20:21],v2 offset:44296\n"
M_"v[68:71],v[32:35],v[132:135],0\n"
W_"lgkmcnt(4)\n"
T_"v[22:23],v2 offset:44552\n"
M_"v[72:75],v[36:39],v[128:131],0\n"
T_"v[24:25],v2 offset:45312\n"
M_"v[76:79],v[36:39],v[132:135],0\n"
T_"v[26:27],v2 offset:45568\n"
L_"v125,s[56:59],s86 offen offset:1024 lds\n"
M_"v[64:67],v[40:43],v[136:139],v[64:67]\n"
T_"v[28:29],v2 offset:45320\n"
C_"v192,v48,v49\n"
M_"v[68:71],v[40:43],v[140:143],v[68:71]\n"
W_"lgkmcnt(4)\n"
T_"v[30:31],v2 offset:45576\n"
C_"v193,v50,v51\n"
M_"v[72:75],v[44:47],v[136:139],v[72:75]\n"
"s_add_u32 m0, s84, 92928\n"
T_"v[32:33],v1 offset:16896\n"
C_"v194,v56,v57\n"
M_"v[76:79],v[44:47],v[140:143],v[76:79]\n"
"s_mov_b32 s86, 115456\n"
T_"v[34:35],v1 offset:17152\n"
L_"v124,s[56:59],s86 offen lds\n"
C_"v195,v58,v59\n"
M_"v[64:67],v[16:19],v[144:147],v[64:67]\n"
T_"v[36:37],v1 offset:16904\n"
C_"v196,v52,v53\n"
M_"v[68:71],v[16:19],v[148:151],v[68:71]\n"
W_"lgkmcnt(4)\n"
T_"v[38:39],v1 offset:17160\n"
C_"v197,v54,v55\n"
M_"v[72:75],v[20:23],v[144:147],v[72:75]\n"
T_"v[40:41],v1 offset:17920\n"
C_"v198,v60,v61\n"
M_"v[76:79],v[20:23],v[148:151],v[76:79]\n"
T_"v[42:43],v1 offset:18176\n"
C_"v199,v62,v63\n"
M_"v[64:67],v[24:27],v[152:155],v[64:67]\n"
T_"v[44:45],v1 offset:17928\n"
L_"v125,s[56:59],s86 offen offset:1024 lds\n"
M_"v[68:71],v[24:27],v[156:159],v[68:71]\n"
W_"lgkmcnt(4)\n"
T_"v[46:47],v1 offset:18184\n"
M_"v[72:75],v[28:31],v[152:155],v[72:75]\n"
W_"vmcnt(27)\n"
"s_barrier\n"
T_"v[16:17],v2 offset:35840\n"
M_"v[76:79],v[28:31],v[156:159],v[76:79]\n"
"s_add_u32 m0, s84, 50688\n"
T_"v[18:19],v2 offset:36096\n"
M_"v[64:67],v[32:35],v[160:163],v[64:67]\n"
"s_mov_b32 s86, 0\n"
T_"v[20:21],v2 offset:35848\n"
L_"v246,s[44:47],s86 offen lds\n"
M_"v[68:71],v[32:35],v[164:167],v[68:71]\n"
W_"lgkmcnt(4)\n"
T_"v[22:23],v2 offset:36104\n"
M_"v[72:75],v[36:39],v[160:163],v[72:75]\n"
T_"v[24:25],v2 offset:36864\n"
M_"v[76:79],v[36:39],v[164:167],v[76:79]\n"
T_"v[26:27],v2 offset:37120\n"
L_"v247,s[44:47],s86 offen offset:1024 lds\n"
M_"v[64:67],v[40:43],v[168:171],v[64:67]\n"
T_"v[28:29],v2 offset:36872\n"
M_"v[68:71],v[40:43],v[172:175],v[68:71]\n"
W_"lgkmcnt(4)\n"
T_"v[30:31],v2 offset:37128\n"
M_"v[72:75],v[44:47],v[168:171],v[72:75]\n"
"s_add_u32 m0, s84, 109824\n"
T_"v[32:33],v2 offset:52736\n"
M_"v[76:79],v[44:47],v[172:175],v[76:79]\n"
"s_mov_b32 s86, 16384\n"
T_"v[34:35],v2 offset:52992\n"
L_"v246,s[44:47],s86 offen lds\n"
M_"v[48:51],v[16:19],v[128:131],0\n"
T_"v[36:37],v2 offset:52744\n"
M_"v[52:55],v[16:19],v[132:135],0\n"
W_"lgkmcnt(4)\n"
T_"v[38:39],v2 offset:53000\n"
M_"v[56:59],v[20:23],v[128:131],0\n"
T_"v[40:41],v2 offset:53760\n"
M_"v[60:63],v[20:23],v[132:135],0\n"
T_"v[42:43],v2 offset:54016\n"
L_"v247,s[44:47],s86 offen offset:1024 lds\n"
M_"v[48:51],v[24:27],v[136:139],v[48:51]\n"
T_"v[44:45],v2 offset:53768\n"
C_"v200,v64,v65\n"
M_"v[52:55],v[24:27],v[140:143],v[52:55]\n"
W_"lgkmcnt(4)\n"
T_"v[46:47],v2 offset:54024\n"
C_"v201,v66,v67\n"
M_"v[56:59],v[28:31],v[136:139],v[56:59]\n"
"s_add_u32 m0, s84, 152064\n"
T_"v[16:17],v2 offset:61184\n"
C_"v202,v72,v73\n"
M_"v[60:63],v[28:31],v[140:143],v[60:63]\n"
"s_mov_b32 s86, 32768\n"
T_"v[18:19],v2 offset:61440\n"
L_"v246,s[44:47],s86 offen lds\n"
C_"v203,v74,v75\n"
M_"v[48:51],v[32:35],v[144:147],v[48:51]\n"
T_"v[20:21],v2 offset:61192\n"
C_"v204,v68,v69\n"
M_"v[52:55],v[32:35],v[148:151],v[52:55]\n"
W_"lgkmcnt(4)\n"
T_"v[22:23],v2 offset:61448\n"
C_"v205,v70,v71\n"
M_"v[56:59],v[36:39],v[144:147],v[56:59]\n"
T_"v[24:25],v2 offset:62208\n"
C_"v206,v76,v77\n"
M_"v[60:63],v[36:39],v[148:151],v[60:63]\n"
T_"v[26:27],v2 offset:62464\n"
C_"v207,v78,v79\n"
M_"v[48:51],v[40:43],v[152:155],v[48:51]\n"
T_"v[28:29],v2 offset:62216\n"
L_"v247,s[44:47],s86 offen offset:1024 lds\n"
M_"v[52:55],v[40:43],v[156:159],v[52:55]\n"
W_"lgkmcnt(4)\n"
T_"v[30:31],v2 offset:62472\n"
M_"v[56:59],v[44:47],v[152:155],v[56:59]\n"
W_"vmcnt(24)\n"
"s_barrier\n"
T_"v[32:33],v1 offset:0\n"
M_"v[60:63],v[44:47],v[156:159],v[60:63]\n"
"s_add_u32 m0, s84, 16896\n"
T_"v[34:35],v1 offset:256\n"
M_"v[48:51],v[16:19],v[160:163],v[48:51]\n"
"s_mov_b32 s86, 49152\n"
T_"v[36:37],v1 offset:8\n"
L_"v246,s[44:47],s86 offen lds\n"
M_"v[52:55],v[16:19],v[164:167],v[52:55]\n"
W_"lgkmcnt(4)\n"
T_"v[38:39],v1 offset:264\n"
M_"v[56:59],v[20:23],v[160:163],v[56:59]\n"
T_"v[40:41],v1 offset:1024\n"
M_"v[60:63],v[20:23],v[164:167],v[60:63]\n"
T_"v[42:43],v1 offset:1280\n"
L_"v247,s[44:47],s86 offen offset:1024 lds\n"
M_"v[48:51],v[24:27],v[168:171],v[48:51]\n"
T_"v[44:45],v1 offset:1032\n"
M_"v[52:55],v[24:27],v[172:175],v[52:55]\n"
W_"lgkmcnt(4)\n"
T_"v[46:47],v1 offset:1288\n"
M_"v[56:59],v[28:31],v[168:171],v[56:59]\n"
"s_add_u32 m0, s84, 101376\n"
T_"v[16:17],v1 offset:33792\n"
M_"v[60:63],v[28:31],v[172:175],v[60:63]\n"
"s_mov_b32 s86, 65536\n"
T_"v[18:19],v1 offset:34048\n"
L_"v246,s[44:47],s86 offen lds\n"
M_"v[64:67],v[32:35],v[128:131],0\n"
T_"v[20:21],v1 offset:33800\n"
M_"v[68:71],v[32:35],v[132:135],0\n"
W_"lgkmcnt(4)\n"
T_"v[22:23],v1 offset:34056\n"
M_"v[72:75],v[36:39],v[128:131],0\n"
T_"v[24:25],v1 offset:34816\n"
M_"v[76:79],v[36:39],v[132:135],0\n"
T_"v[26:27],v1 offset:35072\n"
L_"v247,s[44:47],s86 offen offset:1024 lds\n"
M_"v[64:67],v[40:43],v[136:139],v[64:67]\n"
T_"v[28:29],v1 offset:34824\n"
C_"v208,v48,v49\n"
M_"v[68:71],v[40:43],v[140:143],v[68:71]\n"
W_"lgkmcnt(4)\n"
T_"v[30:31],v1 offset:35080\n"
C_"v209,v50,v51\n"
M_"v[72:75],v[44:47],v[136:139],v[72:75]\n"
"s_add_u32 m0, s84, 118272\n"
T_"v[32:33],v1 offset:42240\n"
C_"v210,v56,v57\n"
M_"v[76:79],v[44:47],v[140:143],v[76:79]\n"
"s_mov_b32 s86, 81920\n"
T_"v[34:35],v1 offset:42496\n"
L_"v246,s[44:47],s86 offen lds\n"
C_"v211,v58,v59\n"
M_"v[64:67],v[16:19],v[144:147],v[64:67]\n"
T_"v[36:37],v1 offset:42248\n"
C_"v212,v52,v53\n"
M_"v[68:71],v[16:19],v[148:151],v[68:71]\n"
W_"lgkmcnt(4)\n"
T_"v[38:39],v1 offset:42504\n"
C_"v213,v54,v55\n"
M_"v[72:75],v[20:23],v[144:147],v[72:75]\n"
T_"v[40:41],v1 offset:43264\n"
C_"v214,v60,v61\n"
M_"v[76:79],v[20:23],v[148:151],v[76:79]\n"
T_"v[42:43],v1 offset:43520\n"
C_"v215,v62,v63\n"
M_"v[64:67],v[24:27],v[152:155],v[64:67]\n"
T_"v[44:45],v1 offset:43272\n"
L_"v247,s[44:47],s86 offen offset:1024 lds\n"
M_"v[68:71],v[24:27],v[156:159],v[68:71]\n"
W_"lgkmcnt(4)\n"
T_"v[46:47],v1 offset:43528\n"
M_"v[72:75],v[28:31],v[152:155],v[72:75]\n"
W_"vmcnt(24)\n"
"s_barrier\n"
T_"v[16:17],v1 offset:59136\n"
M_"v[76:79],v[28:31],v[156:159],v[76:79]\n"
"s_add_u32 m0, s84, 0\n"
T_"v[18:19],v1 offset:59392\n"
M_"v[64:67],v[32:35],v[160:163],v[64:67]\n"
"s_mov_b32 s86, 98304\n"
T_"v[20:21],v1 offset:59144\n"
L_"v246,s[44:47],s86 offen lds\n"
M_"v[68:71],v[32:35],v[164:167],v[68:71]\n"
W_"lgkmcnt(4)\n"
T_"v[22:23],v1 offset:59400\n"
M_"v[72:75],v[36:39],v[160:163],v[72:75]\n"
T_"v[24:25],v1 offset:60160\n"
M_"v[76:79],v[36:39],v[164:167],v[76:79]\n"
T_"v[26:27],v1 offset:60416\n"
L_"v247,s[44:47],s86 offen offset:1024 lds\n"
M_"v[64:67],v[40:43],v[168:171],v[64:67]\n"
T_"v[28:29],v1 offset:60168\n"
M_"v[68:71],v[40:43],v[172:175],v[68:71]\n"
W_"lgkmcnt(4)\n"
T_"v[30:31],v1 offset:60424\n"
M_"v[72:75],v[44:47],v[168:171],v[72:75]\n"
"s_add_u32 m0, s84, 33792\n"
T_"v[32:33],v1 offset:8448\n"
M_"v[76:79],v[44:47],v[172:175],v[76:79]\n"
"s_mov_b32 s86, 114688\n"
T_"v[34:35],v1 offset:8704\n"
L_"v246,s[44:47],s86 offen lds\n"
M_"v[48:51],v[16:19],v[128:131],0\n"
T_"v[36:37],v1 offset:8456\n"
M_"v[52:55],v[16:19],v[132:135],0\n"
W_"lgkmcnt(4)\n"
T_"v[38:39],v1 offset:8712\n"
M_"v[56:59],v[20:23],v[128:131],0\n"
T_"v[40:41],v1 offset:9472\n"
M_"v[60:63],v[20:23],v[132:135],0\n"
T_"v[42:43],v1 offset:9728\n"
L_"v247,s[44:47],s86 offen offset:1024 lds\n"
M_"v[48:51],v[24:27],v[136:139],v[48:51]\n"
T_"v[44:45],v1 offset:9480\n"
C_"v216,v64,v65\n"
M_"v[52:55],v[24:27],v[140:143],v[52:55]\n"
W_"lgkmcnt(4)\n"
T_"v[46:47],v1 offset:9736\n"
C_"v217,v66,v67\n"
M_"v[56:59],v[28:31],v[136:139],v[56:59]\n"
"s_add_u32 m0, s84, 126720\n"
T_"v[16:17],v2 offset:2048\n"
C_"v218,v72,v73\n"
M_"v[60:63],v[28:31],v[140:143],v[60:63]\n"
"s_mov_b32 s86, 131072\n"
T_"v[18:19],v2 offset:2304\n"
L_"v246,s[44:47],s86 offen lds\n"
C_"v219,v74,v75\n"
M_"v[48:51],v[32:35],v[144:147],v[48:51]\n"
T_"v[20:21],v2 offset:2056\n"
C_"v220,v68,v69\n"
M_"v[52:55],v[32:35],v[148:151],v[52:55]\n"
W_"lgkmcnt(4)\n"
T_"v[22:23],v2 offset:2312\n"
C_"v221,v70,v71\n"
M_"v[56:59],v[36:39],v[144:147],v[56:59]\n"
T_"v[24:25],v2 offset:3072\n"
C_"v222,v76,v77\n"
M_"v[60:63],v[36:39],v[148:151],v[60:63]\n"
T_"v[26:27],v2 offset:3328\n"
C_"v223,v78,v79\n"
M_"v[48:51],v[40:43],v[152:155],v[48:51]\n"
T_"v[28:29],v2 offset:3080\n"
L_"v247,s[44:47],s86 offen offset:1024 lds\n"
M_"v[52:55],v[40:43],v[156:159],v[52:55]\n"
W_"lgkmcnt(4)\n"
T_"v[30:31],v2 offset:3336\n"
M_"v[56:59],v[44:47],v[152:155],v[56:59]\n"
W_"vmcnt(24)\n"
"s_barrier\n"
T_"v[32:33],v2 offset:10496\n"
M_"v[60:63],v[44:47],v[156:159],v[60:63]\n"
"s_add_u32 m0, s84, 8448\n"
T_"v[34:35],v2 offset:10752\n"
M_"v[48:51],v[16:19],v[160:163],v[48:51]\n"
"s_mov_b32 s86, 147456\n"
T_"v[36:37],v2 offset:10504\n"
L_"v246,s[44:47],s86 offen lds\n"
M_"v[52:55],v[16:19],v[164:167],v[52:55]\n"
W_"lgkmcnt(4)\n"
T_"v[38:39],v2 offset:10760\n"
M_"v[56:59],v[20:23],v[160:163],v[56:59]\n"
T_"v[40:41],v2 offset:11520\n"
M_"v[60:63],v[20:23],v[164:167],v[60:63]\n"
T_"v[42:43],v2 offset:11776\n"
L_"v247,s[44:47],s86 offen offset:1024 lds\n"
M_"v[48:51],v[24:27],v[168:171],v[48:51]\n"
T_"v[44:45],v2 offset:11528\n"
M_"v[52:55],v[24:27],v[172:175],v[52:55]\n"
W_"lgkmcnt(4)\n"
T_"v[46:47],v2 offset:11784\n"
M_"v[56:59],v[28:31],v[168:171],v[56:59]\n"
"s_add_u32 m0, s84, 42240\n"
T_"v[16:17],v3 offset:4096\n"
M_"v[60:63],v[28:31],v[172:175],v[60:63]\n"
"s_mov_b32 s86, 163840\n"
T_"v[18:19],v3 offset:4352\n"
L_"v246,s[44:47],s86 offen lds\n"
M_"v[64:67],v[32:35],v[128:131],0\n"
T_"v[20:21],v3 offset:4104\n"
M_"v[68:71],v[32:35],v[132:135],0\n"
W_"lgkmcnt(4)\n"
T_"v[22:23],v3 offset:4360\n"
M_"v[72:75],v[36:39],v[128:131],0\n"
T_"v[24:25],v3 offset:5120\n"
M_"v[76:79],v[36:39],v[132:135],0\n"
T_"v[26:27],v3 offset:5376\n"
L_"v247,s[44:47],s86 offen offset:1024 lds\n"
M_"v[64:67],v[40:43],v[136:139],v[64:67]\n"
T_"v[28:29],v3 offset:5128\n"
C_"v224,v48,v49\n"
M_"v[68:71],v[40:43],v[140:143],v[68:71]\n"
W_"lgkmcnt(4)\n"
T_"v[30:31],v3 offset:5384\n"
C_"v225,v50,v51\n"
M_"v[72:75],v[44:47],v[136:139],v[72:75]\n"
"s_add_u32 m0, s84, 59136\n"
T_"v[32:33],v3 offset:12544\n"
C_"v226,v56,v57\n"
M_"v[76:79],v[44:47],v[140:143],v[76:79]\n"
"s_mov_b32 s86, 180224\n"
T_"v[34:35],v3 offset:12800\n"
L_"v246,s[44:47],s86 offen lds\n"
C_"v227,v58,v59\n"
M_"v[64:67],v[16:19],v[144:147],v[64:67]\n"
T_"v[36:37],v3 offset:12552\n"
C_"v228,v52,v53\n"
M_"v[68:71],v[16:19],v[148:151],v[68:71]\n"
W_"lgkmcnt(4)\n"
T_"v[38:39],v3 offset:12808\n"
C_"v229,v54,v55\n"
M_"v[72:75],v[20:23],v[144:147],v[72:75]\n"
T_"v[40:41],v3 offset:13568\n"
C_"v230,v60,v61\n"
M_"v[76:79],v[20:23],v[148:151],v[76:79]\n"
T_"v[42:43],v3 offset:13824\n"
C_"v231,v62,v63\n"
M_"v[64:67],v[24:27],v[152:155],v[64:67]\n"
T_"v[44:45],v3 offset:13576\n"
L_"v247,s[44:47],s86 offen offset:1024 lds\n"
M_"v[68:71],v[24:27],v[156:159],v[68:71]\n"
W_"lgkmcnt(4)\n"
T_"v[46:47],v3 offset:13832\n"
M_"v[72:75],v[28:31],v[152:155],v[72:75]\n"
W_"vmcnt(24)\n"
"s_barrier\n"
T_"v[16:17],v1 offset:25344\n"
M_"v[76:79],v[28:31],v[156:159],v[76:79]\n"
"s_add_u32 m0, s84, 67584\n"
T_"v[18:19],v1 offset:25600\n"
M_"v[64:67],v[32:35],v[160:163],v[64:67]\n"
"s_mov_b32 s86, 196608\n"
T_"v[20:21],v1 offset:25352\n"
L_"v246,s[44:47],s86 offen lds\n"
M_"v[68:71],v[32:35],v[164:167],v[68:71]\n"
W_"lgkmcnt(4)\n"
T_"v[22:23],v1 offset:25608\n"
M_"v[72:75],v[36:39],v[160:163],v[72:75]\n"
T_"v[24:25],v1 offset:26368\n"
M_"v[76:79],v[36:39],v[164:167],v[76:79]\n"
T_"v[26:27],v1 offset:26624\n"
L_"v247,s[44:47],s86 offen offset:1024 lds\n"
M_"v[64:67],v[40:43],v[168:171],v[64:67]\n"
T_"v[28:29],v1 offset:26376\n"
M_"v[68:71],v[40:43],v[172:175],v[68:71]\n"
W_"lgkmcnt(4)\n"
T_"v[30:31],v1 offset:26632\n"
M_"v[72:75],v[44:47],v[168:171],v[72:75]\n"
"s_add_u32 m0, s84, 76032\n"
T_"v[32:33],v2 offset:18944\n"
M_"v[76:79],v[44:47],v[172:175],v[76:79]\n"
"s_mov_b32 s86, 212992\n"
T_"v[34:35],v2 offset:19200\n"
L_"v246,s[44:47],s86 offen lds\n"
M_"v[48:51],v[16:19],v[128:131],0\n"
T_"v[36:37],v2 offset:18952\n"
M_"v[52:55],v[16:19],v[132:135],0\n"
W_"lgkmcnt(4)\n"
T_"v[38:39],v2 offset:19208\n"
M_"v[56:59],v[20:23],v[128:131],0\n"
T_"v[40:41],v2 offset:19968\n"
M_"v[60:63],v[20:23],v[132:135],0\n"
T_"v[42:43],v2 offset:20224\n"
L_"v247,s[44:47],s86 offen offset:1024 lds\n"
M_"v[48:51],v[24:27],v[136:139],v[48:51]\n"
T_"v[44:45],v2 offset:19976\n"
C_"v232,v64,v65\n"
M_"v[52:55],v[24:27],v[140:143],v[52:55]\n"
W_"lgkmcnt(4)\n"
T_"v[46:47],v2 offset:20232\n"
C_"v233,v66,v67\n"
M_"v[56:59],v[28:31],v[136:139],v[56:59]\n"
"s_add_u32 m0, s84, 135168\n"
T_"v[16:17],v2 offset:27392\n"
C_"v234,v72,v73\n"
M_"v[60:63],v[28:31],v[140:143],v[60:63]\n"
"s_mov_b32 s86, 229376\n"
T_"v[18:19],v2 offset:27648\n"
L_"v246,s[44:47],s86 offen lds\n"
C_"v235,v74,v75\n"
M_"v[48:51],v[32:35],v[144:147],v[48:51]\n"
T_"v[20:21],v2 offset:27400\n"
C_"v236,v68,v69\n"
M_"v[52:55],v[32:35],v[148:151],v[52:55]\n"
W_"lgkmcnt(4)\n"
T_"v[22:23],v2 offset:27656\n"
C_"v237,v70,v71\n"
M_"v[56:59],v[36:39],v[144:147],v[56:59]\n"
T_"v[24:25],v2 offset:28416\n"
C_"v238,v76,v77\n"
M_"v[60:63],v[36:39],v[148:151],v[60:63]\n"
T_"v[26:27],v2 offset:28672\n"
C_"v239,v78,v79\n"
M_"v[48:51],v[40:43],v[152:155],v[48:51]\n"
T_"v[28:29],v2 offset:28424\n"
L_"v247,s[44:47],s86 offen offset:1024 lds\n"
M_"v[52:55],v[40:43],v[156:159],v[52:55]\n"
W_"lgkmcnt(4)\n"
T_"v[30:31],v2 offset:28680\n"
M_"v[56:59],v[44:47],v[152:155],v[56:59]\n"
W_"vmcnt(26)\n"
"s_barrier\n"
M_"v[60:63],v[44:47],v[156:159],v[60:63]\n"
M_"v[48:51],v[16:19],v[160:163],v[48:51]\n"
M_"v[52:55],v[16:19],v[164:167],v[52:55]\n"
W_"lgkmcnt(1)\n"
M_"v[56:59],v[20:23],v[160:163],v[56:59]\n"
M_"v[60:63],v[20:23],v[164:167],v[60:63]\n"
M_"v[48:51],v[24:27],v[168:171],v[48:51]\n"
M_"v[52:55],v[24:27],v[172:175],v[52:55]\n"
W_"lgkmcnt(0)\n"
M_"v[56:59],v[28:31],v[168:171],v[56:59]\n"
M_"v[60:63],v[28:31],v[172:175],v[60:63]\n"
"s_add_u32 m0, s84, 25344\n"
"s_mov_b32 s86, 245760\n"
"s_setprio 0\n"
L_"v246,s[44:47],s86 offen lds\n"
C_"v80,v48,v49\n"
C_"v81,v50,v51\n"
L_"v247,s[44:47],s86 offen offset:1024 lds\n"
C_"v82,v56,v57\n"
C_"v83,v58,v59\n"
"s_add_u32 m0, s84, 84480\n"
"s_mov_b32 s86, 256\n"
L_"v246,s[44:47],s86 offen lds\n"
C_"v84,v52,v53\n"
C_"v85,v54,v55\n"
L_"v247,s[44:47],s86 offen offset:1024 lds\n"
C_"v86,v60,v61\n"
C_"v87,v62,v63\n"
"s_add_u32 m0, s84, 143616\n"
"s_mov_b32 s86, 16640\n"
L_"v246,s[44:47],s86 offen lds\n"
"s_add_u32 s87, s90, 0\n"
"buffer_store_dwordx4 v[192:195], v248, s[60:63], s87 offen sc1\n"
L_"v247,s[44:47],s86 offen offset:1024 lds\n"
"s_add_u32 s87, s91, 229376\n"
"buffer_store_dwordx4 v[196:199], v248, s[60:63], s87 offen sc1\n"
W_"vmcnt(30)\n"
"s_barrier\n"
"s_add_u32 m0, s84, 50688\n"
"s_mov_b32 s86, 33024\n"
L_"v246,s[44:47],s86 offen lds\n"
"s_add_u32 s87, s90, 8192\n"
L_"v247,s[44:47],s86 offen offset:1024 lds\n"
"buffer_store_dwordx4 v[200:203], v248, s[60:63], s87 offen sc1\n"
"s_add_u32 m0, s84, 92928\n"
"s_mov_b32 s86, 49408\n"
L_"v246,s[44:47],s86 offen lds\n"
"s_add_u32 s87, s91, 237568\n"
L_"v247,s[44:47],s86 offen offset:1024 lds\n"
"buffer_store_dwordx4 v[204:207], v248, s[60:63], s87 offen sc1\n"
W_"vmcnt(32)\n"
"s_barrier\n"
"s_add_u32 m0, s84, 109824\n"
"s_mov_b32 s86, 65792\n"
L_"v246,s[44:47],s86 offen lds\n"
"s_add_u32 s87, s90, 16384\n"
"buffer_store_dwordx4 v[208:211], v248, s[60:63], s87 offen sc1\n"
L_"v247,s[44:47],s86 offen offset:1024 lds\n"
"s_add_u32 s87, s91, 245760\n"
"buffer_store_dwordx4 v[212:215], v248, s[60:63], s87 offen sc1\n"
"s_add_u32 m0, s84, 152064\n"
"s_mov_b32 s86, 82176\n"
L_"v246,s[44:47],s86 offen lds\n"
"s_lshl1_add_u32 s4, s35, s34\n"
"s_lshl_b32 s4, s4, 9\n"
L_"v247,s[44:47],s86 offen offset:1024 lds\n"
"v_lshl_add_u32 v6, v240, 4, s4\n"
W_"vmcnt(34)\n"
"s_barrier\n"
"s_add_u32 m0, s84, 16896\n"
"s_mov_b32 s86, 98560\n"
L_"v246,s[44:47],s86 offen lds\n"
L_"v247,s[44:47],s86 offen offset:1024 lds\n"
"s_add_u32 s87, s90, 24576\n"
"s_add_u32 m0, s84, 101376\n"
"s_mov_b32 s86, 114944\n"
L_"v246,s[44:47],s86 offen lds\n"
"buffer_store_dwordx4 v[216:219], v248, s[60:63], s87 offen sc1\n"
L_"v247,s[44:47],s86 offen offset:1024 lds\n"
W_"vmcnt(35)\n"
"s_barrier\n"
"s_add_u32 m0, s84, 0\n"
"s_mov_b32 s86, 131328\n"
L_"v246,s[44:47],s86 offen lds\n"
"s_add_u32 s87, s91, 253952\n"
L_"v247,s[44:47],s86 offen offset:1024 lds\n"
"buffer_store_dwordx4 v[220:223], v248, s[60:63], s87 offen sc1\n"
"s_add_u32 m0, s84, 118272\n"
"s_mov_b32 s86, 147712\n"
L_"v246,s[44:47],s86 offen lds\n"
"s_add_u32 s87, s90, 32768\n"
L_"v247,s[44:47],s86 offen offset:1024 lds\n"
"buffer_store_dwordx4 v[224:227], v248, s[60:63], s87 offen sc1\n"
W_"vmcnt(37)\n"
"s_barrier\n"
"s_add_u32 m0, s84, 33792\n"
"s_mov_b32 s86, 164096\n"
L_"v246,s[44:47],s86 offen lds\n"
"s_add_u32 s87, s91, 262144\n"
L_"v247,s[44:47],s86 offen offset:1024 lds\n"
"buffer_store_dwordx4 v[228:231], v248, s[60:63], s87 offen sc1\n"
"s_add_u32 m0, s84, 126720\n"
"s_mov_b32 s86, 180480\n"
"PTL4:\n"
"s_cbranch_execz PTL12\n"
L_"v246,s[44:47],s86 offen lds\n"
"s_add_u32 s87, s90, 40960\n"
L_"v247,s[44:47],s86 offen offset:1024 lds\n"
"buffer_store_dwordx4 v[232:235], v248, s[60:63], s87 offen sc1\n"
W_"vmcnt(39)\n"
"s_barrier\n"
"s_add_u32 m0, s84, 8448\n"
"s_mov_b32 s86, 196864\n"
L_"v246,s[44:47],s86 offen lds\n"
"s_add_u32 s87, s91, 270336\n"
"buffer_store_dwordx4 v[236:239], v248, s[60:63], s87 offen sc1\n"
L_"v247,s[44:47],s86 offen offset:1024 lds\n"
"s_add_u32 s87, s90, 49152\n"
"buffer_store_dwordx4 v[80:83], v248, s[60:63], s87 offen sc1\n"
W_"vmcnt(39)\n"
"s_barrier\n"
"s_add_u32 s87, s91, 278528\n"
"buffer_store_dwordx4 v[84:87], v248, s[60:63], s87 offen sc1\n"
"v_mbcnt_lo_u32_b32 v5, -1, 0\n"
"v_mbcnt_hi_u32_b32 v5, -1, v5\n"
"s_mov_b32 s4, 42240\n"
"s_setprio 2\n"
"v_lshl_add_u32 v5, v5, 4, s4\n"
"s_bitcmp1_b32 s90, 30\n"
"s_cbranch_scc0 OWF0_L\n"
"ds_write_b128 v5, v[192:195] offset:0\n"
"ds_write_b128 v5, v[200:203] offset:1024\n"
"ds_write_b128 v5, v[208:211] offset:2048\n"
"ds_write_b128 v5, v[216:219] offset:3072\n"
"ds_write_b128 v5, v[224:227] offset:4096\n"
"ds_write_b128 v5, v[232:235] offset:5120\n"
"ds_write_b128 v5, v[80:83] offset:6144\n"
"OWF0_L:\n"
"s_bitcmp1_b32 s91, 30\n"
"s_cbranch_scc0 OWF1_L\n"
"ds_write_b128 v5, v[196:199] offset:0\n"
"ds_write_b128 v5, v[204:207] offset:1024\n"
"ds_write_b128 v5, v[212:215] offset:2048\n"
"ds_write_b128 v5, v[220:223] offset:3072\n"
"ds_write_b128 v5, v[228:231] offset:4096\n"
"ds_write_b128 v5, v[236:239] offset:5120\n"
"ds_write_b128 v5, v[84:87] offset:6144\n"
"OWF1_L:\n"
W_"vmcnt(35) lgkmcnt(0)\n"
"s_barrier\n"
"s_mov_b64 exec, 0xffff\n"
R_"v[128:131],v6 offset:42240\n"
R_"v[132:135],v6 offset:42496\n"
R_"v[144:147],v6 offset:44288\n"
R_"v[148:151],v6 offset:44544\n"
R_"v[160:163],v6 offset:46336\n"
R_"v[164:167],v6 offset:46592\n"
R_"v[176:179],v6 offset:48384\n"
R_"v[180:183],v6 offset:48640\n"
"s_mov_b64 exec, -1\n"
W_"lgkmcnt(0)\n"
W_"vmcnt(0)\n"
W_"lgkmcnt(0)\n"
"s_barrier\n"
"s_add_u32 m0, s84, 25344\n"
"s_mov_b32 s86, 213248\n"
"s_setprio 0\n"
L_"v246,s[44:47],s86 offen lds\n"
L_"v247,s[44:47],s86 offen offset:1024 lds\n"
"s_add_u32 m0, s84, 42240\n"
"s_mov_b32 s86, 229632\n"
L_"v246,s[44:47],s86 offen lds\n"
"buffer_load_dwordx2 v[40:41], v244, s[76:79], 0 offen\n"
L_"v247,s[44:47],s86 offen offset:1024 lds\n"
"s_add_u32 m0, s84, 84480\n"
"s_mov_b32 s86, 246016\n"
L_"v246,s[44:47],s86 offen lds\n"
L_"v247,s[44:47],s86 offen offset:1024 lds\n"
"s_barrier\n"
"s_add_u32 m0, s84, 50688\n"
"s_mov_b32 s86, 0\n"
L_"v124,s[48:51],s86 offen lds\n"
"s_lshl_b32 s4, s30, 7\n"
L_"v125,s[48:51],s86 offen offset:1024 lds\n"
"v_cmp_eq_u32 vcc, s4, v242\n"
"s_nop 1\n"
"v_cndmask_b32 v17, 0, 16, vcc\n"
"s_add_u32 m0, s84, 143616\n"
"s_mov_b32 s86, 57344\n"
L_"v124,s[48:51],s86 offen lds\n"
"v_sub_u32 v243, v243, v17\n"
L_"v125,s[48:51],s86 offen offset:1024 lds\n"
"buffer_load_dwordx2 v[42:43], v244, s[76:79], 0 offen offset:256\n"
"s_barrier\n"
"s_add_u32 m0, s84, 92928\n"
"s_mov_b32 s86, 229376\n"
L_"v124,s[48:51],s86 offen lds\n"
L_"v125,s[48:51],s86 offen offset:1024 lds\n"
"buffer_load_dwordx2 v[44:45], v244, s[76:79], 0 offen offset:512\n"
"s_add_u32 m0, s84, 109824\n"
"s_mov_b32 s86, 286720\n"
L_"v124,s[48:51],s86 offen lds\n"
L_"v125,s[48:51],s86 offen offset:1024 lds\n"
"s_barrier\n"
"s_add_u32 m0, s84, 16896\n"
"s_mov_b32 s86, 114688\n"
L_"v124,s[48:51],s86 offen lds\n"
L_"v125,s[48:51],s86 offen offset:1024 lds\n"
"buffer_load_dwordx2 v[46:47], v244, s[76:79], 0 offen offset:768\n"
"s_add_u32 m0, s84, 152064\n"
"s_mov_b32 s86, 172032\n"
L_"v124,s[48:51],s86 offen lds\n"
L_"v125,s[48:51],s86 offen offset:1024 lds\n"
"s_barrier\n"
"s_add_u32 m0, s84, 0\n"
"s_mov_b32 s86, 344064\n"
L_"v124,s[48:51],s86 offen lds\n"
L_"v125,s[48:51],s86 offen offset:1024 lds\n"
"s_add_u32 m0, s84, 59136\n"
"s_mov_b32 s86, 401408\n"
L_"v124,s[48:51],s86 offen lds\n"
L_"v125,s[48:51],s86 offen offset:1024 lds\n"
"s_add_u32 m0, s84, 67584\n"
"s_mov_b32 s86, 128\n"
L_"v124,s[48:51],s86 offen lds\n"
L_"v125,s[48:51],s86 offen offset:1024 lds\n"
"s_add_u32 m0, s84, 101376\n"
"s_mov_b32 s86, 57472\n"
L_"v124,s[48:51],s86 offen lds\n"
L_"v125,s[48:51],s86 offen offset:1024 lds\n"
W_"vmcnt(28)\n"
"s_barrier\n"
"s_add_u32 m0, s84, 33792\n"
"s_mov_b32 s86, 229504\n"
L_"v124,s[48:51],s86 offen lds\n"
L_"v125,s[48:51],s86 offen offset:1024 lds\n"
"s_add_u32 m0, s84, 76032\n"
"s_mov_b32 s86, 286848\n"
L_"v124,s[48:51],s86 offen lds\n"
L_"v125,s[48:51],s86 offen offset:1024 lds\n"
"buffer_load_dword v18, v242, s[80:83], 0 offen sc1\n"
"s_add_u32 m0, s84, 118272\n"
"s_mov_b32 s86, 114816\n"
L_"v124,s[48:51],s86 offen lds\n"
L_"v125,s[48:51],s86 offen offset:1024 lds\n"
"s_add_u32 m0, s84, 135168\n"
"s_mov_b32 s86, 172160\n"
L_"v124,s[48:51],s86 offen lds\n"
L_"v125,s[48:51],s86 offen offset:1024 lds\n"
W_"vmcnt(32)\n"
"s_barrier\n"
W_"vmcnt(4)\n"
"v_sub_u32 v18, v18, v243\n"
"v_cmp_gt_u32 vcc, 1, v18\n"
"s_mov_b32 s4, 0\n"
"s_nop 3\n"
"s_cbranch_vccz FOK1_L\n"
"FPOLL1_L:\n"
"s_sleep 2\n"
"buffer_load_dword v18, v242, s[80:83], 0 offen sc1\n"
W_"vmcnt(0)\n"
"v_sub_u32 v18, v18, v243\n"
"v_cmp_gt_u32 vcc, 1, v18\n"
"s_add_u32 s4, s4, 1\n"
"s_nop 3\n"
"s_cbranch_vccz FOK1_L\n"
"s_cmp_lt_u32 s4, 0x40000\n"
"s_cbranch_scc1 FPOLL1_L\n"
"FOK1_L:\n"
"s_mov_b32 s87, 0\n"
"s_bfm_b64 exec, 48, 16\n"
L_"v[128:131],v255,s[64:67],s87 offen sc1\n"
L_"v[132:135],v255,s[64:67],s87 offen offset:256 sc1\n"
"s_mov_b64 exec, -1\n"
"s_mov_b32 s87, 4096\n"
L_"v[136:139],v255,s[64:67],s87 offen sc1\n"
L_"v[140:143],v255,s[64:67],s87 offen offset:256 sc1\n"
"s_mov_b32 s87, 16384\n"
"s_bfm_b64 exec, 48, 16\n"
L_"v[144:147],v255,s[64:67],s87 offen sc1\n"
L_"v[148:151],v255,s[64:67],s87 offen offset:256 sc1\n"
"s_mov_b64 exec, -1\n"
"s_mov_b32 s87, 20480\n"
L_"v[152:155],v255,s[64:67],s87 offen sc1\n"
L_"v[156:159],v255,s[64:67],s87 offen offset:256 sc1\n"
"s_barrier\n"
"s_mov_b32 s87, 32768\n"
"s_bfm_b64 exec, 48, 16\n"
L_"v[160:163],v255,s[64:67],s87 offen sc1\n"
L_"v[164:167],v255,s[64:67],s87 offen offset:256 sc1\n"
"s_mov_b64 exec, -1\n"
"s_mov_b32 s87, 36864\n"
L_"v[168:171],v255,s[64:67],s87 offen sc1\n"
L_"v[172:175],v255,s[64:67],s87 offen offset:256 sc1\n"
"s_lshl_b32 s87, s35, 24\n"
"s_add_u32 s87, s87, 49152\n"
"s_bfm_b64 exec, 48, 16\n"
L_"v[176:179],v255,s[64:67],s87 offen sc1\n"
L_"v[180:183],v255,s[64:67],s87 offen offset:256 sc1\n"
"s_mov_b64 exec, -1\n"
"s_lshl_b32 s87, s35, 24\n"
"s_add_u32 s87, s87, 53248\n"
L_"v[184:187],v255,s[64:67],s87 offen sc1\n"
L_"v[188:191],v255,s[64:67],s87 offen offset:256 sc1\n"
"s_barrier\n"
"s_add_u32 m0, s84, 50688\n"
"s_mov_b32 s86, 344192\n"
L_"v124,s[48:51],s86 offen lds\n"
L_"v125,s[48:51],s86 offen offset:1024 lds\n"
"s_add_u32 m0, s84, 84480\n"
"s_mov_b32 s86, 401536\n"
L_"v124,s[48:51],s86 offen lds\n"
L_"v125,s[48:51],s86 offen offset:1024 lds\n"
"s_add_u32 m0, s84, 92928\n"
"s_mov_b32 s86, 256\n"
L_"v124,s[48:51],s86 offen lds\n"
L_"v125,s[48:51],s86 offen offset:1024 lds\n"
"s_add_u32 m0, s84, 143616\n"
"s_mov_b32 s86, 57600\n"
L_"v124,s[48:51],s86 offen lds\n"
L_"v125,s[48:51],s86 offen offset:1024 lds\n"
"s_barrier\n"
"s_add_u32 m0, s84, 0\n"
"s_mov_b32 s86, 229632\n"
L_"v124,s[48:51],s86 offen lds\n"
L_"v125,s[48:51],s86 offen offset:1024 lds\n"
"s_add_u32 m0, s84, 8448\n"
"s_mov_b32 s86, 286976\n"
L_"v124,s[48:51],s86 offen lds\n"
L_"v125,s[48:51],s86 offen offset:1024 lds\n"
"s_add_u32 m0, s84, 16896\n"
"s_mov_b32 s86, 114944\n"
L_"v124,s[48:51],s86 offen lds\n"
L_"v125,s[48:51],s86 offen offset:1024 lds\n"
"s_add_u32 m0, s84, 25344\n"
"s_mov_b32 s86, 172288\n"
L_"v124,s[48:51],s86 offen lds\n"
L_"v125,s[48:51],s86 offen offset:1024 lds\n"
W_"vmcnt(12)\n"
"s_barrier\n"
"s_add_u32 m0, s84, 42240\n"
"s_mov_b32 s86, 344320\n"
L_"v124,s[48:51],s86 offen lds\n"
L_"v125,s[48:51],s86 offen offset:1024 lds\n"
"s_add_u32 m0, s84, 59136\n"
"s_mov_b32 s86, 401664\n"
L_"v124,s[48:51],s86 offen lds\n"
L_"v125,s[48:51],s86 offen offset:1024 lds\n"
"s_add_u32 m0, s84, 67584\n"
"s_mov_b32 s86, 384\n"
L_"v124,s[48:51],s86 offen lds\n"
L_"v125,s[48:51],s86 offen offset:1024 lds\n"
W_"vmcnt(10)\n"
"s_barrier\n"
"s_add_u32 m0, s84, 33792\n"
"s_mov_b32 s86, 57728\n"
L_"v124,s[48:51],s86 offen lds\n"
L_"v125,s[48:51],s86 offen offset:1024 lds\n"
"s_add_u32 m0, s84, 76032\n"
"s_mov_b32 s86, 229760\n"
L_"v124,s[48:51],s86 offen lds\n"
L_"v125,s[48:51],s86 offen offset:1024 lds\n"
"s_add_u32 m0, s84, 101376\n"
"s_mov_b32 s86, 287104\n"
L_"v124,s[48:51],s86 offen lds\n"
L_"v125,s[48:51],s86 offen offset:1024 lds\n"
"s_add_u32 m0, s84, 109824\n"
"s_mov_b32 s86, 115072\n"
L_"v124,s[48:51],s86 offen lds\n"
L_"v125,s[48:51],s86 offen offset:1024 lds\n"
W_"vmcnt(10)\n"
"s_barrier\n"
"s_add_u32 m0, s84, 50688\n"
"s_mov_b32 s86, 172416\n"
L_"v124,s[48:51],s86 offen lds\n"
L_"v125,s[48:51],s86 offen offset:1024 lds\n"
"s_add_u32 m0, s84, 84480\n"
"s_mov_b32 s86, 344448\n"
L_"v124,s[48:51],s86 offen lds\n"
L_"v125,s[48:51],s86 offen offset:1024 lds\n"
"s_add_u32 m0, s84, 92928\n"
"s_mov_b32 s86, 401792\n"
L_"v124,s[48:51],s86 offen lds\n"
L_"v125,s[48:51],s86 offen offset:1024 lds\n"
"s_add_u32 m0, s84, 118272\n"
"s_mov_b32 s86, 512\n"
L_"v124,s[48:51],s86 offen lds\n"
L_"v125,s[48:51],s86 offen offset:1024 lds\n"
W_"vmcnt(10)\n"
"s_barrier\n"
"s_add_u32 m0, s84, 0\n"
"s_mov_b32 s86, 57856\n"
L_"v124,s[48:51],s86 offen lds\n"
L_"v125,s[48:51],s86 offen offset:1024 lds\n"
"s_add_u32 m0, s84, 8448\n"
"s_mov_b32 s86, 229888\n"
L_"v124,s[48:51],s86 offen lds\n"
L_"v125,s[48:51],s86 offen offset:1024 lds\n"
"s_add_u32 m0, s84, 16896\n"
"s_mov_b32 s86, 287232\n"
L_"v124,s[48:51],s86 offen lds\n"
L_"v125,s[48:51],s86 offen offset:1024 lds\n"
W_"vmcnt(8)\n"
"s_barrier\n"
"s_add_u32 m0, s84, 25344\n"
"s_mov_b32 s86, 115200\n"
L_"v124,s[48:51],s86 offen lds\n"
"s_setprio 2\n"
M_"v[48:51],v[112:115],v[128:131],0\n"
M_"v[48:51],v[116:119],v[132:135],v[48:51]\n"
M_"v[48:51],v[112:115],v[136:139],v[48:51]\n"
M_"v[48:51],v[116:119],v[140:143],v[48:51]\n"
"s_setprio 0\n"
L_"v125,s[48:51],s86 offen offset:1024 lds\n"
"v_lshlrev_b32 v64, 16, v8\n"
"v_and_b32 v65, s93, v8\n"
"v_lshlrev_b32 v66, 16, v9\n"
"v_and_b32 v67, s93, v9\n"
"v_lshlrev_b32 v108, 16, v40\n"
"s_add_u32 m0, s84, 33792\n"
"s_mov_b32 s86, 172544\n"
L_"v124,s[48:51],s86 offen lds\n"
"v_and_b32 v109, s93, v40\n"
"v_lshlrev_b32 v110, 16, v41\n"
"v_and_b32 v111, s93, v41\n"
"v_fma_f32 v48, v48, s96, v108\n"
L_"v125,s[48:51],s86 offen offset:1024 lds\n"
"v_fma_f32 v49, v49, s96, v109\n"
"v_fma_f32 v50, v50, s96, v110\n"
"v_fma_f32 v51, v51, s96, v111\n"
"v_mul_f32 v104, s97, v48\n"
"v_mul_f32 v105, s97, v49\n"
"s_add_u32 m0, s84, 42240\n"
"s_mov_b32 s86, 344576\n"
L_"v124,s[48:51],s86 offen lds\n"
"v_mul_f32 v106, s97, v50\n"
"v_mul_f32 v107, s97, v51\n"
E_"v104,v104\n"
E_"v105,v105\n"
E_"v106,v106\n"
L_"v125,s[48:51],s86 offen offset:1024 lds\n"
E_"v107,v107\n"
"v_add_f32 v104, 1.0, v104\n"
"v_add_f32 v105, 1.0, v105\n"
"v_add_f32 v106, 1.0, v106\n"
"s_add_u32 m0, s84, 59136\n"
"s_mov_b32 s86, 401920\n"
L_"v124,s[48:51],s86 offen lds\n"
"v_add_f32 v107, 1.0, v107\n"
"v_rcp_f32 v104, v104\n"
"v_rcp_f32 v105, v105\n"
"v_rcp_f32 v106, v106\n"
"v_rcp_f32 v107, v107\n"
L_"v125,s[48:51],s86 offen offset:1024 lds\n"
"v_mul_f32 v48, v48, v104\n"
"v_mul_f32 v49, v49, v105\n"
"v_mul_f32 v50, v50, v106\n"
"v_mul_f32 v51, v51, v107\n"
W_"vmcnt(8)\n"
"s_barrier\n"
"s_add_u32 m0, s84, 50688\n"
"s_mov_b32 s86, 640\n"
L_"v124,s[48:51],s86 offen lds\n"
"s_setprio 2\n"
M_"v[52:55],v[112:115],v[144:147],0\n"
M_"v[52:55],v[116:119],v[148:151],v[52:55]\n"
M_"v[52:55],v[112:115],v[152:155],v[52:55]\n"
M_"v[52:55],v[116:119],v[156:159],v[52:55]\n"
"s_setprio 0\n"
L_"v125,s[48:51],s86 offen offset:1024 lds\n"
"v_lshlrev_b32 v68, 16, v12\n"
"v_and_b32 v69, s93, v12\n"
"v_lshlrev_b32 v70, 16, v13\n"
"v_and_b32 v71, s93, v13\n"
"v_lshlrev_b32 v108, 16, v42\n"
"s_add_u32 m0, s84, 67584\n"
"s_mov_b32 s86, 57984\n"
L_"v124,s[48:51],s86 offen lds\n"
"v_and_b32 v109, s93, v42\n"
"v_lshlrev_b32 v110, 16, v43\n"
"v_and_b32 v111, s93, v43\n"
"v_fma_f32 v52, v52, s96, v108\n"
L_"v125,s[48:51],s86 offen offset:1024 lds\n"
"v_fma_f32 v53, v53, s96, v109\n"
"v_fma_f32 v54, v54, s96, v110\n"
"v_fma_f32 v55, v55, s96, v111\n"
"v_mul_f32 v104, s97, v52\n"
"v_mul_f32 v105, s97, v53\n"
"s_add_u32 m0, s84, 76032\n"
"s_mov_b32 s86, 230016\n"
L_"v124,s[48:51],s86 offen lds\n"
"v_mul_f32 v106, s97, v54\n"
"v_mul_f32 v107, s97, v55\n"
E_"v104,v104\n"
E_"v105,v105\n"
E_"v106,v106\n"
L_"v125,s[48:51],s86 offen offset:1024 lds\n"
E_"v107,v107\n"
"v_add_f32 v104, 1.0, v104\n"
"v_add_f32 v105, 1.0, v105\n"
"v_add_f32 v106, 1.0, v106\n"
"s_add_u32 m0, s84, 101376\n"
"s_mov_b32 s86, 287360\n"
L_"v124,s[48:51],s86 offen lds\n"
"v_add_f32 v107, 1.0, v107\n"
"v_rcp_f32 v104, v104\n"
"v_rcp_f32 v105, v105\n"
"v_rcp_f32 v106, v106\n"
"v_rcp_f32 v107, v107\n"
L_"v125,s[48:51],s86 offen offset:1024 lds\n"
"v_mul_f32 v52, v52, v104\n"
"v_mul_f32 v53, v53, v105\n"
"v_mul_f32 v54, v54, v106\n"
"v_mul_f32 v55, v55, v107\n"
W_"vmcnt(8)\n"
"s_barrier\n"
"s_add_u32 m0, s84, 0\n"
"s_mov_b32 s86, 115328\n"
L_"v124,s[48:51],s86 offen lds\n"
"s_setprio 2\n"
M_"v[56:59],v[112:115],v[160:163],0\n"
M_"v[56:59],v[116:119],v[164:167],v[56:59]\n"
M_"v[56:59],v[112:115],v[168:171],v[56:59]\n"
M_"v[56:59],v[116:119],v[172:175],v[56:59]\n"
"s_setprio 0\n"
L_"v125,s[48:51],s86 offen offset:1024 lds\n"
"v_lshlrev_b32 v72, 16, v14\n"
"v_and_b32 v73, s93, v14\n"
"v_lshlrev_b32 v74, 16, v15\n"
"v_and_b32 v75, s93, v15\n"
"v_lshlrev_b32 v108, 16, v44\n"
"s_add_u32 m0, s84, 84480\n"
"s_mov_b32 s86, 172672\n"
L_"v124,s[48:51],s86 offen lds\n"
"v_and_b32 v109, s93, v44\n"
"v_lshlrev_b32 v110, 16, v45\n"
"v_and_b32 v111, s93, v45\n"
"v_fma_f32 v56, v56, s96, v108\n"
L_"v125,s[48:51],s86 offen offset:1024 lds\n"
"v_fma_f32 v57, v57, s96, v109\n"
"v_fma_f32 v58, v58, s96, v110\n"
"v_fma_f32 v59, v59, s96, v111\n"
"v_mul_f32 v104, s97, v56\n"
"v_mul_f32 v105, s97, v57\n"
"s_add_u32 m0, s84, 92928\n"
"s_mov_b32 s86, 344704\n"
L_"v124,s[48:51],s86 offen lds\n"
"v_mul_f32 v106, s97, v58\n"
"v_mul_f32 v107, s97, v59\n"
E_"v104,v104\n"
E_"v105,v105\n"
E_"v106,v106\n"
L_"v125,s[48:51],s86 offen offset:1024 lds\n"
E_"v107,v107\n"
"v_add_f32 v104, 1.0, v104\n"
"v_add_f32 v105, 1.0, v105\n"
"v_add_f32 v106, 1.0, v106\n"
"s_add_u32 m0, s84, 109824\n"
"s_mov_b32 s86, 402048\n"
L_"v124,s[48:51],s86 offen lds\n"
"v_add_f32 v107, 1.0, v107\n"
"v_rcp_f32 v104, v104\n"
"v_rcp_f32 v105, v105\n"
"v_rcp_f32 v106, v106\n"
"v_rcp_f32 v107, v107\n"
L_"v125,s[48:51],s86 offen offset:1024 lds\n"
"v_mul_f32 v56, v56, v104\n"
"v_mul_f32 v57, v57, v105\n"
"v_mul_f32 v58, v58, v106\n"
"v_mul_f32 v59, v59, v107\n"
W_"vmcnt(8)\n"
"s_barrier\n"
"s_add_u32 m0, s84, 8448\n"
"s_mov_b32 s86, 768\n"
L_"v124,s[48:51],s86 offen lds\n"
"s_setprio 2\n"
M_"v[60:63],v[112:115],v[176:179],0\n"
M_"v[60:63],v[116:119],v[180:183],v[60:63]\n"
M_"v[60:63],v[112:115],v[184:187],v[60:63]\n"
M_"v[60:63],v[116:119],v[188:191],v[60:63]\n"
"s_setprio 0\n"
L_"v125,s[48:51],s86 offen offset:1024 lds\n"
"v_lshlrev_b32 v76, 16, v126\n"
"v_and_b32 v77, s93, v126\n"
"v_lshlrev_b32 v78, 16, v127\n"
"v_and_b32 v79, s93, v127\n"
"v_lshlrev_b32 v108, 16, v46\n"
"s_add_u32 m0, s84, 16896\n"
"s_mov_b32 s86, 58112\n"
L_"v124,s[48:51],s86 offen lds\n"
"v_and_b32 v109, s93, v46\n"
"v_lshlrev_b32 v110, 16, v47\n"
"v_and_b32 v111, s93, v47\n"
"v_fma_f32 v60, v60, s96, v108\n"
L_"v125,s[48:51],s86 offen offset:1024 lds\n"
"v_fma_f32 v61, v61, s96, v109\n"
"v_fma_f32 v62, v62, s96, v110\n"
"v_fma_f32 v63, v63, s96, v111\n"
"v_mul_f32 v104, s97, v60\n"
"v_mul_f32 v105, s97, v61\n"
"s_add_u32 m0, s84, 25344\n"
"s_mov_b32 s86, 230144\n"
L_"v124,s[48:51],s86 offen lds\n"
"v_mul_f32 v106, s97, v62\n"
"v_mul_f32 v107, s97, v63\n"
E_"v104,v104\n"
E_"v105,v105\n"
E_"v106,v106\n"
L_"v125,s[48:51],s86 offen offset:1024 lds\n"
E_"v107,v107\n"
"v_add_f32 v104, 1.0, v104\n"
"v_add_f32 v105, 1.0, v105\n"
"v_add_f32 v106, 1.0, v106\n"
"s_add_u32 m0, s84, 33792\n"
"s_mov_b32 s86, 287488\n"
L_"v124,s[48:51],s86 offen lds\n"
"v_add_f32 v107, 1.0, v107\n"
"v_rcp_f32 v104, v104\n"
"v_rcp_f32 v105, v105\n"
"v_rcp_f32 v106, v106\n"
"v_rcp_f32 v107, v107\n"
L_"v125,s[48:51],s86 offen offset:1024 lds\n"
"v_mul_f32 v60, v60, v104\n"
"v_mul_f32 v61, v61, v105\n"
"v_mul_f32 v62, v62, v106\n"
"v_mul_f32 v63, v63, v107\n"
W_"vmcnt(8)\n"
"s_barrier\n"
"s_add_u32 m0, s84, 42240\n"
"s_mov_b32 s86, 115456\n"
L_"v124,s[48:51],s86 offen lds\n"
L_"v125,s[48:51],s86 offen offset:1024 lds\n"
"s_add_u32 m0, s84, 50688\n"
"s_mov_b32 s86, 172800\n"
L_"v124,s[48:51],s86 offen lds\n"
L_"v125,s[48:51],s86 offen offset:1024 lds\n"
"buffer_load_dword v18, v242, s[80:83], 0 offen sc1\n"
"s_add_u32 m0, s84, 59136\n"
"s_mov_b32 s86, 344832\n"
L_"v124,s[48:51],s86 offen lds\n"
L_"v125,s[48:51],s86 offen offset:1024 lds\n"
"s_add_u32 m0, s84, 67584\n"
"s_mov_b32 s86, 402176\n"
L_"v124,s[48:51],s86 offen lds\n"
L_"v125,s[48:51],s86 offen offset:1024 lds\n"
W_"vmcnt(9)\n"
"s_barrier\n"
W_"vmcnt(4)\n"
"v_sub_u32 v18, v18, v243\n"
"v_cmp_gt_u32 vcc, 2, v18\n"
"s_mov_b32 s4, 0\n"
"s_nop 3\n"
"s_cbranch_vccz FOK2_L\n"
"FPOLL2_L:\n"
"s_sleep 2\n"
"buffer_load_dword v18, v242, s[80:83], 0 offen sc1\n"
W_"vmcnt(0)\n"
"v_sub_u32 v18, v18, v243\n"
"v_cmp_gt_u32 vcc, 2, v18\n"
"s_add_u32 s4, s4, 1\n"
"s_nop 3\n"
"s_cbranch_vccz FOK2_L\n"
"s_cmp_lt_u32 s4, 0x40000\n"
"s_cbranch_scc1 FPOLL2_L\n"
"FOK2_L:\n"
"s_mov_b32 s87, 57344\n"
"s_bfm_b64 exec, 48, 16\n"
L_"v[128:131],v255,s[64:67],s87 offen sc1\n"
L_"v[132:135],v255,s[64:67],s87 offen offset:256 sc1\n"
"s_mov_b64 exec, -1\n"
"s_mov_b32 s87, 61440\n"
L_"v[136:139],v255,s[64:67],s87 offen sc1\n"
L_"v[140:143],v255,s[64:67],s87 offen offset:256 sc1\n"
W_"vmcnt(4)\n"
"s_barrier\n"
"s_cmp_lg_u32 s35, 0\n"
"s_cbranch_scc1 NQ2_L\n"
"s_mov_b32 s87, 73728\n"
"s_bfm_b64 exec, 48, 16\n"
L_"v[144:147],v255,s[64:67],s87 offen sc1\n"
L_"v[148:151],v255,s[64:67],s87 offen offset:256 sc1\n"
"s_mov_b64 exec, -1\n"
"s_mov_b32 s87, 77824\n"
L_"v[152:155],v255,s[64:67],s87 offen sc1\n"
L_"v[156:159],v255,s[64:67],s87 offen offset:256 sc1\n"
"NQ2_L:\n"
"s_barrier\n"
"buffer_load_dword v18, v242, s[80:83], 0 offen sc1\n"
"s_setprio 2\n"
"s_mov_b64 exec, 0xffff\n"
R_"v[128:131],v6 offset:0\n"
R_"v[132:135],v6 offset:256\n"
R_"v[144:147],v6 offset:2048\n"
R_"v[148:151],v6 offset:2304\n"
R_"v[160:163],v6 offset:4096\n"
R_"v[164:167],v6 offset:4352\n"
R_"v[176:179],v6 offset:6144\n"
R_"v[180:183],v6 offset:6400\n"
"s_mov_b64 exec, -1\n"
W_"lgkmcnt(0)\n"
"s_cmp_lg_u32 s35, 0\n"
"s_cbranch_scc1 ODD_L\n"
W_"vmcnt(1)\n"
M_"v[108:111],v[112:115],v[128:131],0\n"
M_"v[100:103],v[112:115],v[144:147],0\n"
M_"v[108:111],v[116:119],v[132:135],v[108:111]\n"
M_"v[100:103],v[116:119],v[148:151],v[100:103]\n"
M_"v[108:111],v[112:115],v[136:139],v[108:111]\n"
M_"v[100:103],v[112:115],v[152:155],v[100:103]\n"
M_"v[108:111],v[116:119],v[140:143],v[108:111]\n"
M_"v[100:103],v[116:119],v[156:159],v[100:103]\n"
"s_setprio 0\n"
"s_nop 5\n"
"v_fma_f32 v64, v108, v48, v64\n"
"v_fma_f32 v65, v109, v49, v65\n"
"v_fma_f32 v66, v110, v50, v66\n"
"v_fma_f32 v67, v111, v51, v67\n"
C_"v64,v64,v65\n"
C_"v65,v66,v67\n"
"buffer_store_dwordx2 v[64:65], v245, s[72:75], 0 offen\n"
"v_fma_f32 v68, v100, v52, v68\n"
"v_fma_f32 v69, v101, v53, v69\n"
"v_fma_f32 v70, v102, v54, v70\n"
"v_fma_f32 v71, v103, v55, v71\n"
C_"v68,v68,v69\n"
C_"v69,v70,v71\n"
"buffer_store_dwordx2 v[68:69], v245, s[72:75], 0 offen offset:256\n"
W_"vmcnt(2)\n"
"v_sub_u32 v18, v18, v243\n"
"v_cmp_gt_u32 vcc, 3, v18\n"
"s_mov_b32 s4, 0\n"
"s_nop 3\n"
"s_cbranch_vccz FOK3_L\n"
"FPOLL3_L:\n"
"s_sleep 2\n"
"buffer_load_dword v18, v242, s[80:83], 0 offen sc1\n"
W_"vmcnt(0)\n"
"v_sub_u32 v18, v18, v243\n"
"v_cmp_gt_u32 vcc, 3, v18\n"
"s_add_u32 s4, s4, 1\n"
"s_nop 3\n"
"s_cbranch_vccz FOK3_L\n"
"s_cmp_lt_u32 s4, 0x40000\n"
"s_cbranch_scc1 FPOLL3_L\n"
"FOK3_L:\n"
"s_mov_b32 s87, 90112\n"
"s_bfm_b64 exec, 48, 16\n"
L_"v[160:163],v255,s[64:67],s87 offen sc1\n"
L_"v[164:167],v255,s[64:67],s87 offen offset:256 sc1\n"
"s_mov_b64 exec, -1\n"
"PTL5:\n"
"s_cbranch_execz PTL13\n"
"s_mov_b32 s87, 94208\n"
L_"v[168:171],v255,s[64:67],s87 offen sc1\n"
L_"v[172:175],v255,s[64:67],s87 offen offset:256 sc1\n"
"s_mov_b32 s87, 106496\n"
"s_bfm_b64 exec, 48, 16\n"
L_"v[176:179],v255,s[64:67],s87 offen sc1\n"
L_"v[180:183],v255,s[64:67],s87 offen offset:256 sc1\n"
"s_mov_b64 exec, -1\n"
"s_mov_b32 s87, 110592\n"
L_"v[184:187],v255,s[64:67],s87 offen sc1\n"
L_"v[188:191],v255,s[64:67],s87 offen offset:256 sc1\n"
"s_setprio 2\n"
W_"vmcnt(7)\n"
M_"v[108:111],v[112:115],v[160:163],0\n"
W_"vmcnt(6)\n"
M_"v[108:111],v[116:119],v[164:167],v[108:111]\n"
W_"vmcnt(5)\n"
M_"v[108:111],v[112:115],v[168:171],v[108:111]\n"
W_"vmcnt(4)\n"
M_"v[108:111],v[116:119],v[172:175],v[108:111]\n"
"s_setprio 0\n"
"s_nop 6\n"
"v_fma_f32 v72, v108, v56, v72\n"
"v_fma_f32 v73, v109, v57, v73\n"
"v_fma_f32 v74, v110, v58, v74\n"
"v_fma_f32 v75, v111, v59, v75\n"
C_"v72,v72,v73\n"
C_"v73,v74,v75\n"
"buffer_store_dwordx2 v[72:73], v245, s[72:75], 0 offen offset:512\n"
W_"vmcnt(1)\n"
"v_mov_b32 v191, v191\n"
"s_mov_b32 s4, 0\n"
"SCL_L:\n"
"v_max3_u32 v104, v176, v177, v178\n"
"v_max3_u32 v105, v179, v180, v181\n"
"v_max3_u32 v104, v104, v182, v183\n"
"v_max3_u32 v105, v105, v184, v185\n"
"v_max3_u32 v104, v104, v186, v187\n"
"v_max3_u32 v105, v105, v188, v189\n"
"v_max3_u32 v104, v104, v190, v191\n"
"v_max_u32 v104, v104, v105\n"
"v_cmp_eq_u32 vcc, -1, v104\n"
"s_add_u32 s4, s4, 1\n"
"s_nop 3\n"
"s_cbranch_vccz SCD_L\n"
"s_cmp_lt_u32 s4, 0x40000\n"
"s_cbranch_scc0 SCD_L\n"
"s_sleep 10\n"
"s_mov_b32 s87, 106496\n"
"s_bfm_b64 exec, 48, 16\n"
L_"v[176:179],v255,s[64:67],s87 offen sc1\n"
L_"v[180:183],v255,s[64:67],s87 offen offset:256 sc1\n"
"s_mov_b64 exec, -1\n"
"s_mov_b32 s87, 110592\n"
L_"v[184:187],v255,s[64:67],s87 offen sc1\n"
L_"v[188:191],v255,s[64:67],s87 offen offset:256 sc1\n"
W_"vmcnt(0)\n"
"s_branch SCL_L\n"
"SCD_L:\n"
"s_setprio 2\n"
M_"v[108:111],v[112:115],v[176:179],0\n"
M_"v[108:111],v[116:119],v[180:183],v[108:111]\n"
M_"v[108:111],v[112:115],v[184:187],v[108:111]\n"
M_"v[108:111],v[116:119],v[188:191],v[108:111]\n"
"s_setprio 0\n"
"s_nop 6\n"
"v_fma_f32 v76, v108, v60, v76\n"
"v_fma_f32 v77, v109, v61, v77\n"
"v_fma_f32 v78, v110, v62, v78\n"
"v_fma_f32 v79, v111, v63, v79\n"
C_"v76,v76,v77\n"
C_"v77,v78,v79\n"
"buffer_store_dwordx2 v[76:77], v245, s[72:75], 0 offen offset:768\n"
"s_branch DONE_L\n"
"ODD_L:\n"
"PTL6:\n"
"s_cbranch_execz PTL14\n"
W_"vmcnt(1)\n"
"s_setprio 2\n"
M_"v[108:111],v[112:115],v[128:131],0\n"
M_"v[108:111],v[116:119],v[132:135],v[108:111]\n"
M_"v[108:111],v[112:115],v[136:139],v[108:111]\n"
M_"v[108:111],v[116:119],v[140:143],v[108:111]\n"
"s_setprio 0\n"
"s_nop 6\n"
"v_fma_f32 v64, v108, v48, v64\n"
"v_fma_f32 v65, v109, v49, v65\n"
"v_fma_f32 v66, v110, v50, v66\n"
"v_fma_f32 v67, v111, v51, v67\n"
C_"v64,v64,v65\n"
C_"v65,v66,v67\n"
"buffer_store_dwordx2 v[64:65], v245, s[72:75], 0 offen\n"
W_"vmcnt(1)\n"
"v_sub_u32 v18, v18, v243\n"
"v_cmp_gt_u32 vcc, 3, v18\n"
"s_mov_b32 s4, 0\n"
"s_nop 3\n"
"s_cbranch_vccz FOKB3_L\n"
"FPOLLB3_L:\n"
"s_sleep 2\n"
"buffer_load_dword v18, v242, s[80:83], 0 offen sc1\n"
W_"vmcnt(0)\n"
"v_sub_u32 v18, v18, v243\n"
"v_cmp_gt_u32 vcc, 3, v18\n"
"s_add_u32 s4, s4, 1\n"
"s_nop 3\n"
"s_cbranch_vccz FOKB3_L\n"
"s_cmp_lt_u32 s4, 0x40000\n"
"s_cbranch_scc1 FPOLLB3_L\n"
"FOKB3_L:\n"
"s_mov_b32 s87, 73728\n"
"s_bfm_b64 exec, 48, 16\n"
L_"v[144:147],v255,s[64:67],s87 offen sc1\n"
L_"v[148:151],v255,s[64:67],s87 offen offset:256 sc1\n"
"s_mov_b64 exec, -1\n"
"s_mov_b32 s87, 77824\n"
L_"v[152:155],v255,s[64:67],s87 offen sc1\n"
L_"v[156:159],v255,s[64:67],s87 offen offset:256 sc1\n"
"s_mov_b32 s87, 90112\n"
"s_bfm_b64 exec, 48, 16\n"
L_"v[160:163],v255,s[64:67],s87 offen sc1\n"
L_"v[164:167],v255,s[64:67],s87 offen offset:256 sc1\n"
"s_mov_b64 exec, -1\n"
"s_mov_b32 s87, 94208\n"
L_"v[168:171],v255,s[64:67],s87 offen sc1\n"
L_"v[172:175],v255,s[64:67],s87 offen offset:256 sc1\n"
"s_setprio 2\n"
W_"vmcnt(7)\n"
M_"v[108:111],v[112:115],v[144:147],0\n"
W_"vmcnt(6)\n"
M_"v[108:111],v[116:119],v[148:151],v[108:111]\n"
W_"vmcnt(5)\n"
M_"v[108:111],v[112:115],v[152:155],v[108:111]\n"
W_"vmcnt(4)\n"
M_"v[108:111],v[116:119],v[156:159],v[108:111]\n"
"s_setprio 0\n"
"s_nop 6\n"
"v_fma_f32 v68, v108, v52, v68\n"
"v_fma_f32 v69, v109, v53, v69\n"
"v_fma_f32 v70, v110, v54, v70\n"
"v_fma_f32 v71, v111, v55, v71\n"
C_"v68,v68,v69\n"
C_"v69,v70,v71\n"
"buffer_store_dwordx2 v[68:69], v245, s[72:75], 0 offen offset:256\n"
W_"vmcnt(1)\n"
"v_mov_b32 v175, v175\n"
"s_mov_b32 s4, 0\n"
"SDL_L:\n"
"v_max3_u32 v104, v160, v161, v162\n"
"v_max3_u32 v105, v163, v164, v165\n"
"v_max3_u32 v104, v104, v166, v167\n"
"v_max3_u32 v105, v105, v168, v169\n"
"v_max3_u32 v104, v104, v170, v171\n"
"v_max3_u32 v105, v105, v172, v173\n"
"v_max3_u32 v104, v104, v174, v175\n"
"v_max_u32 v104, v104, v105\n"
"v_cmp_eq_u32 vcc, -1, v104\n"
"s_add_u32 s4, s4, 1\n"
"s_nop 3\n"
"s_cbranch_vccz SDD_L\n"
"s_cmp_lt_u32 s4, 0x40000\n"
"s_cbranch_scc0 SDD_L\n"
"s_sleep 10\n"
"s_mov_b32 s87, 90112\n"
"s_bfm_b64 exec, 48, 16\n"
L_"v[160:163],v255,s[64:67],s87 offen sc1\n"
L_"v[164:167],v255,s[64:67],s87 offen offset:256 sc1\n"
"s_mov_b64 exec, -1\n"
"s_mov_b32 s87, 94208\n"
L_"v[168:171],v255,s[64:67],s87 offen sc1\n"
L_"v[172:175],v255,s[64:67],s87 offen offset:256 sc1\n"
W_"vmcnt(0)\n"
"s_branch SDL_L\n"
"SDD_L:\n"
"s_setprio 2\n"
M_"v[108:111],v[112:115],v[160:163],0\n"
M_"v[108:111],v[116:119],v[164:167],v[108:111]\n"
M_"v[108:111],v[112:115],v[168:171],v[108:111]\n"
M_"v[108:111],v[116:119],v[172:175],v[108:111]\n"
"s_setprio 0\n"
"s_nop 6\n"
"v_fma_f32 v72, v108, v56, v72\n"
"v_fma_f32 v73, v109, v57, v73\n"
"v_fma_f32 v74, v110, v58, v74\n"
"v_fma_f32 v75, v111, v59, v75\n"
C_"v72,v72,v73\n"
C_"v73,v74,v75\n"
"buffer_store_dwordx2 v[72:73], v245, s[72:75], 0 offen offset:512\n"
"DONE_L:\n"
"s_endpgm\n"
".p2align 6\n"
"AROLE_L:\n"
"PTL7:\n"
"s_cbranch_execz PTL15\n"
"s_add_u32 m0, s84, 0\n"
"s_mov_b32 s86, 0\n"
L_"v8,s[36:39],s86 offen lds\n"
L_"v9,s[36:39],s86 offen offset:1024 lds\n"
"s_add_u32 m0, s84, 8448\n"
"s_mov_b32 s86, 32768\n"
L_"v8,s[36:39],s86 offen lds\n"
L_"v9,s[36:39],s86 offen offset:1024 lds\n"
"s_add_u32 m0, s84, 16896\n"
"s_mov_b32 s86, 256\n"
L_"v8,s[36:39],s86 offen lds\n"
L_"v9,s[36:39],s86 offen offset:1024 lds\n"
"s_add_u32 m0, s84, 25344\n"
"s_mov_b32 s86, 33024\n"
L_"v8,s[36:39],s86 offen lds\n"
L_"v9,s[36:39],s86 offen offset:1024 lds\n"
"s_add_u32 m0, s84, 33792\n"
"s_mov_b32 s86, 262144\n"
L_"v8,s[36:39],s86 offen lds\n"
L_"v9,s[36:39],s86 offen offset:1024 lds\n"
"s_add_u32 m0, s84, 42240\n"
"s_mov_b32 s86, 294912\n"
L_"v8,s[36:39],s86 offen lds\n"
L_"v9,s[36:39],s86 offen offset:1024 lds\n"
"s_add_u32 m0, s84, 50688\n"
"s_mov_b32 s86, 262400\n"
L_"v8,s[36:39],s86 offen lds\n"
L_"v9,s[36:39],s86 offen offset:1024 lds\n"
"s_add_u32 m0, s84, 59136\n"
"s_mov_b32 s86, 295168\n"
L_"v8,s[36:39],s86 offen lds\n"
L_"v9,s[36:39],s86 offen offset:1024 lds\n"
"s_bfm_b64 exec, 44, 0\n"
"buffer_load_dword v6, v20, s[36:39], 0 offen\n"
"s_mov_b64 exec, -1\n"
"s_bfm_b64 exec, 8, 0\n"
"buffer_load_dword v6, v21, s[40:43], 0 offen offset:64\n"
"s_mov_b64 exec, -1\n"
"buffer_load_dword v6, v24, s[72:75], 0 offen offset:64\n"
"v_mov_b32 v0, v11\n"
"s_setprio 1\n"
W_"vmcnt(11) lgkmcnt(6)\n"
"s_barrier\n"
R_"v[40:43],v4 offset:0\n"
R_"v[44:47],v4 offset:1024\n"
T_"v[8:9],v1 offset:0\n"
T_"v[10:11],v1 offset:8448\n"
T_"v[12:13],v1 offset:8\n"
T_"v[14:15],v1 offset:8456\n"
T_"v[16:17],v1 offset:256\n"
T_"v[18:19],v1 offset:8704\n"
T_"v[20:21],v1 offset:264\n"
T_"v[22:23],v1 offset:8712\n"
T_"v[24:25],v1 offset:512\n"
T_"v[26:27],v1 offset:8960\n"
T_"v[28:29],v1 offset:520\n"
T_"v[30:31],v1 offset:8968\n"
W_"lgkmcnt(10)\n"
M_"v[128:131],v[8:11],v[40:43],0\n"
T_"v[32:33],v1 offset:768\n"
M_"v[132:135],v[8:11],v[44:47],0\n"
W_"lgkmcnt(9)\n"
T_"v[34:35],v1 offset:9216\n"
M_"v[136:139],v[12:15],v[40:43],0\n"
T_"v[36:37],v1 offset:776\n"
M_"v[140:143],v[12:15],v[44:47],0\n"
W_"lgkmcnt(9)\n"
T_"v[38:39],v1 offset:9224\n"
M_"v[144:147],v[16:19],v[40:43],0\n"
T_"v[8:9],v1 offset:16896\n"
R_"v[48:51],v4 offset:4096\n"
M_"v[148:151],v[16:19],v[44:47],0\n"
W_"lgkmcnt(10)\n"
T_"v[10:11],v1 offset:25344\n"
M_"v[152:155],v[20:23],v[40:43],0\n"
T_"v[12:13],v1 offset:16904\n"
M_"v[156:159],v[20:23],v[44:47],0\n"
W_"lgkmcnt(7)\n"
T_"v[14:15],v1 offset:25352\n"
M_"v[160:163],v[24:27],v[40:43],0\n"
T_"v[16:17],v1 offset:17152\n"
R_"v[52:55],v4 offset:5120\n"
M_"v[164:167],v[24:27],v[44:47],0\n"
T_"v[18:19],v1 offset:25600\n"
M_"v[168:171],v[28:31],v[40:43],0\n"
T_"v[20:21],v1 offset:17160\n"
M_"v[172:175],v[28:31],v[44:47],0\n"
W_"lgkmcnt(7)\n"
T_"v[22:23],v1 offset:25608\n"
M_"v[176:179],v[32:35],v[40:43],0\n"
T_"v[24:25],v1 offset:17408\n"
M_"v[180:183],v[32:35],v[44:47],0\n"
T_"v[26:27],v1 offset:25856\n"
M_"v[184:187],v[36:39],v[40:43],0\n"
T_"v[28:29],v1 offset:17416\n"
M_"v[188:191],v[36:39],v[44:47],0\n"
W_"lgkmcnt(6)\n"
T_"v[30:31],v1 offset:25864\n"
M_"v[192:195],v[8:11],v[40:43],0\n"
T_"v[32:33],v1 offset:17664\n"
M_"v[196:199],v[8:11],v[44:47],0\n"
T_"v[34:35],v1 offset:26112\n"
M_"v[200:203],v[12:15],v[40:43],0\n"
T_"v[36:37],v1 offset:17672\n"
M_"v[204:207],v[12:15],v[44:47],0\n"
W_"lgkmcnt(6)\n"
T_"v[38:39],v1 offset:26120\n"
M_"v[208:211],v[16:19],v[40:43],0\n"
W_"vmcnt(3)\n"
"s_barrier\n"
T_"v[8:9],v1 offset:33792\n"
M_"v[212:215],v[16:19],v[44:47],0\n"
T_"v[10:11],v1 offset:42240\n"
M_"v[216:219],v[20:23],v[40:43],0\n"
T_"v[12:13],v1 offset:33800\n"
M_"v[220:223],v[20:23],v[44:47],0\n"
W_"lgkmcnt(6)\n"
T_"v[14:15],v1 offset:42248\n"
M_"v[224:227],v[24:27],v[40:43],0\n"
T_"v[16:17],v1 offset:34048\n"
M_"v[228:231],v[24:27],v[44:47],0\n"
T_"v[18:19],v1 offset:42496\n"
M_"v[232:235],v[28:31],v[40:43],0\n"
T_"v[20:21],v1 offset:34056\n"
M_"v[236:239],v[28:31],v[44:47],0\n"
W_"lgkmcnt(6)\n"
T_"v[22:23],v1 offset:42504\n"
M_"v[240:243],v[32:35],v[40:43],0\n"
T_"v[24:25],v1 offset:34304\n"
M_"v[244:247],v[32:35],v[44:47],0\n"
T_"v[26:27],v1 offset:42752\n"
M_"v[248:251],v[36:39],v[40:43],0\n"
T_"v[28:29],v1 offset:34312\n"
M_"v[252:255],v[36:39],v[44:47],0\n"
W_"lgkmcnt(6)\n"
T_"v[30:31],v1 offset:42760\n"
M_"v[128:131],v[8:11],v[48:51],v[128:131]\n"
T_"v[32:33],v1 offset:34560\n"
M_"v[132:135],v[8:11],v[52:55],v[132:135]\n"
T_"v[34:35],v1 offset:43008\n"
M_"v[136:139],v[12:15],v[48:51],v[136:139]\n"
T_"v[36:37],v1 offset:34568\n"
M_"v[140:143],v[12:15],v[52:55],v[140:143]\n"
W_"lgkmcnt(6)\n"
T_"v[38:39],v1 offset:43016\n"
M_"v[144:147],v[16:19],v[48:51],v[144:147]\n"
T_"v[8:9],v1 offset:50688\n"
R_"v[40:43],v4 offset:8448\n"
M_"v[148:151],v[16:19],v[52:55],v[148:151]\n"
T_"v[10:11],v1 offset:59136\n"
M_"v[152:155],v[20:23],v[48:51],v[152:155]\n"
T_"v[12:13],v1 offset:50696\n"
M_"v[156:159],v[20:23],v[52:55],v[156:159]\n"
W_"lgkmcnt(7)\n"
T_"v[14:15],v1 offset:59144\n"
M_"v[160:163],v[24:27],v[48:51],v[160:163]\n"
T_"v[16:17],v1 offset:50944\n"
R_"v[44:47],v4 offset:9472\n"
M_"v[164:167],v[24:27],v[52:55],v[164:167]\n"
T_"v[18:19],v1 offset:59392\n"
M_"v[168:171],v[28:31],v[48:51],v[168:171]\n"
T_"v[20:21],v1 offset:50952\n"
M_"v[172:175],v[28:31],v[52:55],v[172:175]\n"
W_"lgkmcnt(7)\n"
T_"v[22:23],v1 offset:59400\n"
M_"v[176:179],v[32:35],v[48:51],v[176:179]\n"
T_"v[24:25],v1 offset:51200\n"
M_"v[180:183],v[32:35],v[52:55],v[180:183]\n"
T_"v[26:27],v1 offset:59648\n"
M_"v[184:187],v[36:39],v[48:51],v[184:187]\n"
T_"v[28:29],v1 offset:51208\n"
M_"v[188:191],v[36:39],v[52:55],v[188:191]\n"
W_"lgkmcnt(6)\n"
T_"v[30:31],v1 offset:59656\n"
M_"v[192:195],v[8:11],v[48:51],v[192:195]\n"
T_"v[32:33],v1 offset:51456\n"
M_"v[196:199],v[8:11],v[52:55],v[196:199]\n"
T_"v[34:35],v1 offset:59904\n"
M_"v[200:203],v[12:15],v[48:51],v[200:203]\n"
T_"v[36:37],v1 offset:51464\n"
M_"v[204:207],v[12:15],v[52:55],v[204:207]\n"
W_"lgkmcnt(6)\n"
T_"v[38:39],v1 offset:59912\n"
M_"v[208:211],v[16:19],v[48:51],v[208:211]\n"
"s_barrier\n"
T_"v[8:9],v2 offset:2048\n"
M_"v[212:215],v[16:19],v[52:55],v[212:215]\n"
T_"v[10:11],v2 offset:10496\n"
M_"v[216:219],v[20:23],v[48:51],v[216:219]\n"
T_"v[12:13],v2 offset:2056\n"
M_"v[220:223],v[20:23],v[52:55],v[220:223]\n"
W_"lgkmcnt(6)\n"
T_"v[14:15],v2 offset:10504\n"
M_"v[224:227],v[24:27],v[48:51],v[224:227]\n"
T_"v[16:17],v2 offset:2304\n"
M_"v[228:231],v[24:27],v[52:55],v[228:231]\n"
T_"v[18:19],v2 offset:10752\n"
M_"v[232:235],v[28:31],v[48:51],v[232:235]\n"
T_"v[20:21],v2 offset:2312\n"
M_"v[236:239],v[28:31],v[52:55],v[236:239]\n"
W_"lgkmcnt(6)\n"
T_"v[22:23],v2 offset:10760\n"
M_"v[240:243],v[32:35],v[48:51],v[240:243]\n"
T_"v[24:25],v2 offset:2560\n"
M_"v[244:247],v[32:35],v[52:55],v[244:247]\n"
T_"v[26:27],v2 offset:11008\n"
M_"v[248:251],v[36:39],v[48:51],v[248:251]\n"
T_"v[28:29],v2 offset:2568\n"
M_"v[252:255],v[36:39],v[52:55],v[252:255]\n"
W_"lgkmcnt(6)\n"
T_"v[30:31],v2 offset:11016\n"
M_"v[128:131],v[8:11],v[40:43],v[128:131]\n"
T_"v[32:33],v2 offset:2816\n"
M_"v[132:135],v[8:11],v[44:47],v[132:135]\n"
T_"v[34:35],v2 offset:11264\n"
M_"v[136:139],v[12:15],v[40:43],v[136:139]\n"
T_"v[36:37],v2 offset:2824\n"
M_"v[140:143],v[12:15],v[44:47],v[140:143]\n"
W_"lgkmcnt(6)\n"
T_"v[38:39],v2 offset:11272\n"
M_"v[144:147],v[16:19],v[40:43],v[144:147]\n"
T_"v[8:9],v2 offset:18944\n"
R_"v[48:51],v4 offset:12544\n"
M_"v[148:151],v[16:19],v[44:47],v[148:151]\n"
T_"v[10:11],v2 offset:27392\n"
M_"v[152:155],v[20:23],v[40:43],v[152:155]\n"
T_"v[12:13],v2 offset:18952\n"
M_"v[156:159],v[20:23],v[44:47],v[156:159]\n"
W_"lgkmcnt(7)\n"
T_"v[14:15],v2 offset:27400\n"
M_"v[160:163],v[24:27],v[40:43],v[160:163]\n"
T_"v[16:17],v2 offset:19200\n"
R_"v[52:55],v4 offset:13568\n"
M_"v[164:167],v[24:27],v[44:47],v[164:167]\n"
T_"v[18:19],v2 offset:27648\n"
M_"v[168:171],v[28:31],v[40:43],v[168:171]\n"
T_"v[20:21],v2 offset:19208\n"
M_"v[172:175],v[28:31],v[44:47],v[172:175]\n"
W_"lgkmcnt(7)\n"
T_"v[22:23],v2 offset:27656\n"
M_"v[176:179],v[32:35],v[40:43],v[176:179]\n"
T_"v[24:25],v2 offset:19456\n"
M_"v[180:183],v[32:35],v[44:47],v[180:183]\n"
T_"v[26:27],v2 offset:27904\n"
M_"v[184:187],v[36:39],v[40:43],v[184:187]\n"
T_"v[28:29],v2 offset:19464\n"
M_"v[188:191],v[36:39],v[44:47],v[188:191]\n"
W_"lgkmcnt(6)\n"
T_"v[30:31],v2 offset:27912\n"
M_"v[192:195],v[8:11],v[40:43],v[192:195]\n"
T_"v[32:33],v2 offset:19712\n"
M_"v[196:199],v[8:11],v[44:47],v[196:199]\n"
T_"v[34:35],v2 offset:28160\n"
M_"v[200:203],v[12:15],v[40:43],v[200:203]\n"
T_"v[36:37],v2 offset:19720\n"
M_"v[204:207],v[12:15],v[44:47],v[204:207]\n"
W_"lgkmcnt(6)\n"
T_"v[38:39],v2 offset:28168\n"
M_"v[208:211],v[16:19],v[40:43],v[208:211]\n"
"s_barrier\n"
T_"v[8:9],v1 offset:0\n"
M_"v[212:215],v[16:19],v[44:47],v[212:215]\n"
T_"v[10:11],v1 offset:8448\n"
M_"v[216:219],v[20:23],v[40:43],v[216:219]\n"
T_"v[12:13],v1 offset:8\n"
M_"v[220:223],v[20:23],v[44:47],v[220:223]\n"
W_"lgkmcnt(6)\n"
T_"v[14:15],v1 offset:8456\n"
M_"v[224:227],v[24:27],v[40:43],v[224:227]\n"
T_"v[16:17],v1 offset:256\n"
M_"v[228:231],v[24:27],v[44:47],v[228:231]\n"
T_"v[18:19],v1 offset:8704\n"
M_"v[232:235],v[28:31],v[40:43],v[232:235]\n"
T_"v[20:21],v1 offset:264\n"
M_"v[236:239],v[28:31],v[44:47],v[236:239]\n"
W_"lgkmcnt(6)\n"
T_"v[22:23],v1 offset:8712\n"
M_"v[240:243],v[32:35],v[40:43],v[240:243]\n"
T_"v[24:25],v1 offset:512\n"
M_"v[244:247],v[32:35],v[44:47],v[244:247]\n"
T_"v[26:27],v1 offset:8960\n"
M_"v[248:251],v[36:39],v[40:43],v[248:251]\n"
T_"v[28:29],v1 offset:520\n"
M_"v[252:255],v[36:39],v[44:47],v[252:255]\n"
W_"lgkmcnt(6)\n"
T_"v[30:31],v1 offset:8968\n"
M_"v[128:131],v[8:11],v[48:51],v[128:131]\n"
T_"v[32:33],v1 offset:768\n"
M_"v[132:135],v[8:11],v[52:55],v[132:135]\n"
T_"v[34:35],v1 offset:9216\n"
M_"v[136:139],v[12:15],v[48:51],v[136:139]\n"
T_"v[36:37],v1 offset:776\n"
M_"v[140:143],v[12:15],v[52:55],v[140:143]\n"
W_"lgkmcnt(6)\n"
T_"v[38:39],v1 offset:9224\n"
M_"v[144:147],v[16:19],v[48:51],v[144:147]\n"
T_"v[8:9],v2 offset:35840\n"
R_"v[40:43],v4 offset:16896\n"
M_"v[148:151],v[16:19],v[52:55],v[148:151]\n"
T_"v[10:11],v1 offset:16896\n"
M_"v[152:155],v[20:23],v[48:51],v[152:155]\n"
T_"v[12:13],v2 offset:35848\n"
M_"v[156:159],v[20:23],v[52:55],v[156:159]\n"
W_"lgkmcnt(7)\n"
T_"v[14:15],v1 offset:16904\n"
M_"v[160:163],v[24:27],v[48:51],v[160:163]\n"
T_"v[16:17],v2 offset:36096\n"
R_"v[44:47],v4 offset:17920\n"
M_"v[164:167],v[24:27],v[52:55],v[164:167]\n"
T_"v[18:19],v1 offset:17152\n"
M_"v[168:171],v[28:31],v[48:51],v[168:171]\n"
T_"v[20:21],v2 offset:36104\n"
M_"v[172:175],v[28:31],v[52:55],v[172:175]\n"
W_"lgkmcnt(7)\n"
T_"v[22:23],v1 offset:17160\n"
M_"v[176:179],v[32:35],v[48:51],v[176:179]\n"
T_"v[24:25],v2 offset:36352\n"
M_"v[180:183],v[32:35],v[52:55],v[180:183]\n"
T_"v[26:27],v1 offset:17408\n"
M_"v[184:187],v[36:39],v[48:51],v[184:187]\n"
T_"v[28:29],v2 offset:36360\n"
M_"v[188:191],v[36:39],v[52:55],v[188:191]\n"
W_"lgkmcnt(6)\n"
T_"v[30:31],v1 offset:17416\n"
M_"v[192:195],v[8:11],v[48:51],v[192:195]\n"
T_"v[32:33],v2 offset:36608\n"
M_"v[196:199],v[8:11],v[52:55],v[196:199]\n"
T_"v[34:35],v1 offset:17664\n"
M_"v[200:203],v[12:15],v[48:51],v[200:203]\n"
T_"v[36:37],v2 offset:36616\n"
M_"v[204:207],v[12:15],v[52:55],v[204:207]\n"
W_"lgkmcnt(6)\n"
T_"v[38:39],v1 offset:17672\n"
M_"v[208:211],v[16:19],v[48:51],v[208:211]\n"
"s_barrier\n"
T_"v[8:9],v1 offset:25344\n"
M_"v[212:215],v[16:19],v[52:55],v[212:215]\n"
T_"v[10:11],v1 offset:33792\n"
M_"v[216:219],v[20:23],v[48:51],v[216:219]\n"
T_"v[12:13],v1 offset:25352\n"
M_"v[220:223],v[20:23],v[52:55],v[220:223]\n"
W_"lgkmcnt(6)\n"
T_"v[14:15],v1 offset:33800\n"
M_"v[224:227],v[24:27],v[48:51],v[224:227]\n"
T_"v[16:17],v1 offset:25600\n"
M_"v[228:231],v[24:27],v[52:55],v[228:231]\n"
T_"v[18:19],v1 offset:34048\n"
M_"v[232:235],v[28:31],v[48:51],v[232:235]\n"
T_"v[20:21],v1 offset:25608\n"
M_"v[236:239],v[28:31],v[52:55],v[236:239]\n"
W_"lgkmcnt(6)\n"
T_"v[22:23],v1 offset:34056\n"
M_"v[240:243],v[32:35],v[48:51],v[240:243]\n"
T_"v[24:25],v1 offset:25856\n"
M_"v[244:247],v[32:35],v[52:55],v[244:247]\n"
T_"v[26:27],v1 offset:34304\n"
M_"v[248:251],v[36:39],v[48:51],v[248:251]\n"
T_"v[28:29],v1 offset:25864\n"
M_"v[252:255],v[36:39],v[52:55],v[252:255]\n"
W_"lgkmcnt(6)\n"
T_"v[30:31],v1 offset:34312\n"
M_"v[128:131],v[8:11],v[40:43],v[128:131]\n"
T_"v[32:33],v1 offset:26112\n"
M_"v[132:135],v[8:11],v[44:47],v[132:135]\n"
T_"v[34:35],v1 offset:34560\n"
M_"v[136:139],v[12:15],v[40:43],v[136:139]\n"
T_"v[36:37],v1 offset:26120\n"
M_"v[140:143],v[12:15],v[44:47],v[140:143]\n"
W_"lgkmcnt(6)\n"
T_"v[38:39],v1 offset:34568\n"
M_"v[144:147],v[16:19],v[40:43],v[144:147]\n"
T_"v[8:9],v1 offset:42240\n"
R_"v[48:51],v4 offset:20992\n"
M_"v[148:151],v[16:19],v[44:47],v[148:151]\n"
T_"v[10:11],v1 offset:50688\n"
M_"v[152:155],v[20:23],v[40:43],v[152:155]\n"
T_"v[12:13],v1 offset:42248\n"
M_"v[156:159],v[20:23],v[44:47],v[156:159]\n"
W_"lgkmcnt(7)\n"
T_"v[14:15],v1 offset:50696\n"
M_"v[160:163],v[24:27],v[40:43],v[160:163]\n"
T_"v[16:17],v1 offset:42496\n"
R_"v[52:55],v4 offset:22016\n"
M_"v[164:167],v[24:27],v[44:47],v[164:167]\n"
T_"v[18:19],v1 offset:50944\n"
M_"v[168:171],v[28:31],v[40:43],v[168:171]\n"
"PTL8:\n"
"s_cbranch_execz PTL16\n"
T_"v[20:21],v1 offset:42504\n"
M_"v[172:175],v[28:31],v[44:47],v[172:175]\n"
W_"lgkmcnt(7)\n"
T_"v[22:23],v1 offset:50952\n"
M_"v[176:179],v[32:35],v[40:43],v[176:179]\n"
T_"v[24:25],v1 offset:42752\n"
M_"v[180:183],v[32:35],v[44:47],v[180:183]\n"
T_"v[26:27],v1 offset:51200\n"
M_"v[184:187],v[36:39],v[40:43],v[184:187]\n"
T_"v[28:29],v1 offset:42760\n"
M_"v[188:191],v[36:39],v[44:47],v[188:191]\n"
W_"lgkmcnt(6)\n"
T_"v[30:31],v1 offset:51208\n"
M_"v[192:195],v[8:11],v[40:43],v[192:195]\n"
T_"v[32:33],v1 offset:43008\n"
M_"v[196:199],v[8:11],v[44:47],v[196:199]\n"
T_"v[34:35],v1 offset:51456\n"
M_"v[200:203],v[12:15],v[40:43],v[200:203]\n"
T_"v[36:37],v1 offset:43016\n"
M_"v[204:207],v[12:15],v[44:47],v[204:207]\n"
W_"lgkmcnt(6)\n"
T_"v[38:39],v1 offset:51464\n"
M_"v[208:211],v[16:19],v[40:43],v[208:211]\n"
"s_barrier\n"
T_"v[8:9],v1 offset:59136\n"
M_"v[212:215],v[16:19],v[44:47],v[212:215]\n"
T_"v[10:11],v2 offset:2048\n"
M_"v[216:219],v[20:23],v[40:43],v[216:219]\n"
T_"v[12:13],v1 offset:59144\n"
M_"v[220:223],v[20:23],v[44:47],v[220:223]\n"
W_"lgkmcnt(6)\n"
T_"v[14:15],v2 offset:2056\n"
M_"v[224:227],v[24:27],v[40:43],v[224:227]\n"
T_"v[16:17],v1 offset:59392\n"
M_"v[228:231],v[24:27],v[44:47],v[228:231]\n"
T_"v[18:19],v2 offset:2304\n"
M_"v[232:235],v[28:31],v[40:43],v[232:235]\n"
T_"v[20:21],v1 offset:59400\n"
M_"v[236:239],v[28:31],v[44:47],v[236:239]\n"
W_"lgkmcnt(6)\n"
T_"v[22:23],v2 offset:2312\n"
M_"v[240:243],v[32:35],v[40:43],v[240:243]\n"
T_"v[24:25],v1 offset:59648\n"
M_"v[244:247],v[32:35],v[44:47],v[244:247]\n"
T_"v[26:27],v2 offset:2560\n"
M_"v[248:251],v[36:39],v[40:43],v[248:251]\n"
T_"v[28:29],v1 offset:59656\n"
M_"v[252:255],v[36:39],v[44:47],v[252:255]\n"
W_"lgkmcnt(6)\n"
T_"v[30:31],v2 offset:2568\n"
M_"v[128:131],v[8:11],v[48:51],v[128:131]\n"
T_"v[32:33],v1 offset:59904\n"
M_"v[132:135],v[8:11],v[52:55],v[132:135]\n"
T_"v[34:35],v2 offset:2816\n"
M_"v[136:139],v[12:15],v[48:51],v[136:139]\n"
T_"v[36:37],v1 offset:59912\n"
M_"v[140:143],v[12:15],v[52:55],v[140:143]\n"
W_"lgkmcnt(6)\n"
T_"v[38:39],v2 offset:2824\n"
M_"v[144:147],v[16:19],v[48:51],v[144:147]\n"
T_"v[8:9],v2 offset:10496\n"
R_"v[40:43],v4 offset:25344\n"
M_"v[148:151],v[16:19],v[52:55],v[148:151]\n"
T_"v[10:11],v2 offset:44288\n"
M_"v[152:155],v[20:23],v[48:51],v[152:155]\n"
T_"v[12:13],v2 offset:10504\n"
M_"v[156:159],v[20:23],v[52:55],v[156:159]\n"
W_"lgkmcnt(7)\n"
T_"v[14:15],v2 offset:44296\n"
M_"v[160:163],v[24:27],v[48:51],v[160:163]\n"
T_"v[16:17],v2 offset:10752\n"
R_"v[44:47],v4 offset:26368\n"
M_"v[164:167],v[24:27],v[52:55],v[164:167]\n"
T_"v[18:19],v2 offset:44544\n"
M_"v[168:171],v[28:31],v[48:51],v[168:171]\n"
T_"v[20:21],v2 offset:10760\n"
M_"v[172:175],v[28:31],v[52:55],v[172:175]\n"
W_"lgkmcnt(7)\n"
T_"v[22:23],v2 offset:44552\n"
M_"v[176:179],v[32:35],v[48:51],v[176:179]\n"
T_"v[24:25],v2 offset:11008\n"
M_"v[180:183],v[32:35],v[52:55],v[180:183]\n"
T_"v[26:27],v2 offset:44800\n"
M_"v[184:187],v[36:39],v[48:51],v[184:187]\n"
T_"v[28:29],v2 offset:11016\n"
M_"v[188:191],v[36:39],v[52:55],v[188:191]\n"
W_"lgkmcnt(6)\n"
T_"v[30:31],v2 offset:44808\n"
M_"v[192:195],v[8:11],v[48:51],v[192:195]\n"
T_"v[32:33],v2 offset:11264\n"
M_"v[196:199],v[8:11],v[52:55],v[196:199]\n"
T_"v[34:35],v2 offset:45056\n"
M_"v[200:203],v[12:15],v[48:51],v[200:203]\n"
T_"v[36:37],v2 offset:11272\n"
M_"v[204:207],v[12:15],v[52:55],v[204:207]\n"
W_"lgkmcnt(6)\n"
T_"v[38:39],v2 offset:45064\n"
M_"v[208:211],v[16:19],v[48:51],v[208:211]\n"
"s_barrier\n"
T_"v[8:9],v1 offset:0\n"
M_"v[212:215],v[16:19],v[52:55],v[212:215]\n"
T_"v[10:11],v1 offset:8448\n"
M_"v[216:219],v[20:23],v[48:51],v[216:219]\n"
T_"v[12:13],v1 offset:8\n"
M_"v[220:223],v[20:23],v[52:55],v[220:223]\n"
W_"lgkmcnt(6)\n"
T_"v[14:15],v1 offset:8456\n"
M_"v[224:227],v[24:27],v[48:51],v[224:227]\n"
T_"v[16:17],v1 offset:256\n"
M_"v[228:231],v[24:27],v[52:55],v[228:231]\n"
T_"v[18:19],v1 offset:8704\n"
M_"v[232:235],v[28:31],v[48:51],v[232:235]\n"
T_"v[20:21],v1 offset:264\n"
M_"v[236:239],v[28:31],v[52:55],v[236:239]\n"
W_"lgkmcnt(6)\n"
T_"v[22:23],v1 offset:8712\n"
M_"v[240:243],v[32:35],v[48:51],v[240:243]\n"
T_"v[24:25],v1 offset:512\n"
M_"v[244:247],v[32:35],v[52:55],v[244:247]\n"
T_"v[26:27],v1 offset:8960\n"
M_"v[248:251],v[36:39],v[48:51],v[248:251]\n"
T_"v[28:29],v1 offset:520\n"
M_"v[252:255],v[36:39],v[52:55],v[252:255]\n"
W_"lgkmcnt(6)\n"
T_"v[30:31],v1 offset:8968\n"
M_"v[128:131],v[8:11],v[40:43],v[128:131]\n"
T_"v[32:33],v1 offset:768\n"
M_"v[132:135],v[8:11],v[44:47],v[132:135]\n"
T_"v[34:35],v1 offset:9216\n"
M_"v[136:139],v[12:15],v[40:43],v[136:139]\n"
T_"v[36:37],v1 offset:776\n"
M_"v[140:143],v[12:15],v[44:47],v[140:143]\n"
W_"lgkmcnt(6)\n"
T_"v[38:39],v1 offset:9224\n"
M_"v[144:147],v[16:19],v[40:43],v[144:147]\n"
T_"v[8:9],v2 offset:18944\n"
R_"v[48:51],v4 offset:29440\n"
M_"v[148:151],v[16:19],v[44:47],v[148:151]\n"
T_"v[10:11],v2 offset:27392\n"
M_"v[152:155],v[20:23],v[40:43],v[152:155]\n"
T_"v[12:13],v2 offset:18952\n"
M_"v[156:159],v[20:23],v[44:47],v[156:159]\n"
W_"lgkmcnt(7)\n"
T_"v[14:15],v2 offset:27400\n"
M_"v[160:163],v[24:27],v[40:43],v[160:163]\n"
T_"v[16:17],v2 offset:19200\n"
R_"v[52:55],v4 offset:30464\n"
M_"v[164:167],v[24:27],v[44:47],v[164:167]\n"
T_"v[18:19],v2 offset:27648\n"
M_"v[168:171],v[28:31],v[40:43],v[168:171]\n"
T_"v[20:21],v2 offset:19208\n"
M_"v[172:175],v[28:31],v[44:47],v[172:175]\n"
W_"lgkmcnt(7)\n"
T_"v[22:23],v2 offset:27656\n"
M_"v[176:179],v[32:35],v[40:43],v[176:179]\n"
T_"v[24:25],v2 offset:19456\n"
M_"v[180:183],v[32:35],v[44:47],v[180:183]\n"
T_"v[26:27],v2 offset:27904\n"
M_"v[184:187],v[36:39],v[40:43],v[184:187]\n"
T_"v[28:29],v2 offset:19464\n"
M_"v[188:191],v[36:39],v[44:47],v[188:191]\n"
W_"lgkmcnt(6)\n"
T_"v[30:31],v2 offset:27912\n"
M_"v[192:195],v[8:11],v[40:43],v[192:195]\n"
T_"v[32:33],v2 offset:19712\n"
M_"v[196:199],v[8:11],v[44:47],v[196:199]\n"
T_"v[34:35],v2 offset:28160\n"
M_"v[200:203],v[12:15],v[40:43],v[200:203]\n"
T_"v[36:37],v2 offset:19720\n"
M_"v[204:207],v[12:15],v[44:47],v[204:207]\n"
W_"lgkmcnt(6)\n"
T_"v[38:39],v2 offset:28168\n"
M_"v[208:211],v[16:19],v[40:43],v[208:211]\n"
"s_barrier\n"
T_"v[8:9],v1 offset:16896\n"
M_"v[212:215],v[16:19],v[44:47],v[212:215]\n"
T_"v[10:11],v1 offset:25344\n"
M_"v[216:219],v[20:23],v[40:43],v[216:219]\n"
T_"v[12:13],v1 offset:16904\n"
M_"v[220:223],v[20:23],v[44:47],v[220:223]\n"
W_"lgkmcnt(6)\n"
T_"v[14:15],v1 offset:25352\n"
M_"v[224:227],v[24:27],v[40:43],v[224:227]\n"
T_"v[16:17],v1 offset:17152\n"
M_"v[228:231],v[24:27],v[44:47],v[228:231]\n"
T_"v[18:19],v1 offset:25600\n"
M_"v[232:235],v[28:31],v[40:43],v[232:235]\n"
T_"v[20:21],v1 offset:17160\n"
M_"v[236:239],v[28:31],v[44:47],v[236:239]\n"
W_"lgkmcnt(6)\n"
T_"v[22:23],v1 offset:25608\n"
M_"v[240:243],v[32:35],v[40:43],v[240:243]\n"
T_"v[24:25],v1 offset:17408\n"
M_"v[244:247],v[32:35],v[44:47],v[244:247]\n"
T_"v[26:27],v1 offset:25856\n"
M_"v[248:251],v[36:39],v[40:43],v[248:251]\n"
T_"v[28:29],v1 offset:17416\n"
M_"v[252:255],v[36:39],v[44:47],v[252:255]\n"
W_"lgkmcnt(6)\n"
T_"v[30:31],v1 offset:25864\n"
M_"v[128:131],v[8:11],v[48:51],v[128:131]\n"
T_"v[32:33],v1 offset:17664\n"
M_"v[132:135],v[8:11],v[52:55],v[132:135]\n"
T_"v[34:35],v1 offset:26112\n"
M_"v[136:139],v[12:15],v[48:51],v[136:139]\n"
T_"v[36:37],v1 offset:17672\n"
M_"v[140:143],v[12:15],v[52:55],v[140:143]\n"
W_"lgkmcnt(6)\n"
T_"v[38:39],v1 offset:26120\n"
M_"v[144:147],v[16:19],v[48:51],v[144:147]\n"
T_"v[8:9],v1 offset:33792\n"
R_"v[40:43],v4 offset:33792\n"
M_"v[148:151],v[16:19],v[52:55],v[148:151]\n"
T_"v[10:11],v2 offset:35840\n"
M_"v[152:155],v[20:23],v[48:51],v[152:155]\n"
T_"v[12:13],v1 offset:33800\n"
M_"v[156:159],v[20:23],v[52:55],v[156:159]\n"
W_"lgkmcnt(7)\n"
T_"v[14:15],v2 offset:35848\n"
M_"v[160:163],v[24:27],v[48:51],v[160:163]\n"
T_"v[16:17],v1 offset:34048\n"
R_"v[44:47],v4 offset:34816\n"
M_"v[164:167],v[24:27],v[52:55],v[164:167]\n"
T_"v[18:19],v2 offset:36096\n"
M_"v[168:171],v[28:31],v[48:51],v[168:171]\n"
T_"v[20:21],v1 offset:34056\n"
M_"v[172:175],v[28:31],v[52:55],v[172:175]\n"
W_"lgkmcnt(7)\n"
T_"v[22:23],v2 offset:36104\n"
M_"v[176:179],v[32:35],v[48:51],v[176:179]\n"
T_"v[24:25],v1 offset:34304\n"
M_"v[180:183],v[32:35],v[52:55],v[180:183]\n"
T_"v[26:27],v2 offset:36352\n"
M_"v[184:187],v[36:39],v[48:51],v[184:187]\n"
T_"v[28:29],v1 offset:34312\n"
M_"v[188:191],v[36:39],v[52:55],v[188:191]\n"
W_"lgkmcnt(6)\n"
T_"v[30:31],v2 offset:36360\n"
M_"v[192:195],v[8:11],v[48:51],v[192:195]\n"
T_"v[32:33],v1 offset:34560\n"
M_"v[196:199],v[8:11],v[52:55],v[196:199]\n"
T_"v[34:35],v2 offset:36608\n"
M_"v[200:203],v[12:15],v[48:51],v[200:203]\n"
T_"v[36:37],v1 offset:34568\n"
M_"v[204:207],v[12:15],v[52:55],v[204:207]\n"
W_"lgkmcnt(6)\n"
T_"v[38:39],v2 offset:36616\n"
M_"v[208:211],v[16:19],v[48:51],v[208:211]\n"
"s_barrier\n"
T_"v[8:9],v1 offset:42240\n"
M_"v[212:215],v[16:19],v[52:55],v[212:215]\n"
T_"v[10:11],v1 offset:50688\n"
M_"v[216:219],v[20:23],v[48:51],v[216:219]\n"
T_"v[12:13],v1 offset:42248\n"
M_"v[220:223],v[20:23],v[52:55],v[220:223]\n"
W_"lgkmcnt(6)\n"
T_"v[14:15],v1 offset:50696\n"
M_"v[224:227],v[24:27],v[48:51],v[224:227]\n"
T_"v[16:17],v1 offset:42496\n"
M_"v[228:231],v[24:27],v[52:55],v[228:231]\n"
T_"v[18:19],v1 offset:50944\n"
M_"v[232:235],v[28:31],v[48:51],v[232:235]\n"
T_"v[20:21],v1 offset:42504\n"
M_"v[236:239],v[28:31],v[52:55],v[236:239]\n"
W_"lgkmcnt(6)\n"
T_"v[22:23],v1 offset:50952\n"
M_"v[240:243],v[32:35],v[48:51],v[240:243]\n"
T_"v[24:25],v1 offset:42752\n"
M_"v[244:247],v[32:35],v[52:55],v[244:247]\n"
T_"v[26:27],v1 offset:51200\n"
M_"v[248:251],v[36:39],v[48:51],v[248:251]\n"
T_"v[28:29],v1 offset:42760\n"
M_"v[252:255],v[36:39],v[52:55],v[252:255]\n"
W_"lgkmcnt(6)\n"
T_"v[30:31],v1 offset:51208\n"
M_"v[128:131],v[8:11],v[40:43],v[128:131]\n"
T_"v[32:33],v1 offset:43008\n"
M_"v[132:135],v[8:11],v[44:47],v[132:135]\n"
T_"v[34:35],v1 offset:51456\n"
M_"v[136:139],v[12:15],v[40:43],v[136:139]\n"
T_"v[36:37],v1 offset:43016\n"
M_"v[140:143],v[12:15],v[44:47],v[140:143]\n"
W_"lgkmcnt(6)\n"
T_"v[38:39],v1 offset:51464\n"
M_"v[144:147],v[16:19],v[40:43],v[144:147]\n"
T_"v[8:9],v1 offset:59136\n"
R_"v[48:51],v4 offset:37888\n"
M_"v[148:151],v[16:19],v[44:47],v[148:151]\n"
T_"v[10:11],v2 offset:2048\n"
M_"v[152:155],v[20:23],v[40:43],v[152:155]\n"
T_"v[12:13],v1 offset:59144\n"
M_"v[156:159],v[20:23],v[44:47],v[156:159]\n"
W_"lgkmcnt(7)\n"
T_"v[14:15],v2 offset:2056\n"
M_"v[160:163],v[24:27],v[40:43],v[160:163]\n"
T_"v[16:17],v1 offset:59392\n"
R_"v[52:55],v4 offset:38912\n"
M_"v[164:167],v[24:27],v[44:47],v[164:167]\n"
T_"v[18:19],v2 offset:2304\n"
M_"v[168:171],v[28:31],v[40:43],v[168:171]\n"
T_"v[20:21],v1 offset:59400\n"
M_"v[172:175],v[28:31],v[44:47],v[172:175]\n"
W_"lgkmcnt(7)\n"
T_"v[22:23],v2 offset:2312\n"
M_"v[176:179],v[32:35],v[40:43],v[176:179]\n"
T_"v[24:25],v1 offset:59648\n"
M_"v[180:183],v[32:35],v[44:47],v[180:183]\n"
T_"v[26:27],v2 offset:2560\n"
M_"v[184:187],v[36:39],v[40:43],v[184:187]\n"
T_"v[28:29],v1 offset:59656\n"
M_"v[188:191],v[36:39],v[44:47],v[188:191]\n"
W_"lgkmcnt(6)\n"
T_"v[30:31],v2 offset:2568\n"
M_"v[192:195],v[8:11],v[40:43],v[192:195]\n"
T_"v[32:33],v1 offset:59904\n"
M_"v[196:199],v[8:11],v[44:47],v[196:199]\n"
T_"v[34:35],v2 offset:2816\n"
M_"v[200:203],v[12:15],v[40:43],v[200:203]\n"
T_"v[36:37],v1 offset:59912\n"
M_"v[204:207],v[12:15],v[44:47],v[204:207]\n"
W_"lgkmcnt(6)\n"
T_"v[38:39],v2 offset:2824\n"
M_"v[208:211],v[16:19],v[40:43],v[208:211]\n"
"s_barrier\n"
T_"v[8:9],v1 offset:0\n"
M_"v[212:215],v[16:19],v[44:47],v[212:215]\n"
T_"v[10:11],v1 offset:8448\n"
M_"v[216:219],v[20:23],v[40:43],v[216:219]\n"
T_"v[12:13],v1 offset:8\n"
M_"v[220:223],v[20:23],v[44:47],v[220:223]\n"
W_"lgkmcnt(6)\n"
T_"v[14:15],v1 offset:8456\n"
M_"v[224:227],v[24:27],v[40:43],v[224:227]\n"
T_"v[16:17],v1 offset:256\n"
M_"v[228:231],v[24:27],v[44:47],v[228:231]\n"
T_"v[18:19],v1 offset:8704\n"
M_"v[232:235],v[28:31],v[40:43],v[232:235]\n"
T_"v[20:21],v1 offset:264\n"
M_"v[236:239],v[28:31],v[44:47],v[236:239]\n"
W_"lgkmcnt(6)\n"
T_"v[22:23],v1 offset:8712\n"
M_"v[240:243],v[32:35],v[40:43],v[240:243]\n"
T_"v[24:25],v1 offset:512\n"
M_"v[244:247],v[32:35],v[44:47],v[244:247]\n"
T_"v[26:27],v1 offset:8960\n"
M_"v[248:251],v[36:39],v[40:43],v[248:251]\n"
T_"v[28:29],v1 offset:520\n"
M_"v[252:255],v[36:39],v[44:47],v[252:255]\n"
W_"lgkmcnt(6)\n"
T_"v[30:31],v1 offset:8968\n"
M_"v[128:131],v[8:11],v[48:51],v[128:131]\n"
T_"v[32:33],v1 offset:768\n"
M_"v[132:135],v[8:11],v[52:55],v[132:135]\n"
T_"v[34:35],v1 offset:9216\n"
M_"v[136:139],v[12:15],v[48:51],v[136:139]\n"
T_"v[36:37],v1 offset:776\n"
M_"v[140:143],v[12:15],v[52:55],v[140:143]\n"
W_"lgkmcnt(6)\n"
T_"v[38:39],v1 offset:9224\n"
M_"v[144:147],v[16:19],v[48:51],v[144:147]\n"
T_"v[8:9],v2 offset:10496\n"
R_"v[40:43],v4 offset:42240\n"
M_"v[148:151],v[16:19],v[52:55],v[148:151]\n"
T_"v[10:11],v2 offset:44288\n"
M_"v[152:155],v[20:23],v[48:51],v[152:155]\n"
T_"v[12:13],v2 offset:10504\n"
M_"v[156:159],v[20:23],v[52:55],v[156:159]\n"
W_"lgkmcnt(7)\n"
T_"v[14:15],v2 offset:44296\n"
M_"v[160:163],v[24:27],v[48:51],v[160:163]\n"
T_"v[16:17],v2 offset:10752\n"
R_"v[44:47],v4 offset:43264\n"
M_"v[164:167],v[24:27],v[52:55],v[164:167]\n"
T_"v[18:19],v2 offset:44544\n"
M_"v[168:171],v[28:31],v[48:51],v[168:171]\n"
T_"v[20:21],v2 offset:10760\n"
M_"v[172:175],v[28:31],v[52:55],v[172:175]\n"
W_"lgkmcnt(7)\n"
T_"v[22:23],v2 offset:44552\n"
M_"v[176:179],v[32:35],v[48:51],v[176:179]\n"
T_"v[24:25],v2 offset:11008\n"
M_"v[180:183],v[32:35],v[52:55],v[180:183]\n"
T_"v[26:27],v2 offset:44800\n"
M_"v[184:187],v[36:39],v[48:51],v[184:187]\n"
T_"v[28:29],v2 offset:11016\n"
M_"v[188:191],v[36:39],v[52:55],v[188:191]\n"
W_"lgkmcnt(6)\n"
T_"v[30:31],v2 offset:44808\n"
M_"v[192:195],v[8:11],v[48:51],v[192:195]\n"
T_"v[32:33],v2 offset:11264\n"
M_"v[196:199],v[8:11],v[52:55],v[196:199]\n"
T_"v[34:35],v2 offset:45056\n"
M_"v[200:203],v[12:15],v[48:51],v[200:203]\n"
T_"v[36:37],v2 offset:11272\n"
M_"v[204:207],v[12:15],v[52:55],v[204:207]\n"
W_"lgkmcnt(6)\n"
T_"v[38:39],v2 offset:45064\n"
M_"v[208:211],v[16:19],v[48:51],v[208:211]\n"
"s_barrier\n"
T_"v[8:9],v1 offset:16896\n"
M_"v[212:215],v[16:19],v[52:55],v[212:215]\n"
T_"v[10:11],v1 offset:25344\n"
M_"v[216:219],v[20:23],v[48:51],v[216:219]\n"
T_"v[12:13],v1 offset:16904\n"
M_"v[220:223],v[20:23],v[52:55],v[220:223]\n"
W_"lgkmcnt(6)\n"
T_"v[14:15],v1 offset:25352\n"
M_"v[224:227],v[24:27],v[48:51],v[224:227]\n"
T_"v[16:17],v1 offset:17152\n"
M_"v[228:231],v[24:27],v[52:55],v[228:231]\n"
T_"v[18:19],v1 offset:25600\n"
M_"v[232:235],v[28:31],v[48:51],v[232:235]\n"
T_"v[20:21],v1 offset:17160\n"
M_"v[236:239],v[28:31],v[52:55],v[236:239]\n"
W_"lgkmcnt(6)\n"
T_"v[22:23],v1 offset:25608\n"
M_"v[240:243],v[32:35],v[48:51],v[240:243]\n"
T_"v[24:25],v1 offset:17408\n"
M_"v[244:247],v[32:35],v[52:55],v[244:247]\n"
T_"v[26:27],v1 offset:25856\n"
M_"v[248:251],v[36:39],v[48:51],v[248:251]\n"
T_"v[28:29],v1 offset:17416\n"
M_"v[252:255],v[36:39],v[52:55],v[252:255]\n"
W_"lgkmcnt(6)\n"
T_"v[30:31],v1 offset:25864\n"
M_"v[128:131],v[8:11],v[40:43],v[128:131]\n"
T_"v[32:33],v1 offset:17664\n"
M_"v[132:135],v[8:11],v[44:47],v[132:135]\n"
T_"v[34:35],v1 offset:26112\n"
M_"v[136:139],v[12:15],v[40:43],v[136:139]\n"
T_"v[36:37],v1 offset:17672\n"
M_"v[140:143],v[12:15],v[44:47],v[140:143]\n"
W_"lgkmcnt(6)\n"
T_"v[38:39],v1 offset:26120\n"
M_"v[144:147],v[16:19],v[40:43],v[144:147]\n"
T_"v[8:9],v2 offset:18944\n"
R_"v[48:51],v4 offset:46336\n"
M_"v[148:151],v[16:19],v[44:47],v[148:151]\n"
T_"v[10:11],v2 offset:27392\n"
M_"v[152:155],v[20:23],v[40:43],v[152:155]\n"
T_"v[12:13],v2 offset:18952\n"
M_"v[156:159],v[20:23],v[44:47],v[156:159]\n"
W_"lgkmcnt(7)\n"
T_"v[14:15],v2 offset:27400\n"
M_"v[160:163],v[24:27],v[40:43],v[160:163]\n"
T_"v[16:17],v2 offset:19200\n"
R_"v[52:55],v4 offset:47360\n"
M_"v[164:167],v[24:27],v[44:47],v[164:167]\n"
T_"v[18:19],v2 offset:27648\n"
M_"v[168:171],v[28:31],v[40:43],v[168:171]\n"
T_"v[20:21],v2 offset:19208\n"
M_"v[172:175],v[28:31],v[44:47],v[172:175]\n"
W_"lgkmcnt(7)\n"
T_"v[22:23],v2 offset:27656\n"
M_"v[176:179],v[32:35],v[40:43],v[176:179]\n"
T_"v[24:25],v2 offset:19456\n"
M_"v[180:183],v[32:35],v[44:47],v[180:183]\n"
T_"v[26:27],v2 offset:27904\n"
M_"v[184:187],v[36:39],v[40:43],v[184:187]\n"
T_"v[28:29],v2 offset:19464\n"
M_"v[188:191],v[36:39],v[44:47],v[188:191]\n"
W_"lgkmcnt(6)\n"
T_"v[30:31],v2 offset:27912\n"
M_"v[192:195],v[8:11],v[40:43],v[192:195]\n"
T_"v[32:33],v2 offset:19712\n"
M_"v[196:199],v[8:11],v[44:47],v[196:199]\n"
T_"v[34:35],v2 offset:28160\n"
M_"v[200:203],v[12:15],v[40:43],v[200:203]\n"
T_"v[36:37],v2 offset:19720\n"
M_"v[204:207],v[12:15],v[44:47],v[204:207]\n"
W_"lgkmcnt(6)\n"
T_"v[38:39],v2 offset:28168\n"
M_"v[208:211],v[16:19],v[40:43],v[208:211]\n"
"s_barrier\n"
T_"v[8:9],v1 offset:33792\n"
M_"v[212:215],v[16:19],v[44:47],v[212:215]\n"
T_"v[10:11],v1 offset:42240\n"
M_"v[216:219],v[20:23],v[40:43],v[216:219]\n"
T_"v[12:13],v1 offset:33800\n"
M_"v[220:223],v[20:23],v[44:47],v[220:223]\n"
W_"lgkmcnt(6)\n"
T_"v[14:15],v1 offset:42248\n"
M_"v[224:227],v[24:27],v[40:43],v[224:227]\n"
T_"v[16:17],v1 offset:34048\n"
M_"v[228:231],v[24:27],v[44:47],v[228:231]\n"
T_"v[18:19],v1 offset:42496\n"
M_"v[232:235],v[28:31],v[40:43],v[232:235]\n"
T_"v[20:21],v1 offset:34056\n"
M_"v[236:239],v[28:31],v[44:47],v[236:239]\n"
W_"lgkmcnt(6)\n"
T_"v[22:23],v1 offset:42504\n"
M_"v[240:243],v[32:35],v[40:43],v[240:243]\n"
T_"v[24:25],v1 offset:34304\n"
M_"v[244:247],v[32:35],v[44:47],v[244:247]\n"
T_"v[26:27],v1 offset:42752\n"
M_"v[248:251],v[36:39],v[40:43],v[248:251]\n"
T_"v[28:29],v1 offset:34312\n"
M_"v[252:255],v[36:39],v[44:47],v[252:255]\n"
W_"lgkmcnt(6)\n"
T_"v[30:31],v1 offset:42760\n"
M_"v[128:131],v[8:11],v[48:51],v[128:131]\n"
T_"v[32:33],v1 offset:34560\n"
M_"v[132:135],v[8:11],v[52:55],v[132:135]\n"
T_"v[34:35],v1 offset:43008\n"
M_"v[136:139],v[12:15],v[48:51],v[136:139]\n"
T_"v[36:37],v1 offset:34568\n"
M_"v[140:143],v[12:15],v[52:55],v[140:143]\n"
W_"lgkmcnt(6)\n"
T_"v[38:39],v1 offset:43016\n"
M_"v[144:147],v[16:19],v[48:51],v[144:147]\n"
T_"v[8:9],v1 offset:50688\n"
R_"v[40:43],v4 offset:50688\n"
M_"v[148:151],v[16:19],v[52:55],v[148:151]\n"
T_"v[10:11],v2 offset:35840\n"
M_"v[152:155],v[20:23],v[48:51],v[152:155]\n"
T_"v[12:13],v1 offset:50696\n"
M_"v[156:159],v[20:23],v[52:55],v[156:159]\n"
W_"lgkmcnt(7)\n"
T_"v[14:15],v2 offset:35848\n"
M_"v[160:163],v[24:27],v[48:51],v[160:163]\n"
T_"v[16:17],v1 offset:50944\n"
R_"v[44:47],v4 offset:51712\n"
M_"v[164:167],v[24:27],v[52:55],v[164:167]\n"
T_"v[18:19],v2 offset:36096\n"
M_"v[168:171],v[28:31],v[48:51],v[168:171]\n"
T_"v[20:21],v1 offset:50952\n"
M_"v[172:175],v[28:31],v[52:55],v[172:175]\n"
W_"lgkmcnt(7)\n"
T_"v[22:23],v2 offset:36104\n"
M_"v[176:179],v[32:35],v[48:51],v[176:179]\n"
T_"v[24:25],v1 offset:51200\n"
M_"v[180:183],v[32:35],v[52:55],v[180:183]\n"
T_"v[26:27],v2 offset:36352\n"
M_"v[184:187],v[36:39],v[48:51],v[184:187]\n"
T_"v[28:29],v1 offset:51208\n"
M_"v[188:191],v[36:39],v[52:55],v[188:191]\n"
W_"lgkmcnt(6)\n"
T_"v[30:31],v2 offset:36360\n"
M_"v[192:195],v[8:11],v[48:51],v[192:195]\n"
T_"v[32:33],v1 offset:51456\n"
M_"v[196:199],v[8:11],v[52:55],v[196:199]\n"
T_"v[34:35],v2 offset:36608\n"
M_"v[200:203],v[12:15],v[48:51],v[200:203]\n"
T_"v[36:37],v1 offset:51464\n"
M_"v[204:207],v[12:15],v[52:55],v[204:207]\n"
W_"lgkmcnt(6)\n"
T_"v[38:39],v2 offset:36616\n"
M_"v[208:211],v[16:19],v[48:51],v[208:211]\n"
"s_barrier\n"
T_"v[8:9],v1 offset:0\n"
"PTL9:\n"
"s_cbranch_execz PTL17\n"
M_"v[212:215],v[16:19],v[52:55],v[212:215]\n"
T_"v[10:11],v1 offset:8448\n"
M_"v[216:219],v[20:23],v[48:51],v[216:219]\n"
T_"v[12:13],v1 offset:8\n"
M_"v[220:223],v[20:23],v[52:55],v[220:223]\n"
W_"lgkmcnt(6)\n"
T_"v[14:15],v1 offset:8456\n"
M_"v[224:227],v[24:27],v[48:51],v[224:227]\n"
T_"v[16:17],v1 offset:256\n"
M_"v[228:231],v[24:27],v[52:55],v[228:231]\n"
T_"v[18:19],v1 offset:8704\n"
M_"v[232:235],v[28:31],v[48:51],v[232:235]\n"
T_"v[20:21],v1 offset:264\n"
M_"v[236:239],v[28:31],v[52:55],v[236:239]\n"
W_"lgkmcnt(6)\n"
T_"v[22:23],v1 offset:8712\n"
M_"v[240:243],v[32:35],v[48:51],v[240:243]\n"
T_"v[24:25],v1 offset:512\n"
M_"v[244:247],v[32:35],v[52:55],v[244:247]\n"
T_"v[26:27],v1 offset:8960\n"
M_"v[248:251],v[36:39],v[48:51],v[248:251]\n"
T_"v[28:29],v1 offset:520\n"
M_"v[252:255],v[36:39],v[52:55],v[252:255]\n"
W_"lgkmcnt(6)\n"
T_"v[30:31],v1 offset:8968\n"
M_"v[128:131],v[8:11],v[40:43],v[128:131]\n"
T_"v[32:33],v1 offset:768\n"
M_"v[132:135],v[8:11],v[44:47],v[132:135]\n"
T_"v[34:35],v1 offset:9216\n"
M_"v[136:139],v[12:15],v[40:43],v[136:139]\n"
T_"v[36:37],v1 offset:776\n"
M_"v[140:143],v[12:15],v[44:47],v[140:143]\n"
W_"lgkmcnt(6)\n"
T_"v[38:39],v1 offset:9224\n"
M_"v[144:147],v[16:19],v[40:43],v[144:147]\n"
T_"v[8:9],v1 offset:59136\n"
R_"v[48:51],v4 offset:54784\n"
M_"v[148:151],v[16:19],v[44:47],v[148:151]\n"
T_"v[10:11],v2 offset:2048\n"
M_"v[152:155],v[20:23],v[40:43],v[152:155]\n"
T_"v[12:13],v1 offset:59144\n"
M_"v[156:159],v[20:23],v[44:47],v[156:159]\n"
W_"lgkmcnt(7)\n"
T_"v[14:15],v2 offset:2056\n"
M_"v[160:163],v[24:27],v[40:43],v[160:163]\n"
T_"v[16:17],v1 offset:59392\n"
R_"v[52:55],v4 offset:55808\n"
M_"v[164:167],v[24:27],v[44:47],v[164:167]\n"
T_"v[18:19],v2 offset:2304\n"
M_"v[168:171],v[28:31],v[40:43],v[168:171]\n"
T_"v[20:21],v1 offset:59400\n"
M_"v[172:175],v[28:31],v[44:47],v[172:175]\n"
W_"lgkmcnt(7)\n"
T_"v[22:23],v2 offset:2312\n"
M_"v[176:179],v[32:35],v[40:43],v[176:179]\n"
T_"v[24:25],v1 offset:59648\n"
M_"v[180:183],v[32:35],v[44:47],v[180:183]\n"
T_"v[26:27],v2 offset:2560\n"
M_"v[184:187],v[36:39],v[40:43],v[184:187]\n"
T_"v[28:29],v1 offset:59656\n"
M_"v[188:191],v[36:39],v[44:47],v[188:191]\n"
W_"lgkmcnt(6)\n"
T_"v[30:31],v2 offset:2568\n"
M_"v[192:195],v[8:11],v[40:43],v[192:195]\n"
T_"v[32:33],v1 offset:59904\n"
M_"v[196:199],v[8:11],v[44:47],v[196:199]\n"
T_"v[34:35],v2 offset:2816\n"
M_"v[200:203],v[12:15],v[40:43],v[200:203]\n"
T_"v[36:37],v1 offset:59912\n"
M_"v[204:207],v[12:15],v[44:47],v[204:207]\n"
W_"lgkmcnt(6)\n"
T_"v[38:39],v2 offset:2824\n"
M_"v[208:211],v[16:19],v[40:43],v[208:211]\n"
"s_barrier\n"
T_"v[8:9],v1 offset:16896\n"
M_"v[212:215],v[16:19],v[44:47],v[212:215]\n"
T_"v[10:11],v1 offset:25344\n"
M_"v[216:219],v[20:23],v[40:43],v[216:219]\n"
T_"v[12:13],v1 offset:16904\n"
M_"v[220:223],v[20:23],v[44:47],v[220:223]\n"
W_"lgkmcnt(6)\n"
T_"v[14:15],v1 offset:25352\n"
M_"v[224:227],v[24:27],v[40:43],v[224:227]\n"
T_"v[16:17],v1 offset:17152\n"
M_"v[228:231],v[24:27],v[44:47],v[228:231]\n"
T_"v[18:19],v1 offset:25600\n"
M_"v[232:235],v[28:31],v[40:43],v[232:235]\n"
T_"v[20:21],v1 offset:17160\n"
M_"v[236:239],v[28:31],v[44:47],v[236:239]\n"
W_"lgkmcnt(6)\n"
T_"v[22:23],v1 offset:25608\n"
M_"v[240:243],v[32:35],v[40:43],v[240:243]\n"
T_"v[24:25],v1 offset:17408\n"
M_"v[244:247],v[32:35],v[44:47],v[244:247]\n"
T_"v[26:27],v1 offset:25856\n"
M_"v[248:251],v[36:39],v[40:43],v[248:251]\n"
T_"v[28:29],v1 offset:17416\n"
M_"v[252:255],v[36:39],v[44:47],v[252:255]\n"
W_"lgkmcnt(6)\n"
T_"v[30:31],v1 offset:25864\n"
M_"v[128:131],v[8:11],v[48:51],v[128:131]\n"
T_"v[32:33],v1 offset:17664\n"
M_"v[132:135],v[8:11],v[52:55],v[132:135]\n"
T_"v[34:35],v1 offset:26112\n"
M_"v[136:139],v[12:15],v[48:51],v[136:139]\n"
T_"v[36:37],v1 offset:17672\n"
M_"v[140:143],v[12:15],v[52:55],v[140:143]\n"
W_"lgkmcnt(6)\n"
T_"v[38:39],v1 offset:26120\n"
M_"v[144:147],v[16:19],v[48:51],v[144:147]\n"
T_"v[8:9],v2 offset:10496\n"
M_"v[148:151],v[16:19],v[52:55],v[148:151]\n"
T_"v[10:11],v2 offset:44288\n"
M_"v[152:155],v[20:23],v[48:51],v[152:155]\n"
T_"v[12:13],v2 offset:10504\n"
C_"v64,v128,v129\n"
M_"v[156:159],v[20:23],v[52:55],v[156:159]\n"
W_"lgkmcnt(6)\n"
T_"v[14:15],v2 offset:44296\n"
C_"v65,v130,v131\n"
M_"v[160:163],v[24:27],v[48:51],v[160:163]\n"
T_"v[16:17],v2 offset:10752\n"
C_"v66,v136,v137\n"
M_"v[164:167],v[24:27],v[52:55],v[164:167]\n"
T_"v[18:19],v2 offset:44544\n"
C_"v67,v138,v139\n"
M_"v[168:171],v[28:31],v[48:51],v[168:171]\n"
T_"v[20:21],v2 offset:10760\n"
C_"v68,v132,v133\n"
M_"v[172:175],v[28:31],v[52:55],v[172:175]\n"
W_"lgkmcnt(6)\n"
T_"v[22:23],v2 offset:44552\n"
C_"v69,v134,v135\n"
M_"v[176:179],v[32:35],v[48:51],v[176:179]\n"
T_"v[24:25],v2 offset:11008\n"
C_"v70,v140,v141\n"
M_"v[180:183],v[32:35],v[52:55],v[180:183]\n"
T_"v[26:27],v2 offset:44800\n"
C_"v71,v142,v143\n"
M_"v[184:187],v[36:39],v[48:51],v[184:187]\n"
T_"v[28:29],v2 offset:11016\n"
C_"v72,v144,v145\n"
M_"v[188:191],v[36:39],v[52:55],v[188:191]\n"
W_"lgkmcnt(6)\n"
T_"v[30:31],v2 offset:44808\n"
C_"v73,v146,v147\n"
M_"v[192:195],v[8:11],v[48:51],v[192:195]\n"
T_"v[32:33],v2 offset:11264\n"
C_"v74,v152,v153\n"
M_"v[196:199],v[8:11],v[52:55],v[196:199]\n"
T_"v[34:35],v2 offset:45056\n"
C_"v75,v154,v155\n"
M_"v[200:203],v[12:15],v[48:51],v[200:203]\n"
T_"v[36:37],v2 offset:11272\n"
C_"v76,v148,v149\n"
C_"v77,v150,v151\n"
M_"v[204:207],v[12:15],v[52:55],v[204:207]\n"
W_"lgkmcnt(6)\n"
T_"v[38:39],v2 offset:45064\n"
C_"v78,v156,v157\n"
C_"v79,v158,v159\n"
M_"v[208:211],v[16:19],v[48:51],v[208:211]\n"
"s_barrier\n"
"v_add_u32 v1, 0, v7\n"
"v_add_u32 v2, 0x10000, v1\n"
"v_add_u32 v3, 0x20000, v1\n"
R_"v[8:11],v1 offset:33792\n"
C_"v80,v160,v161\n"
C_"v81,v162,v163\n"
M_"v[212:215],v[16:19],v[52:55],v[212:215]\n"
C_"v82,v168,v169\n"
C_"v83,v170,v171\n"
M_"v[216:219],v[20:23],v[48:51],v[216:219]\n"
R_"v[12:15],v1 offset:33856\n"
C_"v84,v164,v165\n"
C_"v85,v166,v167\n"
M_"v[220:223],v[20:23],v[52:55],v[220:223]\n"
W_"lgkmcnt(5)\n"
C_"v86,v172,v173\n"
C_"v87,v174,v175\n"
M_"v[224:227],v[24:27],v[48:51],v[224:227]\n"
R_"v[16:19],v1 offset:33920\n"
C_"v88,v176,v177\n"
C_"v89,v178,v179\n"
M_"v[228:231],v[24:27],v[52:55],v[228:231]\n"
C_"v90,v184,v185\n"
C_"v91,v186,v187\n"
M_"v[232:235],v[28:31],v[48:51],v[232:235]\n"
R_"v[20:23],v1 offset:33984\n"
C_"v92,v180,v181\n"
C_"v93,v182,v183\n"
M_"v[236:239],v[28:31],v[52:55],v[236:239]\n"
W_"lgkmcnt(3)\n"
C_"v94,v188,v189\n"
C_"v95,v190,v191\n"
M_"v[240:243],v[32:35],v[48:51],v[240:243]\n"
R_"v[24:27],v1 offset:34048\n"
C_"v96,v192,v193\n"
C_"v97,v194,v195\n"
M_"v[244:247],v[32:35],v[52:55],v[244:247]\n"
C_"v98,v200,v201\n"
C_"v99,v202,v203\n"
M_"v[248:251],v[36:39],v[48:51],v[248:251]\n"
R_"v[28:31],v1 offset:34112\n"
C_"v100,v196,v197\n"
M_"v[252:255],v[36:39],v[52:55],v[252:255]\n"
C_"v101,v198,v199\n"
C_"v102,v204,v205\n"
M_"v[40:43],v[8:11],v[64:67],0\n"
R_"v[32:35],v1 offset:34176\n"
C_"v103,v206,v207\n"
C_"v104,v208,v209\n"
M_"v[44:47],v[8:11],v[68:71],0\n"
W_"lgkmcnt(3)\n"
C_"v105,v210,v211\n"
C_"v106,v216,v217\n"
M_"v[40:43],v[12:15],v[72:75],v[40:43]\n"
R_"v[36:39],v1 offset:34240\n"
C_"v107,v218,v219\n"
C_"v108,v212,v213\n"
M_"v[44:47],v[12:15],v[76:79],v[44:47]\n"
C_"v109,v214,v215\n"
C_"v110,v220,v221\n"
M_"v[40:43],v[16:19],v[80:83],v[40:43]\n"
R_"v[8:11],v1 offset:42240\n"
C_"v111,v222,v223\n"
C_"v112,v224,v225\n"
M_"v[44:47],v[16:19],v[84:87],v[44:47]\n"
C_"v113,v226,v227\n"
C_"v114,v232,v233\n"
M_"v[40:43],v[20:23],v[88:91],v[40:43]\n"
R_"v[12:15],v1 offset:42304\n"
C_"v115,v234,v235\n"
C_"v116,v228,v229\n"
M_"v[44:47],v[20:23],v[92:95],v[44:47]\n"
W_"lgkmcnt(3)\n"
C_"v117,v230,v231\n"
C_"v118,v236,v237\n"
M_"v[40:43],v[24:27],v[96:99],v[40:43]\n"
R_"v[16:19],v1 offset:42368\n"
C_"v119,v238,v239\n"
C_"v120,v240,v241\n"
M_"v[44:47],v[24:27],v[100:103],v[44:47]\n"
C_"v121,v242,v243\n"
C_"v122,v248,v249\n"
M_"v[40:43],v[28:31],v[104:107],v[40:43]\n"
R_"v[20:23],v1 offset:42432\n"
C_"v123,v250,v251\n"
C_"v124,v244,v245\n"
M_"v[44:47],v[28:31],v[108:111],v[44:47]\n"
C_"v125,v246,v247\n"
C_"v126,v252,v253\n"
M_"v[40:43],v[32:35],v[112:115],v[40:43]\n"
R_"v[24:27],v1 offset:42496\n"
C_"v127,v254,v255\n"
M_"v[44:47],v[32:35],v[116:119],v[44:47]\n"
W_"lgkmcnt(3)\n"
M_"v[40:43],v[36:39],v[120:123],v[40:43]\n"
R_"v[28:31],v1 offset:42560\n"
M_"v[44:47],v[36:39],v[124:127],v[44:47]\n"
M_"v[48:51],v[8:11],v[64:67],0\n"
R_"v[32:35],v1 offset:42624\n"
M_"v[52:55],v[8:11],v[68:71],0\n"
"s_lshl_b32 s4, s34, 11\n"
M_"v[48:51],v[12:15],v[72:75],v[48:51]\n"
R_"v[36:39],v1 offset:42688\n"
M_"v[52:55],v[12:15],v[76:79],v[52:55]\n"
W_"lgkmcnt(3)\n"
"v_lshl_add_u32 v4, v63, 4, s4\n"
M_"v[48:51],v[16:19],v[80:83],v[48:51]\n"
"s_barrier\n"
R_"v[8:11],v2 offset:18944\n"
"v_cvt_pk_f16_f32 v192, v40, v41\n"
M_"v[52:55],v[16:19],v[84:87],v[52:55]\n"
"v_cvt_pk_f16_f32 v193, v42, v43\n"
"s_lshl_b32 s4, s35, 12\n"
M_"v[48:51],v[20:23],v[88:91],v[48:51]\n"
R_"v[12:15],v2 offset:19008\n"
M_"v[52:55],v[20:23],v[92:95],v[52:55]\n"
"v_cvt_pk_f16_f32 v196, v44, v45\n"
"v_add_u32 v5, s4, v4\n"
M_"v[48:51],v[24:27],v[96:99],v[48:51]\n"
R_"v[16:19],v2 offset:19072\n"
M_"v[52:55],v[24:27],v[100:103],v[52:55]\n"
W_"lgkmcnt(3)\n"
"v_cvt_pk_f16_f32 v197, v46, v47\n"
"s_lshl2_add_u32 s4, s34, s35\n"
M_"v[48:51],v[28:31],v[104:107],v[48:51]\n"
R_"v[20:23],v2 offset:19136\n"
"v_pk_maximum3_f16 v56, v192, v192, v193\n"
M_"v[52:55],v[28:31],v[108:111],v[52:55]\n"
"s_mul_i32 s4, s4, 114688\n"
M_"v[48:51],v[32:35],v[112:115],v[48:51]\n"
R_"v[24:27],v2 offset:19200\n"
"v_pk_maximum3_f16 v57, v196, v196, v197\n"
M_"v[52:55],v[32:35],v[116:119],v[52:55]\n"
"v_lshl_add_u32 v7, v63, 4, s4\n"
M_"v[48:51],v[36:39],v[120:123],v[48:51]\n"
R_"v[28:31],v2 offset:19264\n"
M_"v[52:55],v[36:39],v[124:127],v[52:55]\n"
W_"lgkmcnt(3)\n"
M_"v[40:43],v[8:11],v[64:67],0\n"
R_"v[32:35],v2 offset:19328\n"
"v_mov_b32 v60, 0x3f803f80\n"
M_"v[44:47],v[8:11],v[68:71],0\n"
M_"v[40:43],v[12:15],v[72:75],v[40:43]\n"
R_"v[36:39],v2 offset:19392\n"
M_"v[44:47],v[12:15],v[76:79],v[44:47]\n"
M_"v[40:43],v[16:19],v[80:83],v[40:43]\n"
R_"v[8:11],v2 offset:27392\n"
"v_cvt_pk_f16_f32 v194, v48, v49\n"
M_"v[44:47],v[16:19],v[84:87],v[44:47]\n"
W_"lgkmcnt(3)\n"
"v_mov_b32 v61, 0x3f803f80\n"
"v_cvt_pk_f16_f32 v195, v50, v51\n"
M_"v[40:43],v[20:23],v[88:91],v[40:43]\n"
R_"v[12:15],v2 offset:27456\n"
M_"v[44:47],v[20:23],v[92:95],v[44:47]\n"
"v_cvt_pk_f16_f32 v198, v52, v53\n"
M_"v[40:43],v[24:27],v[96:99],v[40:43]\n"
R_"v[16:19],v2 offset:27520\n"
"v_mov_b32 v62, 0x3f803f80\n"
M_"v[44:47],v[24:27],v[100:103],v[44:47]\n"
"v_cvt_pk_f16_f32 v199, v54, v55\n"
M_"v[40:43],v[28:31],v[104:107],v[40:43]\n"
R_"v[20:23],v2 offset:27584\n"
"v_pk_maximum3_f16 v56, v56, v194, v195\n"
M_"v[44:47],v[28:31],v[108:111],v[44:47]\n"
W_"lgkmcnt(3)\n"
M_"v[40:43],v[32:35],v[112:115],v[40:43]\n"
R_"v[24:27],v2 offset:27648\n"
"v_pk_maximum3_f16 v57, v57, v198, v199\n"
M_"v[44:47],v[32:35],v[116:119],v[44:47]\n"
"v_mov_b32 v63, 0x3f803f80\n"
M_"v[40:43],v[36:39],v[120:123],v[40:43]\n"
R_"v[28:31],v2 offset:27712\n"
M_"v[44:47],v[36:39],v[124:127],v[44:47]\n"
M_"v[48:51],v[8:11],v[64:67],0\n"
R_"v[32:35],v2 offset:27776\n"
M_"v[52:55],v[8:11],v[68:71],0\n"
W_"lgkmcnt(3)\n"
M_"v[48:51],v[12:15],v[72:75],v[48:51]\n"
R_"v[36:39],v2 offset:27840\n"
M_"v[52:55],v[12:15],v[76:79],v[52:55]\n"
M_"v[48:51],v[16:19],v[80:83],v[48:51]\n"
"s_barrier\n"
R_"v[8:11],v1 offset:0\n"
"v_cvt_pk_f16_f32 v200, v40, v41\n"
M_"v[52:55],v[16:19],v[84:87],v[52:55]\n"
"v_cvt_pk_f16_f32 v201, v42, v43\n"
M_"v[48:51],v[20:23],v[88:91],v[48:51]\n"
R_"v[12:15],v1 offset:64\n"
M_"v[52:55],v[20:23],v[92:95],v[52:55]\n"
W_"lgkmcnt(3)\n"
"v_cvt_pk_f16_f32 v204, v44, v45\n"
M_"v[48:51],v[24:27],v[96:99],v[48:51]\n"
R_"v[16:19],v1 offset:128\n"
M_"v[52:55],v[24:27],v[100:103],v[52:55]\n"
"v_cvt_pk_f16_f32 v205, v46, v47\n"
M_"v[48:51],v[28:31],v[104:107],v[48:51]\n"
R_"v[20:23],v1 offset:192\n"
"v_pk_maximum3_f16 v56, v56, v200, v201\n"
M_"v[52:55],v[28:31],v[108:111],v[52:55]\n"
M_"v[48:51],v[32:35],v[112:115],v[48:51]\n"
R_"v[24:27],v1 offset:256\n"
"v_pk_maximum3_f16 v57, v57, v204, v205\n"
M_"v[52:55],v[32:35],v[116:119],v[52:55]\n"
W_"lgkmcnt(3)\n"
M_"v[48:51],v[36:39],v[120:123],v[48:51]\n"
R_"v[28:31],v1 offset:320\n"
M_"v[52:55],v[36:39],v[124:127],v[52:55]\n"
M_"v[40:43],v[8:11],v[64:67],0\n"
R_"v[32:35],v1 offset:384\n"
M_"v[44:47],v[8:11],v[68:71],0\n"
"s_lshl_b32 s8, s30, 7\n"
M_"v[40:43],v[12:15],v[72:75],v[40:43]\n"
R_"v[36:39],v1 offset:448\n"
M_"v[44:47],v[12:15],v[76:79],v[44:47]\n"
W_"lgkmcnt(3)\n"
"s_lshl_b32 s4, s33, 20\n"
M_"v[40:43],v[16:19],v[80:83],v[40:43]\n"
R_"v[8:11],v1 offset:8448\n"
"v_cvt_pk_f16_f32 v202, v48, v49\n"
M_"v[44:47],v[16:19],v[84:87],v[44:47]\n"
"v_cvt_pk_f16_f32 v203, v50, v51\n"
"s_add_u32 s8, s8, s4\n"
M_"v[40:43],v[20:23],v[88:91],v[40:43]\n"
R_"v[12:15],v1 offset:8512\n"
"v_cvt_pk_f16_f32 v206, v52, v53\n"
M_"v[44:47],v[20:23],v[92:95],v[44:47]\n"
W_"vmcnt(0)\n"
"buffer_load_dword v6, off, s[80:83], s8 sc1\n"
M_"v[40:43],v[24:27],v[96:99],v[40:43]\n"
R_"v[16:19],v1 offset:8576\n"
M_"v[44:47],v[24:27],v[100:103],v[44:47]\n"
W_"lgkmcnt(3)\n"
"v_cvt_pk_f16_f32 v207, v54, v55\n"
M_"v[40:43],v[28:31],v[104:107],v[40:43]\n"
R_"v[20:23],v1 offset:8640\n"
"v_pk_maximum3_f16 v56, v56, v202, v203\n"
M_"v[44:47],v[28:31],v[108:111],v[44:47]\n"
M_"v[40:43],v[32:35],v[112:115],v[40:43]\n"
R_"v[24:27],v1 offset:8704\n"
"v_pk_maximum3_f16 v57, v57, v206, v207\n"
M_"v[44:47],v[32:35],v[116:119],v[44:47]\n"
M_"v[40:43],v[36:39],v[120:123],v[40:43]\n"
R_"v[28:31],v1 offset:8768\n"
M_"v[44:47],v[36:39],v[124:127],v[44:47]\n"
W_"lgkmcnt(3)\n"
M_"v[48:51],v[8:11],v[64:67],0\n"
R_"v[32:35],v1 offset:8832\n"
M_"v[52:55],v[8:11],v[68:71],0\n"
M_"v[48:51],v[12:15],v[72:75],v[48:51]\n"
R_"v[36:39],v1 offset:8896\n"
M_"v[52:55],v[12:15],v[76:79],v[52:55]\n"
M_"v[48:51],v[16:19],v[80:83],v[48:51]\n"
"s_barrier\n"
R_"v[8:11],v1 offset:50688\n"
"v_cvt_pk_f16_f32 v208, v40, v41\n"
M_"v[52:55],v[16:19],v[84:87],v[52:55]\n"
W_"lgkmcnt(3)\n"
"v_cvt_pk_f16_f32 v209, v42, v43\n"
M_"v[48:51],v[20:23],v[88:91],v[48:51]\n"
R_"v[12:15],v1 offset:50752\n"
M_"v[52:55],v[20:23],v[92:95],v[52:55]\n"
"v_cvt_pk_f16_f32 v212, v44, v45\n"
M_"v[48:51],v[24:27],v[96:99],v[48:51]\n"
R_"v[16:19],v1 offset:50816\n"
M_"v[52:55],v[24:27],v[100:103],v[52:55]\n"
"v_cvt_pk_f16_f32 v213, v46, v47\n"
M_"v[48:51],v[28:31],v[104:107],v[48:51]\n"
R_"v[20:23],v1 offset:50880\n"
"v_pk_maximum3_f16 v56, v56, v208, v209\n"
M_"v[52:55],v[28:31],v[108:111],v[52:55]\n"
W_"lgkmcnt(3)\n"
M_"v[48:51],v[32:35],v[112:115],v[48:51]\n"
R_"v[24:27],v1 offset:50944\n"
"v_pk_maximum3_f16 v57, v57, v212, v213\n"
M_"v[52:55],v[32:35],v[116:119],v[52:55]\n"
M_"v[48:51],v[36:39],v[120:123],v[48:51]\n"
R_"v[28:31],v1 offset:51008\n"
M_"v[52:55],v[36:39],v[124:127],v[52:55]\n"
M_"v[40:43],v[8:11],v[64:67],0\n"
R_"v[32:35],v1 offset:51072\n"
M_"v[44:47],v[8:11],v[68:71],0\n"
W_"lgkmcnt(3)\n"
M_"v[40:43],v[12:15],v[72:75],v[40:43]\n"
R_"v[36:39],v1 offset:51136\n"
M_"v[44:47],v[12:15],v[76:79],v[44:47]\n"
M_"v[40:43],v[16:19],v[80:83],v[40:43]\n"
R_"v[8:11],v2 offset:35840\n"
"v_cvt_pk_f16_f32 v210, v48, v49\n"
M_"v[44:47],v[16:19],v[84:87],v[44:47]\n"
"v_cvt_pk_f16_f32 v211, v50, v51\n"
M_"v[40:43],v[20:23],v[88:91],v[40:43]\n"
R_"v[12:15],v2 offset:35904\n"
M_"v[44:47],v[20:23],v[92:95],v[44:47]\n"
W_"lgkmcnt(3)\n"
"v_cvt_pk_f16_f32 v214, v52, v53\n"
M_"v[40:43],v[24:27],v[96:99],v[40:43]\n"
R_"v[16:19],v2 offset:35968\n"
M_"v[44:47],v[24:27],v[100:103],v[44:47]\n"
"v_cvt_pk_f16_f32 v215, v54, v55\n"
M_"v[40:43],v[28:31],v[104:107],v[40:43]\n"
R_"v[20:23],v2 offset:36032\n"
"v_pk_maximum3_f16 v56, v56, v210, v211\n"
M_"v[44:47],v[28:31],v[108:111],v[44:47]\n"
M_"v[40:43],v[32:35],v[112:115],v[40:43]\n"
R_"v[24:27],v2 offset:36096\n"
"v_pk_maximum3_f16 v57, v57, v214, v215\n"
M_"v[44:47],v[32:35],v[116:119],v[44:47]\n"
W_"lgkmcnt(3)\n"
M_"v[40:43],v[36:39],v[120:123],v[40:43]\n"
R_"v[28:31],v2 offset:36160\n"
M_"v[44:47],v[36:39],v[124:127],v[44:47]\n"
M_"v[48:51],v[8:11],v[64:67],0\n"
R_"v[32:35],v2 offset:36224\n"
M_"v[52:55],v[8:11],v[68:71],0\n"
M_"v[48:51],v[12:15],v[72:75],v[48:51]\n"
R_"v[36:39],v2 offset:36288\n"
M_"v[52:55],v[12:15],v[76:79],v[52:55]\n"
W_"lgkmcnt(3)\n"
M_"v[48:51],v[16:19],v[80:83],v[48:51]\n"
"s_barrier\n"
R_"v[8:11],v1 offset:16896\n"
"v_cvt_pk_f16_f32 v216, v40, v41\n"
M_"v[52:55],v[16:19],v[84:87],v[52:55]\n"
"v_cvt_pk_f16_f32 v217, v42, v43\n"
M_"v[48:51],v[20:23],v[88:91],v[48:51]\n"
R_"v[12:15],v1 offset:16960\n"
M_"v[52:55],v[20:23],v[92:95],v[52:55]\n"
"v_cvt_pk_f16_f32 v220, v44, v45\n"
M_"v[48:51],v[24:27],v[96:99],v[48:51]\n"
R_"v[16:19],v1 offset:17024\n"
M_"v[52:55],v[24:27],v[100:103],v[52:55]\n"
W_"lgkmcnt(3)\n"
"v_cvt_pk_f16_f32 v221, v46, v47\n"
M_"v[48:51],v[28:31],v[104:107],v[48:51]\n"
R_"v[20:23],v1 offset:17088\n"
"v_pk_maximum3_f16 v56, v56, v216, v217\n"
M_"v[52:55],v[28:31],v[108:111],v[52:55]\n"
M_"v[48:51],v[32:35],v[112:115],v[48:51]\n"
R_"v[24:27],v1 offset:17152\n"
"v_pk_maximum3_f16 v57, v57, v220, v221\n"
M_"v[52:55],v[32:35],v[116:119],v[52:55]\n"
M_"v[48:51],v[36:39],v[120:123],v[48:51]\n"
R_"v[28:31],v1 offset:17216\n"
M_"v[52:55],v[36:39],v[124:127],v[52:55]\n"
W_"lgkmcnt(3)\n"
M_"v[40:43],v[8:11],v[64:67],0\n"
R_"v[32:35],v1 offset:17280\n"
M_"v[44:47],v[8:11],v[68:71],0\n"
M_"v[40:43],v[12:15],v[72:75],v[40:43]\n"
R_"v[36:39],v1 offset:17344\n"
M_"v[44:47],v[12:15],v[76:79],v[44:47]\n"
M_"v[40:43],v[16:19],v[80:83],v[40:43]\n"
R_"v[8:11],v1 offset:25344\n"
"v_cvt_pk_f16_f32 v218, v48, v49\n"
M_"v[44:47],v[16:19],v[84:87],v[44:47]\n"
W_"lgkmcnt(3)\n"
"v_cvt_pk_f16_f32 v219, v50, v51\n"
M_"v[40:43],v[20:23],v[88:91],v[40:43]\n"
R_"v[12:15],v1 offset:25408\n"
M_"v[44:47],v[20:23],v[92:95],v[44:47]\n"
"v_cvt_pk_f16_f32 v222, v52, v53\n"
M_"v[40:43],v[24:27],v[96:99],v[40:43]\n"
R_"v[16:19],v1 offset:25472\n"
M_"v[44:47],v[24:27],v[100:103],v[44:47]\n"
"v_cvt_pk_f16_f32 v223, v54, v55\n"
M_"v[40:43],v[28:31],v[104:107],v[40:43]\n"
R_"v[20:23],v1 offset:25536\n"
"v_pk_maximum3_f16 v56, v56, v218, v219\n"
M_"v[44:47],v[28:31],v[108:111],v[44:47]\n"
W_"lgkmcnt(3)\n"
M_"v[40:43],v[32:35],v[112:115],v[40:43]\n"
"PTL10:\n"
"s_cbranch_execz PTR\n"
R_"v[24:27],v1 offset:25600\n"
"v_pk_maximum3_f16 v57, v57, v222, v223\n"
M_"v[44:47],v[32:35],v[116:119],v[44:47]\n"
M_"v[40:43],v[36:39],v[120:123],v[40:43]\n"
R_"v[28:31],v1 offset:25664\n"
M_"v[44:47],v[36:39],v[124:127],v[44:47]\n"
M_"v[48:51],v[8:11],v[64:67],0\n"
R_"v[32:35],v1 offset:25728\n"
M_"v[52:55],v[8:11],v[68:71],0\n"
W_"lgkmcnt(3)\n"
M_"v[48:51],v[12:15],v[72:75],v[48:51]\n"
R_"v[36:39],v1 offset:25792\n"
M_"v[52:55],v[12:15],v[76:79],v[52:55]\n"
M_"v[48:51],v[16:19],v[80:83],v[48:51]\n"
"v_cvt_pk_f16_f32 v224, v40, v41\n"
M_"v[52:55],v[16:19],v[84:87],v[52:55]\n"
"v_cvt_pk_f16_f32 v225, v42, v43\n"
M_"v[48:51],v[20:23],v[88:91],v[48:51]\n"
"s_barrier\n"
M_"v[52:55],v[20:23],v[92:95],v[52:55]\n"
W_"lgkmcnt(1)\n"
"v_cvt_pk_f16_f32 v228, v44, v45\n"
M_"v[48:51],v[24:27],v[96:99],v[48:51]\n"
M_"v[52:55],v[24:27],v[100:103],v[52:55]\n"
"v_cvt_pk_f16_f32 v229, v46, v47\n"
M_"v[48:51],v[28:31],v[104:107],v[48:51]\n"
"v_pk_maximum3_f16 v56, v56, v224, v225\n"
M_"v[52:55],v[28:31],v[108:111],v[52:55]\n"
M_"v[48:51],v[32:35],v[112:115],v[48:51]\n"
"v_pk_maximum3_f16 v57, v57, v228, v229\n"
M_"v[52:55],v[32:35],v[116:119],v[52:55]\n"
W_"lgkmcnt(0)\n"
M_"v[48:51],v[36:39],v[120:123],v[48:51]\n"
M_"v[52:55],v[36:39],v[124:127],v[52:55]\n"
"s_nop 6\n"
"v_cvt_pk_f16_f32 v226, v48, v49\n"
"v_cvt_pk_f16_f32 v227, v50, v51\n"
"v_cvt_pk_f16_f32 v230, v52, v53\n"
"v_cvt_pk_f16_f32 v231, v54, v55\n"
"v_pk_maximum3_f16 v56, v56, v226, v227\n"
"v_pk_maximum3_f16 v57, v57, v230, v231\n"
"v_pk_max_f16 v48, v56, v56 op_sel:[0,1] op_sel_hi:[1,0]\n"
"v_pk_max_f16 v49, v57, v57 op_sel:[0,1] op_sel_hi:[1,0]\n"
"v_bfi_b32 v48, s93, v49, v48\n"
"v_mov_b32 v49, v48\n"
"s_nop 1\n"
"v_permlane32_swap_b32 v48, v49\n"
"v_pk_max_f16 v48, v48, v49\n"
"v_mov_b32 v49, v48\n"
"s_nop 1\n"
"v_permlane16_swap_b32 v48, v49\n"
"v_pk_max_f16 v48, v48, v49\n"
X_"v50,v48,s94,0 op_sel_hi:[1,0,0]\n"
X_"v51,v48,s94,0 op_sel:[1,0,0] op_sel_hi:[1,0,0]\n"
"v_ceil_f32 v50, v50\n"
"v_ceil_f32 v51, v51\n"
"v_sub_f32 v58, 0xc2c80000, v50\n"
"v_sub_f32 v59, 0xc2c80000, v51\n"
X_"v40,v192,s94,v58 op_sel_hi:[1,0,0]\n"
"s_barrier\n"
X_"v41,v192,s94,v58 op_sel:[1,0,0] op_sel_hi:[1,0,0]\n"
X_"v42,v193,s94,v58 op_sel_hi:[1,0,0]\n"
X_"v43,v193,s94,v58 op_sel:[1,0,0] op_sel_hi:[1,0,0]\n"
E_"v40,v40\n"
E_"v41,v41\n"
X_"v44,v194,s94,v58 op_sel_hi:[1,0,0]\n"
X_"v45,v194,s94,v58 op_sel:[1,0,0] op_sel_hi:[1,0,0]\n"
E_"v42,v42\n"
E_"v43,v43\n"
C_"v192,v40,v41\n"
X_"v46,v195,s94,v58 op_sel_hi:[1,0,0]\n"
X_"v47,v195,s94,v58 op_sel:[1,0,0] op_sel_hi:[1,0,0]\n"
E_"v44,v44\n"
E_"v45,v45\n"
C_"v193,v42,v43\n"
E_"v46,v46\n"
E_"v47,v47\n"
C_"v194,v44,v45\n"
C_"v195,v46,v47\n"
X_"v40,v196,s94,v59 op_sel_hi:[1,0,0]\n"
X_"v41,v196,s94,v59 op_sel:[1,0,0] op_sel_hi:[1,0,0]\n"
X_"v42,v197,s94,v59 op_sel_hi:[1,0,0]\n"
X_"v43,v197,s94,v59 op_sel:[1,0,0] op_sel_hi:[1,0,0]\n"
E_"v40,v40\n"
E_"v41,v41\n"
X_"v44,v198,s94,v59 op_sel_hi:[1,0,0]\n"
X_"v45,v198,s94,v59 op_sel:[1,0,0] op_sel_hi:[1,0,0]\n"
E_"v42,v42\n"
E_"v43,v43\n"
C_"v196,v40,v41\n"
"s_barrier\n"
X_"v46,v199,s94,v59 op_sel_hi:[1,0,0]\n"
X_"v47,v199,s94,v59 op_sel:[1,0,0] op_sel_hi:[1,0,0]\n"
E_"v44,v44\n"
E_"v45,v45\n"
C_"v197,v42,v43\n"
E_"v46,v46\n"
E_"v47,v47\n"
C_"v198,v44,v45\n"
C_"v199,v46,v47\n"
X_"v40,v200,s94,v58 op_sel_hi:[1,0,0]\n"
X_"v41,v200,s94,v58 op_sel:[1,0,0] op_sel_hi:[1,0,0]\n"
X_"v42,v201,s94,v58 op_sel_hi:[1,0,0]\n"
X_"v43,v201,s94,v58 op_sel:[1,0,0] op_sel_hi:[1,0,0]\n"
E_"v40,v40\n"
E_"v41,v41\n"
X_"v44,v202,s94,v58 op_sel_hi:[1,0,0]\n"
X_"v45,v202,s94,v58 op_sel:[1,0,0] op_sel_hi:[1,0,0]\n"
E_"v42,v42\n"
E_"v43,v43\n"
C_"v200,v40,v41\n"
X_"v46,v203,s94,v58 op_sel_hi:[1,0,0]\n"
X_"v47,v203,s94,v58 op_sel:[1,0,0] op_sel_hi:[1,0,0]\n"
E_"v44,v44\n"
E_"v45,v45\n"
C_"v201,v42,v43\n"
E_"v46,v46\n"
E_"v47,v47\n"
C_"v202,v44,v45\n"
C_"v203,v46,v47\n"
X_"v40,v204,s94,v59 op_sel_hi:[1,0,0]\n"
"s_barrier\n"
X_"v41,v204,s94,v59 op_sel:[1,0,0] op_sel_hi:[1,0,0]\n"
X_"v42,v205,s94,v59 op_sel_hi:[1,0,0]\n"
X_"v43,v205,s94,v59 op_sel:[1,0,0] op_sel_hi:[1,0,0]\n"
E_"v40,v40\n"
E_"v41,v41\n"
X_"v44,v206,s94,v59 op_sel_hi:[1,0,0]\n"
X_"v45,v206,s94,v59 op_sel:[1,0,0] op_sel_hi:[1,0,0]\n"
E_"v42,v42\n"
E_"v43,v43\n"
C_"v204,v40,v41\n"
X_"v46,v207,s94,v59 op_sel_hi:[1,0,0]\n"
X_"v47,v207,s94,v59 op_sel:[1,0,0] op_sel_hi:[1,0,0]\n"
E_"v44,v44\n"
E_"v45,v45\n"
C_"v205,v42,v43\n"
E_"v46,v46\n"
E_"v47,v47\n"
C_"v206,v44,v45\n"
C_"v207,v46,v47\n"
X_"v40,v208,s94,v58 op_sel_hi:[1,0,0]\n"
X_"v41,v208,s94,v58 op_sel:[1,0,0] op_sel_hi:[1,0,0]\n"
X_"v42,v209,s94,v58 op_sel_hi:[1,0,0]\n"
X_"v43,v209,s94,v58 op_sel:[1,0,0] op_sel_hi:[1,0,0]\n"
E_"v40,v40\n"
E_"v41,v41\n"
X_"v44,v210,s94,v58 op_sel_hi:[1,0,0]\n"
X_"v45,v210,s94,v58 op_sel:[1,0,0] op_sel_hi:[1,0,0]\n"
E_"v42,v42\n"
E_"v43,v43\n"
C_"v208,v40,v41\n"
"s_barrier\n"
X_"v46,v211,s94,v58 op_sel_hi:[1,0,0]\n"
X_"v47,v211,s94,v58 op_sel:[1,0,0] op_sel_hi:[1,0,0]\n"
E_"v44,v44\n"
E_"v45,v45\n"
C_"v209,v42,v43\n"
E_"v46,v46\n"
E_"v47,v47\n"
C_"v210,v44,v45\n"
C_"v211,v46,v47\n"
X_"v40,v212,s94,v59 op_sel_hi:[1,0,0]\n"
X_"v41,v212,s94,v59 op_sel:[1,0,0] op_sel_hi:[1,0,0]\n"
X_"v42,v213,s94,v59 op_sel_hi:[1,0,0]\n"
X_"v43,v213,s94,v59 op_sel:[1,0,0] op_sel_hi:[1,0,0]\n"
E_"v40,v40\n"
E_"v41,v41\n"
X_"v44,v214,s94,v59 op_sel_hi:[1,0,0]\n"
X_"v45,v214,s94,v59 op_sel:[1,0,0] op_sel_hi:[1,0,0]\n"
E_"v42,v42\n"
E_"v43,v43\n"
C_"v212,v40,v41\n"
X_"v46,v215,s94,v59 op_sel_hi:[1,0,0]\n"
X_"v47,v215,s94,v59 op_sel:[1,0,0] op_sel_hi:[1,0,0]\n"
E_"v44,v44\n"
E_"v45,v45\n"
C_"v213,v42,v43\n"
E_"v46,v46\n"
E_"v47,v47\n"
C_"v214,v44,v45\n"
C_"v215,v46,v47\n"
X_"v40,v216,s94,v58 op_sel_hi:[1,0,0]\n"
"s_barrier\n"
X_"v41,v216,s94,v58 op_sel:[1,0,0] op_sel_hi:[1,0,0]\n"
X_"v42,v217,s94,v58 op_sel_hi:[1,0,0]\n"
X_"v43,v217,s94,v58 op_sel:[1,0,0] op_sel_hi:[1,0,0]\n"
E_"v40,v40\n"
E_"v41,v41\n"
X_"v44,v218,s94,v58 op_sel_hi:[1,0,0]\n"
X_"v45,v218,s94,v58 op_sel:[1,0,0] op_sel_hi:[1,0,0]\n"
E_"v42,v42\n"
E_"v43,v43\n"
C_"v216,v40,v41\n"
X_"v46,v219,s94,v58 op_sel_hi:[1,0,0]\n"
X_"v47,v219,s94,v58 op_sel:[1,0,0] op_sel_hi:[1,0,0]\n"
E_"v44,v44\n"
E_"v45,v45\n"
C_"v217,v42,v43\n"
E_"v46,v46\n"
E_"v47,v47\n"
C_"v218,v44,v45\n"
C_"v219,v46,v47\n"
X_"v40,v220,s94,v59 op_sel_hi:[1,0,0]\n"
X_"v41,v220,s94,v59 op_sel:[1,0,0] op_sel_hi:[1,0,0]\n"
X_"v42,v221,s94,v59 op_sel_hi:[1,0,0]\n"
X_"v43,v221,s94,v59 op_sel:[1,0,0] op_sel_hi:[1,0,0]\n"
E_"v40,v40\n"
E_"v41,v41\n"
X_"v44,v222,s94,v59 op_sel_hi:[1,0,0]\n"
X_"v45,v222,s94,v59 op_sel:[1,0,0] op_sel_hi:[1,0,0]\n"
E_"v42,v42\n"
E_"v43,v43\n"
C_"v220,v40,v41\n"
"s_barrier\n"
X_"v46,v223,s94,v59 op_sel_hi:[1,0,0]\n"
X_"v47,v223,s94,v59 op_sel:[1,0,0] op_sel_hi:[1,0,0]\n"
E_"v44,v44\n"
E_"v45,v45\n"
C_"v221,v42,v43\n"
E_"v46,v46\n"
E_"v47,v47\n"
C_"v222,v44,v45\n"
C_"v223,v46,v47\n"
X_"v40,v224,s94,v58 op_sel_hi:[1,0,0]\n"
X_"v41,v224,s94,v58 op_sel:[1,0,0] op_sel_hi:[1,0,0]\n"
X_"v42,v225,s94,v58 op_sel_hi:[1,0,0]\n"
X_"v43,v225,s94,v58 op_sel:[1,0,0] op_sel_hi:[1,0,0]\n"
E_"v40,v40\n"
E_"v41,v41\n"
X_"v44,v226,s94,v58 op_sel_hi:[1,0,0]\n"
X_"v45,v226,s94,v58 op_sel:[1,0,0] op_sel_hi:[1,0,0]\n"
E_"v42,v42\n"
E_"v43,v43\n"
C_"v224,v40,v41\n"
X_"v46,v227,s94,v58 op_sel_hi:[1,0,0]\n"
X_"v47,v227,s94,v58 op_sel:[1,0,0] op_sel_hi:[1,0,0]\n"
E_"v44,v44\n"
E_"v45,v45\n"
C_"v225,v42,v43\n"
E_"v46,v46\n"
E_"v47,v47\n"
C_"v226,v44,v45\n"
C_"v227,v46,v47\n"
"s_barrier\n"
R_"v[8:11],v1 offset:0\n"
R_"v[12:15],v1 offset:64\n"
R_"v[16:19],v1 offset:128\n"
R_"v[20:23],v1 offset:192\n"
R_"v[24:27],v1 offset:256\n"
R_"v[28:31],v1 offset:320\n"
X_"v40,v228,s94,v59 op_sel_hi:[1,0,0]\n"
X_"v41,v228,s94,v59 op_sel:[1,0,0] op_sel_hi:[1,0,0]\n"
X_"v42,v229,s94,v59 op_sel_hi:[1,0,0]\n"
X_"v43,v229,s94,v59 op_sel:[1,0,0] op_sel_hi:[1,0,0]\n"
E_"v40,v40\n"
E_"v41,v41\n"
X_"v44,v230,s94,v59 op_sel_hi:[1,0,0]\n"
X_"v45,v230,s94,v59 op_sel:[1,0,0] op_sel_hi:[1,0,0]\n"
E_"v42,v42\n"
E_"v43,v43\n"
C_"v228,v40,v41\n"
X_"v46,v231,s94,v59 op_sel_hi:[1,0,0]\n"
X_"v47,v231,s94,v59 op_sel:[1,0,0] op_sel_hi:[1,0,0]\n"
E_"v44,v44\n"
E_"v45,v45\n"
C_"v229,v42,v43\n"
E_"v46,v46\n"
E_"v47,v47\n"
C_"v230,v44,v45\n"
C_"v231,v46,v47\n"
W_"lgkmcnt(5)\n"
M_"v[40:43],v[8:11],v[64:67],0\n"
R_"v[32:35],v1 offset:384\n"
M_"v[44:47],v[8:11],v[68:71],0\n"
W_"lgkmcnt(5)\n"
M_"v[40:43],v[12:15],v[72:75],v[40:43]\n"
R_"v[36:39],v1 offset:448\n"
M_"v[44:47],v[12:15],v[76:79],v[44:47]\n"
W_"lgkmcnt(5)\n"
M_"v[40:43],v[16:19],v[80:83],v[40:43]\n"
R_"v[8:11],v1 offset:59136\n"
M_"v[44:47],v[16:19],v[84:87],v[44:47]\n"
W_"lgkmcnt(5)\n"
M_"v[40:43],v[20:23],v[88:91],v[40:43]\n"
R_"v[12:15],v1 offset:59200\n"
M_"v[44:47],v[20:23],v[92:95],v[44:47]\n"
W_"lgkmcnt(3)\n"
M_"v[40:43],v[24:27],v[96:99],v[40:43]\n"
R_"v[16:19],v1 offset:59264\n"
M_"v[44:47],v[24:27],v[100:103],v[44:47]\n"
M_"v[40:43],v[28:31],v[104:107],v[40:43]\n"
R_"v[20:23],v1 offset:59328\n"
M_"v[44:47],v[28:31],v[108:111],v[44:47]\n"
M_"v[40:43],v[32:35],v[112:115],v[40:43]\n"
R_"v[24:27],v1 offset:59392\n"
M_"v[44:47],v[32:35],v[116:119],v[44:47]\n"
W_"lgkmcnt(3)\n"
M_"v[40:43],v[36:39],v[120:123],v[40:43]\n"
R_"v[28:31],v1 offset:59456\n"
M_"v[44:47],v[36:39],v[124:127],v[44:47]\n"
M_"v[48:51],v[8:11],v[64:67],0\n"
R_"v[32:35],v1 offset:59520\n"
M_"v[52:55],v[8:11],v[68:71],0\n"
M_"v[48:51],v[12:15],v[72:75],v[48:51]\n"
R_"v[36:39],v1 offset:59584\n"
M_"v[52:55],v[12:15],v[76:79],v[52:55]\n"
W_"lgkmcnt(3)\n"
M_"v[48:51],v[16:19],v[80:83],v[48:51]\n"
"s_barrier\n"
R_"v[8:11],v2 offset:2048\n"
"v_cvt_pk_f16_f32 v232, v40, v41\n"
M_"v[52:55],v[16:19],v[84:87],v[52:55]\n"
"v_cvt_pk_f16_f32 v233, v42, v43\n"
M_"v[48:51],v[20:23],v[88:91],v[48:51]\n"
R_"v[12:15],v2 offset:2112\n"
M_"v[52:55],v[20:23],v[92:95],v[52:55]\n"
"v_cvt_pk_f16_f32 v236, v44, v45\n"
M_"v[48:51],v[24:27],v[96:99],v[48:51]\n"
R_"v[16:19],v2 offset:2176\n"
M_"v[52:55],v[24:27],v[100:103],v[52:55]\n"
W_"lgkmcnt(3)\n"
"v_cvt_pk_f16_f32 v237, v46, v47\n"
M_"v[48:51],v[28:31],v[104:107],v[48:51]\n"
R_"v[20:23],v2 offset:2240\n"
"v_pk_maximum3_f16 v56, v56, v232, v233\n"
M_"v[52:55],v[28:31],v[108:111],v[52:55]\n"
M_"v[48:51],v[32:35],v[112:115],v[48:51]\n"
R_"v[24:27],v2 offset:2304\n"
"v_pk_maximum3_f16 v57, v57, v236, v237\n"
M_"v[52:55],v[32:35],v[116:119],v[52:55]\n"
M_"v[48:51],v[36:39],v[120:123],v[48:51]\n"
R_"v[28:31],v2 offset:2368\n"
M_"v[52:55],v[36:39],v[124:127],v[52:55]\n"
W_"lgkmcnt(3)\n"
M_"v[40:43],v[8:11],v[64:67],0\n"
R_"v[32:35],v2 offset:2432\n"
M_"v[44:47],v[8:11],v[68:71],0\n"
M_"v[40:43],v[12:15],v[72:75],v[40:43]\n"
R_"v[36:39],v2 offset:2496\n"
M_"v[44:47],v[12:15],v[76:79],v[44:47]\n"
M_"v[40:43],v[16:19],v[80:83],v[40:43]\n"
R_"v[8:11],v2 offset:10496\n"
"v_cvt_pk_f16_f32 v234, v48, v49\n"
M_"v[44:47],v[16:19],v[84:87],v[44:47]\n"
W_"lgkmcnt(3)\n"
"v_cvt_pk_f16_f32 v235, v50, v51\n"
M_"v[40:43],v[20:23],v[88:91],v[40:43]\n"
R_"v[12:15],v2 offset:10560\n"
M_"v[44:47],v[20:23],v[92:95],v[44:47]\n"
"v_cvt_pk_f16_f32 v238, v52, v53\n"
M_"v[40:43],v[24:27],v[96:99],v[40:43]\n"
R_"v[16:19],v2 offset:10624\n"
M_"v[44:47],v[24:27],v[100:103],v[44:47]\n"
"v_cvt_pk_f16_f32 v239, v54, v55\n"
M_"v[40:43],v[28:31],v[104:107],v[40:43]\n"
R_"v[20:23],v2 offset:10688\n"
"v_pk_maximum3_f16 v56, v56, v234, v235\n"
M_"v[44:47],v[28:31],v[108:111],v[44:47]\n"
W_"lgkmcnt(3)\n"
M_"v[40:43],v[32:35],v[112:115],v[40:43]\n"
R_"v[24:27],v2 offset:10752\n"
"v_pk_maximum3_f16 v57, v57, v238, v239\n"
M_"v[44:47],v[32:35],v[116:119],v[44:47]\n"
M_"v[40:43],v[36:39],v[120:123],v[40:43]\n"
R_"v[28:31],v2 offset:10816\n"
M_"v[44:47],v[36:39],v[124:127],v[44:47]\n"
M_"v[48:51],v[8:11],v[64:67],0\n"
R_"v[32:35],v2 offset:10880\n"
M_"v[52:55],v[8:11],v[68:71],0\n"
W_"lgkmcnt(3)\n"
M_"v[48:51],v[12:15],v[72:75],v[48:51]\n"
R_"v[36:39],v2 offset:10944\n"
M_"v[52:55],v[12:15],v[76:79],v[52:55]\n"
M_"v[48:51],v[16:19],v[80:83],v[48:51]\n"
"s_barrier\n"
R_"v[8:11],v2 offset:18944\n"
"v_cvt_pk_f16_f32 v240, v40, v41\n"
M_"v[52:55],v[16:19],v[84:87],v[52:55]\n"
"v_cvt_pk_f16_f32 v241, v42, v43\n"
M_"v[48:51],v[20:23],v[88:91],v[48:51]\n"
R_"v[12:15],v2 offset:19008\n"
M_"v[52:55],v[20:23],v[92:95],v[52:55]\n"
W_"lgkmcnt(3)\n"
"v_cvt_pk_f16_f32 v244, v44, v45\n"
M_"v[48:51],v[24:27],v[96:99],v[48:51]\n"
R_"v[16:19],v2 offset:19072\n"
M_"v[52:55],v[24:27],v[100:103],v[52:55]\n"
"v_cvt_pk_f16_f32 v245, v46, v47\n"
M_"v[48:51],v[28:31],v[104:107],v[48:51]\n"
R_"v[20:23],v2 offset:19136\n"
"v_pk_maximum3_f16 v56, v56, v240, v241\n"
M_"v[52:55],v[28:31],v[108:111],v[52:55]\n"
M_"v[48:51],v[32:35],v[112:115],v[48:51]\n"
R_"v[24:27],v2 offset:19200\n"
"v_pk_maximum3_f16 v57, v57, v244, v245\n"
M_"v[52:55],v[32:35],v[116:119],v[52:55]\n"
W_"lgkmcnt(3)\n"
M_"v[48:51],v[36:39],v[120:123],v[48:51]\n"
R_"v[28:31],v2 offset:19264\n"
M_"v[52:55],v[36:39],v[124:127],v[52:55]\n"
M_"v[40:43],v[8:11],v[64:67],0\n"
R_"v[32:35],v2 offset:19328\n"
M_"v[44:47],v[8:11],v[68:71],0\n"
M_"v[40:43],v[12:15],v[72:75],v[40:43]\n"
R_"v[36:39],v2 offset:19392\n"
M_"v[44:47],v[12:15],v[76:79],v[44:47]\n"
W_"lgkmcnt(3)\n"
M_"v[40:43],v[16:19],v[80:83],v[40:43]\n"
R_"v[8:11],v2 offset:27392\n"
"v_cvt_pk_f16_f32 v242, v48, v49\n"
M_"v[44:47],v[16:19],v[84:87],v[44:47]\n"
"v_cvt_pk_f16_f32 v243, v50, v51\n"
M_"v[40:43],v[20:23],v[88:91],v[40:43]\n"
R_"v[12:15],v2 offset:27456\n"
M_"v[44:47],v[20:23],v[92:95],v[44:47]\n"
"v_cvt_pk_f16_f32 v246, v52, v53\n"
M_"v[40:43],v[24:27],v[96:99],v[40:43]\n"
R_"v[16:19],v2 offset:27520\n"
M_"v[44:47],v[24:27],v[100:103],v[44:47]\n"
W_"lgkmcnt(3)\n"
"v_cvt_pk_f16_f32 v247, v54, v55\n"
M_"v[40:43],v[28:31],v[104:107],v[40:43]\n"
R_"v[20:23],v2 offset:27584\n"
"v_pk_maximum3_f16 v56, v56, v242, v243\n"
M_"v[44:47],v[28:31],v[108:111],v[44:47]\n"
M_"v[40:43],v[32:35],v[112:115],v[40:43]\n"
R_"v[24:27],v2 offset:27648\n"
"v_pk_maximum3_f16 v57, v57, v246, v247\n"
M_"v[44:47],v[32:35],v[116:119],v[44:47]\n"
M_"v[40:43],v[36:39],v[120:123],v[40:43]\n"
R_"v[28:31],v2 offset:27712\n"
M_"v[44:47],v[36:39],v[124:127],v[44:47]\n"
W_"lgkmcnt(3)\n"
M_"v[48:51],v[8:11],v[64:67],0\n"
R_"v[32:35],v2 offset:27776\n"
M_"v[52:55],v[8:11],v[68:71],0\n"
M_"v[48:51],v[12:15],v[72:75],v[48:51]\n"
R_"v[36:39],v2 offset:27840\n"
M_"v[52:55],v[12:15],v[76:79],v[52:55]\n"
M_"v[48:51],v[16:19],v[80:83],v[48:51]\n"
"s_barrier\n"
R_"v[8:11],v1 offset:8448\n"
"v_cvt_pk_f16_f32 v248, v40, v41\n"
M_"v[52:55],v[16:19],v[84:87],v[52:55]\n"
W_"lgkmcnt(3)\n"
"v_cvt_pk_f16_f32 v249, v42, v43\n"
M_"v[48:51],v[20:23],v[88:91],v[48:51]\n"
R_"v[12:15],v1 offset:8512\n"
M_"v[52:55],v[20:23],v[92:95],v[52:55]\n"
"v_cvt_pk_f16_f32 v252, v44, v45\n"
M_"v[48:51],v[24:27],v[96:99],v[48:51]\n"
R_"v[16:19],v1 offset:8576\n"
M_"v[52:55],v[24:27],v[100:103],v[52:55]\n"
"v_cvt_pk_f16_f32 v253, v46, v47\n"
M_"v[48:51],v[28:31],v[104:107],v[48:51]\n"
R_"v[20:23],v1 offset:8640\n"
"v_pk_maximum3_f16 v56, v56, v248, v249\n"
M_"v[52:55],v[28:31],v[108:111],v[52:55]\n"
W_"lgkmcnt(3)\n"
M_"v[48:51],v[32:35],v[112:115],v[48:51]\n"
R_"v[24:27],v1 offset:8704\n"
"v_pk_maximum3_f16 v57, v57, v252, v253\n"
M_"v[52:55],v[32:35],v[116:119],v[52:55]\n"
M_"v[48:51],v[36:39],v[120:123],v[48:51]\n"
R_"v[28:31],v1 offset:8768\n"
M_"v[52:55],v[36:39],v[124:127],v[52:55]\n"
M_"v[40:43],v[8:11],v[64:67],0\n"
R_"v[32:35],v1 offset:8832\n"
M_"v[44:47],v[8:11],v[68:71],0\n"
W_"lgkmcnt(3)\n"
M_"v[40:43],v[12:15],v[72:75],v[40:43]\n"
R_"v[36:39],v1 offset:8896\n"
M_"v[44:47],v[12:15],v[76:79],v[44:47]\n"
M_"v[40:43],v[16:19],v[80:83],v[40:43]\n"
R_"v[8:11],v1 offset:50688\n"
"v_cvt_pk_f16_f32 v250, v48, v49\n"
M_"v[44:47],v[16:19],v[84:87],v[44:47]\n"
"v_cvt_pk_f16_f32 v251, v50, v51\n"
M_"v[40:43],v[20:23],v[88:91],v[40:43]\n"
R_"v[12:15],v1 offset:50752\n"
M_"v[44:47],v[20:23],v[92:95],v[44:47]\n"
W_"lgkmcnt(3)\n"
"v_cvt_pk_f16_f32 v254, v52, v53\n"
M_"v[40:43],v[24:27],v[96:99],v[40:43]\n"
R_"v[16:19],v1 offset:50816\n"
M_"v[44:47],v[24:27],v[100:103],v[44:47]\n"
"v_cvt_pk_f16_f32 v255, v54, v55\n"
M_"v[40:43],v[28:31],v[104:107],v[40:43]\n"
R_"v[20:23],v1 offset:50880\n"
"v_pk_maximum3_f16 v56, v56, v250, v251\n"
M_"v[44:47],v[28:31],v[108:111],v[44:47]\n"
M_"v[40:43],v[32:35],v[112:115],v[40:43]\n"
R_"v[24:27],v1 offset:50944\n"
"v_pk_maximum3_f16 v57, v57, v254, v255\n"
M_"v[44:47],v[32:35],v[116:119],v[44:47]\n"
W_"lgkmcnt(3)\n"
M_"v[40:43],v[36:39],v[120:123],v[40:43]\n"
R_"v[28:31],v1 offset:51008\n"
M_"v[44:47],v[36:39],v[124:127],v[44:47]\n"
M_"v[48:51],v[8:11],v[64:67],0\n"
R_"v[32:35],v1 offset:51072\n"
M_"v[52:55],v[8:11],v[68:71],0\n"
M_"v[48:51],v[12:15],v[72:75],v[48:51]\n"
R_"v[36:39],v1 offset:51136\n"
M_"v[52:55],v[12:15],v[76:79],v[52:55]\n"
W_"lgkmcnt(3)\n"
M_"v[48:51],v[16:19],v[80:83],v[48:51]\n"
"s_barrier\n"
R_"v[8:11],v2 offset:44288\n"
"v_cvt_pk_f16_f32 v128, v40, v41\n"
M_"v[52:55],v[16:19],v[84:87],v[52:55]\n"
"v_cvt_pk_f16_f32 v129, v42, v43\n"
M_"v[48:51],v[20:23],v[88:91],v[48:51]\n"
R_"v[12:15],v2 offset:44352\n"
M_"v[52:55],v[20:23],v[92:95],v[52:55]\n"
"v_cvt_pk_f16_f32 v132, v44, v45\n"
M_"v[48:51],v[24:27],v[96:99],v[48:51]\n"
R_"v[16:19],v2 offset:44416\n"
M_"v[52:55],v[24:27],v[100:103],v[52:55]\n"
W_"lgkmcnt(3)\n"
"v_cvt_pk_f16_f32 v133, v46, v47\n"
M_"v[48:51],v[28:31],v[104:107],v[48:51]\n"
R_"v[20:23],v2 offset:44480\n"
"v_pk_maximum3_f16 v56, v56, v128, v129\n"
M_"v[52:55],v[28:31],v[108:111],v[52:55]\n"
M_"v[48:51],v[32:35],v[112:115],v[48:51]\n"
R_"v[24:27],v2 offset:44544\n"
"v_pk_maximum3_f16 v57, v57, v132, v133\n"
M_"v[52:55],v[32:35],v[116:119],v[52:55]\n"
M_"v[48:51],v[36:39],v[120:123],v[48:51]\n"
R_"v[28:31],v2 offset:44608\n"
M_"v[52:55],v[36:39],v[124:127],v[52:55]\n"
W_"lgkmcnt(3)\n"
M_"v[40:43],v[8:11],v[64:67],0\n"
R_"v[32:35],v2 offset:44672\n"
M_"v[44:47],v[8:11],v[68:71],0\n"
M_"v[40:43],v[12:15],v[72:75],v[40:43]\n"
R_"v[36:39],v2 offset:44736\n"
M_"v[44:47],v[12:15],v[76:79],v[44:47]\n"
M_"v[40:43],v[16:19],v[80:83],v[40:43]\n"
R_"v[8:11],v1 offset:16896\n"
"v_cvt_pk_f16_f32 v130, v48, v49\n"
M_"v[44:47],v[16:19],v[84:87],v[44:47]\n"
W_"lgkmcnt(3)\n"
"v_cvt_pk_f16_f32 v131, v50, v51\n"
M_"v[40:43],v[20:23],v[88:91],v[40:43]\n"
R_"v[12:15],v1 offset:16960\n"
M_"v[44:47],v[20:23],v[92:95],v[44:47]\n"
"v_cvt_pk_f16_f32 v134, v52, v53\n"
M_"v[40:43],v[24:27],v[96:99],v[40:43]\n"
R_"v[16:19],v1 offset:17024\n"
M_"v[44:47],v[24:27],v[100:103],v[44:47]\n"
"v_cvt_pk_f16_f32 v135, v54, v55\n"
M_"v[40:43],v[28:31],v[104:107],v[40:43]\n"
R_"v[20:23],v1 offset:17088\n"
"v_pk_maximum3_f16 v56, v56, v130, v131\n"
M_"v[44:47],v[28:31],v[108:111],v[44:47]\n"
W_"lgkmcnt(3)\n"
M_"v[40:43],v[32:35],v[112:115],v[40:43]\n"
R_"v[24:27],v1 offset:17152\n"
"v_pk_maximum3_f16 v57, v57, v134, v135\n"
M_"v[44:47],v[32:35],v[116:119],v[44:47]\n"
M_"v[40:43],v[36:39],v[120:123],v[40:43]\n"
R_"v[28:31],v1 offset:17216\n"
M_"v[44:47],v[36:39],v[124:127],v[44:47]\n"
M_"v[48:51],v[8:11],v[64:67],0\n"
R_"v[32:35],v1 offset:17280\n"
M_"v[52:55],v[8:11],v[68:71],0\n"
W_"lgkmcnt(3)\n"
M_"v[48:51],v[12:15],v[72:75],v[48:51]\n"
R_"v[36:39],v1 offset:17344\n"
M_"v[52:55],v[12:15],v[76:79],v[52:55]\n"
M_"v[48:51],v[16:19],v[80:83],v[48:51]\n"
"s_barrier\n"
R_"v[8:11],v2 offset:35840\n"
"v_cvt_pk_f16_f32 v136, v40, v41\n"
M_"v[52:55],v[16:19],v[84:87],v[52:55]\n"
"PTL11:\n"
"s_cbranch_execz PTR\n"
"v_cvt_pk_f16_f32 v137, v42, v43\n"
M_"v[48:51],v[20:23],v[88:91],v[48:51]\n"
R_"v[12:15],v2 offset:35904\n"
M_"v[52:55],v[20:23],v[92:95],v[52:55]\n"
W_"lgkmcnt(3)\n"
"v_cvt_pk_f16_f32 v140, v44, v45\n"
M_"v[48:51],v[24:27],v[96:99],v[48:51]\n"
R_"v[16:19],v2 offset:35968\n"
M_"v[52:55],v[24:27],v[100:103],v[52:55]\n"
"v_cvt_pk_f16_f32 v141, v46, v47\n"
M_"v[48:51],v[28:31],v[104:107],v[48:51]\n"
R_"v[20:23],v2 offset:36032\n"
"v_pk_maximum3_f16 v56, v56, v136, v137\n"
M_"v[52:55],v[28:31],v[108:111],v[52:55]\n"
M_"v[48:51],v[32:35],v[112:115],v[48:51]\n"
R_"v[24:27],v2 offset:36096\n"
"v_pk_maximum3_f16 v57, v57, v140, v141\n"
M_"v[52:55],v[32:35],v[116:119],v[52:55]\n"
W_"lgkmcnt(3)\n"
M_"v[48:51],v[36:39],v[120:123],v[48:51]\n"
R_"v[28:31],v2 offset:36160\n"
M_"v[52:55],v[36:39],v[124:127],v[52:55]\n"
M_"v[40:43],v[8:11],v[64:67],0\n"
R_"v[32:35],v2 offset:36224\n"
M_"v[44:47],v[8:11],v[68:71],0\n"
M_"v[40:43],v[12:15],v[72:75],v[40:43]\n"
R_"v[36:39],v2 offset:36288\n"
M_"v[44:47],v[12:15],v[76:79],v[44:47]\n"
W_"lgkmcnt(3)\n"
M_"v[40:43],v[16:19],v[80:83],v[40:43]\n"
R_"v[8:11],v2 offset:52736\n"
"v_cvt_pk_f16_f32 v138, v48, v49\n"
M_"v[44:47],v[16:19],v[84:87],v[44:47]\n"
"v_cvt_pk_f16_f32 v139, v50, v51\n"
M_"v[40:43],v[20:23],v[88:91],v[40:43]\n"
R_"v[12:15],v2 offset:52800\n"
M_"v[44:47],v[20:23],v[92:95],v[44:47]\n"
"v_cvt_pk_f16_f32 v142, v52, v53\n"
M_"v[40:43],v[24:27],v[96:99],v[40:43]\n"
R_"v[16:19],v2 offset:52864\n"
M_"v[44:47],v[24:27],v[100:103],v[44:47]\n"
W_"lgkmcnt(3)\n"
"v_cvt_pk_f16_f32 v143, v54, v55\n"
M_"v[40:43],v[28:31],v[104:107],v[40:43]\n"
R_"v[20:23],v2 offset:52928\n"
"v_pk_maximum3_f16 v56, v56, v138, v139\n"
M_"v[44:47],v[28:31],v[108:111],v[44:47]\n"
M_"v[40:43],v[32:35],v[112:115],v[40:43]\n"
R_"v[24:27],v2 offset:52992\n"
"v_pk_maximum3_f16 v57, v57, v142, v143\n"
M_"v[44:47],v[32:35],v[116:119],v[44:47]\n"
M_"v[40:43],v[36:39],v[120:123],v[40:43]\n"
R_"v[28:31],v2 offset:53056\n"
M_"v[44:47],v[36:39],v[124:127],v[44:47]\n"
W_"lgkmcnt(3)\n"
M_"v[48:51],v[8:11],v[64:67],0\n"
R_"v[32:35],v2 offset:53120\n"
M_"v[52:55],v[8:11],v[68:71],0\n"
M_"v[48:51],v[12:15],v[72:75],v[48:51]\n"
R_"v[36:39],v2 offset:53184\n"
M_"v[52:55],v[12:15],v[76:79],v[52:55]\n"
M_"v[48:51],v[16:19],v[80:83],v[48:51]\n"
"s_barrier\n"
R_"v[8:11],v2 offset:61184\n"
"v_cvt_pk_f16_f32 v144, v40, v41\n"
M_"v[52:55],v[16:19],v[84:87],v[52:55]\n"
W_"lgkmcnt(3)\n"
"v_cvt_pk_f16_f32 v145, v42, v43\n"
M_"v[48:51],v[20:23],v[88:91],v[48:51]\n"
R_"v[12:15],v2 offset:61248\n"
M_"v[52:55],v[20:23],v[92:95],v[52:55]\n"
"v_cvt_pk_f16_f32 v148, v44, v45\n"
M_"v[48:51],v[24:27],v[96:99],v[48:51]\n"
R_"v[16:19],v2 offset:61312\n"
M_"v[52:55],v[24:27],v[100:103],v[52:55]\n"
"v_cvt_pk_f16_f32 v149, v46, v47\n"
M_"v[48:51],v[28:31],v[104:107],v[48:51]\n"
R_"v[20:23],v2 offset:61376\n"
"v_pk_maximum3_f16 v56, v56, v144, v145\n"
M_"v[52:55],v[28:31],v[108:111],v[52:55]\n"
W_"lgkmcnt(3)\n"
M_"v[48:51],v[32:35],v[112:115],v[48:51]\n"
R_"v[24:27],v2 offset:61440\n"
"v_pk_maximum3_f16 v57, v57, v148, v149\n"
M_"v[52:55],v[32:35],v[116:119],v[52:55]\n"
M_"v[48:51],v[36:39],v[120:123],v[48:51]\n"
R_"v[28:31],v2 offset:61504\n"
M_"v[52:55],v[36:39],v[124:127],v[52:55]\n"
M_"v[40:43],v[8:11],v[64:67],0\n"
R_"v[32:35],v2 offset:61568\n"
M_"v[44:47],v[8:11],v[68:71],0\n"
W_"lgkmcnt(3)\n"
M_"v[40:43],v[12:15],v[72:75],v[40:43]\n"
R_"v[36:39],v2 offset:61632\n"
M_"v[44:47],v[12:15],v[76:79],v[44:47]\n"
M_"v[40:43],v[16:19],v[80:83],v[40:43]\n"
R_"v[8:11],v3 offset:4096\n"
"v_cvt_pk_f16_f32 v146, v48, v49\n"
M_"v[44:47],v[16:19],v[84:87],v[44:47]\n"
"v_cvt_pk_f16_f32 v147, v50, v51\n"
M_"v[40:43],v[20:23],v[88:91],v[40:43]\n"
R_"v[12:15],v3 offset:4160\n"
M_"v[44:47],v[20:23],v[92:95],v[44:47]\n"
W_"lgkmcnt(3)\n"
"v_cvt_pk_f16_f32 v150, v52, v53\n"
M_"v[40:43],v[24:27],v[96:99],v[40:43]\n"
R_"v[16:19],v3 offset:4224\n"
M_"v[44:47],v[24:27],v[100:103],v[44:47]\n"
"v_cvt_pk_f16_f32 v151, v54, v55\n"
M_"v[40:43],v[28:31],v[104:107],v[40:43]\n"
R_"v[20:23],v3 offset:4288\n"
"v_pk_maximum3_f16 v56, v56, v146, v147\n"
M_"v[44:47],v[28:31],v[108:111],v[44:47]\n"
M_"v[40:43],v[32:35],v[112:115],v[40:43]\n"
R_"v[24:27],v3 offset:4352\n"
"v_pk_maximum3_f16 v57, v57, v150, v151\n"
M_"v[44:47],v[32:35],v[116:119],v[44:47]\n"
W_"lgkmcnt(3)\n"
M_"v[40:43],v[36:39],v[120:123],v[40:43]\n"
R_"v[28:31],v3 offset:4416\n"
M_"v[44:47],v[36:39],v[124:127],v[44:47]\n"
M_"v[48:51],v[8:11],v[64:67],0\n"
R_"v[32:35],v3 offset:4480\n"
M_"v[52:55],v[8:11],v[68:71],0\n"
M_"v[48:51],v[12:15],v[72:75],v[48:51]\n"
R_"v[36:39],v3 offset:4544\n"
M_"v[52:55],v[12:15],v[76:79],v[52:55]\n"
W_"lgkmcnt(3)\n"
M_"v[48:51],v[16:19],v[80:83],v[48:51]\n"
"s_barrier\n"
R_"v[8:11],v3 offset:12544\n"
"v_cvt_pk_f16_f32 v152, v40, v41\n"
M_"v[52:55],v[16:19],v[84:87],v[52:55]\n"
"v_cvt_pk_f16_f32 v153, v42, v43\n"
M_"v[48:51],v[20:23],v[88:91],v[48:51]\n"
R_"v[12:15],v3 offset:12608\n"
M_"v[52:55],v[20:23],v[92:95],v[52:55]\n"
"v_cvt_pk_f16_f32 v156, v44, v45\n"
M_"v[48:51],v[24:27],v[96:99],v[48:51]\n"
R_"v[16:19],v3 offset:12672\n"
M_"v[52:55],v[24:27],v[100:103],v[52:55]\n"
W_"lgkmcnt(3)\n"
"v_cvt_pk_f16_f32 v157, v46, v47\n"
M_"v[48:51],v[28:31],v[104:107],v[48:51]\n"
R_"v[20:23],v3 offset:12736\n"
"v_pk_maximum3_f16 v56, v56, v152, v153\n"
M_"v[52:55],v[28:31],v[108:111],v[52:55]\n"
M_"v[48:51],v[32:35],v[112:115],v[48:51]\n"
R_"v[24:27],v3 offset:12800\n"
"v_pk_maximum3_f16 v57, v57, v156, v157\n"
M_"v[52:55],v[32:35],v[116:119],v[52:55]\n"
M_"v[48:51],v[36:39],v[120:123],v[48:51]\n"
R_"v[28:31],v3 offset:12864\n"
M_"v[52:55],v[36:39],v[124:127],v[52:55]\n"
W_"lgkmcnt(3)\n"
M_"v[40:43],v[8:11],v[64:67],0\n"
R_"v[32:35],v3 offset:12928\n"
M_"v[44:47],v[8:11],v[68:71],0\n"
M_"v[40:43],v[12:15],v[72:75],v[40:43]\n"
R_"v[36:39],v3 offset:12992\n"
M_"v[44:47],v[12:15],v[76:79],v[44:47]\n"
M_"v[40:43],v[16:19],v[80:83],v[40:43]\n"
R_"v[8:11],v1 offset:33792\n"
"v_cvt_pk_f16_f32 v154, v48, v49\n"
M_"v[44:47],v[16:19],v[84:87],v[44:47]\n"
W_"lgkmcnt(3)\n"
"v_cvt_pk_f16_f32 v155, v50, v51\n"
M_"v[40:43],v[20:23],v[88:91],v[40:43]\n"
R_"v[12:15],v1 offset:33856\n"
M_"v[44:47],v[20:23],v[92:95],v[44:47]\n"
"v_cvt_pk_f16_f32 v158, v52, v53\n"
M_"v[40:43],v[24:27],v[96:99],v[40:43]\n"
R_"v[16:19],v1 offset:33920\n"
M_"v[44:47],v[24:27],v[100:103],v[44:47]\n"
"v_cvt_pk_f16_f32 v159, v54, v55\n"
M_"v[40:43],v[28:31],v[104:107],v[40:43]\n"
R_"v[20:23],v1 offset:33984\n"
"v_pk_maximum3_f16 v56, v56, v154, v155\n"
M_"v[44:47],v[28:31],v[108:111],v[44:47]\n"
W_"lgkmcnt(3)\n"
M_"v[40:43],v[32:35],v[112:115],v[40:43]\n"
R_"v[24:27],v1 offset:34048\n"
"v_pk_maximum3_f16 v57, v57, v158, v159\n"
M_"v[44:47],v[32:35],v[116:119],v[44:47]\n"
M_"v[40:43],v[36:39],v[120:123],v[40:43]\n"
R_"v[28:31],v1 offset:34112\n"
M_"v[44:47],v[36:39],v[124:127],v[44:47]\n"
M_"v[48:51],v[8:11],v[64:67],0\n"
R_"v[32:35],v1 offset:34176\n"
M_"v[52:55],v[8:11],v[68:71],0\n"
W_"lgkmcnt(3)\n"
M_"v[48:51],v[12:15],v[72:75],v[48:51]\n"
R_"v[36:39],v1 offset:34240\n"
M_"v[52:55],v[12:15],v[76:79],v[52:55]\n"
M_"v[48:51],v[16:19],v[80:83],v[48:51]\n"
"s_barrier\n"
R_"v[8:11],v1 offset:42240\n"
"v_cvt_pk_f16_f32 v160, v40, v41\n"
M_"v[52:55],v[16:19],v[84:87],v[52:55]\n"
"v_cvt_pk_f16_f32 v161, v42, v43\n"
M_"v[48:51],v[20:23],v[88:91],v[48:51]\n"
R_"v[12:15],v1 offset:42304\n"
M_"v[52:55],v[20:23],v[92:95],v[52:55]\n"
W_"lgkmcnt(3)\n"
"v_cvt_pk_f16_f32 v164, v44, v45\n"
M_"v[48:51],v[24:27],v[96:99],v[48:51]\n"
R_"v[16:19],v1 offset:42368\n"
M_"v[52:55],v[24:27],v[100:103],v[52:55]\n"
"v_cvt_pk_f16_f32 v165, v46, v47\n"
M_"v[48:51],v[28:31],v[104:107],v[48:51]\n"
R_"v[20:23],v1 offset:42432\n"
"v_pk_maximum3_f16 v56, v56, v160, v161\n"
M_"v[52:55],v[28:31],v[108:111],v[52:55]\n"
M_"v[48:51],v[32:35],v[112:115],v[48:51]\n"
R_"v[24:27],v1 offset:42496\n"
"v_pk_maximum3_f16 v57, v57, v164, v165\n"
M_"v[52:55],v[32:35],v[116:119],v[52:55]\n"
W_"lgkmcnt(3)\n"
M_"v[48:51],v[36:39],v[120:123],v[48:51]\n"
R_"v[28:31],v1 offset:42560\n"
M_"v[52:55],v[36:39],v[124:127],v[52:55]\n"
M_"v[40:43],v[8:11],v[64:67],0\n"
R_"v[32:35],v1 offset:42624\n"
M_"v[44:47],v[8:11],v[68:71],0\n"
M_"v[40:43],v[12:15],v[72:75],v[40:43]\n"
R_"v[36:39],v1 offset:42688\n"
M_"v[44:47],v[12:15],v[76:79],v[44:47]\n"
W_"lgkmcnt(3)\n"
M_"v[40:43],v[16:19],v[80:83],v[40:43]\n"
R_"v[8:11],v1 offset:0\n"
"v_cvt_pk_f16_f32 v162, v48, v49\n"
M_"v[44:47],v[16:19],v[84:87],v[44:47]\n"
"v_cvt_pk_f16_f32 v163, v50, v51\n"
M_"v[40:43],v[20:23],v[88:91],v[40:43]\n"
R_"v[12:15],v1 offset:64\n"
M_"v[44:47],v[20:23],v[92:95],v[44:47]\n"
"v_cvt_pk_f16_f32 v166, v52, v53\n"
M_"v[40:43],v[24:27],v[96:99],v[40:43]\n"
R_"v[16:19],v1 offset:128\n"
M_"v[44:47],v[24:27],v[100:103],v[44:47]\n"
W_"lgkmcnt(3)\n"
"v_cvt_pk_f16_f32 v167, v54, v55\n"
M_"v[40:43],v[28:31],v[104:107],v[40:43]\n"
R_"v[20:23],v1 offset:192\n"
"v_pk_maximum3_f16 v56, v56, v162, v163\n"
M_"v[44:47],v[28:31],v[108:111],v[44:47]\n"
M_"v[40:43],v[32:35],v[112:115],v[40:43]\n"
R_"v[24:27],v1 offset:256\n"
"v_pk_maximum3_f16 v57, v57, v166, v167\n"
M_"v[44:47],v[32:35],v[116:119],v[44:47]\n"
M_"v[40:43],v[36:39],v[120:123],v[40:43]\n"
R_"v[28:31],v1 offset:320\n"
M_"v[44:47],v[36:39],v[124:127],v[44:47]\n"
W_"lgkmcnt(3)\n"
M_"v[48:51],v[8:11],v[64:67],0\n"
R_"v[32:35],v1 offset:384\n"
M_"v[52:55],v[8:11],v[68:71],0\n"
M_"v[48:51],v[12:15],v[72:75],v[48:51]\n"
R_"v[36:39],v1 offset:448\n"
M_"v[52:55],v[12:15],v[76:79],v[52:55]\n"
M_"v[48:51],v[16:19],v[80:83],v[48:51]\n"
"s_barrier\n"
R_"v[8:11],v1 offset:59136\n"
"v_cvt_pk_f16_f32 v168, v40, v41\n"
M_"v[52:55],v[16:19],v[84:87],v[52:55]\n"
W_"lgkmcnt(3)\n"
"v_cvt_pk_f16_f32 v169, v42, v43\n"
M_"v[48:51],v[20:23],v[88:91],v[48:51]\n"
R_"v[12:15],v1 offset:59200\n"
M_"v[52:55],v[20:23],v[92:95],v[52:55]\n"
"v_cvt_pk_f16_f32 v172, v44, v45\n"
M_"v[48:51],v[24:27],v[96:99],v[48:51]\n"
R_"v[16:19],v1 offset:59264\n"
M_"v[52:55],v[24:27],v[100:103],v[52:55]\n"
"v_cvt_pk_f16_f32 v173, v46, v47\n"
M_"v[48:51],v[28:31],v[104:107],v[48:51]\n"
R_"v[20:23],v1 offset:59328\n"
"v_pk_maximum3_f16 v56, v56, v168, v169\n"
M_"v[52:55],v[28:31],v[108:111],v[52:55]\n"
W_"lgkmcnt(3)\n"
M_"v[48:51],v[32:35],v[112:115],v[48:51]\n"
R_"v[24:27],v1 offset:59392\n"
"v_pk_maximum3_f16 v57, v57, v172, v173\n"
M_"v[52:55],v[32:35],v[116:119],v[52:55]\n"
M_"v[48:51],v[36:39],v[120:123],v[48:51]\n"
R_"v[28:31],v1 offset:59456\n"
M_"v[52:55],v[36:39],v[124:127],v[52:55]\n"
M_"v[40:43],v[8:11],v[64:67],0\n"
R_"v[32:35],v1 offset:59520\n"
M_"v[44:47],v[8:11],v[68:71],0\n"
W_"lgkmcnt(3)\n"
M_"v[40:43],v[12:15],v[72:75],v[40:43]\n"
R_"v[36:39],v1 offset:59584\n"
M_"v[44:47],v[12:15],v[76:79],v[44:47]\n"
M_"v[40:43],v[16:19],v[80:83],v[40:43]\n"
R_"v[8:11],v2 offset:2048\n"
"v_cvt_pk_f16_f32 v170, v48, v49\n"
M_"v[44:47],v[16:19],v[84:87],v[44:47]\n"
"v_cvt_pk_f16_f32 v171, v50, v51\n"
M_"v[40:43],v[20:23],v[88:91],v[40:43]\n"
R_"v[12:15],v2 offset:2112\n"
M_"v[44:47],v[20:23],v[92:95],v[44:47]\n"
W_"lgkmcnt(3)\n"
"v_cvt_pk_f16_f32 v174, v52, v53\n"
M_"v[40:43],v[24:27],v[96:99],v[40:43]\n"
R_"v[16:19],v2 offset:2176\n"
M_"v[44:47],v[24:27],v[100:103],v[44:47]\n"
"v_cvt_pk_f16_f32 v175, v54, v55\n"
M_"v[40:43],v[28:31],v[104:107],v[40:43]\n"
R_"v[20:23],v2 offset:2240\n"
"v_pk_maximum3_f16 v56, v56, v170, v171\n"
M_"v[44:47],v[28:31],v[108:111],v[44:47]\n"
M_"v[40:43],v[32:35],v[112:115],v[40:43]\n"
R_"v[24:27],v2 offset:2304\n"
"v_pk_maximum3_f16 v57, v57, v174, v175\n"
M_"v[44:47],v[32:35],v[116:119],v[44:47]\n"
W_"lgkmcnt(3)\n"
M_"v[40:43],v[36:39],v[120:123],v[40:43]\n"
R_"v[28:31],v2 offset:2368\n"
M_"v[44:47],v[36:39],v[124:127],v[44:47]\n"
M_"v[48:51],v[8:11],v[64:67],0\n"
R_"v[32:35],v2 offset:2432\n"
M_"v[52:55],v[8:11],v[68:71],0\n"
M_"v[48:51],v[12:15],v[72:75],v[48:51]\n"
R_"v[36:39],v2 offset:2496\n"
M_"v[52:55],v[12:15],v[76:79],v[52:55]\n"
W_"lgkmcnt(3)\n"
M_"v[48:51],v[16:19],v[80:83],v[48:51]\n"
"s_barrier\n"
R_"v[8:11],v2 offset:10496\n"
"v_cvt_pk_f16_f32 v176, v40, v41\n"
M_"v[52:55],v[16:19],v[84:87],v[52:55]\n"
"v_cvt_pk_f16_f32 v177, v42, v43\n"
M_"v[48:51],v[20:23],v[88:91],v[48:51]\n"
R_"v[12:15],v2 offset:10560\n"
M_"v[52:55],v[20:23],v[92:95],v[52:55]\n"
"v_cvt_pk_f16_f32 v180, v44, v45\n"
M_"v[48:51],v[24:27],v[96:99],v[48:51]\n"
R_"v[16:19],v2 offset:10624\n"
M_"v[52:55],v[24:27],v[100:103],v[52:55]\n"
W_"lgkmcnt(3)\n"
"v_cvt_pk_f16_f32 v181, v46, v47\n"
M_"v[48:51],v[28:31],v[104:107],v[48:51]\n"
R_"v[20:23],v2 offset:10688\n"
"v_pk_maximum3_f16 v56, v56, v176, v177\n"
M_"v[52:55],v[28:31],v[108:111],v[52:55]\n"
M_"v[48:51],v[32:35],v[112:115],v[48:51]\n"
R_"v[24:27],v2 offset:10752\n"
"v_pk_maximum3_f16 v57, v57, v180, v181\n"
M_"v[52:55],v[32:35],v[116:119],v[52:55]\n"
M_"v[48:51],v[36:39],v[120:123],v[48:51]\n"
R_"v[28:31],v2 offset:10816\n"
M_"v[52:55],v[36:39],v[124:127],v[52:55]\n"
W_"lgkmcnt(3)\n"
M_"v[40:43],v[8:11],v[64:67],0\n"
R_"v[32:35],v2 offset:10880\n"
M_"v[44:47],v[8:11],v[68:71],0\n"
M_"v[40:43],v[12:15],v[72:75],v[40:43]\n"
R_"v[36:39],v2 offset:10944\n"
M_"v[44:47],v[12:15],v[76:79],v[44:47]\n"
M_"v[40:43],v[16:19],v[80:83],v[40:43]\n"
R_"v[8:11],v1 offset:25344\n"
"v_cvt_pk_f16_f32 v178, v48, v49\n"
M_"v[44:47],v[16:19],v[84:87],v[44:47]\n"
W_"lgkmcnt(3)\n"
"v_cvt_pk_f16_f32 v179, v50, v51\n"
M_"v[40:43],v[20:23],v[88:91],v[40:43]\n"
R_"v[12:15],v1 offset:25408\n"
M_"v[44:47],v[20:23],v[92:95],v[44:47]\n"
"v_cvt_pk_f16_f32 v182, v52, v53\n"
M_"v[40:43],v[24:27],v[96:99],v[40:43]\n"
R_"v[16:19],v1 offset:25472\n"
M_"v[44:47],v[24:27],v[100:103],v[44:47]\n"
"v_cvt_pk_f16_f32 v183, v54, v55\n"
M_"v[40:43],v[28:31],v[104:107],v[40:43]\n"
R_"v[20:23],v1 offset:25536\n"
"v_pk_maximum3_f16 v56, v56, v178, v179\n"
M_"v[44:47],v[28:31],v[108:111],v[44:47]\n"
W_"lgkmcnt(3)\n"
M_"v[40:43],v[32:35],v[112:115],v[40:43]\n"
R_"v[24:27],v1 offset:25600\n"
"v_pk_maximum3_f16 v57, v57, v182, v183\n"
M_"v[44:47],v[32:35],v[116:119],v[44:47]\n"
M_"v[40:43],v[36:39],v[120:123],v[40:43]\n"
R_"v[28:31],v1 offset:25664\n"
M_"v[44:47],v[36:39],v[124:127],v[44:47]\n"
M_"v[48:51],v[8:11],v[64:67],0\n"
R_"v[32:35],v1 offset:25728\n"
M_"v[52:55],v[8:11],v[68:71],0\n"
W_"lgkmcnt(3)\n"
M_"v[48:51],v[12:15],v[72:75],v[48:51]\n"
R_"v[36:39],v1 offset:25792\n"
M_"v[52:55],v[12:15],v[76:79],v[52:55]\n"
M_"v[48:51],v[16:19],v[80:83],v[48:51]\n"
"v_cvt_pk_f16_f32 v184, v40, v41\n"
M_"v[52:55],v[16:19],v[84:87],v[52:55]\n"
"v_cvt_pk_f16_f32 v185, v42, v43\n"
M_"v[48:51],v[20:23],v[88:91],v[48:51]\n"
"s_barrier\n"
M_"v[52:55],v[20:23],v[92:95],v[52:55]\n"
W_"lgkmcnt(1)\n"
"v_cvt_pk_f16_f32 v188, v44, v45\n"
M_"v[48:51],v[24:27],v[96:99],v[48:51]\n"
M_"v[52:55],v[24:27],v[100:103],v[52:55]\n"
"v_cvt_pk_f16_f32 v189, v46, v47\n"
M_"v[48:51],v[28:31],v[104:107],v[48:51]\n"
"v_pk_maximum3_f16 v56, v56, v184, v185\n"
M_"v[52:55],v[28:31],v[108:111],v[52:55]\n"
M_"v[48:51],v[32:35],v[112:115],v[48:51]\n"
"v_pk_maximum3_f16 v57, v57, v188, v189\n"
M_"v[52:55],v[32:35],v[116:119],v[52:55]\n"
W_"lgkmcnt(0)\n"
M_"v[48:51],v[36:39],v[120:123],v[48:51]\n"
M_"v[52:55],v[36:39],v[124:127],v[52:55]\n"
"s_nop 6\n"
"v_cvt_pk_f16_f32 v186, v48, v49\n"
"v_cvt_pk_f16_f32 v187, v50, v51\n"
"v_cvt_pk_f16_f32 v190, v52, v53\n"
"v_cvt_pk_f16_f32 v191, v54, v55\n"
"v_pk_maximum3_f16 v56, v56, v186, v187\n"
"v_pk_maximum3_f16 v57, v57, v190, v191\n"
"v_pk_max_f16 v48, v56, v56 op_sel:[0,1] op_sel_hi:[1,0]\n"
"v_pk_max_f16 v49, v57, v57 op_sel:[0,1] op_sel_hi:[1,0]\n"
"v_bfi_b32 v48, s93, v49, v48\n"
"v_mov_b32 v49, v48\n"
"s_nop 1\n"
"v_permlane32_swap_b32 v48, v49\n"
"v_pk_max_f16 v48, v48, v49\n"
"v_mov_b32 v49, v48\n"
"s_nop 1\n"
"v_permlane16_swap_b32 v48, v49\n"
"v_pk_max_f16 v48, v48, v49\n"
X_"v50,v48,s94,0 op_sel_hi:[1,0,0]\n"
X_"v51,v48,s94,0 op_sel:[1,0,0] op_sel_hi:[1,0,0]\n"
"v_ceil_f32 v50, v50\n"
"v_ceil_f32 v51, v51\n"
"v_mov_b32 v56, v58\n"
"v_mov_b32 v57, v59\n"
"v_sub_f32 v58, 0xc2c80000, v50\n"
"v_sub_f32 v59, 0xc2c80000, v51\n"
"v_sub_f32 v56, v56, v58\n"
"v_sub_f32 v57, v57, v59\n"
"v_cvt_u32_f32 v56, v56\n"
"v_cvt_u32_f32 v57, v57\n"
"v_min_u32 v56, 0xff, v56\n"
"v_min_u32 v57, 0xff, v57\n"
"v_mul_u32_u24 v56, 0x800080, v56\n"
"v_mul_u32_u24 v57, 0x800080, v57\n"
"v_pk_sub_u16 v192, v192, v56 clamp\n"
"v_pk_sub_u16 v193, v193, v56 clamp\n"
"v_pk_sub_u16 v194, v194, v56 clamp\n"
"v_pk_sub_u16 v195, v195, v56 clamp\n"
"v_pk_sub_u16 v196, v196, v57 clamp\n"
"v_pk_sub_u16 v197, v197, v57 clamp\n"
"s_barrier\n"
"v_pk_sub_u16 v198, v198, v57 clamp\n"
"v_pk_sub_u16 v199, v199, v57 clamp\n"
X_"v40,v232,s94,v58 op_sel_hi:[1,0,0]\n"
X_"v41,v232,s94,v58 op_sel:[1,0,0] op_sel_hi:[1,0,0]\n"
X_"v42,v233,s94,v58 op_sel_hi:[1,0,0]\n"
X_"v43,v233,s94,v58 op_sel:[1,0,0] op_sel_hi:[1,0,0]\n"
E_"v40,v40\n"
E_"v41,v41\n"
X_"v44,v234,s94,v58 op_sel_hi:[1,0,0]\n"
X_"v45,v234,s94,v58 op_sel:[1,0,0] op_sel_hi:[1,0,0]\n"
E_"v42,v42\n"
E_"v43,v43\n"
C_"v232,v40,v41\n"
X_"v46,v235,s94,v58 op_sel_hi:[1,0,0]\n"
X_"v47,v235,s94,v58 op_sel:[1,0,0] op_sel_hi:[1,0,0]\n"
E_"v44,v44\n"
E_"v45,v45\n"
C_"v233,v42,v43\n"
E_"v46,v46\n"
E_"v47,v47\n"
C_"v234,v44,v45\n"
C_"v235,v46,v47\n"
X_"v40,v236,s94,v59 op_sel_hi:[1,0,0]\n"
X_"v41,v236,s94,v59 op_sel:[1,0,0] op_sel_hi:[1,0,0]\n"
X_"v42,v237,s94,v59 op_sel_hi:[1,0,0]\n"
X_"v43,v237,s94,v59 op_sel:[1,0,0] op_sel_hi:[1,0,0]\n"
E_"v40,v40\n"
E_"v41,v41\n"
X_"v44,v238,s94,v59 op_sel_hi:[1,0,0]\n"
X_"v45,v238,s94,v59 op_sel:[1,0,0] op_sel_hi:[1,0,0]\n"
E_"v42,v42\n"
E_"v43,v43\n"
C_"v236,v40,v41\n"
X_"v46,v239,s94,v59 op_sel_hi:[1,0,0]\n"
X_"v47,v239,s94,v59 op_sel:[1,0,0] op_sel_hi:[1,0,0]\n"
E_"v44,v44\n"
E_"v45,v45\n"
C_"v237,v42,v43\n"
E_"v46,v46\n"
E_"v47,v47\n"
"s_barrier\n"
C_"v238,v44,v45\n"
C_"v239,v46,v47\n"
X_"v40,v240,s94,v58 op_sel_hi:[1,0,0]\n"
X_"v41,v240,s94,v58 op_sel:[1,0,0] op_sel_hi:[1,0,0]\n"
X_"v42,v241,s94,v58 op_sel_hi:[1,0,0]\n"
X_"v43,v241,s94,v58 op_sel:[1,0,0] op_sel_hi:[1,0,0]\n"
E_"v40,v40\n"
E_"v41,v41\n"
X_"v44,v242,s94,v58 op_sel_hi:[1,0,0]\n"
X_"v45,v242,s94,v58 op_sel:[1,0,0] op_sel_hi:[1,0,0]\n"
E_"v42,v42\n"
E_"v43,v43\n"
C_"v240,v40,v41\n"
X_"v46,v243,s94,v58 op_sel_hi:[1,0,0]\n"
X_"v47,v243,s94,v58 op_sel:[1,0,0] op_sel_hi:[1,0,0]\n"
E_"v44,v44\n"
E_"v45,v45\n"
C_"v241,v42,v43\n"
E_"v46,v46\n"
E_"v47,v47\n"
C_"v242,v44,v45\n"
C_"v243,v46,v47\n"
X_"v40,v244,s94,v59 op_sel_hi:[1,0,0]\n"
X_"v41,v244,s94,v59 op_sel:[1,0,0] op_sel_hi:[1,0,0]\n"
X_"v42,v245,s94,v59 op_sel_hi:[1,0,0]\n"
X_"v43,v245,s94,v59 op_sel:[1,0,0] op_sel_hi:[1,0,0]\n"
E_"v40,v40\n"
E_"v41,v41\n"
X_"v44,v246,s94,v59 op_sel_hi:[1,0,0]\n"
X_"v45,v246,s94,v59 op_sel:[1,0,0] op_sel_hi:[1,0,0]\n"
E_"v42,v42\n"
E_"v43,v43\n"
C_"v244,v40,v41\n"
X_"v46,v247,s94,v59 op_sel_hi:[1,0,0]\n"
X_"v47,v247,s94,v59 op_sel:[1,0,0] op_sel_hi:[1,0,0]\n"
E_"v44,v44\n"
E_"v45,v45\n"
C_"v245,v42,v43\n"
E_"v46,v46\n"
E_"v47,v47\n"
C_"v246,v44,v45\n"
"s_barrier\n"
C_"v247,v46,v47\n"
X_"v40,v248,s94,v58 op_sel_hi:[1,0,0]\n"
X_"v41,v248,s94,v58 op_sel:[1,0,0] op_sel_hi:[1,0,0]\n"
X_"v42,v249,s94,v58 op_sel_hi:[1,0,0]\n"
X_"v43,v249,s94,v58 op_sel:[1,0,0] op_sel_hi:[1,0,0]\n"
E_"v40,v40\n"
E_"v41,v41\n"
X_"v44,v250,s94,v58 op_sel_hi:[1,0,0]\n"
X_"v45,v250,s94,v58 op_sel:[1,0,0] op_sel_hi:[1,0,0]\n"
E_"v42,v42\n"
E_"v43,v43\n"
C_"v248,v40,v41\n"
X_"v46,v251,s94,v58 op_sel_hi:[1,0,0]\n"
X_"v47,v251,s94,v58 op_sel:[1,0,0] op_sel_hi:[1,0,0]\n"
E_"v44,v44\n"
E_"v45,v45\n"
C_"v249,v42,v43\n"
E_"v46,v46\n"
E_"v47,v47\n"
C_"v250,v44,v45\n"
C_"v251,v46,v47\n"
X_"v40,v252,s94,v59 op_sel_hi:[1,0,0]\n"
X_"v41,v252,s94,v59 op_sel:[1,0,0] op_sel_hi:[1,0,0]\n"
X_"v42,v253,s94,v59 op_sel_hi:[1,0,0]\n"
X_"v43,v253,s94,v59 op_sel:[1,0,0] op_sel_hi:[1,0,0]\n"
E_"v40,v40\n"
E_"v41,v41\n"
X_"v44,v254,s94,v59 op_sel_hi:[1,0,0]\n"
X_"v45,v254,s94,v59 op_sel:[1,0,0] op_sel_hi:[1,0,0]\n"
"PTL12:\n"
"s_cbranch_execz PTR\n"
E_"v42,v42\n"
E_"v43,v43\n"
C_"v252,v40,v41\n"
X_"v46,v255,s94,v59 op_sel_hi:[1,0,0]\n"
X_"v47,v255,s94,v59 op_sel:[1,0,0] op_sel_hi:[1,0,0]\n"
E_"v44,v44\n"
E_"v45,v45\n"
C_"v253,v42,v43\n"
E_"v46,v46\n"
E_"v47,v47\n"
C_"v254,v44,v45\n"
"s_barrier\n"
C_"v255,v46,v47\n"
X_"v40,v128,s94,v58 op_sel_hi:[1,0,0]\n"
X_"v41,v128,s94,v58 op_sel:[1,0,0] op_sel_hi:[1,0,0]\n"
X_"v42,v129,s94,v58 op_sel_hi:[1,0,0]\n"
X_"v43,v129,s94,v58 op_sel:[1,0,0] op_sel_hi:[1,0,0]\n"
E_"v40,v40\n"
E_"v41,v41\n"
X_"v44,v130,s94,v58 op_sel_hi:[1,0,0]\n"
X_"v45,v130,s94,v58 op_sel:[1,0,0] op_sel_hi:[1,0,0]\n"
E_"v42,v42\n"
E_"v43,v43\n"
C_"v128,v40,v41\n"
X_"v46,v131,s94,v58 op_sel_hi:[1,0,0]\n"
X_"v47,v131,s94,v58 op_sel:[1,0,0] op_sel_hi:[1,0,0]\n"
E_"v44,v44\n"
E_"v45,v45\n"
C_"v129,v42,v43\n"
E_"v46,v46\n"
E_"v47,v47\n"
C_"v130,v44,v45\n"
C_"v131,v46,v47\n"
X_"v40,v132,s94,v59 op_sel_hi:[1,0,0]\n"
X_"v41,v132,s94,v59 op_sel:[1,0,0] op_sel_hi:[1,0,0]\n"
X_"v42,v133,s94,v59 op_sel_hi:[1,0,0]\n"
X_"v43,v133,s94,v59 op_sel:[1,0,0] op_sel_hi:[1,0,0]\n"
E_"v40,v40\n"
E_"v41,v41\n"
X_"v44,v134,s94,v59 op_sel_hi:[1,0,0]\n"
X_"v45,v134,s94,v59 op_sel:[1,0,0] op_sel_hi:[1,0,0]\n"
E_"v42,v42\n"
E_"v43,v43\n"
C_"v132,v40,v41\n"
X_"v46,v135,s94,v59 op_sel_hi:[1,0,0]\n"
X_"v47,v135,s94,v59 op_sel:[1,0,0] op_sel_hi:[1,0,0]\n"
E_"v44,v44\n"
E_"v45,v45\n"
C_"v133,v42,v43\n"
E_"v46,v46\n"
E_"v47,v47\n"
C_"v134,v44,v45\n"
C_"v135,v46,v47\n"
"s_barrier\n"
X_"v40,v136,s94,v58 op_sel_hi:[1,0,0]\n"
X_"v41,v136,s94,v58 op_sel:[1,0,0] op_sel_hi:[1,0,0]\n"
X_"v42,v137,s94,v58 op_sel_hi:[1,0,0]\n"
X_"v43,v137,s94,v58 op_sel:[1,0,0] op_sel_hi:[1,0,0]\n"
E_"v40,v40\n"
E_"v41,v41\n"
X_"v44,v138,s94,v58 op_sel_hi:[1,0,0]\n"
X_"v45,v138,s94,v58 op_sel:[1,0,0] op_sel_hi:[1,0,0]\n"
E_"v42,v42\n"
E_"v43,v43\n"
C_"v136,v40,v41\n"
X_"v46,v139,s94,v58 op_sel_hi:[1,0,0]\n"
X_"v47,v139,s94,v58 op_sel:[1,0,0] op_sel_hi:[1,0,0]\n"
E_"v44,v44\n"
E_"v45,v45\n"
C_"v137,v42,v43\n"
E_"v46,v46\n"
E_"v47,v47\n"
C_"v138,v44,v45\n"
C_"v139,v46,v47\n"
X_"v40,v140,s94,v59 op_sel_hi:[1,0,0]\n"
X_"v41,v140,s94,v59 op_sel:[1,0,0] op_sel_hi:[1,0,0]\n"
X_"v42,v141,s94,v59 op_sel_hi:[1,0,0]\n"
X_"v43,v141,s94,v59 op_sel:[1,0,0] op_sel_hi:[1,0,0]\n"
E_"v40,v40\n"
E_"v41,v41\n"
X_"v44,v142,s94,v59 op_sel_hi:[1,0,0]\n"
X_"v45,v142,s94,v59 op_sel:[1,0,0] op_sel_hi:[1,0,0]\n"
E_"v42,v42\n"
E_"v43,v43\n"
C_"v140,v40,v41\n"
X_"v46,v143,s94,v59 op_sel_hi:[1,0,0]\n"
X_"v47,v143,s94,v59 op_sel:[1,0,0] op_sel_hi:[1,0,0]\n"
E_"v44,v44\n"
E_"v45,v45\n"
C_"v141,v42,v43\n"
E_"v46,v46\n"
E_"v47,v47\n"
C_"v142,v44,v45\n"
C_"v143,v46,v47\n"
"s_barrier\n"
X_"v40,v144,s94,v58 op_sel_hi:[1,0,0]\n"
X_"v41,v144,s94,v58 op_sel:[1,0,0] op_sel_hi:[1,0,0]\n"
X_"v42,v145,s94,v58 op_sel_hi:[1,0,0]\n"
X_"v43,v145,s94,v58 op_sel:[1,0,0] op_sel_hi:[1,0,0]\n"
E_"v40,v40\n"
E_"v41,v41\n"
X_"v44,v146,s94,v58 op_sel_hi:[1,0,0]\n"
X_"v45,v146,s94,v58 op_sel:[1,0,0] op_sel_hi:[1,0,0]\n"
E_"v42,v42\n"
E_"v43,v43\n"
C_"v144,v40,v41\n"
X_"v46,v147,s94,v58 op_sel_hi:[1,0,0]\n"
X_"v47,v147,s94,v58 op_sel:[1,0,0] op_sel_hi:[1,0,0]\n"
E_"v44,v44\n"
E_"v45,v45\n"
C_"v145,v42,v43\n"
E_"v46,v46\n"
E_"v47,v47\n"
C_"v146,v44,v45\n"
C_"v147,v46,v47\n"
X_"v40,v148,s94,v59 op_sel_hi:[1,0,0]\n"
X_"v41,v148,s94,v59 op_sel:[1,0,0] op_sel_hi:[1,0,0]\n"
X_"v42,v149,s94,v59 op_sel_hi:[1,0,0]\n"
X_"v43,v149,s94,v59 op_sel:[1,0,0] op_sel_hi:[1,0,0]\n"
E_"v40,v40\n"
E_"v41,v41\n"
X_"v44,v150,s94,v59 op_sel_hi:[1,0,0]\n"
X_"v45,v150,s94,v59 op_sel:[1,0,0] op_sel_hi:[1,0,0]\n"
E_"v42,v42\n"
E_"v43,v43\n"
C_"v148,v40,v41\n"
X_"v46,v151,s94,v59 op_sel_hi:[1,0,0]\n"
X_"v47,v151,s94,v59 op_sel:[1,0,0] op_sel_hi:[1,0,0]\n"
E_"v44,v44\n"
E_"v45,v45\n"
C_"v149,v42,v43\n"
E_"v46,v46\n"
E_"v47,v47\n"
C_"v150,v44,v45\n"
C_"v151,v46,v47\n"
"s_barrier\n"
"v_add_u32 v1, 0, v0\n"
"v_add_u32 v2, 0x10000, v1\n"
"v_add_u32 v3, 0x20000, v1\n"
T_"v[8:9],v1 offset:50688\n"
T_"v[10:11],v1 offset:51712\n"
T_"v[12:13],v1 offset:50696\n"
T_"v[14:15],v1 offset:51720\n"
T_"v[16:17],v1 offset:50944\n"
T_"v[18:19],v1 offset:51968\n"
T_"v[20:21],v1 offset:50952\n"
T_"v[22:23],v1 offset:51976\n"
T_"v[24:25],v1 offset:51200\n"
T_"v[26:27],v1 offset:52224\n"
T_"v[28:29],v1 offset:51208\n"
T_"v[30:31],v1 offset:52232\n"
X_"v40,v152,s94,v58 op_sel_hi:[1,0,0]\n"
X_"v41,v152,s94,v58 op_sel:[1,0,0] op_sel_hi:[1,0,0]\n"
X_"v42,v153,s94,v58 op_sel_hi:[1,0,0]\n"
X_"v43,v153,s94,v58 op_sel:[1,0,0] op_sel_hi:[1,0,0]\n"
E_"v40,v40\n"
E_"v41,v41\n"
X_"v44,v154,s94,v58 op_sel_hi:[1,0,0]\n"
X_"v45,v154,s94,v58 op_sel:[1,0,0] op_sel_hi:[1,0,0]\n"
E_"v42,v42\n"
E_"v43,v43\n"
C_"v152,v40,v41\n"
X_"v46,v155,s94,v58 op_sel_hi:[1,0,0]\n"
X_"v47,v155,s94,v58 op_sel:[1,0,0] op_sel_hi:[1,0,0]\n"
E_"v44,v44\n"
E_"v45,v45\n"
C_"v153,v42,v43\n"
E_"v46,v46\n"
E_"v47,v47\n"
C_"v154,v44,v45\n"
C_"v155,v46,v47\n"
M_"v[48:51],v[60:63],v[192:195],0\n"
"v_pk_sub_u16 v200, v200, v56 clamp\n"
M_"v[52:55],v[60:63],v[196:199],0\n"
W_"lgkmcnt(10)\n"
"v_pk_sub_u16 v201, v201, v56 clamp\n"
M_"v[64:67],v[8:11],v[192:195],0\n"
T_"v[32:33],v1 offset:51456\n"
"v_pk_sub_u16 v202, v202, v56 clamp\n"
M_"v[68:71],v[8:11],v[196:199],0\n"
W_"lgkmcnt(9)\n"
T_"v[34:35],v1 offset:52480\n"
"v_pk_sub_u16 v203, v203, v56 clamp\n"
M_"v[72:75],v[12:15],v[192:195],0\n"
T_"v[36:37],v1 offset:51464\n"
"v_pk_sub_u16 v204, v204, v57 clamp\n"
M_"v[76:79],v[12:15],v[196:199],0\n"
W_"lgkmcnt(9)\n"
T_"v[38:39],v1 offset:52488\n"
"v_pk_sub_u16 v205, v205, v57 clamp\n"
M_"v[80:83],v[16:19],v[192:195],0\n"
T_"v[8:9],v2 offset:44288\n"
"v_pk_sub_u16 v206, v206, v57 clamp\n"
M_"v[84:87],v[16:19],v[196:199],0\n"
W_"lgkmcnt(5)\n"
T_"v[10:11],v2 offset:45312\n"
"v_pk_sub_u16 v207, v207, v57 clamp\n"
M_"v[88:91],v[20:23],v[192:195],0\n"
T_"v[12:13],v2 offset:44296\n"
"v_pk_sub_u16 v208, v208, v56 clamp\n"
M_"v[92:95],v[20:23],v[196:199],0\n"
T_"v[14:15],v2 offset:45320\n"
"v_pk_sub_u16 v209, v209, v56 clamp\n"
M_"v[96:99],v[24:27],v[192:195],0\n"
T_"v[16:17],v2 offset:44544\n"
"v_pk_sub_u16 v210, v210, v56 clamp\n"
M_"v[100:103],v[24:27],v[196:199],0\n"
T_"v[18:19],v2 offset:45568\n"
"v_pk_sub_u16 v211, v211, v56 clamp\n"
M_"v[104:107],v[28:31],v[192:195],0\n"
T_"v[20:21],v2 offset:44552\n"
"v_pk_sub_u16 v212, v212, v57 clamp\n"
M_"v[108:111],v[28:31],v[196:199],0\n"
W_"lgkmcnt(6)\n"
T_"v[22:23],v2 offset:45576\n"
"v_pk_sub_u16 v213, v213, v57 clamp\n"
M_"v[112:115],v[32:35],v[192:195],0\n"
T_"v[24:25],v2 offset:44800\n"
"v_pk_sub_u16 v214, v214, v57 clamp\n"
M_"v[116:119],v[32:35],v[196:199],0\n"
T_"v[26:27],v2 offset:45824\n"
"v_pk_sub_u16 v215, v215, v57 clamp\n"
M_"v[120:123],v[36:39],v[192:195],0\n"
T_"v[28:29],v2 offset:44808\n"
"v_pk_sub_u16 v216, v216, v56 clamp\n"
M_"v[124:127],v[36:39],v[196:199],0\n"
T_"v[30:31],v2 offset:45832\n"
"v_pk_sub_u16 v217, v217, v56 clamp\n"
M_"v[48:51],v[60:63],v[200:203],v[48:51]\n"
"v_pk_sub_u16 v218, v218, v56 clamp\n"
M_"v[52:55],v[60:63],v[204:207],v[52:55]\n"
W_"lgkmcnt(5)\n"
"v_pk_sub_u16 v219, v219, v56 clamp\n"
M_"v[64:67],v[8:11],v[200:203],v[64:67]\n"
T_"v[32:33],v2 offset:45056\n"
"v_pk_sub_u16 v220, v220, v57 clamp\n"
M_"v[68:71],v[8:11],v[204:207],v[68:71]\n"
T_"v[34:35],v2 offset:46080\n"
"v_pk_sub_u16 v221, v221, v57 clamp\n"
M_"v[72:75],v[12:15],v[200:203],v[72:75]\n"
T_"v[36:37],v2 offset:45064\n"
"v_pk_sub_u16 v222, v222, v57 clamp\n"
M_"v[76:79],v[12:15],v[204:207],v[76:79]\n"
T_"v[38:39],v2 offset:46088\n"
"v_pk_sub_u16 v223, v223, v57 clamp\n"
M_"v[80:83],v[16:19],v[200:203],v[80:83]\n"
"s_barrier\n"
T_"v[8:9],v3 offset:20992\n"
"v_pk_sub_u16 v224, v224, v56 clamp\n"
M_"v[84:87],v[16:19],v[204:207],v[84:87]\n"
W_"lgkmcnt(5)\n"
T_"v[10:11],v3 offset:22016\n"
"v_pk_sub_u16 v225, v225, v56 clamp\n"
M_"v[88:91],v[20:23],v[200:203],v[88:91]\n"
T_"v[12:13],v3 offset:21000\n"
"v_pk_sub_u16 v226, v226, v56 clamp\n"
M_"v[92:95],v[20:23],v[204:207],v[92:95]\n"
T_"v[14:15],v3 offset:22024\n"
"v_pk_sub_u16 v227, v227, v56 clamp\n"
M_"v[96:99],v[24:27],v[200:203],v[96:99]\n"
T_"v[16:17],v3 offset:21248\n"
"v_pk_sub_u16 v228, v228, v57 clamp\n"
M_"v[100:103],v[24:27],v[204:207],v[100:103]\n"
T_"v[18:19],v3 offset:22272\n"
"v_pk_sub_u16 v229, v229, v57 clamp\n"
M_"v[104:107],v[28:31],v[200:203],v[104:107]\n"
T_"v[20:21],v3 offset:21256\n"
"v_pk_sub_u16 v230, v230, v57 clamp\n"
M_"v[108:111],v[28:31],v[204:207],v[108:111]\n"
W_"lgkmcnt(6)\n"
T_"v[22:23],v3 offset:22280\n"
"v_pk_sub_u16 v231, v231, v57 clamp\n"
M_"v[112:115],v[32:35],v[200:203],v[112:115]\n"
T_"v[24:25],v3 offset:21504\n"
X_"v40,v156,s94,v59 op_sel_hi:[1,0,0]\n"
M_"v[116:119],v[32:35],v[204:207],v[116:119]\n"
T_"v[26:27],v3 offset:22528\n"
X_"v41,v156,s94,v59 op_sel:[1,0,0] op_sel_hi:[1,0,0]\n"
M_"v[120:123],v[36:39],v[200:203],v[120:123]\n"
T_"v[28:29],v3 offset:21512\n"
X_"v42,v157,s94,v59 op_sel_hi:[1,0,0]\n"
M_"v[124:127],v[36:39],v[204:207],v[124:127]\n"
T_"v[30:31],v3 offset:22536\n"
X_"v43,v157,s94,v59 op_sel:[1,0,0] op_sel_hi:[1,0,0]\n"
M_"v[48:51],v[60:63],v[208:211],v[48:51]\n"
M_"v[52:55],v[60:63],v[212:215],v[52:55]\n"
W_"lgkmcnt(5)\n"
E_"v40,v40\n"
M_"v[64:67],v[8:11],v[208:211],v[64:67]\n"
T_"v[32:33],v3 offset:21760\n"
E_"v41,v41\n"
M_"v[68:71],v[8:11],v[212:215],v[68:71]\n"
T_"v[34:35],v3 offset:22784\n"
X_"v44,v158,s94,v59 op_sel_hi:[1,0,0]\n"
M_"v[72:75],v[12:15],v[208:211],v[72:75]\n"
T_"v[36:37],v3 offset:21768\n"
M_"v[76:79],v[12:15],v[212:215],v[76:79]\n"
T_"v[38:39],v3 offset:22792\n"
X_"v45,v158,s94,v59 op_sel:[1,0,0] op_sel_hi:[1,0,0]\n"
M_"v[80:83],v[16:19],v[208:211],v[80:83]\n"
T_"v[8:9],v1 offset:16896\n"
E_"v42,v42\n"
M_"v[84:87],v[16:19],v[212:215],v[84:87]\n"
W_"lgkmcnt(5)\n"
T_"v[10:11],v1 offset:17920\n"
E_"v43,v43\n"
M_"v[88:91],v[20:23],v[208:211],v[88:91]\n"
T_"v[12:13],v1 offset:16904\n"
M_"v[92:95],v[20:23],v[212:215],v[92:95]\n"
T_"v[14:15],v1 offset:17928\n"
C_"v156,v40,v41\n"
M_"v[96:99],v[24:27],v[208:211],v[96:99]\n"
T_"v[16:17],v1 offset:17152\n"
X_"v46,v159,s94,v59 op_sel_hi:[1,0,0]\n"
M_"v[100:103],v[24:27],v[212:215],v[100:103]\n"
T_"v[18:19],v1 offset:18176\n"
X_"v47,v159,s94,v59 op_sel:[1,0,0] op_sel_hi:[1,0,0]\n"
M_"v[104:107],v[28:31],v[208:211],v[104:107]\n"
T_"v[20:21],v1 offset:17160\n"
T_"v[22:23],v1 offset:18184\n"
M_"v[108:111],v[28:31],v[212:215],v[108:111]\n"
W_"lgkmcnt(7)\n"
E_"v44,v44\n"
M_"v[112:115],v[32:35],v[208:211],v[112:115]\n"
T_"v[24:25],v1 offset:17408\n"
E_"v45,v45\n"
M_"v[116:119],v[32:35],v[212:215],v[116:119]\n"
T_"v[26:27],v1 offset:18432\n"
C_"v157,v42,v43\n"
M_"v[120:123],v[36:39],v[208:211],v[120:123]\n"
T_"v[28:29],v1 offset:17416\n"
T_"v[30:31],v1 offset:18440\n"
M_"v[124:127],v[36:39],v[212:215],v[124:127]\n"
E_"v46,v46\n"
M_"v[48:51],v[60:63],v[216:219],v[48:51]\n"
E_"v47,v47\n"
M_"v[52:55],v[60:63],v[220:223],v[52:55]\n"
W_"lgkmcnt(4)\n"
C_"v158,v44,v45\n"
M_"v[64:67],v[8:11],v[216:219],v[64:67]\n"
T_"v[32:33],v1 offset:17664\n"
M_"v[68:71],v[8:11],v[220:223],v[68:71]\n"
T_"v[34:35],v1 offset:18688\n"
C_"v159,v46,v47\n"
M_"v[72:75],v[12:15],v[216:219],v[72:75]\n"
T_"v[36:37],v1 offset:17672\n"
X_"v40,v160,s94,v58 op_sel_hi:[1,0,0]\n"
M_"v[76:79],v[12:15],v[220:223],v[76:79]\n"
T_"v[38:39],v1 offset:18696\n"
X_"v41,v160,s94,v58 op_sel:[1,0,0] op_sel_hi:[1,0,0]\n"
M_"v[80:83],v[16:19],v[216:219],v[80:83]\n"
"s_barrier\n"
T_"v[8:9],v2 offset:35840\n"
X_"v42,v161,s94,v58 op_sel_hi:[1,0,0]\n"
M_"v[84:87],v[16:19],v[220:223],v[84:87]\n"
T_"v[10:11],v2 offset:36864\n"
M_"v[88:91],v[20:23],v[216:219],v[88:91]\n"
T_"v[12:13],v2 offset:35848\n"
X_"v43,v161,s94,v58 op_sel:[1,0,0] op_sel_hi:[1,0,0]\n"
M_"v[92:95],v[20:23],v[220:223],v[92:95]\n"
W_"lgkmcnt(6)\n"
T_"v[14:15],v2 offset:36872\n"
E_"v40,v40\n"
M_"v[96:99],v[24:27],v[216:219],v[96:99]\n"
T_"v[16:17],v2 offset:36096\n"
T_"v[18:19],v2 offset:37120\n"
M_"v[100:103],v[24:27],v[220:223],v[100:103]\n"
E_"v41,v41\n"
M_"v[104:107],v[28:31],v[216:219],v[104:107]\n"
T_"v[20:21],v2 offset:36104\n"
X_"v44,v162,s94,v58 op_sel_hi:[1,0,0]\n"
M_"v[108:111],v[28:31],v[220:223],v[108:111]\n"
W_"lgkmcnt(6)\n"
T_"v[22:23],v2 offset:37128\n"
X_"v45,v162,s94,v58 op_sel:[1,0,0] op_sel_hi:[1,0,0]\n"
M_"v[112:115],v[32:35],v[216:219],v[112:115]\n"
T_"v[24:25],v2 offset:36352\n"
T_"v[26:27],v2 offset:37376\n"
M_"v[116:119],v[32:35],v[220:223],v[116:119]\n"
E_"v42,v42\n"
M_"v[120:123],v[36:39],v[216:219],v[120:123]\n"
T_"v[28:29],v2 offset:36360\n"
E_"v43,v43\n"
M_"v[124:127],v[36:39],v[220:223],v[124:127]\n"
T_"v[30:31],v2 offset:37384\n"
C_"v160,v40,v41\n"
M_"v[48:51],v[60:63],v[224:227],v[48:51]\n"
X_"v46,v163,s94,v58 op_sel_hi:[1,0,0]\n"
M_"v[52:55],v[60:63],v[228:231],v[52:55]\n"
W_"lgkmcnt(5)\n"
M_"v[64:67],v[8:11],v[224:227],v[64:67]\n"
T_"v[32:33],v2 offset:36608\n"
X_"v47,v163,s94,v58 op_sel:[1,0,0] op_sel_hi:[1,0,0]\n"
M_"v[68:71],v[8:11],v[228:231],v[68:71]\n"
T_"v[34:35],v2 offset:37632\n"
E_"v44,v44\n"
M_"v[72:75],v[12:15],v[224:227],v[72:75]\n"
T_"v[36:37],v2 offset:36616\n"
T_"v[38:39],v2 offset:37640\n"
M_"v[76:79],v[12:15],v[228:231],v[76:79]\n"
E_"v45,v45\n"
M_"v[80:83],v[16:19],v[224:227],v[80:83]\n"
T_"v[8:9],v2 offset:52736\n"
C_"v161,v42,v43\n"
M_"v[84:87],v[16:19],v[228:231],v[84:87]\n"
W_"lgkmcnt(5)\n"
T_"v[10:11],v2 offset:53760\n"
E_"v46,v46\n"
M_"v[88:91],v[20:23],v[224:227],v[88:91]\n"
T_"v[12:13],v2 offset:52744\n"
T_"v[14:15],v2 offset:53768\n"
M_"v[92:95],v[20:23],v[228:231],v[92:95]\n"
E_"v47,v47\n"
M_"v[96:99],v[24:27],v[224:227],v[96:99]\n"
T_"v[16:17],v2 offset:52992\n"
C_"v162,v44,v45\n"
M_"v[100:103],v[24:27],v[228:231],v[100:103]\n"
T_"v[18:19],v2 offset:54016\n"
C_"v163,v46,v47\n"
M_"v[104:107],v[28:31],v[224:227],v[104:107]\n"
T_"v[20:21],v2 offset:53000\n"
X_"v40,v164,s94,v59 op_sel_hi:[1,0,0]\n"
M_"v[108:111],v[28:31],v[228:231],v[108:111]\n"
W_"lgkmcnt(6)\n"
T_"v[22:23],v2 offset:54024\n"
X_"v41,v164,s94,v59 op_sel:[1,0,0] op_sel_hi:[1,0,0]\n"
M_"v[112:115],v[32:35],v[224:227],v[112:115]\n"
T_"v[24:25],v2 offset:53248\n"
M_"v[116:119],v[32:35],v[228:231],v[116:119]\n"
T_"v[26:27],v2 offset:54272\n"
X_"v42,v165,s94,v59 op_sel_hi:[1,0,0]\n"
M_"v[120:123],v[36:39],v[224:227],v[120:123]\n"
T_"v[28:29],v2 offset:53256\n"
X_"v43,v165,s94,v59 op_sel:[1,0,0] op_sel_hi:[1,0,0]\n"
M_"v[124:127],v[36:39],v[228:231],v[124:127]\n"
T_"v[30:31],v2 offset:54280\n"
E_"v40,v40\n"
M_"v[48:51],v[60:63],v[232:235],v[48:51]\n"
M_"v[52:55],v[60:63],v[236:239],v[52:55]\n"
W_"lgkmcnt(5)\n"
E_"v41,v41\n"
M_"v[64:67],v[8:11],v[232:235],v[64:67]\n"
T_"v[32:33],v2 offset:53504\n"
X_"v44,v166,s94,v59 op_sel_hi:[1,0,0]\n"
M_"v[68:71],v[8:11],v[236:239],v[68:71]\n"
T_"v[34:35],v2 offset:54528\n"
X_"v45,v166,s94,v59 op_sel:[1,0,0] op_sel_hi:[1,0,0]\n"
M_"v[72:75],v[12:15],v[232:235],v[72:75]\n"
T_"v[36:37],v2 offset:53512\n"
T_"v[38:39],v2 offset:54536\n"
M_"v[76:79],v[12:15],v[236:239],v[76:79]\n"
E_"v42,v42\n"
M_"v[80:83],v[16:19],v[232:235],v[80:83]\n"
"s_barrier\n"
T_"v[8:9],v1 offset:0\n"
E_"v43,v43\n"
M_"v[84:87],v[16:19],v[236:239],v[84:87]\n"
W_"lgkmcnt(5)\n"
T_"v[10:11],v1 offset:1024\n"
C_"v164,v40,v41\n"
M_"v[88:91],v[20:23],v[232:235],v[88:91]\n"
T_"v[12:13],v1 offset:8\n"
M_"v[92:95],v[20:23],v[236:239],v[92:95]\n"
T_"v[14:15],v1 offset:1032\n"
X_"v46,v167,s94,v59 op_sel_hi:[1,0,0]\n"
M_"v[96:99],v[24:27],v[232:235],v[96:99]\n"
T_"v[16:17],v1 offset:256\n"
X_"v47,v167,s94,v59 op_sel:[1,0,0] op_sel_hi:[1,0,0]\n"
M_"v[100:103],v[24:27],v[236:239],v[100:103]\n"
T_"v[18:19],v1 offset:1280\n"
E_"v44,v44\n"
M_"v[104:107],v[28:31],v[232:235],v[104:107]\n"
T_"v[20:21],v1 offset:264\n"
T_"v[22:23],v1 offset:1288\n"
M_"v[108:111],v[28:31],v[236:239],v[108:111]\n"
W_"lgkmcnt(7)\n"
E_"v45,v45\n"
M_"v[112:115],v[32:35],v[232:235],v[112:115]\n"
T_"v[24:25],v1 offset:512\n"
C_"v165,v42,v43\n"
M_"v[116:119],v[32:35],v[236:239],v[116:119]\n"
T_"v[26:27],v1 offset:1536\n"
E_"v46,v46\n"
M_"v[120:123],v[36:39],v[232:235],v[120:123]\n"
T_"v[28:29],v1 offset:520\n"
T_"v[30:31],v1 offset:1544\n"
M_"v[124:127],v[36:39],v[236:239],v[124:127]\n"
E_"v47,v47\n"
M_"v[48:51],v[60:63],v[240:243],v[48:51]\n"
C_"v166,v44,v45\n"
M_"v[52:55],v[60:63],v[244:247],v[52:55]\n"
W_"lgkmcnt(4)\n"
C_"v167,v46,v47\n"
M_"v[64:67],v[8:11],v[240:243],v[64:67]\n"
T_"v[32:33],v1 offset:768\n"
M_"v[68:71],v[8:11],v[244:247],v[68:71]\n"
T_"v[34:35],v1 offset:1792\n"
X_"v40,v168,s94,v58 op_sel_hi:[1,0,0]\n"
M_"v[72:75],v[12:15],v[240:243],v[72:75]\n"
T_"v[36:37],v1 offset:776\n"
X_"v41,v168,s94,v58 op_sel:[1,0,0] op_sel_hi:[1,0,0]\n"
M_"v[76:79],v[12:15],v[244:247],v[76:79]\n"
T_"v[38:39],v1 offset:1800\n"
X_"v42,v169,s94,v58 op_sel_hi:[1,0,0]\n"
M_"v[80:83],v[16:19],v[240:243],v[80:83]\n"
T_"v[8:9],v1 offset:33792\n"
X_"v43,v169,s94,v58 op_sel:[1,0,0] op_sel_hi:[1,0,0]\n"
M_"v[84:87],v[16:19],v[244:247],v[84:87]\n"
T_"v[10:11],v1 offset:34816\n"
M_"v[88:91],v[20:23],v[240:243],v[88:91]\n"
T_"v[12:13],v1 offset:33800\n"
E_"v40,v40\n"
M_"v[92:95],v[20:23],v[244:247],v[92:95]\n"
W_"lgkmcnt(6)\n"
T_"v[14:15],v1 offset:34824\n"
E_"v41,v41\n"
M_"v[96:99],v[24:27],v[240:243],v[96:99]\n"
T_"v[16:17],v1 offset:34048\n"
X_"v44,v170,s94,v58 op_sel_hi:[1,0,0]\n"
M_"v[100:103],v[24:27],v[244:247],v[100:103]\n"
T_"v[18:19],v1 offset:35072\n"
M_"v[104:107],v[28:31],v[240:243],v[104:107]\n"
T_"v[20:21],v1 offset:34056\n"
X_"v45,v170,s94,v58 op_sel:[1,0,0] op_sel_hi:[1,0,0]\n"
M_"v[108:111],v[28:31],v[244:247],v[108:111]\n"
W_"lgkmcnt(6)\n"
T_"v[22:23],v1 offset:35080\n"
E_"v42,v42\n"
M_"v[112:115],v[32:35],v[240:243],v[112:115]\n"
T_"v[24:25],v1 offset:34304\n"
T_"v[26:27],v1 offset:35328\n"
M_"v[116:119],v[32:35],v[244:247],v[116:119]\n"
E_"v43,v43\n"
M_"v[120:123],v[36:39],v[240:243],v[120:123]\n"
T_"v[28:29],v1 offset:34312\n"
C_"v168,v40,v41\n"
M_"v[124:127],v[36:39],v[244:247],v[124:127]\n"
T_"v[30:31],v1 offset:35336\n"
X_"v46,v171,s94,v58 op_sel_hi:[1,0,0]\n"
M_"v[48:51],v[60:63],v[248:251],v[48:51]\n"
X_"v47,v171,s94,v58 op_sel:[1,0,0] op_sel_hi:[1,0,0]\n"
M_"v[52:55],v[60:63],v[252:255],v[52:55]\n"
W_"lgkmcnt(5)\n"
M_"v[64:67],v[8:11],v[248:251],v[64:67]\n"
T_"v[32:33],v1 offset:34560\n"
E_"v44,v44\n"
M_"v[68:71],v[8:11],v[252:255],v[68:71]\n"
T_"v[34:35],v1 offset:35584\n"
E_"v45,v45\n"
M_"v[72:75],v[12:15],v[248:251],v[72:75]\n"
T_"v[36:37],v1 offset:34568\n"
C_"v169,v42,v43\n"
M_"v[76:79],v[12:15],v[252:255],v[76:79]\n"
T_"v[38:39],v1 offset:35592\n"
M_"v[80:83],v[16:19],v[248:251],v[80:83]\n"
"s_barrier\n"
T_"v[8:9],v2 offset:61184\n"
E_"v46,v46\n"
M_"v[84:87],v[16:19],v[252:255],v[84:87]\n"
W_"lgkmcnt(5)\n"
T_"v[10:11],v2 offset:62208\n"
E_"v47,v47\n"
M_"v[88:91],v[20:23],v[248:251],v[88:91]\n"
T_"v[12:13],v2 offset:61192\n"
C_"v170,v44,v45\n"
M_"v[92:95],v[20:23],v[252:255],v[92:95]\n"
T_"v[14:15],v2 offset:62216\n"
M_"v[96:99],v[24:27],v[248:251],v[96:99]\n"
T_"v[16:17],v2 offset:61440\n"
C_"v171,v46,v47\n"
M_"v[100:103],v[24:27],v[252:255],v[100:103]\n"
"PTL13:\n"
"s_cbranch_execz PTR\n"
T_"v[18:19],v2 offset:62464\n"
X_"v40,v172,s94,v59 op_sel_hi:[1,0,0]\n"
M_"v[104:107],v[28:31],v[248:251],v[104:107]\n"
T_"v[20:21],v2 offset:61448\n"
X_"v41,v172,s94,v59 op_sel:[1,0,0] op_sel_hi:[1,0,0]\n"
M_"v[108:111],v[28:31],v[252:255],v[108:111]\n"
W_"lgkmcnt(6)\n"
T_"v[22:23],v2 offset:62472\n"
X_"v42,v173,s94,v59 op_sel_hi:[1,0,0]\n"
M_"v[112:115],v[32:35],v[248:251],v[112:115]\n"
T_"v[24:25],v2 offset:61696\n"
M_"v[116:119],v[32:35],v[252:255],v[116:119]\n"
T_"v[26:27],v2 offset:62720\n"
X_"v43,v173,s94,v59 op_sel:[1,0,0] op_sel_hi:[1,0,0]\n"
M_"v[120:123],v[36:39],v[248:251],v[120:123]\n"
T_"v[28:29],v2 offset:61704\n"
E_"v40,v40\n"
M_"v[124:127],v[36:39],v[252:255],v[124:127]\n"
T_"v[30:31],v2 offset:62728\n"
E_"v41,v41\n"
M_"v[48:51],v[60:63],v[128:131],v[48:51]\n"
M_"v[52:55],v[60:63],v[132:135],v[52:55]\n"
W_"lgkmcnt(5)\n"
X_"v44,v174,s94,v59 op_sel_hi:[1,0,0]\n"
M_"v[64:67],v[8:11],v[128:131],v[64:67]\n"
T_"v[32:33],v2 offset:61952\n"
X_"v45,v174,s94,v59 op_sel:[1,0,0] op_sel_hi:[1,0,0]\n"
M_"v[68:71],v[8:11],v[132:135],v[68:71]\n"
T_"v[34:35],v2 offset:62976\n"
E_"v42,v42\n"
M_"v[72:75],v[12:15],v[128:131],v[72:75]\n"
T_"v[36:37],v2 offset:61960\n"
T_"v[38:39],v2 offset:62984\n"
M_"v[76:79],v[12:15],v[132:135],v[76:79]\n"
E_"v43,v43\n"
M_"v[80:83],v[16:19],v[128:131],v[80:83]\n"
T_"v[8:9],v1 offset:8448\n"
C_"v172,v40,v41\n"
M_"v[84:87],v[16:19],v[132:135],v[84:87]\n"
W_"lgkmcnt(5)\n"
T_"v[10:11],v1 offset:9472\n"
X_"v46,v175,s94,v59 op_sel_hi:[1,0,0]\n"
M_"v[88:91],v[20:23],v[128:131],v[88:91]\n"
T_"v[12:13],v1 offset:8456\n"
M_"v[92:95],v[20:23],v[132:135],v[92:95]\n"
T_"v[14:15],v1 offset:9480\n"
X_"v47,v175,s94,v59 op_sel:[1,0,0] op_sel_hi:[1,0,0]\n"
M_"v[96:99],v[24:27],v[128:131],v[96:99]\n"
T_"v[16:17],v1 offset:8704\n"
E_"v44,v44\n"
M_"v[100:103],v[24:27],v[132:135],v[100:103]\n"
T_"v[18:19],v1 offset:9728\n"
E_"v45,v45\n"
M_"v[104:107],v[28:31],v[128:131],v[104:107]\n"
T_"v[20:21],v1 offset:8712\n"
M_"v[108:111],v[28:31],v[132:135],v[108:111]\n"
W_"lgkmcnt(6)\n"
T_"v[22:23],v1 offset:9736\n"
C_"v173,v42,v43\n"
M_"v[112:115],v[32:35],v[128:131],v[112:115]\n"
T_"v[24:25],v1 offset:8960\n"
E_"v46,v46\n"
M_"v[116:119],v[32:35],v[132:135],v[116:119]\n"
T_"v[26:27],v1 offset:9984\n"
E_"v47,v47\n"
M_"v[120:123],v[36:39],v[128:131],v[120:123]\n"
T_"v[28:29],v1 offset:8968\n"
M_"v[124:127],v[36:39],v[132:135],v[124:127]\n"
T_"v[30:31],v1 offset:9992\n"
C_"v174,v44,v45\n"
M_"v[48:51],v[60:63],v[136:139],v[48:51]\n"
C_"v175,v46,v47\n"
M_"v[52:55],v[60:63],v[140:143],v[52:55]\n"
W_"lgkmcnt(5)\n"
X_"v40,v176,s94,v58 op_sel_hi:[1,0,0]\n"
M_"v[64:67],v[8:11],v[136:139],v[64:67]\n"
T_"v[32:33],v1 offset:9216\n"
X_"v41,v176,s94,v58 op_sel:[1,0,0] op_sel_hi:[1,0,0]\n"
M_"v[68:71],v[8:11],v[140:143],v[68:71]\n"
T_"v[34:35],v1 offset:10240\n"
M_"v[72:75],v[12:15],v[136:139],v[72:75]\n"
T_"v[36:37],v1 offset:9224\n"
X_"v42,v177,s94,v58 op_sel_hi:[1,0,0]\n"
M_"v[76:79],v[12:15],v[140:143],v[76:79]\n"
T_"v[38:39],v1 offset:10248\n"
X_"v43,v177,s94,v58 op_sel:[1,0,0] op_sel_hi:[1,0,0]\n"
M_"v[80:83],v[16:19],v[136:139],v[80:83]\n"
"s_barrier\n"
T_"v[8:9],v1 offset:42240\n"
T_"v[10:11],v1 offset:43264\n"
M_"v[84:87],v[16:19],v[140:143],v[84:87]\n"
W_"lgkmcnt(6)\n"
E_"v40,v40\n"
M_"v[88:91],v[20:23],v[136:139],v[88:91]\n"
T_"v[12:13],v1 offset:42248\n"
E_"v41,v41\n"
M_"v[92:95],v[20:23],v[140:143],v[92:95]\n"
T_"v[14:15],v1 offset:43272\n"
X_"v44,v178,s94,v58 op_sel_hi:[1,0,0]\n"
M_"v[96:99],v[24:27],v[136:139],v[96:99]\n"
T_"v[16:17],v1 offset:42496\n"
X_"v45,v178,s94,v58 op_sel:[1,0,0] op_sel_hi:[1,0,0]\n"
M_"v[100:103],v[24:27],v[140:143],v[100:103]\n"
T_"v[18:19],v1 offset:43520\n"
M_"v[104:107],v[28:31],v[136:139],v[104:107]\n"
T_"v[20:21],v1 offset:42504\n"
E_"v42,v42\n"
M_"v[108:111],v[28:31],v[140:143],v[108:111]\n"
W_"lgkmcnt(5)\n"
T_"v[22:23],v1 offset:43528\n"
E_"v43,v43\n"
M_"v[112:115],v[32:35],v[136:139],v[112:115]\n"
T_"v[24:25],v1 offset:42752\n"
C_"v176,v40,v41\n"
M_"v[116:119],v[32:35],v[140:143],v[116:119]\n"
T_"v[26:27],v1 offset:43776\n"
M_"v[120:123],v[36:39],v[136:139],v[120:123]\n"
T_"v[28:29],v1 offset:42760\n"
X_"v46,v179,s94,v58 op_sel_hi:[1,0,0]\n"
M_"v[124:127],v[36:39],v[140:143],v[124:127]\n"
T_"v[30:31],v1 offset:43784\n"
X_"v47,v179,s94,v58 op_sel:[1,0,0] op_sel_hi:[1,0,0]\n"
M_"v[48:51],v[60:63],v[144:147],v[48:51]\n"
E_"v44,v44\n"
M_"v[52:55],v[60:63],v[148:151],v[52:55]\n"
M_"v[64:67],v[8:11],v[144:147],v[64:67]\n"
T_"v[32:33],v1 offset:43008\n"
E_"v45,v45\n"
M_"v[68:71],v[8:11],v[148:151],v[68:71]\n"
W_"lgkmcnt(4)\n"
T_"v[34:35],v1 offset:44032\n"
C_"v177,v42,v43\n"
M_"v[72:75],v[12:15],v[144:147],v[72:75]\n"
T_"v[36:37],v1 offset:43016\n"
T_"v[38:39],v1 offset:44040\n"
M_"v[76:79],v[12:15],v[148:151],v[76:79]\n"
E_"v46,v46\n"
M_"v[80:83],v[16:19],v[144:147],v[80:83]\n"
T_"v[8:9],v1 offset:59136\n"
E_"v47,v47\n"
M_"v[84:87],v[16:19],v[148:151],v[84:87]\n"
T_"v[10:11],v1 offset:60160\n"
C_"v178,v44,v45\n"
M_"v[88:91],v[20:23],v[144:147],v[88:91]\n"
T_"v[12:13],v1 offset:59144\n"
C_"v179,v46,v47\n"
M_"v[92:95],v[20:23],v[148:151],v[92:95]\n"
W_"lgkmcnt(6)\n"
T_"v[14:15],v1 offset:60168\n"
M_"v[96:99],v[24:27],v[144:147],v[96:99]\n"
T_"v[16:17],v1 offset:59392\n"
X_"v40,v180,s94,v59 op_sel_hi:[1,0,0]\n"
M_"v[100:103],v[24:27],v[148:151],v[100:103]\n"
T_"v[18:19],v1 offset:60416\n"
X_"v41,v180,s94,v59 op_sel:[1,0,0] op_sel_hi:[1,0,0]\n"
M_"v[104:107],v[28:31],v[144:147],v[104:107]\n"
T_"v[20:21],v1 offset:59400\n"
X_"v42,v181,s94,v59 op_sel_hi:[1,0,0]\n"
M_"v[108:111],v[28:31],v[148:151],v[108:111]\n"
W_"lgkmcnt(6)\n"
T_"v[22:23],v1 offset:60424\n"
X_"v43,v181,s94,v59 op_sel:[1,0,0] op_sel_hi:[1,0,0]\n"
M_"v[112:115],v[32:35],v[144:147],v[112:115]\n"
T_"v[24:25],v1 offset:59648\n"
T_"v[26:27],v1 offset:60672\n"
M_"v[116:119],v[32:35],v[148:151],v[116:119]\n"
E_"v40,v40\n"
M_"v[120:123],v[36:39],v[144:147],v[120:123]\n"
T_"v[28:29],v1 offset:59656\n"
E_"v41,v41\n"
M_"v[124:127],v[36:39],v[148:151],v[124:127]\n"
T_"v[30:31],v1 offset:60680\n"
X_"v44,v182,s94,v59 op_sel_hi:[1,0,0]\n"
M_"v[48:51],v[60:63],v[152:155],v[48:51]\n"
M_"v[52:55],v[60:63],v[156:159],v[52:55]\n"
W_"lgkmcnt(5)\n"
X_"v45,v182,s94,v59 op_sel:[1,0,0] op_sel_hi:[1,0,0]\n"
M_"v[64:67],v[8:11],v[152:155],v[64:67]\n"
T_"v[32:33],v1 offset:59904\n"
E_"v42,v42\n"
M_"v[68:71],v[8:11],v[156:159],v[68:71]\n"
T_"v[34:35],v1 offset:60928\n"
E_"v43,v43\n"
M_"v[72:75],v[12:15],v[152:155],v[72:75]\n"
T_"v[36:37],v1 offset:59912\n"
M_"v[76:79],v[12:15],v[156:159],v[76:79]\n"
T_"v[38:39],v1 offset:60936\n"
C_"v180,v40,v41\n"
M_"v[80:83],v[16:19],v[152:155],v[80:83]\n"
"s_barrier\n"
T_"v[8:9],v2 offset:2048\n"
X_"v46,v183,s94,v59 op_sel_hi:[1,0,0]\n"
M_"v[84:87],v[16:19],v[156:159],v[84:87]\n"
W_"lgkmcnt(5)\n"
T_"v[10:11],v2 offset:3072\n"
X_"v47,v183,s94,v59 op_sel:[1,0,0] op_sel_hi:[1,0,0]\n"
M_"v[88:91],v[20:23],v[152:155],v[88:91]\n"
T_"v[12:13],v2 offset:2056\n"
T_"v[14:15],v2 offset:3080\n"
M_"v[92:95],v[20:23],v[156:159],v[92:95]\n"
E_"v44,v44\n"
M_"v[96:99],v[24:27],v[152:155],v[96:99]\n"
T_"v[16:17],v2 offset:2304\n"
E_"v45,v45\n"
M_"v[100:103],v[24:27],v[156:159],v[100:103]\n"
T_"v[18:19],v2 offset:3328\n"
C_"v181,v42,v43\n"
M_"v[104:107],v[28:31],v[152:155],v[104:107]\n"
T_"v[20:21],v2 offset:2312\n"
T_"v[22:23],v2 offset:3336\n"
M_"v[108:111],v[28:31],v[156:159],v[108:111]\n"
W_"lgkmcnt(7)\n"
E_"v46,v46\n"
M_"v[112:115],v[32:35],v[152:155],v[112:115]\n"
T_"v[24:25],v2 offset:2560\n"
E_"v47,v47\n"
M_"v[116:119],v[32:35],v[156:159],v[116:119]\n"
T_"v[26:27],v2 offset:3584\n"
C_"v182,v44,v45\n"
M_"v[120:123],v[36:39],v[152:155],v[120:123]\n"
T_"v[28:29],v2 offset:2568\n"
M_"v[124:127],v[36:39],v[156:159],v[124:127]\n"
T_"v[30:31],v2 offset:3592\n"
C_"v183,v46,v47\n"
M_"v[48:51],v[60:63],v[160:163],v[48:51]\n"
X_"v40,v184,s94,v58 op_sel_hi:[1,0,0]\n"
M_"v[52:55],v[60:63],v[164:167],v[52:55]\n"
W_"lgkmcnt(4)\n"
X_"v41,v184,s94,v58 op_sel:[1,0,0] op_sel_hi:[1,0,0]\n"
M_"v[64:67],v[8:11],v[160:163],v[64:67]\n"
T_"v[32:33],v2 offset:2816\n"
X_"v42,v185,s94,v58 op_sel_hi:[1,0,0]\n"
M_"v[68:71],v[8:11],v[164:167],v[68:71]\n"
T_"v[34:35],v2 offset:3840\n"
M_"v[72:75],v[12:15],v[160:163],v[72:75]\n"
T_"v[36:37],v2 offset:2824\n"
X_"v43,v185,s94,v58 op_sel:[1,0,0] op_sel_hi:[1,0,0]\n"
M_"v[76:79],v[12:15],v[164:167],v[76:79]\n"
T_"v[38:39],v2 offset:3848\n"
E_"v40,v40\n"
M_"v[80:83],v[16:19],v[160:163],v[80:83]\n"
T_"v[8:9],v2 offset:10496\n"
T_"v[10:11],v2 offset:11520\n"
M_"v[84:87],v[16:19],v[164:167],v[84:87]\n"
E_"v41,v41\n"
M_"v[88:91],v[20:23],v[160:163],v[88:91]\n"
T_"v[12:13],v2 offset:10504\n"
X_"v44,v186,s94,v58 op_sel_hi:[1,0,0]\n"
M_"v[92:95],v[20:23],v[164:167],v[92:95]\n"
W_"lgkmcnt(6)\n"
T_"v[14:15],v2 offset:11528\n"
X_"v45,v186,s94,v58 op_sel:[1,0,0] op_sel_hi:[1,0,0]\n"
M_"v[96:99],v[24:27],v[160:163],v[96:99]\n"
T_"v[16:17],v2 offset:10752\n"
T_"v[18:19],v2 offset:11776\n"
M_"v[100:103],v[24:27],v[164:167],v[100:103]\n"
E_"v42,v42\n"
M_"v[104:107],v[28:31],v[160:163],v[104:107]\n"
T_"v[20:21],v2 offset:10760\n"
E_"v43,v43\n"
M_"v[108:111],v[28:31],v[164:167],v[108:111]\n"
W_"lgkmcnt(5)\n"
T_"v[22:23],v2 offset:11784\n"
C_"v184,v40,v41\n"
M_"v[112:115],v[32:35],v[160:163],v[112:115]\n"
T_"v[24:25],v2 offset:11008\n"
X_"v46,v187,s94,v58 op_sel_hi:[1,0,0]\n"
M_"v[116:119],v[32:35],v[164:167],v[116:119]\n"
T_"v[26:27],v2 offset:12032\n"
M_"v[120:123],v[36:39],v[160:163],v[120:123]\n"
T_"v[28:29],v2 offset:11016\n"
X_"v47,v187,s94,v58 op_sel:[1,0,0] op_sel_hi:[1,0,0]\n"
M_"v[124:127],v[36:39],v[164:167],v[124:127]\n"
T_"v[30:31],v2 offset:12040\n"
E_"v44,v44\n"
M_"v[48:51],v[60:63],v[168:171],v[48:51]\n"
E_"v45,v45\n"
M_"v[52:55],v[60:63],v[172:175],v[52:55]\n"
M_"v[64:67],v[8:11],v[168:171],v[64:67]\n"
T_"v[32:33],v2 offset:11264\n"
C_"v185,v42,v43\n"
M_"v[68:71],v[8:11],v[172:175],v[68:71]\n"
W_"lgkmcnt(4)\n"
T_"v[34:35],v2 offset:12288\n"
E_"v46,v46\n"
M_"v[72:75],v[12:15],v[168:171],v[72:75]\n"
T_"v[36:37],v2 offset:11272\n"
T_"v[38:39],v2 offset:12296\n"
M_"v[76:79],v[12:15],v[172:175],v[76:79]\n"
E_"v47,v47\n"
M_"v[80:83],v[16:19],v[168:171],v[80:83]\n"
"s_barrier\n"
T_"v[8:9],v3 offset:4096\n"
C_"v186,v44,v45\n"
M_"v[84:87],v[16:19],v[172:175],v[84:87]\n"
T_"v[10:11],v3 offset:5120\n"
C_"v187,v46,v47\n"
M_"v[88:91],v[20:23],v[168:171],v[88:91]\n"
T_"v[12:13],v3 offset:4104\n"
X_"v40,v188,s94,v59 op_sel_hi:[1,0,0]\n"
M_"v[92:95],v[20:23],v[172:175],v[92:95]\n"
W_"lgkmcnt(6)\n"
T_"v[14:15],v3 offset:5128\n"
X_"v41,v188,s94,v59 op_sel:[1,0,0] op_sel_hi:[1,0,0]\n"
M_"v[96:99],v[24:27],v[168:171],v[96:99]\n"
T_"v[16:17],v3 offset:4352\n"
M_"v[100:103],v[24:27],v[172:175],v[100:103]\n"
T_"v[18:19],v3 offset:5376\n"
X_"v42,v189,s94,v59 op_sel_hi:[1,0,0]\n"
M_"v[104:107],v[28:31],v[168:171],v[104:107]\n"
T_"v[20:21],v3 offset:4360\n"
X_"v43,v189,s94,v59 op_sel:[1,0,0] op_sel_hi:[1,0,0]\n"
M_"v[108:111],v[28:31],v[172:175],v[108:111]\n"
W_"lgkmcnt(6)\n"
T_"v[22:23],v3 offset:5384\n"
E_"v40,v40\n"
M_"v[112:115],v[32:35],v[168:171],v[112:115]\n"
T_"v[24:25],v3 offset:4608\n"
T_"v[26:27],v3 offset:5632\n"
M_"v[116:119],v[32:35],v[172:175],v[116:119]\n"
E_"v41,v41\n"
M_"v[120:123],v[36:39],v[168:171],v[120:123]\n"
T_"v[28:29],v3 offset:4616\n"
X_"v44,v190,s94,v59 op_sel_hi:[1,0,0]\n"
M_"v[124:127],v[36:39],v[172:175],v[124:127]\n"
T_"v[30:31],v3 offset:5640\n"
X_"v45,v190,s94,v59 op_sel:[1,0,0] op_sel_hi:[1,0,0]\n"
M_"v[48:51],v[60:63],v[176:179],v[48:51]\n"
M_"v[52:55],v[60:63],v[180:183],v[52:55]\n"
W_"lgkmcnt(5)\n"
E_"v42,v42\n"
M_"v[64:67],v[8:11],v[176:179],v[64:67]\n"
T_"v[32:33],v3 offset:4864\n"
E_"v43,v43\n"
M_"v[68:71],v[8:11],v[180:183],v[68:71]\n"
T_"v[34:35],v3 offset:5888\n"
C_"v188,v40,v41\n"
M_"v[72:75],v[12:15],v[176:179],v[72:75]\n"
T_"v[36:37],v3 offset:4872\n"
M_"v[76:79],v[12:15],v[180:183],v[76:79]\n"
T_"v[38:39],v3 offset:5896\n"
X_"v46,v191,s94,v59 op_sel_hi:[1,0,0]\n"
M_"v[80:83],v[16:19],v[176:179],v[80:83]\n"
T_"v[8:9],v1 offset:25344\n"
X_"v47,v191,s94,v59 op_sel:[1,0,0] op_sel_hi:[1,0,0]\n"
M_"v[84:87],v[16:19],v[180:183],v[84:87]\n"
W_"lgkmcnt(5)\n"
T_"v[10:11],v1 offset:26368\n"
E_"v44,v44\n"
M_"v[88:91],v[20:23],v[176:179],v[88:91]\n"
T_"v[12:13],v1 offset:25352\n"
T_"v[14:15],v1 offset:26376\n"
M_"v[92:95],v[20:23],v[180:183],v[92:95]\n"
E_"v45,v45\n"
M_"v[96:99],v[24:27],v[176:179],v[96:99]\n"
T_"v[16:17],v1 offset:25600\n"
C_"v189,v42,v43\n"
M_"v[100:103],v[24:27],v[180:183],v[100:103]\n"
T_"v[18:19],v1 offset:26624\n"
E_"v46,v46\n"
M_"v[104:107],v[28:31],v[176:179],v[104:107]\n"
T_"v[20:21],v1 offset:25608\n"
T_"v[22:23],v1 offset:26632\n"
M_"v[108:111],v[28:31],v[180:183],v[108:111]\n"
W_"lgkmcnt(7)\n"
E_"v47,v47\n"
M_"v[112:115],v[32:35],v[176:179],v[112:115]\n"
T_"v[24:25],v1 offset:25856\n"
C_"v190,v44,v45\n"
M_"v[116:119],v[32:35],v[180:183],v[116:119]\n"
T_"v[26:27],v1 offset:26880\n"
C_"v191,v46,v47\n"
M_"v[120:123],v[36:39],v[176:179],v[120:123]\n"
T_"v[28:29],v1 offset:25864\n"
M_"v[124:127],v[36:39],v[180:183],v[124:127]\n"
T_"v[30:31],v1 offset:26888\n"
M_"v[48:51],v[60:63],v[184:187],v[48:51]\n"
M_"v[52:55],v[60:63],v[188:191],v[52:55]\n"
W_"lgkmcnt(4)\n"
M_"v[64:67],v[8:11],v[184:187],v[64:67]\n"
T_"v[32:33],v1 offset:26112\n"
M_"v[68:71],v[8:11],v[188:191],v[68:71]\n"
T_"v[34:35],v1 offset:27136\n"
M_"v[72:75],v[12:15],v[184:187],v[72:75]\n"
T_"v[36:37],v1 offset:26120\n"
T_"v[38:39],v1 offset:27144\n"
M_"v[76:79],v[12:15],v[188:191],v[76:79]\n"
"v_rcp_f32 v58, v48\n"
M_"v[80:83],v[16:19],v[184:187],v[80:83]\n"
"s_barrier\n"
T_"v[8:9],v2 offset:18944\n"
T_"v[10:11],v2 offset:19968\n"
W_"vmcnt(0)\n"
"v_add_u32 v6, 1, v6\n"
M_"v[84:87],v[16:19],v[188:191],v[84:87]\n"
"v_rcp_f32 v59, v52\n"
M_"v[88:91],v[20:23],v[184:187],v[88:91]\n"
T_"v[12:13],v2 offset:18952\n"
M_"v[92:95],v[20:23],v[188:191],v[92:95]\n"
W_"lgkmcnt(6)\n"
T_"v[14:15],v2 offset:19976\n"
M_"v[96:99],v[24:27],v[184:187],v[96:99]\n"
T_"v[16:17],v2 offset:19200\n"
M_"v[100:103],v[24:27],v[188:191],v[100:103]\n"
T_"v[18:19],v2 offset:20224\n"
M_"v[104:107],v[28:31],v[184:187],v[104:107]\n"
T_"v[20:21],v2 offset:19208\n"
M_"v[108:111],v[28:31],v[188:191],v[108:111]\n"
W_"lgkmcnt(5)\n"
T_"v[22:23],v2 offset:20232\n"
M_"v[112:115],v[32:35],v[184:187],v[112:115]\n"
T_"v[24:25],v2 offset:19456\n"
M_"v[116:119],v[32:35],v[188:191],v[116:119]\n"
T_"v[26:27],v2 offset:20480\n"
M_"v[120:123],v[36:39],v[184:187],v[120:123]\n"
T_"v[28:29],v2 offset:19464\n"
T_"v[30:31],v2 offset:20488\n"
M_"v[124:127],v[36:39],v[188:191],v[124:127]\n"
"v_pk_mul_f32 v[64:65], v[64:65], v[58:59] op_sel_hi:[1,0]\n"
"v_pk_mul_f32 v[66:67], v[66:67], v[58:59] op_sel_hi:[1,0]\n"
"v_pk_mul_f32 v[72:73], v[72:73], v[58:59] op_sel_hi:[1,0]\n"
"v_pk_mul_f32 v[74:75], v[74:75], v[58:59] op_sel_hi:[1,0]\n"
C_"v64,v64,v65\n"
C_"v65,v66,v67\n"
C_"v66,v72,v73\n"
C_"v67,v74,v75\n"
"v_pk_mul_f32 v[68:69], v[68:69], v[58:59] op_sel:[0,1] op_sel_hi:[1,1]\n"
"v_pk_mul_f32 v[70:71], v[70:71], v[58:59] op_sel:[0,1] op_sel_hi:[1,1]\n"
"v_pk_mul_f32 v[76:77], v[76:77], v[58:59] op_sel:[0,1] op_sel_hi:[1,1]\n"
"v_pk_mul_f32 v[78:79], v[78:79], v[58:59] op_sel:[0,1] op_sel_hi:[1,1]\n"
"ds_write_b128 v5, v[64:67] offset:59136\n"
C_"v68,v68,v69\n"
C_"v69,v70,v71\n"
C_"v70,v76,v77\n"
C_"v71,v78,v79\n"
"v_pk_mul_f32 v[80:81], v[80:81], v[58:59] op_sel_hi:[1,0]\n"
"v_pk_mul_f32 v[82:83], v[82:83], v[58:59] op_sel_hi:[1,0]\n"
"v_pk_mul_f32 v[88:89], v[88:89], v[58:59] op_sel_hi:[1,0]\n"
"v_pk_mul_f32 v[90:91], v[90:91], v[58:59] op_sel_hi:[1,0]\n"
"ds_write_b128 v5, v[68:71] offset:60160\n"
C_"v80,v80,v81\n"
C_"v81,v82,v83\n"
C_"v82,v88,v89\n"
C_"v83,v90,v91\n"
"v_pk_mul_f32 v[84:85], v[84:85], v[58:59] op_sel:[0,1] op_sel_hi:[1,1]\n"
"v_pk_mul_f32 v[86:87], v[86:87], v[58:59] op_sel:[0,1] op_sel_hi:[1,1]\n"
"v_pk_mul_f32 v[92:93], v[92:93], v[58:59] op_sel:[0,1] op_sel_hi:[1,1]\n"
"v_pk_mul_f32 v[94:95], v[94:95], v[58:59] op_sel:[0,1] op_sel_hi:[1,1]\n"
"v_add_u32 v5, 0x10000, v5\n"
"ds_write_b128 v5, v[80:83] offset:2048\n"
C_"v84,v84,v85\n"
C_"v85,v86,v87\n"
C_"v86,v92,v93\n"
C_"v87,v94,v95\n"
"v_pk_mul_f32 v[96:97], v[96:97], v[58:59] op_sel_hi:[1,0]\n"
"v_pk_mul_f32 v[98:99], v[98:99], v[58:59] op_sel_hi:[1,0]\n"
"v_pk_mul_f32 v[104:105], v[104:105], v[58:59] op_sel_hi:[1,0]\n"
"v_pk_mul_f32 v[106:107], v[106:107], v[58:59] op_sel_hi:[1,0]\n"
"ds_write_b128 v5, v[84:87] offset:3072\n"
C_"v96,v96,v97\n"
C_"v97,v98,v99\n"
C_"v98,v104,v105\n"
C_"v99,v106,v107\n"
"v_pk_mul_f32 v[100:101], v[100:101], v[58:59] op_sel:[0,1] op_sel_hi:[1,1]\n"
"v_pk_mul_f32 v[102:103], v[102:103], v[58:59] op_sel:[0,1] op_sel_hi:[1,1]\n"
"v_pk_mul_f32 v[108:109], v[108:109], v[58:59] op_sel:[0,1] op_sel_hi:[1,1]\n"
"v_pk_mul_f32 v[110:111], v[110:111], v[58:59] op_sel:[0,1] op_sel_hi:[1,1]\n"
"ds_write_b128 v5, v[96:99] offset:10496\n"
C_"v100,v100,v101\n"
C_"v101,v102,v103\n"
C_"v102,v108,v109\n"
C_"v103,v110,v111\n"
"v_pk_mul_f32 v[112:113], v[112:113], v[58:59] op_sel_hi:[1,0]\n"
"v_pk_mul_f32 v[114:115], v[114:115], v[58:59] op_sel_hi:[1,0]\n"
"v_pk_mul_f32 v[120:121], v[120:121], v[58:59] op_sel_hi:[1,0]\n"
"v_pk_mul_f32 v[122:123], v[122:123], v[58:59] op_sel_hi:[1,0]\n"
"ds_write_b128 v5, v[100:103] offset:11520\n"
C_"v112,v112,v113\n"
C_"v113,v114,v115\n"
C_"v114,v120,v121\n"
C_"v115,v122,v123\n"
"v_pk_mul_f32 v[116:117], v[116:117], v[58:59] op_sel:[0,1] op_sel_hi:[1,1]\n"
"v_pk_mul_f32 v[118:119], v[118:119], v[58:59] op_sel:[0,1] op_sel_hi:[1,1]\n"
"v_pk_mul_f32 v[124:125], v[124:125], v[58:59] op_sel:[0,1] op_sel_hi:[1,1]\n"
"v_pk_mul_f32 v[126:127], v[126:127], v[58:59] op_sel:[0,1] op_sel_hi:[1,1]\n"
"v_add_u32 v5, 0x10000, v5\n"
"ds_write_b128 v5, v[112:115] offset:4096\n"
C_"v116,v116,v117\n"
C_"v117,v118,v119\n"
C_"v118,v124,v125\n"
C_"v119,v126,v127\n"
"ds_write_b128 v5, v[116:119] offset:5120\n"
M_"v[64:67],v[8:11],v[192:195],0\n"
T_"v[32:33],v2 offset:19712\n"
M_"v[68:71],v[8:11],v[196:199],0\n"
W_"lgkmcnt(14)\n"
T_"v[34:35],v2 offset:20736\n"
M_"v[72:75],v[12:15],v[192:195],0\n"
T_"v[36:37],v2 offset:19720\n"
M_"v[76:79],v[12:15],v[196:199],0\n"
T_"v[38:39],v2 offset:20744\n"
M_"v[80:83],v[16:19],v[192:195],0\n"
T_"v[8:9],v3 offset:12544\n"
M_"v[84:87],v[16:19],v[196:199],0\n"
W_"lgkmcnt(13)\n"
T_"v[10:11],v3 offset:13568\n"
M_"v[88:91],v[20:23],v[192:195],0\n"
T_"v[12:13],v3 offset:12552\n"
M_"v[92:95],v[20:23],v[196:199],0\n"
T_"v[14:15],v3 offset:13576\n"
M_"v[96:99],v[24:27],v[192:195],0\n"
T_"v[16:17],v3 offset:12800\n"
M_"v[100:103],v[24:27],v[196:199],0\n"
T_"v[18:19],v3 offset:13824\n"
M_"v[104:107],v[28:31],v[192:195],0\n"
T_"v[20:21],v3 offset:12808\n"
M_"v[108:111],v[28:31],v[196:199],0\n"
W_"lgkmcnt(6)\n"
T_"v[22:23],v3 offset:13832\n"
M_"v[112:115],v[32:35],v[192:195],0\n"
T_"v[24:25],v3 offset:13056\n"
M_"v[116:119],v[32:35],v[196:199],0\n"
T_"v[26:27],v3 offset:14080\n"
M_"v[120:123],v[36:39],v[192:195],0\n"
T_"v[28:29],v3 offset:13064\n"
M_"v[124:127],v[36:39],v[196:199],0\n"
W_"lgkmcnt(6)\n"
T_"v[30:31],v3 offset:14088\n"
M_"v[64:67],v[8:11],v[200:203],v[64:67]\n"
T_"v[32:33],v3 offset:13312\n"
M_"v[68:71],v[8:11],v[204:207],v[68:71]\n"
T_"v[34:35],v3 offset:14336\n"
M_"v[72:75],v[12:15],v[200:203],v[72:75]\n"
T_"v[36:37],v3 offset:13320\n"
M_"v[76:79],v[12:15],v[204:207],v[76:79]\n"
W_"lgkmcnt(6)\n"
T_"v[38:39],v3 offset:14344\n"
M_"v[80:83],v[16:19],v[200:203],v[80:83]\n"
"s_barrier\n"
"s_mov_b64 exec, 1\n"
"buffer_store_dword v6, off, s[80:83], s8 sc1\n"
"s_mov_b64 exec, -1\n"
T_"v[8:9],v1 offset:50688\n"
M_"v[84:87],v[16:19],v[204:207],v[84:87]\n"
T_"v[10:11],v1 offset:51712\n"
M_"v[88:91],v[20:23],v[200:203],v[88:91]\n"
T_"v[12:13],v1 offset:50696\n"
M_"v[92:95],v[20:23],v[204:207],v[92:95]\n"
W_"lgkmcnt(6)\n"
T_"v[14:15],v1 offset:51720\n"
M_"v[96:99],v[24:27],v[200:203],v[96:99]\n"
"PTL14:\n"
"s_cbranch_execz PTR\n"
T_"v[16:17],v1 offset:50944\n"
M_"v[100:103],v[24:27],v[204:207],v[100:103]\n"
T_"v[18:19],v1 offset:51968\n"
M_"v[104:107],v[28:31],v[200:203],v[104:107]\n"
T_"v[20:21],v1 offset:50952\n"
M_"v[108:111],v[28:31],v[204:207],v[108:111]\n"
W_"lgkmcnt(6)\n"
T_"v[22:23],v1 offset:51976\n"
M_"v[112:115],v[32:35],v[200:203],v[112:115]\n"
T_"v[24:25],v1 offset:51200\n"
M_"v[116:119],v[32:35],v[204:207],v[116:119]\n"
T_"v[26:27],v1 offset:52224\n"
M_"v[120:123],v[36:39],v[200:203],v[120:123]\n"
T_"v[28:29],v1 offset:51208\n"
M_"v[124:127],v[36:39],v[204:207],v[124:127]\n"
W_"lgkmcnt(6)\n"
T_"v[30:31],v1 offset:52232\n"
M_"v[64:67],v[8:11],v[208:211],v[64:67]\n"
T_"v[32:33],v1 offset:51456\n"
M_"v[68:71],v[8:11],v[212:215],v[68:71]\n"
T_"v[34:35],v1 offset:52480\n"
M_"v[72:75],v[12:15],v[208:211],v[72:75]\n"
T_"v[36:37],v1 offset:51464\n"
M_"v[76:79],v[12:15],v[212:215],v[76:79]\n"
W_"lgkmcnt(6)\n"
T_"v[38:39],v1 offset:52488\n"
M_"v[80:83],v[16:19],v[208:211],v[80:83]\n"
T_"v[8:9],v2 offset:27392\n"
M_"v[84:87],v[16:19],v[212:215],v[84:87]\n"
T_"v[10:11],v2 offset:28416\n"
M_"v[88:91],v[20:23],v[208:211],v[88:91]\n"
T_"v[12:13],v2 offset:27400\n"
M_"v[92:95],v[20:23],v[212:215],v[92:95]\n"
W_"lgkmcnt(6)\n"
T_"v[14:15],v2 offset:28424\n"
M_"v[96:99],v[24:27],v[208:211],v[96:99]\n"
T_"v[16:17],v2 offset:27648\n"
M_"v[100:103],v[24:27],v[212:215],v[100:103]\n"
T_"v[18:19],v2 offset:28672\n"
M_"v[104:107],v[28:31],v[208:211],v[104:107]\n"
T_"v[20:21],v2 offset:27656\n"
M_"v[108:111],v[28:31],v[212:215],v[108:111]\n"
W_"lgkmcnt(6)\n"
T_"v[22:23],v2 offset:28680\n"
M_"v[112:115],v[32:35],v[208:211],v[112:115]\n"
T_"v[24:25],v2 offset:27904\n"
M_"v[116:119],v[32:35],v[212:215],v[116:119]\n"
T_"v[26:27],v2 offset:28928\n"
M_"v[120:123],v[36:39],v[208:211],v[120:123]\n"
T_"v[28:29],v2 offset:27912\n"
M_"v[124:127],v[36:39],v[212:215],v[124:127]\n"
W_"lgkmcnt(6)\n"
T_"v[30:31],v2 offset:28936\n"
M_"v[64:67],v[8:11],v[216:219],v[64:67]\n"
T_"v[32:33],v2 offset:28160\n"
M_"v[68:71],v[8:11],v[220:223],v[68:71]\n"
T_"v[34:35],v2 offset:29184\n"
M_"v[72:75],v[12:15],v[216:219],v[72:75]\n"
T_"v[36:37],v2 offset:28168\n"
R_"v[192:195],v4 offset:59136\n"
M_"v[76:79],v[12:15],v[220:223],v[76:79]\n"
W_"lgkmcnt(7)\n"
T_"v[38:39],v2 offset:29192\n"
M_"v[80:83],v[16:19],v[216:219],v[80:83]\n"
"s_barrier\n"
T_"v[8:9],v2 offset:44288\n"
M_"v[84:87],v[16:19],v[220:223],v[84:87]\n"
T_"v[10:11],v2 offset:45312\n"
M_"v[88:91],v[20:23],v[216:219],v[88:91]\n"
T_"v[12:13],v2 offset:44296\n"
M_"v[92:95],v[20:23],v[220:223],v[92:95]\n"
W_"lgkmcnt(7)\n"
T_"v[14:15],v2 offset:45320\n"
M_"v[96:99],v[24:27],v[216:219],v[96:99]\n"
T_"v[16:17],v2 offset:44544\n"
R_"v[196:199],v4 offset:60160\n"
M_"v[100:103],v[24:27],v[220:223],v[100:103]\n"
T_"v[18:19],v2 offset:45568\n"
M_"v[104:107],v[28:31],v[216:219],v[104:107]\n"
T_"v[20:21],v2 offset:44552\n"
M_"v[108:111],v[28:31],v[220:223],v[108:111]\n"
W_"lgkmcnt(7)\n"
T_"v[22:23],v2 offset:45576\n"
M_"v[112:115],v[32:35],v[216:219],v[112:115]\n"
T_"v[24:25],v2 offset:44800\n"
M_"v[116:119],v[32:35],v[220:223],v[116:119]\n"
T_"v[26:27],v2 offset:45824\n"
M_"v[120:123],v[36:39],v[216:219],v[120:123]\n"
T_"v[28:29],v2 offset:44808\n"
M_"v[124:127],v[36:39],v[220:223],v[124:127]\n"
W_"lgkmcnt(6)\n"
T_"v[30:31],v2 offset:45832\n"
M_"v[64:67],v[8:11],v[224:227],v[64:67]\n"
T_"v[32:33],v2 offset:45056\n"
M_"v[68:71],v[8:11],v[228:231],v[68:71]\n"
T_"v[34:35],v2 offset:46080\n"
M_"v[72:75],v[12:15],v[224:227],v[72:75]\n"
T_"v[36:37],v2 offset:45064\n"
"v_add_u32 v4, 0x10000, v4\n"
R_"v[200:203],v4 offset:2048\n"
M_"v[76:79],v[12:15],v[228:231],v[76:79]\n"
W_"lgkmcnt(7)\n"
T_"v[38:39],v2 offset:46088\n"
M_"v[80:83],v[16:19],v[224:227],v[80:83]\n"
T_"v[8:9],v3 offset:20992\n"
M_"v[84:87],v[16:19],v[228:231],v[84:87]\n"
T_"v[10:11],v3 offset:22016\n"
M_"v[88:91],v[20:23],v[224:227],v[88:91]\n"
T_"v[12:13],v3 offset:21000\n"
M_"v[92:95],v[20:23],v[228:231],v[92:95]\n"
W_"lgkmcnt(7)\n"
T_"v[14:15],v3 offset:22024\n"
M_"v[96:99],v[24:27],v[224:227],v[96:99]\n"
T_"v[16:17],v3 offset:21248\n"
R_"v[204:207],v4 offset:3072\n"
M_"v[100:103],v[24:27],v[228:231],v[100:103]\n"
T_"v[18:19],v3 offset:22272\n"
M_"v[104:107],v[28:31],v[224:227],v[104:107]\n"
T_"v[20:21],v3 offset:21256\n"
M_"v[108:111],v[28:31],v[228:231],v[108:111]\n"
W_"lgkmcnt(7)\n"
T_"v[22:23],v3 offset:22280\n"
M_"v[112:115],v[32:35],v[224:227],v[112:115]\n"
T_"v[24:25],v3 offset:21504\n"
M_"v[116:119],v[32:35],v[228:231],v[116:119]\n"
T_"v[26:27],v3 offset:22528\n"
M_"v[120:123],v[36:39],v[224:227],v[120:123]\n"
T_"v[28:29],v3 offset:21512\n"
M_"v[124:127],v[36:39],v[228:231],v[124:127]\n"
W_"lgkmcnt(6)\n"
T_"v[30:31],v3 offset:22536\n"
M_"v[64:67],v[8:11],v[232:235],v[64:67]\n"
T_"v[32:33],v3 offset:21760\n"
M_"v[68:71],v[8:11],v[236:239],v[68:71]\n"
T_"v[34:35],v3 offset:22784\n"
M_"v[72:75],v[12:15],v[232:235],v[72:75]\n"
T_"v[36:37],v3 offset:21768\n"
R_"v[208:211],v4 offset:10496\n"
M_"v[76:79],v[12:15],v[236:239],v[76:79]\n"
W_"lgkmcnt(7)\n"
T_"v[38:39],v3 offset:22792\n"
M_"v[80:83],v[16:19],v[232:235],v[80:83]\n"
"s_barrier\n"
T_"v[8:9],v1 offset:16896\n"
M_"v[84:87],v[16:19],v[236:239],v[84:87]\n"
T_"v[10:11],v1 offset:17920\n"
M_"v[88:91],v[20:23],v[232:235],v[88:91]\n"
T_"v[12:13],v1 offset:16904\n"
M_"v[92:95],v[20:23],v[236:239],v[92:95]\n"
W_"lgkmcnt(7)\n"
T_"v[14:15],v1 offset:17928\n"
M_"v[96:99],v[24:27],v[232:235],v[96:99]\n"
T_"v[16:17],v1 offset:17152\n"
R_"v[212:215],v4 offset:11520\n"
M_"v[100:103],v[24:27],v[236:239],v[100:103]\n"
T_"v[18:19],v1 offset:18176\n"
M_"v[104:107],v[28:31],v[232:235],v[104:107]\n"
T_"v[20:21],v1 offset:17160\n"
M_"v[108:111],v[28:31],v[236:239],v[108:111]\n"
W_"lgkmcnt(7)\n"
T_"v[22:23],v1 offset:18184\n"
M_"v[112:115],v[32:35],v[232:235],v[112:115]\n"
T_"v[24:25],v1 offset:17408\n"
M_"v[116:119],v[32:35],v[236:239],v[116:119]\n"
T_"v[26:27],v1 offset:18432\n"
M_"v[120:123],v[36:39],v[232:235],v[120:123]\n"
T_"v[28:29],v1 offset:17416\n"
M_"v[124:127],v[36:39],v[236:239],v[124:127]\n"
W_"lgkmcnt(6)\n"
T_"v[30:31],v1 offset:18440\n"
M_"v[64:67],v[8:11],v[240:243],v[64:67]\n"
T_"v[32:33],v1 offset:17664\n"
M_"v[68:71],v[8:11],v[244:247],v[68:71]\n"
T_"v[34:35],v1 offset:18688\n"
M_"v[72:75],v[12:15],v[240:243],v[72:75]\n"
T_"v[36:37],v1 offset:17672\n"
"v_add_u32 v4, 0x10000, v4\n"
R_"v[216:219],v4 offset:4096\n"
M_"v[76:79],v[12:15],v[244:247],v[76:79]\n"
W_"lgkmcnt(7)\n"
T_"v[38:39],v1 offset:18696\n"
M_"v[80:83],v[16:19],v[240:243],v[80:83]\n"
T_"v[8:9],v2 offset:35840\n"
M_"v[84:87],v[16:19],v[244:247],v[84:87]\n"
T_"v[10:11],v2 offset:36864\n"
M_"v[88:91],v[20:23],v[240:243],v[88:91]\n"
T_"v[12:13],v2 offset:35848\n"
M_"v[92:95],v[20:23],v[244:247],v[92:95]\n"
W_"lgkmcnt(7)\n"
T_"v[14:15],v2 offset:36872\n"
M_"v[96:99],v[24:27],v[240:243],v[96:99]\n"
T_"v[16:17],v2 offset:36096\n"
R_"v[220:223],v4 offset:5120\n"
M_"v[100:103],v[24:27],v[244:247],v[100:103]\n"
T_"v[18:19],v2 offset:37120\n"
M_"v[104:107],v[28:31],v[240:243],v[104:107]\n"
T_"v[20:21],v2 offset:36104\n"
M_"v[108:111],v[28:31],v[244:247],v[108:111]\n"
W_"lgkmcnt(7)\n"
T_"v[22:23],v2 offset:37128\n"
M_"v[112:115],v[32:35],v[240:243],v[112:115]\n"
T_"v[24:25],v2 offset:36352\n"
M_"v[116:119],v[32:35],v[244:247],v[116:119]\n"
T_"v[26:27],v2 offset:37376\n"
M_"v[120:123],v[36:39],v[240:243],v[120:123]\n"
T_"v[28:29],v2 offset:36360\n"
M_"v[124:127],v[36:39],v[244:247],v[124:127]\n"
W_"lgkmcnt(6)\n"
T_"v[30:31],v2 offset:37384\n"
M_"v[64:67],v[8:11],v[248:251],v[64:67]\n"
T_"v[32:33],v2 offset:36608\n"
M_"v[68:71],v[8:11],v[252:255],v[68:71]\n"
T_"v[34:35],v2 offset:37632\n"
M_"v[72:75],v[12:15],v[248:251],v[72:75]\n"
T_"v[36:37],v2 offset:36616\n"
"v_add_u32 v4, 0xfffe0000, v4\n"
R_"v[224:227],v4 offset:63232\n"
M_"v[76:79],v[12:15],v[252:255],v[76:79]\n"
W_"lgkmcnt(7)\n"
T_"v[38:39],v2 offset:37640\n"
M_"v[80:83],v[16:19],v[248:251],v[80:83]\n"
"s_barrier\n"
T_"v[8:9],v1 offset:0\n"
M_"v[84:87],v[16:19],v[252:255],v[84:87]\n"
T_"v[10:11],v1 offset:1024\n"
M_"v[88:91],v[20:23],v[248:251],v[88:91]\n"
T_"v[12:13],v1 offset:8\n"
M_"v[92:95],v[20:23],v[252:255],v[92:95]\n"
W_"lgkmcnt(7)\n"
T_"v[14:15],v1 offset:1032\n"
M_"v[96:99],v[24:27],v[248:251],v[96:99]\n"
T_"v[16:17],v1 offset:256\n"
R_"v[228:231],v4 offset:64256\n"
M_"v[100:103],v[24:27],v[252:255],v[100:103]\n"
T_"v[18:19],v1 offset:1280\n"
M_"v[104:107],v[28:31],v[248:251],v[104:107]\n"
T_"v[20:21],v1 offset:264\n"
M_"v[108:111],v[28:31],v[252:255],v[108:111]\n"
W_"lgkmcnt(7)\n"
T_"v[22:23],v1 offset:1288\n"
M_"v[112:115],v[32:35],v[248:251],v[112:115]\n"
T_"v[24:25],v1 offset:512\n"
M_"v[116:119],v[32:35],v[252:255],v[116:119]\n"
T_"v[26:27],v1 offset:1536\n"
M_"v[120:123],v[36:39],v[248:251],v[120:123]\n"
T_"v[28:29],v1 offset:520\n"
M_"v[124:127],v[36:39],v[252:255],v[124:127]\n"
W_"lgkmcnt(6)\n"
T_"v[30:31],v1 offset:1544\n"
M_"v[64:67],v[8:11],v[128:131],v[64:67]\n"
T_"v[32:33],v1 offset:768\n"
M_"v[68:71],v[8:11],v[132:135],v[68:71]\n"
T_"v[34:35],v1 offset:1792\n"
M_"v[72:75],v[12:15],v[128:131],v[72:75]\n"
T_"v[36:37],v1 offset:776\n"
"v_add_u32 v4, 0x10000, v4\n"
R_"v[232:235],v4 offset:6144\n"
M_"v[76:79],v[12:15],v[132:135],v[76:79]\n"
W_"lgkmcnt(7)\n"
T_"v[38:39],v1 offset:1800\n"
M_"v[80:83],v[16:19],v[128:131],v[80:83]\n"
T_"v[8:9],v2 offset:52736\n"
M_"v[84:87],v[16:19],v[132:135],v[84:87]\n"
T_"v[10:11],v2 offset:53760\n"
M_"v[88:91],v[20:23],v[128:131],v[88:91]\n"
T_"v[12:13],v2 offset:52744\n"
M_"v[92:95],v[20:23],v[132:135],v[92:95]\n"
W_"lgkmcnt(7)\n"
T_"v[14:15],v2 offset:53768\n"
M_"v[96:99],v[24:27],v[128:131],v[96:99]\n"
T_"v[16:17],v2 offset:52992\n"
R_"v[236:239],v4 offset:7168\n"
M_"v[100:103],v[24:27],v[132:135],v[100:103]\n"
T_"v[18:19],v2 offset:54016\n"
M_"v[104:107],v[28:31],v[128:131],v[104:107]\n"
T_"v[20:21],v2 offset:53000\n"
M_"v[108:111],v[28:31],v[132:135],v[108:111]\n"
W_"lgkmcnt(7)\n"
T_"v[22:23],v2 offset:54024\n"
M_"v[112:115],v[32:35],v[128:131],v[112:115]\n"
T_"v[24:25],v2 offset:53248\n"
M_"v[116:119],v[32:35],v[132:135],v[116:119]\n"
T_"v[26:27],v2 offset:54272\n"
M_"v[120:123],v[36:39],v[128:131],v[120:123]\n"
T_"v[28:29],v2 offset:53256\n"
M_"v[124:127],v[36:39],v[132:135],v[124:127]\n"
W_"lgkmcnt(6)\n"
T_"v[30:31],v2 offset:54280\n"
M_"v[64:67],v[8:11],v[136:139],v[64:67]\n"
T_"v[32:33],v2 offset:53504\n"
M_"v[68:71],v[8:11],v[140:143],v[68:71]\n"
T_"v[34:35],v2 offset:54528\n"
M_"v[72:75],v[12:15],v[136:139],v[72:75]\n"
T_"v[36:37],v2 offset:53512\n"
R_"v[240:243],v4 offset:14592\n"
M_"v[76:79],v[12:15],v[140:143],v[76:79]\n"
W_"lgkmcnt(7)\n"
T_"v[38:39],v2 offset:54536\n"
M_"v[80:83],v[16:19],v[136:139],v[80:83]\n"
"s_barrier\n"
T_"v[8:9],v1 offset:33792\n"
M_"v[84:87],v[16:19],v[140:143],v[84:87]\n"
T_"v[10:11],v1 offset:34816\n"
M_"v[88:91],v[20:23],v[136:139],v[88:91]\n"
T_"v[12:13],v1 offset:33800\n"
M_"v[92:95],v[20:23],v[140:143],v[92:95]\n"
W_"lgkmcnt(7)\n"
T_"v[14:15],v1 offset:34824\n"
M_"v[96:99],v[24:27],v[136:139],v[96:99]\n"
T_"v[16:17],v1 offset:34048\n"
R_"v[244:247],v4 offset:15616\n"
M_"v[100:103],v[24:27],v[140:143],v[100:103]\n"
T_"v[18:19],v1 offset:35072\n"
M_"v[104:107],v[28:31],v[136:139],v[104:107]\n"
T_"v[20:21],v1 offset:34056\n"
M_"v[108:111],v[28:31],v[140:143],v[108:111]\n"
W_"lgkmcnt(7)\n"
T_"v[22:23],v1 offset:35080\n"
M_"v[112:115],v[32:35],v[136:139],v[112:115]\n"
T_"v[24:25],v1 offset:34304\n"
M_"v[116:119],v[32:35],v[140:143],v[116:119]\n"
T_"v[26:27],v1 offset:35328\n"
M_"v[120:123],v[36:39],v[136:139],v[120:123]\n"
T_"v[28:29],v1 offset:34312\n"
M_"v[124:127],v[36:39],v[140:143],v[124:127]\n"
W_"lgkmcnt(6)\n"
T_"v[30:31],v1 offset:35336\n"
M_"v[64:67],v[8:11],v[144:147],v[64:67]\n"
T_"v[32:33],v1 offset:34560\n"
M_"v[68:71],v[8:11],v[148:151],v[68:71]\n"
T_"v[34:35],v1 offset:35584\n"
M_"v[72:75],v[12:15],v[144:147],v[72:75]\n"
T_"v[36:37],v1 offset:34568\n"
"v_add_u32 v4, 0x10000, v4\n"
R_"v[248:251],v4 offset:8192\n"
M_"v[76:79],v[12:15],v[148:151],v[76:79]\n"
W_"lgkmcnt(7)\n"
T_"v[38:39],v1 offset:35592\n"
M_"v[80:83],v[16:19],v[144:147],v[80:83]\n"
T_"v[8:9],v2 offset:61184\n"
M_"v[84:87],v[16:19],v[148:151],v[84:87]\n"
T_"v[10:11],v2 offset:62208\n"
M_"v[88:91],v[20:23],v[144:147],v[88:91]\n"
T_"v[12:13],v2 offset:61192\n"
M_"v[92:95],v[20:23],v[148:151],v[92:95]\n"
W_"lgkmcnt(7)\n"
T_"v[14:15],v2 offset:62216\n"
M_"v[96:99],v[24:27],v[144:147],v[96:99]\n"
T_"v[16:17],v2 offset:61440\n"
R_"v[252:255],v4 offset:9216\n"
M_"v[100:103],v[24:27],v[148:151],v[100:103]\n"
T_"v[18:19],v2 offset:62464\n"
M_"v[104:107],v[28:31],v[144:147],v[104:107]\n"
T_"v[20:21],v2 offset:61448\n"
M_"v[108:111],v[28:31],v[148:151],v[108:111]\n"
W_"lgkmcnt(7)\n"
T_"v[22:23],v2 offset:62472\n"
M_"v[112:115],v[32:35],v[144:147],v[112:115]\n"
T_"v[24:25],v2 offset:61696\n"
M_"v[116:119],v[32:35],v[148:151],v[116:119]\n"
T_"v[26:27],v2 offset:62720\n"
M_"v[120:123],v[36:39],v[144:147],v[120:123]\n"
T_"v[28:29],v2 offset:61704\n"
M_"v[124:127],v[36:39],v[148:151],v[124:127]\n"
W_"lgkmcnt(6)\n"
T_"v[30:31],v2 offset:62728\n"
M_"v[64:67],v[8:11],v[152:155],v[64:67]\n"
T_"v[32:33],v2 offset:61952\n"
M_"v[68:71],v[8:11],v[156:159],v[68:71]\n"
T_"v[34:35],v2 offset:62976\n"
M_"v[72:75],v[12:15],v[152:155],v[72:75]\n"
T_"v[36:37],v2 offset:61960\n"
M_"v[76:79],v[12:15],v[156:159],v[76:79]\n"
W_"lgkmcnt(6)\n"
T_"v[38:39],v2 offset:62984\n"
M_"v[80:83],v[16:19],v[152:155],v[80:83]\n"
"s_barrier\n"
T_"v[8:9],v1 offset:8448\n"
M_"v[84:87],v[16:19],v[156:159],v[84:87]\n"
T_"v[10:11],v1 offset:9472\n"
M_"v[88:91],v[20:23],v[152:155],v[88:91]\n"
T_"v[12:13],v1 offset:8456\n"
M_"v[92:95],v[20:23],v[156:159],v[92:95]\n"
W_"lgkmcnt(6)\n"
T_"v[14:15],v1 offset:9480\n"
M_"v[96:99],v[24:27],v[152:155],v[96:99]\n"
T_"v[16:17],v1 offset:8704\n"
M_"v[100:103],v[24:27],v[156:159],v[100:103]\n"
T_"v[18:19],v1 offset:9728\n"
M_"v[104:107],v[28:31],v[152:155],v[104:107]\n"
T_"v[20:21],v1 offset:8712\n"
M_"v[108:111],v[28:31],v[156:159],v[108:111]\n"
W_"lgkmcnt(6)\n"
T_"v[22:23],v1 offset:9736\n"
M_"v[112:115],v[32:35],v[152:155],v[112:115]\n"
T_"v[24:25],v1 offset:8960\n"
M_"v[116:119],v[32:35],v[156:159],v[116:119]\n"
T_"v[26:27],v1 offset:9984\n"
M_"v[120:123],v[36:39],v[152:155],v[120:123]\n"
T_"v[28:29],v1 offset:8968\n"
M_"v[124:127],v[36:39],v[156:159],v[124:127]\n"
W_"lgkmcnt(6)\n"
T_"v[30:31],v1 offset:9992\n"
M_"v[64:67],v[8:11],v[160:163],v[64:67]\n"
T_"v[32:33],v1 offset:9216\n"
M_"v[68:71],v[8:11],v[164:167],v[68:71]\n"
T_"v[34:35],v1 offset:10240\n"
M_"v[72:75],v[12:15],v[160:163],v[72:75]\n"
T_"v[36:37],v1 offset:9224\n"
M_"v[76:79],v[12:15],v[164:167],v[76:79]\n"
W_"lgkmcnt(6)\n"
T_"v[38:39],v1 offset:10248\n"
M_"v[80:83],v[16:19],v[160:163],v[80:83]\n"
T_"v[8:9],v1 offset:25344\n"
M_"v[84:87],v[16:19],v[164:167],v[84:87]\n"
T_"v[10:11],v1 offset:26368\n"
M_"v[88:91],v[20:23],v[160:163],v[88:91]\n"
T_"v[12:13],v1 offset:25352\n"
M_"v[92:95],v[20:23],v[164:167],v[92:95]\n"
W_"lgkmcnt(6)\n"
T_"v[14:15],v1 offset:26376\n"
M_"v[96:99],v[24:27],v[160:163],v[96:99]\n"
T_"v[16:17],v1 offset:25600\n"
M_"v[100:103],v[24:27],v[164:167],v[100:103]\n"
T_"v[18:19],v1 offset:26624\n"
M_"v[104:107],v[28:31],v[160:163],v[104:107]\n"
T_"v[20:21],v1 offset:25608\n"
M_"v[108:111],v[28:31],v[164:167],v[108:111]\n"
W_"lgkmcnt(6)\n"
T_"v[22:23],v1 offset:26632\n"
M_"v[112:115],v[32:35],v[160:163],v[112:115]\n"
T_"v[24:25],v1 offset:25856\n"
M_"v[116:119],v[32:35],v[164:167],v[116:119]\n"
T_"v[26:27],v1 offset:26880\n"
M_"v[120:123],v[36:39],v[160:163],v[120:123]\n"
T_"v[28:29],v1 offset:25864\n"
M_"v[124:127],v[36:39],v[164:167],v[124:127]\n"
W_"lgkmcnt(6)\n"
T_"v[30:31],v1 offset:26888\n"
M_"v[64:67],v[8:11],v[168:171],v[64:67]\n"
T_"v[32:33],v1 offset:26112\n"
M_"v[68:71],v[8:11],v[172:175],v[68:71]\n"
T_"v[34:35],v1 offset:27136\n"
M_"v[72:75],v[12:15],v[168:171],v[72:75]\n"
T_"v[36:37],v1 offset:26120\n"
M_"v[76:79],v[12:15],v[172:175],v[76:79]\n"
W_"lgkmcnt(6)\n"
T_"v[38:39],v1 offset:27144\n"
M_"v[80:83],v[16:19],v[168:171],v[80:83]\n"
"s_barrier\n"
T_"v[8:9],v1 offset:42240\n"
M_"v[84:87],v[16:19],v[172:175],v[84:87]\n"
T_"v[10:11],v1 offset:43264\n"
M_"v[88:91],v[20:23],v[168:171],v[88:91]\n"
T_"v[12:13],v1 offset:42248\n"
M_"v[92:95],v[20:23],v[172:175],v[92:95]\n"
W_"lgkmcnt(6)\n"
T_"v[14:15],v1 offset:43272\n"
M_"v[96:99],v[24:27],v[168:171],v[96:99]\n"
T_"v[16:17],v1 offset:42496\n"
M_"v[100:103],v[24:27],v[172:175],v[100:103]\n"
T_"v[18:19],v1 offset:43520\n"
M_"v[104:107],v[28:31],v[168:171],v[104:107]\n"
T_"v[20:21],v1 offset:42504\n"
M_"v[108:111],v[28:31],v[172:175],v[108:111]\n"
W_"lgkmcnt(6)\n"
T_"v[22:23],v1 offset:43528\n"
M_"v[112:115],v[32:35],v[168:171],v[112:115]\n"
T_"v[24:25],v1 offset:42752\n"
M_"v[116:119],v[32:35],v[172:175],v[116:119]\n"
T_"v[26:27],v1 offset:43776\n"
M_"v[120:123],v[36:39],v[168:171],v[120:123]\n"
T_"v[28:29],v1 offset:42760\n"
M_"v[124:127],v[36:39],v[172:175],v[124:127]\n"
W_"lgkmcnt(6)\n"
T_"v[30:31],v1 offset:43784\n"
M_"v[64:67],v[8:11],v[176:179],v[64:67]\n"
T_"v[32:33],v1 offset:43008\n"
M_"v[68:71],v[8:11],v[180:183],v[68:71]\n"
T_"v[34:35],v1 offset:44032\n"
M_"v[72:75],v[12:15],v[176:179],v[72:75]\n"
T_"v[36:37],v1 offset:43016\n"
M_"v[76:79],v[12:15],v[180:183],v[76:79]\n"
W_"lgkmcnt(6)\n"
T_"v[38:39],v1 offset:44040\n"
M_"v[80:83],v[16:19],v[176:179],v[80:83]\n"
T_"v[8:9],v2 offset:18944\n"
M_"v[84:87],v[16:19],v[180:183],v[84:87]\n"
T_"v[10:11],v2 offset:19968\n"
M_"v[88:91],v[20:23],v[176:179],v[88:91]\n"
T_"v[12:13],v2 offset:18952\n"
M_"v[92:95],v[20:23],v[180:183],v[92:95]\n"
W_"lgkmcnt(6)\n"
T_"v[14:15],v2 offset:19976\n"
M_"v[96:99],v[24:27],v[176:179],v[96:99]\n"
T_"v[16:17],v2 offset:19200\n"
M_"v[100:103],v[24:27],v[180:183],v[100:103]\n"
T_"v[18:19],v2 offset:20224\n"
M_"v[104:107],v[28:31],v[176:179],v[104:107]\n"
T_"v[20:21],v2 offset:19208\n"
M_"v[108:111],v[28:31],v[180:183],v[108:111]\n"
W_"lgkmcnt(6)\n"
T_"v[22:23],v2 offset:20232\n"
M_"v[112:115],v[32:35],v[176:179],v[112:115]\n"
T_"v[24:25],v2 offset:19456\n"
M_"v[116:119],v[32:35],v[180:183],v[116:119]\n"
T_"v[26:27],v2 offset:20480\n"
M_"v[120:123],v[36:39],v[176:179],v[120:123]\n"
T_"v[28:29],v2 offset:19464\n"
M_"v[124:127],v[36:39],v[180:183],v[124:127]\n"
W_"lgkmcnt(6)\n"
T_"v[30:31],v2 offset:20488\n"
M_"v[64:67],v[8:11],v[184:187],v[64:67]\n"
T_"v[32:33],v2 offset:19712\n"
M_"v[68:71],v[8:11],v[188:191],v[68:71]\n"
T_"v[34:35],v2 offset:20736\n"
M_"v[72:75],v[12:15],v[184:187],v[72:75]\n"
T_"v[36:37],v2 offset:19720\n"
M_"v[76:79],v[12:15],v[188:191],v[76:79]\n"
W_"lgkmcnt(6)\n"
T_"v[38:39],v2 offset:20744\n"
M_"v[80:83],v[16:19],v[184:187],v[80:83]\n"
"s_barrier\n"
"s_lshl_b32 s4, s35, 9\n"
"v_add_u32 v1, s4, v0\n"
"v_add_u32 v2, 0x10000, v1\n"
"v_add_u32 v3, 0x20000, v1\n"
T_"v[8:9],v1 offset:50688\n"
M_"v[84:87],v[16:19],v[188:191],v[84:87]\n"
T_"v[10:11],v1 offset:50944\n"
M_"v[88:91],v[20:23],v[184:187],v[88:91]\n"
T_"v[12:13],v1 offset:50696\n"
M_"v[92:95],v[20:23],v[188:191],v[92:95]\n"
W_"lgkmcnt(6)\n"
T_"v[14:15],v1 offset:50952\n"
M_"v[96:99],v[24:27],v[184:187],v[96:99]\n"
T_"v[16:17],v1 offset:51712\n"
M_"v[100:103],v[24:27],v[188:191],v[100:103]\n"
T_"v[18:19],v1 offset:51968\n"
M_"v[104:107],v[28:31],v[184:187],v[104:107]\n"
T_"v[20:21],v1 offset:51720\n"
M_"v[108:111],v[28:31],v[188:191],v[108:111]\n"
W_"lgkmcnt(6)\n"
T_"v[22:23],v1 offset:51976\n"
M_"v[112:115],v[32:35],v[184:187],v[112:115]\n"
"PTL15:\n"
"s_cbranch_execz PTR\n"
T_"v[24:25],v3 offset:12544\n"
M_"v[116:119],v[32:35],v[188:191],v[116:119]\n"
T_"v[26:27],v3 offset:12800\n"
M_"v[120:123],v[36:39],v[184:187],v[120:123]\n"
T_"v[28:29],v3 offset:12552\n"
T_"v[30:31],v3 offset:12808\n"
M_"v[124:127],v[36:39],v[188:191],v[124:127]\n"
W_"lgkmcnt(7)\n"
"v_pk_mul_f32 v[64:65], v[64:65], v[58:59] op_sel_hi:[1,0]\n"
"v_pk_mul_f32 v[66:67], v[66:67], v[58:59] op_sel_hi:[1,0]\n"
"v_pk_mul_f32 v[72:73], v[72:73], v[58:59] op_sel_hi:[1,0]\n"
"v_pk_mul_f32 v[74:75], v[74:75], v[58:59] op_sel_hi:[1,0]\n"
C_"v64,v64,v65\n"
C_"v65,v66,v67\n"
C_"v66,v72,v73\n"
C_"v67,v74,v75\n"
"v_pk_mul_f32 v[68:69], v[68:69], v[58:59] op_sel:[0,1] op_sel_hi:[1,1]\n"
"v_pk_mul_f32 v[70:71], v[70:71], v[58:59] op_sel:[0,1] op_sel_hi:[1,1]\n"
"v_pk_mul_f32 v[76:77], v[76:77], v[58:59] op_sel:[0,1] op_sel_hi:[1,1]\n"
"v_pk_mul_f32 v[78:79], v[78:79], v[58:59] op_sel:[0,1] op_sel_hi:[1,1]\n"
"v_add_u32 v5, 0xfffe0000, v5\n"
"ds_write_b128 v5, v[64:67] offset:8448\n"
C_"v68,v68,v69\n"
C_"v69,v70,v71\n"
C_"v70,v76,v77\n"
C_"v71,v78,v79\n"
"v_pk_mul_f32 v[80:81], v[80:81], v[58:59] op_sel_hi:[1,0]\n"
"v_pk_mul_f32 v[82:83], v[82:83], v[58:59] op_sel_hi:[1,0]\n"
"v_pk_mul_f32 v[88:89], v[88:89], v[58:59] op_sel_hi:[1,0]\n"
"v_pk_mul_f32 v[90:91], v[90:91], v[58:59] op_sel_hi:[1,0]\n"
"ds_write_b128 v5, v[68:71] offset:9472\n"
C_"v80,v80,v81\n"
C_"v81,v82,v83\n"
C_"v82,v88,v89\n"
C_"v83,v90,v91\n"
"v_pk_mul_f32 v[84:85], v[84:85], v[58:59] op_sel:[0,1] op_sel_hi:[1,1]\n"
"v_pk_mul_f32 v[86:87], v[86:87], v[58:59] op_sel:[0,1] op_sel_hi:[1,1]\n"
"v_pk_mul_f32 v[92:93], v[92:93], v[58:59] op_sel:[0,1] op_sel_hi:[1,1]\n"
"v_pk_mul_f32 v[94:95], v[94:95], v[58:59] op_sel:[0,1] op_sel_hi:[1,1]\n"
"ds_write_b128 v5, v[80:83] offset:25344\n"
C_"v84,v84,v85\n"
C_"v85,v86,v87\n"
C_"v86,v92,v93\n"
C_"v87,v94,v95\n"
"v_pk_mul_f32 v[96:97], v[96:97], v[58:59] op_sel_hi:[1,0]\n"
"v_pk_mul_f32 v[98:99], v[98:99], v[58:59] op_sel_hi:[1,0]\n"
"v_pk_mul_f32 v[104:105], v[104:105], v[58:59] op_sel_hi:[1,0]\n"
"v_pk_mul_f32 v[106:107], v[106:107], v[58:59] op_sel_hi:[1,0]\n"
"ds_write_b128 v5, v[84:87] offset:26368\n"
C_"v96,v96,v97\n"
C_"v97,v98,v99\n"
C_"v98,v104,v105\n"
C_"v99,v106,v107\n"
"v_pk_mul_f32 v[100:101], v[100:101], v[58:59] op_sel:[0,1] op_sel_hi:[1,1]\n"
"v_pk_mul_f32 v[102:103], v[102:103], v[58:59] op_sel:[0,1] op_sel_hi:[1,1]\n"
"v_pk_mul_f32 v[108:109], v[108:109], v[58:59] op_sel:[0,1] op_sel_hi:[1,1]\n"
"v_pk_mul_f32 v[110:111], v[110:111], v[58:59] op_sel:[0,1] op_sel_hi:[1,1]\n"
"ds_write_b128 v5, v[96:99] offset:42240\n"
C_"v100,v100,v101\n"
C_"v101,v102,v103\n"
C_"v102,v108,v109\n"
C_"v103,v110,v111\n"
"v_pk_mul_f32 v[112:113], v[112:113], v[58:59] op_sel_hi:[1,0]\n"
"v_pk_mul_f32 v[114:115], v[114:115], v[58:59] op_sel_hi:[1,0]\n"
"v_pk_mul_f32 v[120:121], v[120:121], v[58:59] op_sel_hi:[1,0]\n"
"v_pk_mul_f32 v[122:123], v[122:123], v[58:59] op_sel_hi:[1,0]\n"
"ds_write_b128 v5, v[100:103] offset:43264\n"
C_"v112,v112,v113\n"
C_"v113,v114,v115\n"
C_"v114,v120,v121\n"
C_"v115,v122,v123\n"
"v_pk_mul_f32 v[116:117], v[116:117], v[58:59] op_sel:[0,1] op_sel_hi:[1,1]\n"
"v_pk_mul_f32 v[118:119], v[118:119], v[58:59] op_sel:[0,1] op_sel_hi:[1,1]\n"
"v_pk_mul_f32 v[124:125], v[124:125], v[58:59] op_sel:[0,1] op_sel_hi:[1,1]\n"
"v_pk_mul_f32 v[126:127], v[126:127], v[58:59] op_sel:[0,1] op_sel_hi:[1,1]\n"
"v_add_u32 v5, 0x10000, v5\n"
"ds_write_b128 v5, v[112:115] offset:61184\n"
C_"v116,v116,v117\n"
C_"v117,v118,v119\n"
C_"v118,v124,v125\n"
C_"v119,v126,v127\n"
"ds_write_b128 v5, v[116:119] offset:62208\n"
M_"v[40:43],v[8:11],v[192:195],0\n"
T_"v[32:33],v3 offset:13568\n"
M_"v[44:47],v[8:11],v[196:199],0\n"
T_"v[34:35],v3 offset:13824\n"
M_"v[48:51],v[12:15],v[192:195],0\n"
T_"v[36:37],v3 offset:13576\n"
M_"v[52:55],v[12:15],v[196:199],0\n"
W_"lgkmcnt(14)\n"
T_"v[38:39],v3 offset:13832\n"
M_"v[40:43],v[16:19],v[200:203],v[40:43]\n"
T_"v[8:9],v2 offset:27392\n"
M_"v[44:47],v[16:19],v[204:207],v[44:47]\n"
T_"v[10:11],v2 offset:27648\n"
M_"v[48:51],v[20:23],v[200:203],v[48:51]\n"
T_"v[12:13],v2 offset:27400\n"
M_"v[52:55],v[20:23],v[204:207],v[52:55]\n"
W_"lgkmcnt(6)\n"
T_"v[14:15],v2 offset:27656\n"
M_"v[40:43],v[24:27],v[208:211],v[40:43]\n"
T_"v[16:17],v2 offset:28416\n"
M_"v[44:47],v[24:27],v[212:215],v[44:47]\n"
T_"v[18:19],v2 offset:28672\n"
M_"v[48:51],v[28:31],v[208:211],v[48:51]\n"
T_"v[20:21],v2 offset:28424\n"
M_"v[52:55],v[28:31],v[212:215],v[52:55]\n"
W_"lgkmcnt(6)\n"
T_"v[22:23],v2 offset:28680\n"
M_"v[40:43],v[32:35],v[216:219],v[40:43]\n"
T_"v[24:25],v2 offset:44288\n"
M_"v[44:47],v[32:35],v[220:223],v[44:47]\n"
T_"v[26:27],v2 offset:44544\n"
M_"v[48:51],v[36:39],v[216:219],v[48:51]\n"
T_"v[28:29],v2 offset:44296\n"
M_"v[52:55],v[36:39],v[220:223],v[52:55]\n"
W_"lgkmcnt(6)\n"
T_"v[30:31],v2 offset:44552\n"
M_"v[40:43],v[8:11],v[224:227],v[40:43]\n"
T_"v[32:33],v2 offset:45312\n"
M_"v[44:47],v[8:11],v[228:231],v[44:47]\n"
T_"v[34:35],v2 offset:45568\n"
M_"v[48:51],v[12:15],v[224:227],v[48:51]\n"
T_"v[36:37],v2 offset:45320\n"
M_"v[52:55],v[12:15],v[228:231],v[52:55]\n"
W_"lgkmcnt(6)\n"
T_"v[38:39],v2 offset:45576\n"
M_"v[40:43],v[16:19],v[232:235],v[40:43]\n"
"s_barrier\n"
"v_add_u32 v4, 0xfffe0000, v4\n"
R_"v[128:131],v4 offset:8448\n"
R_"v[132:135],v4 offset:9472\n"
M_"v[44:47],v[16:19],v[236:239],v[44:47]\n"
T_"v[8:9],v1 offset:16896\n"
T_"v[10:11],v1 offset:17152\n"
M_"v[48:51],v[20:23],v[232:235],v[48:51]\n"
T_"v[12:13],v1 offset:16904\n"
M_"v[52:55],v[20:23],v[236:239],v[52:55]\n"
W_"lgkmcnt(8)\n"
T_"v[14:15],v1 offset:17160\n"
M_"v[40:43],v[24:27],v[240:243],v[40:43]\n"
R_"v[136:139],v4 offset:25344\n"
R_"v[140:143],v4 offset:26368\n"
M_"v[44:47],v[24:27],v[244:247],v[44:47]\n"
T_"v[16:17],v1 offset:17920\n"
T_"v[18:19],v1 offset:18176\n"
M_"v[48:51],v[28:31],v[240:243],v[48:51]\n"
T_"v[20:21],v1 offset:17928\n"
M_"v[52:55],v[28:31],v[244:247],v[52:55]\n"
W_"lgkmcnt(9)\n"
T_"v[22:23],v1 offset:18184\n"
M_"v[40:43],v[32:35],v[248:251],v[40:43]\n"
R_"v[144:147],v4 offset:42240\n"
R_"v[148:151],v4 offset:43264\n"
M_"v[44:47],v[32:35],v[252:255],v[44:47]\n"
T_"v[24:25],v3 offset:20992\n"
T_"v[26:27],v3 offset:21248\n"
M_"v[48:51],v[36:39],v[248:251],v[48:51]\n"
T_"v[28:29],v3 offset:21000\n"
M_"v[52:55],v[36:39],v[252:255],v[52:55]\n"
W_"lgkmcnt(9)\n"
T_"v[30:31],v3 offset:21256\n"
M_"v[40:43],v[8:11],v[128:131],v[40:43]\n"
"v_add_u32 v4, 0x10000, v4\n"
R_"v[152:155],v4 offset:61184\n"
R_"v[156:159],v4 offset:62208\n"
M_"v[44:47],v[8:11],v[132:135],v[44:47]\n"
T_"v[32:33],v3 offset:22016\n"
T_"v[34:35],v3 offset:22272\n"
M_"v[48:51],v[12:15],v[128:131],v[48:51]\n"
T_"v[36:37],v3 offset:22024\n"
M_"v[52:55],v[12:15],v[132:135],v[52:55]\n"
W_"lgkmcnt(9)\n"
T_"v[38:39],v3 offset:22280\n"
M_"v[40:43],v[16:19],v[136:139],v[40:43]\n"
"v_add_u32 v4, 0xffff0000, v4\n"
R_"v[160:163],v4 offset:12544\n"
R_"v[164:167],v4 offset:13568\n"
M_"v[44:47],v[16:19],v[140:143],v[44:47]\n"
T_"v[8:9],v1 offset:0\n"
T_"v[10:11],v1 offset:256\n"
M_"v[48:51],v[20:23],v[136:139],v[48:51]\n"
T_"v[12:13],v1 offset:8\n"
M_"v[52:55],v[20:23],v[140:143],v[52:55]\n"
W_"lgkmcnt(9)\n"
T_"v[14:15],v1 offset:264\n"
M_"v[40:43],v[24:27],v[144:147],v[40:43]\n"
R_"v[168:171],v4 offset:29440\n"
R_"v[172:175],v4 offset:30464\n"
M_"v[44:47],v[24:27],v[148:151],v[44:47]\n"
T_"v[16:17],v1 offset:1024\n"
T_"v[18:19],v1 offset:1280\n"
M_"v[48:51],v[28:31],v[144:147],v[48:51]\n"
T_"v[20:21],v1 offset:1032\n"
M_"v[52:55],v[28:31],v[148:151],v[52:55]\n"
W_"lgkmcnt(9)\n"
T_"v[22:23],v1 offset:1288\n"
M_"v[40:43],v[32:35],v[152:155],v[40:43]\n"
R_"v[176:179],v4 offset:46336\n"
R_"v[180:183],v4 offset:47360\n"
M_"v[44:47],v[32:35],v[156:159],v[44:47]\n"
T_"v[24:25],v1 offset:59136\n"
T_"v[26:27],v1 offset:59392\n"
M_"v[48:51],v[36:39],v[152:155],v[48:51]\n"
T_"v[28:29],v1 offset:59144\n"
M_"v[52:55],v[36:39],v[156:159],v[52:55]\n"
W_"lgkmcnt(9)\n"
T_"v[30:31],v1 offset:59400\n"
M_"v[40:43],v[8:11],v[160:163],v[40:43]\n"
"v_add_u32 v4, 0x10000, v4\n"
R_"v[184:187],v4 offset:65280\n"
"v_add_u32 v4, 0x10000, v4\n"
R_"v[188:191],v4 offset:768\n"
M_"v[44:47],v[8:11],v[164:167],v[44:47]\n"
T_"v[32:33],v1 offset:60160\n"
T_"v[34:35],v1 offset:60416\n"
M_"v[48:51],v[12:15],v[160:163],v[48:51]\n"
T_"v[36:37],v1 offset:60168\n"
M_"v[52:55],v[12:15],v[164:167],v[52:55]\n"
W_"lgkmcnt(9)\n"
T_"v[38:39],v1 offset:60424\n"
M_"v[40:43],v[16:19],v[168:171],v[40:43]\n"
"s_barrier\n"
T_"v[8:9],v2 offset:2048\n"
M_"v[44:47],v[16:19],v[172:175],v[44:47]\n"
T_"v[10:11],v2 offset:2304\n"
M_"v[48:51],v[20:23],v[168:171],v[48:51]\n"
T_"v[12:13],v2 offset:2056\n"
M_"v[52:55],v[20:23],v[172:175],v[52:55]\n"
W_"lgkmcnt(7)\n"
T_"v[14:15],v2 offset:2312\n"
M_"v[40:43],v[24:27],v[176:179],v[40:43]\n"
T_"v[16:17],v2 offset:3072\n"
M_"v[44:47],v[24:27],v[180:183],v[44:47]\n"
T_"v[18:19],v2 offset:3328\n"
M_"v[48:51],v[28:31],v[176:179],v[48:51]\n"
T_"v[20:21],v2 offset:3080\n"
M_"v[52:55],v[28:31],v[180:183],v[52:55]\n"
W_"lgkmcnt(6)\n"
T_"v[22:23],v2 offset:3336\n"
M_"v[40:43],v[32:35],v[184:187],v[40:43]\n"
T_"v[24:25],v2 offset:35840\n"
M_"v[44:47],v[32:35],v[188:191],v[44:47]\n"
T_"v[26:27],v2 offset:36096\n"
M_"v[48:51],v[36:39],v[184:187],v[48:51]\n"
T_"v[28:29],v2 offset:35848\n"
M_"v[52:55],v[36:39],v[188:191],v[52:55]\n"
W_"lgkmcnt(6)\n"
T_"v[30:31],v2 offset:36104\n"
M_"v[64:67],v[8:11],v[192:195],0\n"
T_"v[32:33],v2 offset:36864\n"
M_"v[68:71],v[8:11],v[196:199],0\n"
T_"v[34:35],v2 offset:37120\n"
M_"v[72:75],v[12:15],v[192:195],0\n"
T_"v[36:37],v2 offset:36872\n"
M_"v[76:79],v[12:15],v[196:199],0\n"
W_"lgkmcnt(6)\n"
T_"v[38:39],v2 offset:37128\n"
M_"v[64:67],v[16:19],v[200:203],v[64:67]\n"
T_"v[8:9],v1 offset:33792\n"
C_"v80,v40,v41\n"
M_"v[68:71],v[16:19],v[204:207],v[68:71]\n"
T_"v[10:11],v1 offset:34048\n"
C_"v81,v42,v43\n"
M_"v[72:75],v[20:23],v[200:203],v[72:75]\n"
T_"v[12:13],v1 offset:33800\n"
C_"v82,v48,v49\n"
M_"v[76:79],v[20:23],v[204:207],v[76:79]\n"
W_"lgkmcnt(6)\n"
T_"v[14:15],v1 offset:34056\n"
C_"v83,v50,v51\n"
M_"v[64:67],v[24:27],v[208:211],v[64:67]\n"
T_"v[16:17],v1 offset:34816\n"
T_"v[18:19],v1 offset:35072\n"
M_"v[68:71],v[24:27],v[212:215],v[68:71]\n"
"s_add_u32 s87, s90, 57344\n"
"buffer_store_dwordx4 v[80:83], v7, s[60:63], s87 offen sc1\n"
M_"v[72:75],v[28:31],v[208:211],v[72:75]\n"
T_"v[20:21],v1 offset:34824\n"
M_"v[76:79],v[28:31],v[212:215],v[76:79]\n"
W_"lgkmcnt(6)\n"
T_"v[22:23],v1 offset:35080\n"
M_"v[64:67],v[32:35],v[216:219],v[64:67]\n"
T_"v[24:25],v2 offset:10496\n"
M_"v[68:71],v[32:35],v[220:223],v[68:71]\n"
T_"v[26:27],v2 offset:10752\n"
M_"v[72:75],v[36:39],v[216:219],v[72:75]\n"
T_"v[28:29],v2 offset:10504\n"
M_"v[76:79],v[36:39],v[220:223],v[76:79]\n"
W_"lgkmcnt(5)\n"
T_"v[30:31],v2 offset:10760\n"
M_"v[64:67],v[8:11],v[224:227],v[64:67]\n"
T_"v[32:33],v2 offset:11520\n"
M_"v[68:71],v[8:11],v[228:231],v[68:71]\n"
T_"v[34:35],v2 offset:11776\n"
M_"v[72:75],v[12:15],v[224:227],v[72:75]\n"
T_"v[36:37],v2 offset:11528\n"
M_"v[76:79],v[12:15],v[228:231],v[76:79]\n"
T_"v[38:39],v2 offset:11784\n"
M_"v[64:67],v[16:19],v[232:235],v[64:67]\n"
"s_barrier\n"
T_"v[8:9],v2 offset:52736\n"
M_"v[68:71],v[16:19],v[236:239],v[68:71]\n"
W_"lgkmcnt(6)\n"
T_"v[10:11],v2 offset:52992\n"
M_"v[72:75],v[20:23],v[232:235],v[72:75]\n"
T_"v[12:13],v2 offset:52744\n"
M_"v[76:79],v[20:23],v[236:239],v[76:79]\n"
T_"v[14:15],v2 offset:53000\n"
M_"v[64:67],v[24:27],v[240:243],v[64:67]\n"
T_"v[16:17],v2 offset:53760\n"
M_"v[68:71],v[24:27],v[244:247],v[68:71]\n"
W_"lgkmcnt(6)\n"
T_"v[18:19],v2 offset:54016\n"
M_"v[72:75],v[28:31],v[240:243],v[72:75]\n"
T_"v[20:21],v2 offset:53768\n"
M_"v[76:79],v[28:31],v[244:247],v[76:79]\n"
T_"v[22:23],v2 offset:54024\n"
M_"v[64:67],v[32:35],v[248:251],v[64:67]\n"
T_"v[24:25],v3 offset:4096\n"
M_"v[68:71],v[32:35],v[252:255],v[68:71]\n"
W_"lgkmcnt(6)\n"
T_"v[26:27],v3 offset:4352\n"
M_"v[72:75],v[36:39],v[248:251],v[72:75]\n"
T_"v[28:29],v3 offset:4104\n"
M_"v[76:79],v[36:39],v[252:255],v[76:79]\n"
T_"v[30:31],v3 offset:4360\n"
M_"v[64:67],v[8:11],v[128:131],v[64:67]\n"
T_"v[32:33],v3 offset:5120\n"
M_"v[68:71],v[8:11],v[132:135],v[68:71]\n"
W_"lgkmcnt(6)\n"
T_"v[34:35],v3 offset:5376\n"
M_"v[72:75],v[12:15],v[128:131],v[72:75]\n"
T_"v[36:37],v3 offset:5128\n"
M_"v[76:79],v[12:15],v[132:135],v[76:79]\n"
T_"v[38:39],v3 offset:5384\n"
M_"v[64:67],v[16:19],v[136:139],v[64:67]\n"
T_"v[8:9],v1 offset:50688\n"
C_"v84,v44,v45\n"
M_"v[68:71],v[16:19],v[140:143],v[68:71]\n"
W_"lgkmcnt(6)\n"
T_"v[10:11],v1 offset:50944\n"
C_"v85,v46,v47\n"
M_"v[72:75],v[20:23],v[136:139],v[72:75]\n"
T_"v[12:13],v1 offset:50696\n"
C_"v86,v52,v53\n"
M_"v[76:79],v[20:23],v[140:143],v[76:79]\n"
T_"v[14:15],v1 offset:50952\n"
C_"v87,v54,v55\n"
M_"v[64:67],v[24:27],v[144:147],v[64:67]\n"
T_"v[16:17],v1 offset:51712\n"
T_"v[18:19],v1 offset:51968\n"
M_"v[68:71],v[24:27],v[148:151],v[68:71]\n"
W_"lgkmcnt(7)\n"
"s_add_u32 s87, s91, 286720\n"
"buffer_store_dwordx4 v[84:87], v7, s[60:63], s87 offen sc1\n"
M_"v[72:75],v[28:31],v[144:147],v[72:75]\n"
T_"v[20:21],v1 offset:51720\n"
M_"v[76:79],v[28:31],v[148:151],v[76:79]\n"
T_"v[22:23],v1 offset:51976\n"
M_"v[64:67],v[32:35],v[152:155],v[64:67]\n"
T_"v[24:25],v2 offset:18944\n"
M_"v[68:71],v[32:35],v[156:159],v[68:71]\n"
W_"lgkmcnt(6)\n"
T_"v[26:27],v2 offset:19200\n"
M_"v[72:75],v[36:39],v[152:155],v[72:75]\n"
T_"v[28:29],v2 offset:18952\n"
M_"v[76:79],v[36:39],v[156:159],v[76:79]\n"
T_"v[30:31],v2 offset:19208\n"
M_"v[64:67],v[8:11],v[160:163],v[64:67]\n"
T_"v[32:33],v2 offset:19968\n"
M_"v[68:71],v[8:11],v[164:167],v[68:71]\n"
W_"lgkmcnt(6)\n"
T_"v[34:35],v2 offset:20224\n"
M_"v[72:75],v[12:15],v[160:163],v[72:75]\n"
T_"v[36:37],v2 offset:19976\n"
M_"v[76:79],v[12:15],v[164:167],v[76:79]\n"
T_"v[38:39],v2 offset:20232\n"
M_"v[64:67],v[16:19],v[168:171],v[64:67]\n"
"s_barrier\n"
T_"v[8:9],v2 offset:27392\n"
M_"v[68:71],v[16:19],v[172:175],v[68:71]\n"
W_"lgkmcnt(6)\n"
T_"v[10:11],v2 offset:27648\n"
M_"v[72:75],v[20:23],v[168:171],v[72:75]\n"
T_"v[12:13],v2 offset:27400\n"
M_"v[76:79],v[20:23],v[172:175],v[76:79]\n"
T_"v[14:15],v2 offset:27656\n"
M_"v[64:67],v[24:27],v[176:179],v[64:67]\n"
T_"v[16:17],v2 offset:28416\n"
M_"v[68:71],v[24:27],v[180:183],v[68:71]\n"
W_"lgkmcnt(6)\n"
T_"v[18:19],v2 offset:28672\n"
M_"v[72:75],v[28:31],v[176:179],v[72:75]\n"
T_"v[20:21],v2 offset:28424\n"
M_"v[76:79],v[28:31],v[180:183],v[76:79]\n"
T_"v[22:23],v2 offset:28680\n"
M_"v[64:67],v[32:35],v[184:187],v[64:67]\n"
T_"v[24:25],v3 offset:12544\n"
M_"v[68:71],v[32:35],v[188:191],v[68:71]\n"
W_"lgkmcnt(6)\n"
T_"v[26:27],v3 offset:12800\n"
M_"v[72:75],v[36:39],v[184:187],v[72:75]\n"
T_"v[28:29],v3 offset:12552\n"
M_"v[76:79],v[36:39],v[188:191],v[76:79]\n"
T_"v[30:31],v3 offset:12808\n"
M_"v[40:43],v[8:11],v[192:195],0\n"
T_"v[32:33],v3 offset:13568\n"
M_"v[44:47],v[8:11],v[196:199],0\n"
W_"lgkmcnt(6)\n"
T_"v[34:35],v3 offset:13824\n"
M_"v[48:51],v[12:15],v[192:195],0\n"
T_"v[36:37],v3 offset:13576\n"
M_"v[52:55],v[12:15],v[196:199],0\n"
T_"v[38:39],v3 offset:13832\n"
M_"v[40:43],v[16:19],v[200:203],v[40:43]\n"
T_"v[8:9],v1 offset:0\n"
C_"v88,v64,v65\n"
M_"v[44:47],v[16:19],v[204:207],v[44:47]\n"
W_"lgkmcnt(6)\n"
T_"v[10:11],v1 offset:256\n"
C_"v89,v66,v67\n"
M_"v[48:51],v[20:23],v[200:203],v[48:51]\n"
T_"v[12:13],v1 offset:8\n"
C_"v90,v72,v73\n"
M_"v[52:55],v[20:23],v[204:207],v[52:55]\n"
T_"v[14:15],v1 offset:264\n"
C_"v91,v74,v75\n"
M_"v[40:43],v[24:27],v[208:211],v[40:43]\n"
T_"v[16:17],v1 offset:1024\n"
T_"v[18:19],v1 offset:1280\n"
M_"v[44:47],v[24:27],v[212:215],v[44:47]\n"
W_"lgkmcnt(7)\n"
"s_add_u32 s87, s90, 65536\n"
"buffer_store_dwordx4 v[88:91], v7, s[60:63], s87 offen sc1\n"
M_"v[48:51],v[28:31],v[208:211],v[48:51]\n"
T_"v[20:21],v1 offset:1032\n"
M_"v[52:55],v[28:31],v[212:215],v[52:55]\n"
T_"v[22:23],v1 offset:1288\n"
M_"v[40:43],v[32:35],v[216:219],v[40:43]\n"
T_"v[24:25],v1 offset:8448\n"
M_"v[44:47],v[32:35],v[220:223],v[44:47]\n"
W_"lgkmcnt(6)\n"
T_"v[26:27],v1 offset:8704\n"
M_"v[48:51],v[36:39],v[216:219],v[48:51]\n"
T_"v[28:29],v1 offset:8456\n"
M_"v[52:55],v[36:39],v[220:223],v[52:55]\n"
T_"v[30:31],v1 offset:8712\n"
M_"v[40:43],v[8:11],v[224:227],v[40:43]\n"
T_"v[32:33],v1 offset:9472\n"
M_"v[44:47],v[8:11],v[228:231],v[44:47]\n"
W_"lgkmcnt(6)\n"
T_"v[34:35],v1 offset:9728\n"
M_"v[48:51],v[12:15],v[224:227],v[48:51]\n"
T_"v[36:37],v1 offset:9480\n"
M_"v[52:55],v[12:15],v[228:231],v[52:55]\n"
T_"v[38:39],v1 offset:9736\n"
M_"v[40:43],v[16:19],v[232:235],v[40:43]\n"
"s_barrier\n"
T_"v[8:9],v1 offset:16896\n"
M_"v[44:47],v[16:19],v[236:239],v[44:47]\n"
W_"lgkmcnt(6)\n"
T_"v[10:11],v1 offset:17152\n"
M_"v[48:51],v[20:23],v[232:235],v[48:51]\n"
T_"v[12:13],v1 offset:16904\n"
M_"v[52:55],v[20:23],v[236:239],v[52:55]\n"
T_"v[14:15],v1 offset:17160\n"
M_"v[40:43],v[24:27],v[240:243],v[40:43]\n"
T_"v[16:17],v1 offset:17920\n"
M_"v[44:47],v[24:27],v[244:247],v[44:47]\n"
W_"lgkmcnt(6)\n"
T_"v[18:19],v1 offset:18176\n"
M_"v[48:51],v[28:31],v[240:243],v[48:51]\n"
T_"v[20:21],v1 offset:17928\n"
M_"v[52:55],v[28:31],v[244:247],v[52:55]\n"
T_"v[22:23],v1 offset:18184\n"
M_"v[40:43],v[32:35],v[248:251],v[40:43]\n"
T_"v[24:25],v1 offset:25344\n"
M_"v[44:47],v[32:35],v[252:255],v[44:47]\n"
W_"lgkmcnt(6)\n"
T_"v[26:27],v1 offset:25600\n"
M_"v[48:51],v[36:39],v[248:251],v[48:51]\n"
T_"v[28:29],v1 offset:25352\n"
M_"v[52:55],v[36:39],v[252:255],v[52:55]\n"
T_"v[30:31],v1 offset:25608\n"
M_"v[40:43],v[8:11],v[128:131],v[40:43]\n"
T_"v[32:33],v1 offset:26368\n"
M_"v[44:47],v[8:11],v[132:135],v[44:47]\n"
W_"lgkmcnt(6)\n"
T_"v[34:35],v1 offset:26624\n"
M_"v[48:51],v[12:15],v[128:131],v[48:51]\n"
T_"v[36:37],v1 offset:26376\n"
M_"v[52:55],v[12:15],v[132:135],v[52:55]\n"
T_"v[38:39],v1 offset:26632\n"
M_"v[40:43],v[16:19],v[136:139],v[40:43]\n"
T_"v[8:9],v1 offset:42240\n"
C_"v92,v68,v69\n"
M_"v[44:47],v[16:19],v[140:143],v[44:47]\n"
W_"lgkmcnt(6)\n"
T_"v[10:11],v1 offset:42496\n"
C_"v93,v70,v71\n"
M_"v[48:51],v[20:23],v[136:139],v[48:51]\n"
T_"v[12:13],v1 offset:42248\n"
C_"v94,v76,v77\n"
M_"v[52:55],v[20:23],v[140:143],v[52:55]\n"
T_"v[14:15],v1 offset:42504\n"
C_"v95,v78,v79\n"
M_"v[40:43],v[24:27],v[144:147],v[40:43]\n"
T_"v[16:17],v1 offset:43264\n"
T_"v[18:19],v1 offset:43520\n"
M_"v[44:47],v[24:27],v[148:151],v[44:47]\n"
W_"lgkmcnt(7)\n"
"s_add_u32 s87, s91, 294912\n"
"buffer_store_dwordx4 v[92:95], v7, s[60:63], s87 offen sc1\n"
M_"v[48:51],v[28:31],v[144:147],v[48:51]\n"
T_"v[20:21],v1 offset:43272\n"
M_"v[52:55],v[28:31],v[148:151],v[52:55]\n"
T_"v[22:23],v1 offset:43528\n"
M_"v[40:43],v[32:35],v[152:155],v[40:43]\n"
T_"v[24:25],v1 offset:59136\n"
M_"v[44:47],v[32:35],v[156:159],v[44:47]\n"
W_"lgkmcnt(6)\n"
T_"v[26:27],v1 offset:59392\n"
M_"v[48:51],v[36:39],v[152:155],v[48:51]\n"
T_"v[28:29],v1 offset:59144\n"
M_"v[52:55],v[36:39],v[156:159],v[52:55]\n"
T_"v[30:31],v1 offset:59400\n"
M_"v[40:43],v[8:11],v[160:163],v[40:43]\n"
T_"v[32:33],v1 offset:60160\n"
M_"v[44:47],v[8:11],v[164:167],v[44:47]\n"
W_"lgkmcnt(6)\n"
T_"v[34:35],v1 offset:60416\n"
M_"v[48:51],v[12:15],v[160:163],v[48:51]\n"
T_"v[36:37],v1 offset:60168\n"
M_"v[52:55],v[12:15],v[164:167],v[52:55]\n"
T_"v[38:39],v1 offset:60424\n"
M_"v[40:43],v[16:19],v[168:171],v[40:43]\n"
"s_barrier\n"
T_"v[8:9],v2 offset:2048\n"
M_"v[44:47],v[16:19],v[172:175],v[44:47]\n"
W_"lgkmcnt(6)\n"
T_"v[10:11],v2 offset:2304\n"
M_"v[48:51],v[20:23],v[168:171],v[48:51]\n"
"PTL16:\n"
"s_cbranch_execz PTR\n"
T_"v[12:13],v2 offset:2056\n"
M_"v[52:55],v[20:23],v[172:175],v[52:55]\n"
T_"v[14:15],v2 offset:2312\n"
M_"v[40:43],v[24:27],v[176:179],v[40:43]\n"
T_"v[16:17],v2 offset:3072\n"
M_"v[44:47],v[24:27],v[180:183],v[44:47]\n"
W_"lgkmcnt(6)\n"
T_"v[18:19],v2 offset:3328\n"
M_"v[48:51],v[28:31],v[176:179],v[48:51]\n"
T_"v[20:21],v2 offset:3080\n"
M_"v[52:55],v[28:31],v[180:183],v[52:55]\n"
T_"v[22:23],v2 offset:3336\n"
M_"v[40:43],v[32:35],v[184:187],v[40:43]\n"
T_"v[24:25],v1 offset:33792\n"
M_"v[44:47],v[32:35],v[188:191],v[44:47]\n"
W_"lgkmcnt(6)\n"
T_"v[26:27],v1 offset:34048\n"
M_"v[48:51],v[36:39],v[184:187],v[48:51]\n"
T_"v[28:29],v1 offset:33800\n"
M_"v[52:55],v[36:39],v[188:191],v[52:55]\n"
T_"v[30:31],v1 offset:34056\n"
M_"v[64:67],v[8:11],v[192:195],0\n"
T_"v[32:33],v1 offset:34816\n"
M_"v[68:71],v[8:11],v[196:199],0\n"
W_"lgkmcnt(6)\n"
T_"v[34:35],v1 offset:35072\n"
M_"v[72:75],v[12:15],v[192:195],0\n"
T_"v[36:37],v1 offset:34824\n"
M_"v[76:79],v[12:15],v[196:199],0\n"
T_"v[38:39],v1 offset:35080\n"
M_"v[64:67],v[16:19],v[200:203],v[64:67]\n"
T_"v[8:9],v2 offset:10496\n"
C_"v96,v40,v41\n"
M_"v[68:71],v[16:19],v[204:207],v[68:71]\n"
W_"lgkmcnt(6)\n"
T_"v[10:11],v2 offset:10752\n"
C_"v97,v42,v43\n"
M_"v[72:75],v[20:23],v[200:203],v[72:75]\n"
T_"v[12:13],v2 offset:10504\n"
C_"v98,v48,v49\n"
M_"v[76:79],v[20:23],v[204:207],v[76:79]\n"
T_"v[14:15],v2 offset:10760\n"
C_"v99,v50,v51\n"
M_"v[64:67],v[24:27],v[208:211],v[64:67]\n"
T_"v[16:17],v2 offset:11520\n"
T_"v[18:19],v2 offset:11776\n"
M_"v[68:71],v[24:27],v[212:215],v[68:71]\n"
W_"lgkmcnt(7)\n"
"s_add_u32 s87, s90, 73728\n"
"buffer_store_dwordx4 v[96:99], v7, s[60:63], s87 offen sc1\n"
M_"v[72:75],v[28:31],v[208:211],v[72:75]\n"
T_"v[20:21],v2 offset:11528\n"
M_"v[76:79],v[28:31],v[212:215],v[76:79]\n"
T_"v[22:23],v2 offset:11784\n"
M_"v[64:67],v[32:35],v[216:219],v[64:67]\n"
T_"v[24:25],v2 offset:35840\n"
M_"v[68:71],v[32:35],v[220:223],v[68:71]\n"
W_"lgkmcnt(6)\n"
T_"v[26:27],v2 offset:36096\n"
M_"v[72:75],v[36:39],v[216:219],v[72:75]\n"
T_"v[28:29],v2 offset:35848\n"
M_"v[76:79],v[36:39],v[220:223],v[76:79]\n"
T_"v[30:31],v2 offset:36104\n"
M_"v[64:67],v[8:11],v[224:227],v[64:67]\n"
T_"v[32:33],v2 offset:36864\n"
M_"v[68:71],v[8:11],v[228:231],v[68:71]\n"
W_"lgkmcnt(6)\n"
T_"v[34:35],v2 offset:37120\n"
M_"v[72:75],v[12:15],v[224:227],v[72:75]\n"
T_"v[36:37],v2 offset:36872\n"
M_"v[76:79],v[12:15],v[228:231],v[76:79]\n"
T_"v[38:39],v2 offset:37128\n"
M_"v[64:67],v[16:19],v[232:235],v[64:67]\n"
"s_barrier\n"
T_"v[8:9],v2 offset:44288\n"
M_"v[68:71],v[16:19],v[236:239],v[68:71]\n"
W_"lgkmcnt(6)\n"
T_"v[10:11],v2 offset:44544\n"
M_"v[72:75],v[20:23],v[232:235],v[72:75]\n"
T_"v[12:13],v2 offset:44296\n"
M_"v[76:79],v[20:23],v[236:239],v[76:79]\n"
T_"v[14:15],v2 offset:44552\n"
M_"v[64:67],v[24:27],v[240:243],v[64:67]\n"
T_"v[16:17],v2 offset:45312\n"
M_"v[68:71],v[24:27],v[244:247],v[68:71]\n"
W_"lgkmcnt(6)\n"
T_"v[18:19],v2 offset:45568\n"
M_"v[72:75],v[28:31],v[240:243],v[72:75]\n"
T_"v[20:21],v2 offset:45320\n"
M_"v[76:79],v[28:31],v[244:247],v[76:79]\n"
T_"v[22:23],v2 offset:45576\n"
M_"v[64:67],v[32:35],v[248:251],v[64:67]\n"
T_"v[24:25],v1 offset:50688\n"
M_"v[68:71],v[32:35],v[252:255],v[68:71]\n"
W_"lgkmcnt(6)\n"
T_"v[26:27],v1 offset:50944\n"
M_"v[72:75],v[36:39],v[248:251],v[72:75]\n"
T_"v[28:29],v1 offset:50696\n"
M_"v[76:79],v[36:39],v[252:255],v[76:79]\n"
T_"v[30:31],v1 offset:50952\n"
M_"v[64:67],v[8:11],v[128:131],v[64:67]\n"
T_"v[32:33],v1 offset:51712\n"
M_"v[68:71],v[8:11],v[132:135],v[68:71]\n"
W_"lgkmcnt(6)\n"
T_"v[34:35],v1 offset:51968\n"
M_"v[72:75],v[12:15],v[128:131],v[72:75]\n"
T_"v[36:37],v1 offset:51720\n"
M_"v[76:79],v[12:15],v[132:135],v[76:79]\n"
T_"v[38:39],v1 offset:51976\n"
M_"v[64:67],v[16:19],v[136:139],v[64:67]\n"
T_"v[8:9],v2 offset:18944\n"
C_"v100,v44,v45\n"
M_"v[68:71],v[16:19],v[140:143],v[68:71]\n"
W_"lgkmcnt(6)\n"
T_"v[10:11],v2 offset:19200\n"
C_"v101,v46,v47\n"
M_"v[72:75],v[20:23],v[136:139],v[72:75]\n"
T_"v[12:13],v2 offset:18952\n"
C_"v102,v52,v53\n"
M_"v[76:79],v[20:23],v[140:143],v[76:79]\n"
T_"v[14:15],v2 offset:19208\n"
C_"v103,v54,v55\n"
M_"v[64:67],v[24:27],v[144:147],v[64:67]\n"
T_"v[16:17],v2 offset:19968\n"
T_"v[18:19],v2 offset:20224\n"
M_"v[68:71],v[24:27],v[148:151],v[68:71]\n"
W_"lgkmcnt(7)\n"
"s_add_u32 s87, s91, 303104\n"
"buffer_store_dwordx4 v[100:103], v7, s[60:63], s87 offen sc1\n"
M_"v[72:75],v[28:31],v[144:147],v[72:75]\n"
T_"v[20:21],v2 offset:19976\n"
M_"v[76:79],v[28:31],v[148:151],v[76:79]\n"
T_"v[22:23],v2 offset:20232\n"
M_"v[64:67],v[32:35],v[152:155],v[64:67]\n"
T_"v[24:25],v2 offset:27392\n"
M_"v[68:71],v[32:35],v[156:159],v[68:71]\n"
W_"lgkmcnt(6)\n"
T_"v[26:27],v2 offset:27648\n"
M_"v[72:75],v[36:39],v[152:155],v[72:75]\n"
T_"v[28:29],v2 offset:27400\n"
M_"v[76:79],v[36:39],v[156:159],v[76:79]\n"
T_"v[30:31],v2 offset:27656\n"
M_"v[64:67],v[8:11],v[160:163],v[64:67]\n"
T_"v[32:33],v2 offset:28416\n"
M_"v[68:71],v[8:11],v[164:167],v[68:71]\n"
W_"lgkmcnt(6)\n"
T_"v[34:35],v2 offset:28672\n"
M_"v[72:75],v[12:15],v[160:163],v[72:75]\n"
T_"v[36:37],v2 offset:28424\n"
M_"v[76:79],v[12:15],v[164:167],v[76:79]\n"
T_"v[38:39],v2 offset:28680\n"
M_"v[64:67],v[16:19],v[168:171],v[64:67]\n"
"s_barrier\n"
T_"v[8:9],v2 offset:52736\n"
"v_add_u32 v6, 1, v6\n"
M_"v[68:71],v[16:19],v[172:175],v[68:71]\n"
W_"lgkmcnt(6)\n"
T_"v[10:11],v2 offset:52992\n"
M_"v[72:75],v[20:23],v[168:171],v[72:75]\n"
T_"v[12:13],v2 offset:52744\n"
M_"v[76:79],v[20:23],v[172:175],v[76:79]\n"
T_"v[14:15],v2 offset:53000\n"
M_"v[64:67],v[24:27],v[176:179],v[64:67]\n"
T_"v[16:17],v2 offset:53760\n"
M_"v[68:71],v[24:27],v[180:183],v[68:71]\n"
W_"lgkmcnt(6)\n"
T_"v[18:19],v2 offset:54016\n"
M_"v[72:75],v[28:31],v[176:179],v[72:75]\n"
T_"v[20:21],v2 offset:53768\n"
M_"v[76:79],v[28:31],v[180:183],v[76:79]\n"
T_"v[22:23],v2 offset:54024\n"
M_"v[64:67],v[32:35],v[184:187],v[64:67]\n"
T_"v[24:25],v1 offset:0\n"
M_"v[68:71],v[32:35],v[188:191],v[68:71]\n"
W_"lgkmcnt(6)\n"
T_"v[26:27],v1 offset:256\n"
M_"v[72:75],v[36:39],v[184:187],v[72:75]\n"
T_"v[28:29],v1 offset:8\n"
M_"v[76:79],v[36:39],v[188:191],v[76:79]\n"
T_"v[30:31],v1 offset:264\n"
M_"v[40:43],v[8:11],v[192:195],0\n"
T_"v[32:33],v1 offset:1024\n"
M_"v[44:47],v[8:11],v[196:199],0\n"
W_"lgkmcnt(6)\n"
T_"v[34:35],v1 offset:1280\n"
M_"v[48:51],v[12:15],v[192:195],0\n"
T_"v[36:37],v1 offset:1032\n"
M_"v[52:55],v[12:15],v[196:199],0\n"
T_"v[38:39],v1 offset:1288\n"
M_"v[40:43],v[16:19],v[200:203],v[40:43]\n"
T_"v[8:9],v1 offset:8448\n"
C_"v104,v64,v65\n"
M_"v[44:47],v[16:19],v[204:207],v[44:47]\n"
W_"lgkmcnt(6)\n"
T_"v[10:11],v1 offset:8704\n"
C_"v105,v66,v67\n"
M_"v[48:51],v[20:23],v[200:203],v[48:51]\n"
T_"v[12:13],v1 offset:8456\n"
C_"v106,v72,v73\n"
M_"v[52:55],v[20:23],v[204:207],v[52:55]\n"
T_"v[14:15],v1 offset:8712\n"
C_"v107,v74,v75\n"
M_"v[40:43],v[24:27],v[208:211],v[40:43]\n"
T_"v[16:17],v1 offset:9472\n"
T_"v[18:19],v1 offset:9728\n"
M_"v[44:47],v[24:27],v[212:215],v[44:47]\n"
W_"lgkmcnt(7)\n"
"s_add_u32 s87, s90, 81920\n"
"buffer_store_dwordx4 v[104:107], v7, s[60:63], s87 offen sc1\n"
M_"v[48:51],v[28:31],v[208:211],v[48:51]\n"
T_"v[20:21],v1 offset:9480\n"
M_"v[52:55],v[28:31],v[212:215],v[52:55]\n"
T_"v[22:23],v1 offset:9736\n"
M_"v[40:43],v[32:35],v[216:219],v[40:43]\n"
T_"v[24:25],v1 offset:16896\n"
M_"v[44:47],v[32:35],v[220:223],v[44:47]\n"
W_"lgkmcnt(6)\n"
T_"v[26:27],v1 offset:17152\n"
M_"v[48:51],v[36:39],v[216:219],v[48:51]\n"
T_"v[28:29],v1 offset:16904\n"
M_"v[52:55],v[36:39],v[220:223],v[52:55]\n"
T_"v[30:31],v1 offset:17160\n"
M_"v[40:43],v[8:11],v[224:227],v[40:43]\n"
T_"v[32:33],v1 offset:17920\n"
M_"v[44:47],v[8:11],v[228:231],v[44:47]\n"
W_"lgkmcnt(6)\n"
T_"v[34:35],v1 offset:18176\n"
M_"v[48:51],v[12:15],v[224:227],v[48:51]\n"
T_"v[36:37],v1 offset:17928\n"
M_"v[52:55],v[12:15],v[228:231],v[52:55]\n"
T_"v[38:39],v1 offset:18184\n"
M_"v[40:43],v[16:19],v[232:235],v[40:43]\n"
W_"vmcnt(1)\n"
"s_barrier\n"
"s_mov_b64 exec, 1\n"
"buffer_store_dword v6, off, s[80:83], s8 sc1\n"
"s_mov_b64 exec, -1\n"
T_"v[8:9],v1 offset:25344\n"
M_"v[44:47],v[16:19],v[236:239],v[44:47]\n"
W_"lgkmcnt(6)\n"
T_"v[10:11],v1 offset:25600\n"
M_"v[48:51],v[20:23],v[232:235],v[48:51]\n"
T_"v[12:13],v1 offset:25352\n"
M_"v[52:55],v[20:23],v[236:239],v[52:55]\n"
T_"v[14:15],v1 offset:25608\n"
M_"v[40:43],v[24:27],v[240:243],v[40:43]\n"
T_"v[16:17],v1 offset:26368\n"
M_"v[44:47],v[24:27],v[244:247],v[44:47]\n"
W_"lgkmcnt(6)\n"
T_"v[18:19],v1 offset:26624\n"
M_"v[48:51],v[28:31],v[240:243],v[48:51]\n"
T_"v[20:21],v1 offset:26376\n"
M_"v[52:55],v[28:31],v[244:247],v[52:55]\n"
T_"v[22:23],v1 offset:26632\n"
M_"v[40:43],v[32:35],v[248:251],v[40:43]\n"
T_"v[24:25],v1 offset:33792\n"
M_"v[44:47],v[32:35],v[252:255],v[44:47]\n"
W_"lgkmcnt(6)\n"
T_"v[26:27],v1 offset:34048\n"
M_"v[48:51],v[36:39],v[248:251],v[48:51]\n"
T_"v[28:29],v1 offset:33800\n"
M_"v[52:55],v[36:39],v[252:255],v[52:55]\n"
T_"v[30:31],v1 offset:34056\n"
M_"v[40:43],v[8:11],v[128:131],v[40:43]\n"
T_"v[32:33],v1 offset:34816\n"
M_"v[44:47],v[8:11],v[132:135],v[44:47]\n"
W_"lgkmcnt(6)\n"
T_"v[34:35],v1 offset:35072\n"
M_"v[48:51],v[12:15],v[128:131],v[48:51]\n"
T_"v[36:37],v1 offset:34824\n"
M_"v[52:55],v[12:15],v[132:135],v[52:55]\n"
T_"v[38:39],v1 offset:35080\n"
M_"v[40:43],v[16:19],v[136:139],v[40:43]\n"
T_"v[8:9],v1 offset:42240\n"
C_"v108,v68,v69\n"
M_"v[44:47],v[16:19],v[140:143],v[44:47]\n"
W_"lgkmcnt(6)\n"
T_"v[10:11],v1 offset:42496\n"
C_"v109,v70,v71\n"
M_"v[48:51],v[20:23],v[136:139],v[48:51]\n"
T_"v[12:13],v1 offset:42248\n"
C_"v110,v76,v77\n"
M_"v[52:55],v[20:23],v[140:143],v[52:55]\n"
T_"v[14:15],v1 offset:42504\n"
C_"v111,v78,v79\n"
M_"v[40:43],v[24:27],v[144:147],v[40:43]\n"
T_"v[16:17],v1 offset:43264\n"
T_"v[18:19],v1 offset:43520\n"
M_"v[44:47],v[24:27],v[148:151],v[44:47]\n"
W_"lgkmcnt(7)\n"
"s_add_u32 s87, s91, 311296\n"
"buffer_store_dwordx4 v[108:111], v7, s[60:63], s87 offen sc1\n"
M_"v[48:51],v[28:31],v[144:147],v[48:51]\n"
T_"v[20:21],v1 offset:43272\n"
M_"v[52:55],v[28:31],v[148:151],v[52:55]\n"
T_"v[22:23],v1 offset:43528\n"
M_"v[40:43],v[32:35],v[152:155],v[40:43]\n"
T_"v[24:25],v1 offset:59136\n"
M_"v[44:47],v[32:35],v[156:159],v[44:47]\n"
W_"lgkmcnt(6)\n"
T_"v[26:27],v1 offset:59392\n"
M_"v[48:51],v[36:39],v[152:155],v[48:51]\n"
T_"v[28:29],v1 offset:59144\n"
M_"v[52:55],v[36:39],v[156:159],v[52:55]\n"
T_"v[30:31],v1 offset:59400\n"
M_"v[40:43],v[8:11],v[160:163],v[40:43]\n"
T_"v[32:33],v1 offset:60160\n"
M_"v[44:47],v[8:11],v[164:167],v[44:47]\n"
W_"lgkmcnt(6)\n"
T_"v[34:35],v1 offset:60416\n"
M_"v[48:51],v[12:15],v[160:163],v[48:51]\n"
T_"v[36:37],v1 offset:60168\n"
M_"v[52:55],v[12:15],v[164:167],v[52:55]\n"
T_"v[38:39],v1 offset:60424\n"
M_"v[40:43],v[16:19],v[168:171],v[40:43]\n"
"s_barrier\n"
T_"v[8:9],v1 offset:50688\n"
M_"v[44:47],v[16:19],v[172:175],v[44:47]\n"
W_"lgkmcnt(6)\n"
T_"v[10:11],v1 offset:50944\n"
M_"v[48:51],v[20:23],v[168:171],v[48:51]\n"
T_"v[12:13],v1 offset:50696\n"
M_"v[52:55],v[20:23],v[172:175],v[52:55]\n"
T_"v[14:15],v1 offset:50952\n"
M_"v[40:43],v[24:27],v[176:179],v[40:43]\n"
T_"v[16:17],v1 offset:51712\n"
M_"v[44:47],v[24:27],v[180:183],v[44:47]\n"
W_"lgkmcnt(6)\n"
T_"v[18:19],v1 offset:51968\n"
M_"v[48:51],v[28:31],v[176:179],v[48:51]\n"
T_"v[20:21],v1 offset:51720\n"
M_"v[52:55],v[28:31],v[180:183],v[52:55]\n"
T_"v[22:23],v1 offset:51976\n"
M_"v[40:43],v[32:35],v[184:187],v[40:43]\n"
T_"v[24:25],v2 offset:2048\n"
M_"v[44:47],v[32:35],v[188:191],v[44:47]\n"
W_"lgkmcnt(6)\n"
T_"v[26:27],v2 offset:2304\n"
M_"v[48:51],v[36:39],v[184:187],v[48:51]\n"
T_"v[28:29],v2 offset:2056\n"
M_"v[52:55],v[36:39],v[188:191],v[52:55]\n"
T_"v[30:31],v2 offset:2312\n"
M_"v[64:67],v[8:11],v[192:195],0\n"
T_"v[32:33],v2 offset:3072\n"
M_"v[68:71],v[8:11],v[196:199],0\n"
W_"lgkmcnt(6)\n"
T_"v[34:35],v2 offset:3328\n"
M_"v[72:75],v[12:15],v[192:195],0\n"
T_"v[36:37],v2 offset:3080\n"
M_"v[76:79],v[12:15],v[196:199],0\n"
T_"v[38:39],v2 offset:3336\n"
M_"v[64:67],v[16:19],v[200:203],v[64:67]\n"
T_"v[8:9],v2 offset:10496\n"
C_"v112,v40,v41\n"
M_"v[68:71],v[16:19],v[204:207],v[68:71]\n"
W_"lgkmcnt(6)\n"
T_"v[10:11],v2 offset:10752\n"
C_"v113,v42,v43\n"
M_"v[72:75],v[20:23],v[200:203],v[72:75]\n"
T_"v[12:13],v2 offset:10504\n"
C_"v114,v48,v49\n"
M_"v[76:79],v[20:23],v[204:207],v[76:79]\n"
T_"v[14:15],v2 offset:10760\n"
C_"v115,v50,v51\n"
M_"v[64:67],v[24:27],v[208:211],v[64:67]\n"
T_"v[16:17],v2 offset:11520\n"
T_"v[18:19],v2 offset:11776\n"
M_"v[68:71],v[24:27],v[212:215],v[68:71]\n"
W_"lgkmcnt(7)\n"
"s_add_u32 s87, s90, 90112\n"
"buffer_store_dwordx4 v[112:115], v7, s[60:63], s87 offen sc1\n"
M_"v[72:75],v[28:31],v[208:211],v[72:75]\n"
T_"v[20:21],v2 offset:11528\n"
M_"v[76:79],v[28:31],v[212:215],v[76:79]\n"
T_"v[22:23],v2 offset:11784\n"
M_"v[64:67],v[32:35],v[216:219],v[64:67]\n"
T_"v[24:25],v2 offset:35840\n"
C_"v116,v44,v45\n"
M_"v[68:71],v[32:35],v[220:223],v[68:71]\n"
W_"lgkmcnt(6)\n"
T_"v[26:27],v2 offset:36096\n"
C_"v117,v46,v47\n"
M_"v[72:75],v[36:39],v[216:219],v[72:75]\n"
T_"v[28:29],v2 offset:35848\n"
C_"v118,v52,v53\n"
M_"v[76:79],v[36:39],v[220:223],v[76:79]\n"
T_"v[30:31],v2 offset:36104\n"
C_"v119,v54,v55\n"
M_"v[64:67],v[8:11],v[224:227],v[64:67]\n"
T_"v[32:33],v2 offset:36864\n"
T_"v[34:35],v2 offset:37120\n"
M_"v[68:71],v[8:11],v[228:231],v[68:71]\n"
W_"lgkmcnt(7)\n"
"s_add_u32 s87, s91, 319488\n"
"buffer_store_dwordx4 v[116:119], v7, s[60:63], s87 offen sc1\n"
M_"v[72:75],v[12:15],v[224:227],v[72:75]\n"
T_"v[36:37],v2 offset:36872\n"
M_"v[76:79],v[12:15],v[228:231],v[76:79]\n"
T_"v[38:39],v2 offset:37128\n"
M_"v[64:67],v[16:19],v[232:235],v[64:67]\n"
"s_barrier\n"
T_"v[8:9],v1 offset:0\n"
M_"v[68:71],v[16:19],v[236:239],v[68:71]\n"
W_"lgkmcnt(6)\n"
T_"v[10:11],v1 offset:256\n"
M_"v[72:75],v[20:23],v[232:235],v[72:75]\n"
T_"v[12:13],v1 offset:8\n"
M_"v[76:79],v[20:23],v[236:239],v[76:79]\n"
T_"v[14:15],v1 offset:264\n"
M_"v[64:67],v[24:27],v[240:243],v[64:67]\n"
T_"v[16:17],v1 offset:1024\n"
M_"v[68:71],v[24:27],v[244:247],v[68:71]\n"
W_"lgkmcnt(6)\n"
T_"v[18:19],v1 offset:1280\n"
M_"v[72:75],v[28:31],v[240:243],v[72:75]\n"
T_"v[20:21],v1 offset:1032\n"
M_"v[76:79],v[28:31],v[244:247],v[76:79]\n"
T_"v[22:23],v1 offset:1288\n"
M_"v[64:67],v[32:35],v[248:251],v[64:67]\n"
T_"v[24:25],v2 offset:18944\n"
M_"v[68:71],v[32:35],v[252:255],v[68:71]\n"
W_"lgkmcnt(6)\n"
T_"v[26:27],v2 offset:19200\n"
M_"v[72:75],v[36:39],v[248:251],v[72:75]\n"
T_"v[28:29],v2 offset:18952\n"
M_"v[76:79],v[36:39],v[252:255],v[76:79]\n"
T_"v[30:31],v2 offset:19208\n"
M_"v[64:67],v[8:11],v[128:131],v[64:67]\n"
T_"v[32:33],v2 offset:19968\n"
M_"v[68:71],v[8:11],v[132:135],v[68:71]\n"
W_"lgkmcnt(6)\n"
T_"v[34:35],v2 offset:20224\n"
M_"v[72:75],v[12:15],v[128:131],v[72:75]\n"
T_"v[36:37],v2 offset:19976\n"
M_"v[76:79],v[12:15],v[132:135],v[76:79]\n"
T_"v[38:39],v2 offset:20232\n"
M_"v[64:67],v[16:19],v[136:139],v[64:67]\n"
T_"v[8:9],v2 offset:27392\n"
M_"v[68:71],v[16:19],v[140:143],v[68:71]\n"
W_"lgkmcnt(6)\n"
T_"v[10:11],v2 offset:27648\n"
M_"v[72:75],v[20:23],v[136:139],v[72:75]\n"
T_"v[12:13],v2 offset:27400\n"
M_"v[76:79],v[20:23],v[140:143],v[76:79]\n"
T_"v[14:15],v2 offset:27656\n"
M_"v[64:67],v[24:27],v[144:147],v[64:67]\n"
T_"v[16:17],v2 offset:28416\n"
M_"v[68:71],v[24:27],v[148:151],v[68:71]\n"
W_"lgkmcnt(6)\n"
T_"v[18:19],v2 offset:28672\n"
M_"v[72:75],v[28:31],v[144:147],v[72:75]\n"
T_"v[20:21],v2 offset:28424\n"
M_"v[76:79],v[28:31],v[148:151],v[76:79]\n"
T_"v[22:23],v2 offset:28680\n"
M_"v[64:67],v[32:35],v[152:155],v[64:67]\n"
T_"v[24:25],v2 offset:44288\n"
M_"v[68:71],v[32:35],v[156:159],v[68:71]\n"
W_"lgkmcnt(6)\n"
T_"v[26:27],v2 offset:44544\n"
M_"v[72:75],v[36:39],v[152:155],v[72:75]\n"
T_"v[28:29],v2 offset:44296\n"
M_"v[76:79],v[36:39],v[156:159],v[76:79]\n"
T_"v[30:31],v2 offset:44552\n"
M_"v[64:67],v[8:11],v[160:163],v[64:67]\n"
T_"v[32:33],v2 offset:45312\n"
M_"v[68:71],v[8:11],v[164:167],v[68:71]\n"
W_"lgkmcnt(6)\n"
T_"v[34:35],v2 offset:45568\n"
M_"v[72:75],v[12:15],v[160:163],v[72:75]\n"
T_"v[36:37],v2 offset:45320\n"
M_"v[76:79],v[12:15],v[164:167],v[76:79]\n"
T_"v[38:39],v2 offset:45576\n"
M_"v[64:67],v[16:19],v[168:171],v[64:67]\n"
"s_barrier\n"
T_"v[8:9],v1 offset:8448\n"
"v_add_u32 v6, 1, v6\n"
M_"v[68:71],v[16:19],v[172:175],v[68:71]\n"
W_"lgkmcnt(6)\n"
T_"v[10:11],v1 offset:8704\n"
M_"v[72:75],v[20:23],v[168:171],v[72:75]\n"
T_"v[12:13],v1 offset:8456\n"
M_"v[76:79],v[20:23],v[172:175],v[76:79]\n"
T_"v[14:15],v1 offset:8712\n"
M_"v[64:67],v[24:27],v[176:179],v[64:67]\n"
T_"v[16:17],v1 offset:9472\n"
M_"v[68:71],v[24:27],v[180:183],v[68:71]\n"
W_"lgkmcnt(6)\n"
T_"v[18:19],v1 offset:9728\n"
M_"v[72:75],v[28:31],v[176:179],v[72:75]\n"
T_"v[20:21],v1 offset:9480\n"
M_"v[76:79],v[28:31],v[180:183],v[76:79]\n"
T_"v[22:23],v1 offset:9736\n"
M_"v[64:67],v[32:35],v[184:187],v[64:67]\n"
T_"v[24:25],v1 offset:16896\n"
M_"v[68:71],v[32:35],v[188:191],v[68:71]\n"
W_"lgkmcnt(6)\n"
T_"v[26:27],v1 offset:17152\n"
M_"v[72:75],v[36:39],v[184:187],v[72:75]\n"
T_"v[28:29],v1 offset:16904\n"
M_"v[76:79],v[36:39],v[188:191],v[76:79]\n"
T_"v[30:31],v1 offset:17160\n"
M_"v[40:43],v[8:11],v[192:195],0\n"
T_"v[32:33],v1 offset:17920\n"
M_"v[44:47],v[8:11],v[196:199],0\n"
W_"lgkmcnt(6)\n"
T_"v[34:35],v1 offset:18176\n"
M_"v[48:51],v[12:15],v[192:195],0\n"
T_"v[36:37],v1 offset:17928\n"
M_"v[52:55],v[12:15],v[196:199],0\n"
T_"v[38:39],v1 offset:18184\n"
M_"v[40:43],v[16:19],v[200:203],v[40:43]\n"
T_"v[8:9],v1 offset:25344\n"
C_"v120,v64,v65\n"
M_"v[44:47],v[16:19],v[204:207],v[44:47]\n"
W_"lgkmcnt(6)\n"
T_"v[10:11],v1 offset:25600\n"
C_"v121,v66,v67\n"
M_"v[48:51],v[20:23],v[200:203],v[48:51]\n"
T_"v[12:13],v1 offset:25352\n"
C_"v122,v72,v73\n"
M_"v[52:55],v[20:23],v[204:207],v[52:55]\n"
T_"v[14:15],v1 offset:25608\n"
C_"v123,v74,v75\n"
M_"v[40:43],v[24:27],v[208:211],v[40:43]\n"
T_"v[16:17],v1 offset:26368\n"
T_"v[18:19],v1 offset:26624\n"
M_"v[44:47],v[24:27],v[212:215],v[44:47]\n"
W_"lgkmcnt(7)\n"
"s_add_u32 s87, s90, 98304\n"
"buffer_store_dwordx4 v[120:123], v7, s[60:63], s87 offen sc1\n"
M_"v[48:51],v[28:31],v[208:211],v[48:51]\n"
T_"v[20:21],v1 offset:26376\n"
M_"v[52:55],v[28:31],v[212:215],v[52:55]\n"
T_"v[22:23],v1 offset:26632\n"
M_"v[40:43],v[32:35],v[216:219],v[40:43]\n"
T_"v[24:25],v1 offset:33792\n"
C_"v124,v68,v69\n"
M_"v[44:47],v[32:35],v[220:223],v[44:47]\n"
W_"lgkmcnt(6)\n"
T_"v[26:27],v1 offset:34048\n"
C_"v125,v70,v71\n"
M_"v[48:51],v[36:39],v[216:219],v[48:51]\n"
"PTL17:\n"
"s_cbranch_execz PTR\n"
T_"v[28:29],v1 offset:33800\n"
C_"v126,v76,v77\n"
M_"v[52:55],v[36:39],v[220:223],v[52:55]\n"
T_"v[30:31],v1 offset:34056\n"
C_"v127,v78,v79\n"
M_"v[40:43],v[8:11],v[224:227],v[40:43]\n"
T_"v[32:33],v1 offset:34816\n"
T_"v[34:35],v1 offset:35072\n"
M_"v[44:47],v[8:11],v[228:231],v[44:47]\n"
W_"lgkmcnt(7)\n"
"s_add_u32 s87, s91, 327680\n"
"buffer_store_dwordx4 v[124:127], v7, s[60:63], s87 offen sc1\n"
M_"v[48:51],v[12:15],v[224:227],v[48:51]\n"
T_"v[36:37],v1 offset:34824\n"
M_"v[52:55],v[12:15],v[228:231],v[52:55]\n"
T_"v[38:39],v1 offset:35080\n"
M_"v[40:43],v[16:19],v[232:235],v[40:43]\n"
W_"vmcnt(2)\n"
"s_barrier\n"
"s_mov_b64 exec, 1\n"
"buffer_store_dword v6, off, s[80:83], s8 sc1\n"
"s_mov_b64 exec, -1\n"
T_"v[8:9],v1 offset:42240\n"
M_"v[44:47],v[16:19],v[236:239],v[44:47]\n"
W_"lgkmcnt(6)\n"
T_"v[10:11],v1 offset:42496\n"
M_"v[48:51],v[20:23],v[232:235],v[48:51]\n"
T_"v[12:13],v1 offset:42248\n"
M_"v[52:55],v[20:23],v[236:239],v[52:55]\n"
T_"v[14:15],v1 offset:42504\n"
M_"v[40:43],v[24:27],v[240:243],v[40:43]\n"
T_"v[16:17],v1 offset:43264\n"
M_"v[44:47],v[24:27],v[244:247],v[44:47]\n"
W_"lgkmcnt(6)\n"
T_"v[18:19],v1 offset:43520\n"
M_"v[48:51],v[28:31],v[240:243],v[48:51]\n"
T_"v[20:21],v1 offset:43272\n"
M_"v[52:55],v[28:31],v[244:247],v[52:55]\n"
T_"v[22:23],v1 offset:43528\n"
M_"v[40:43],v[32:35],v[248:251],v[40:43]\n"
T_"v[24:25],v1 offset:50688\n"
M_"v[44:47],v[32:35],v[252:255],v[44:47]\n"
W_"lgkmcnt(6)\n"
T_"v[26:27],v1 offset:50944\n"
M_"v[48:51],v[36:39],v[248:251],v[48:51]\n"
T_"v[28:29],v1 offset:50696\n"
M_"v[52:55],v[36:39],v[252:255],v[52:55]\n"
T_"v[30:31],v1 offset:50952\n"
M_"v[40:43],v[8:11],v[128:131],v[40:43]\n"
T_"v[32:33],v1 offset:51712\n"
M_"v[44:47],v[8:11],v[132:135],v[44:47]\n"
W_"lgkmcnt(6)\n"
T_"v[34:35],v1 offset:51968\n"
M_"v[48:51],v[12:15],v[128:131],v[48:51]\n"
T_"v[36:37],v1 offset:51720\n"
M_"v[52:55],v[12:15],v[132:135],v[52:55]\n"
T_"v[38:39],v1 offset:51976\n"
M_"v[40:43],v[16:19],v[136:139],v[40:43]\n"
T_"v[8:9],v1 offset:59136\n"
M_"v[44:47],v[16:19],v[140:143],v[44:47]\n"
W_"lgkmcnt(6)\n"
T_"v[10:11],v1 offset:59392\n"
M_"v[48:51],v[20:23],v[136:139],v[48:51]\n"
T_"v[12:13],v1 offset:59144\n"
M_"v[52:55],v[20:23],v[140:143],v[52:55]\n"
T_"v[14:15],v1 offset:59400\n"
M_"v[40:43],v[24:27],v[144:147],v[40:43]\n"
T_"v[16:17],v1 offset:60160\n"
M_"v[44:47],v[24:27],v[148:151],v[44:47]\n"
W_"lgkmcnt(6)\n"
T_"v[18:19],v1 offset:60416\n"
M_"v[48:51],v[28:31],v[144:147],v[48:51]\n"
T_"v[20:21],v1 offset:60168\n"
M_"v[52:55],v[28:31],v[148:151],v[52:55]\n"
T_"v[22:23],v1 offset:60424\n"
M_"v[40:43],v[32:35],v[152:155],v[40:43]\n"
T_"v[24:25],v2 offset:2048\n"
M_"v[44:47],v[32:35],v[156:159],v[44:47]\n"
W_"lgkmcnt(6)\n"
T_"v[26:27],v2 offset:2304\n"
M_"v[48:51],v[36:39],v[152:155],v[48:51]\n"
T_"v[28:29],v2 offset:2056\n"
M_"v[52:55],v[36:39],v[156:159],v[52:55]\n"
T_"v[30:31],v2 offset:2312\n"
M_"v[40:43],v[8:11],v[160:163],v[40:43]\n"
T_"v[32:33],v2 offset:3072\n"
M_"v[44:47],v[8:11],v[164:167],v[44:47]\n"
W_"lgkmcnt(6)\n"
T_"v[34:35],v2 offset:3328\n"
M_"v[48:51],v[12:15],v[160:163],v[48:51]\n"
T_"v[36:37],v2 offset:3080\n"
M_"v[52:55],v[12:15],v[164:167],v[52:55]\n"
T_"v[38:39],v2 offset:3336\n"
M_"v[40:43],v[16:19],v[168:171],v[40:43]\n"
M_"v[44:47],v[16:19],v[172:175],v[44:47]\n"
W_"lgkmcnt(5)\n"
M_"v[48:51],v[20:23],v[168:171],v[48:51]\n"
M_"v[52:55],v[20:23],v[172:175],v[52:55]\n"
M_"v[40:43],v[24:27],v[176:179],v[40:43]\n"
M_"v[44:47],v[24:27],v[180:183],v[44:47]\n"
W_"lgkmcnt(1)\n"
M_"v[48:51],v[28:31],v[176:179],v[48:51]\n"
M_"v[52:55],v[28:31],v[180:183],v[52:55]\n"
M_"v[40:43],v[32:35],v[184:187],v[40:43]\n"
M_"v[44:47],v[32:35],v[188:191],v[44:47]\n"
W_"lgkmcnt(0)\n"
M_"v[48:51],v[36:39],v[184:187],v[48:51]\n"
M_"v[52:55],v[36:39],v[188:191],v[52:55]\n"
"s_nop 3\n"
C_"v56,v40,v41\n"
C_"v57,v42,v43\n"
"s_nop 0\n"
C_"v58,v48,v49\n"
C_"v59,v50,v51\n"
"s_add_u32 s87, s90, 106496\n"
"buffer_store_dwordx4 v[56:59], v7, s[60:63], s87 offen sc1\n"
C_"v60,v44,v45\n"
C_"v61,v46,v47\n"
C_"v62,v52,v53\n"
C_"v63,v54,v55\n"
"s_add_u32 s87, s91, 335872\n"
"buffer_store_dwordx4 v[60:63], v7, s[60:63], s87 offen sc1\n"
"v_mbcnt_lo_u32_b32 v5, -1, 0\n"
"v_mbcnt_hi_u32_b32 v5, -1, v5\n"
"s_mov_b32 s4, 0\n"
"v_lshl_add_u32 v5, v5, 4, s4\n"
"s_bitcmp1_b32 s90, 30\n"
"s_cbranch_scc0 OWS0_L\n"
"ds_write_b128 v5, v[80:83] offset:0\n"
"ds_write_b128 v5, v[88:91] offset:1024\n"
"ds_write_b128 v5, v[96:99] offset:2048\n"
"ds_write_b128 v5, v[104:107] offset:3072\n"
"ds_write_b128 v5, v[112:115] offset:4096\n"
"ds_write_b128 v5, v[120:123] offset:5120\n"
"ds_write_b128 v5, v[56:59] offset:6144\n"
"OWS0_L:\n"
"s_bitcmp1_b32 s91, 30\n"
"s_cbranch_scc0 OWS1_L\n"
"ds_write_b128 v5, v[84:87] offset:0\n"
"ds_write_b128 v5, v[92:95] offset:1024\n"
"ds_write_b128 v5, v[100:103] offset:2048\n"
"ds_write_b128 v5, v[108:111] offset:3072\n"
"ds_write_b128 v5, v[116:119] offset:4096\n"
"ds_write_b128 v5, v[124:127] offset:5120\n"
"ds_write_b128 v5, v[60:63] offset:6144\n"
"OWS1_L:\n"
W_"lgkmcnt(0)\n"
"s_barrier\n"
"s_endpgm\n"
"KEND:\n"
  :
  : [ka] "s"(__builtin_amdgcn_kernarg_segment_ptr()), [bid] "s"(blockIdx.x), [tid] "v"(threadIdx.x), [l] "s"((unsigned)(size_t)lds)
  : "v1","v2","v3","v4","v5","v6","v7","v8","v9","v10","v11","v12","v13","v14","v15","v16","v17","v18","v19","v20","v21","v22","v23","v24","v25","v26","v27","v28","v29","v30","v31","v32","v33","v34","v35","v36","v37","v38","v39","v40","v41","v42","v43","v44","v45","v46","v47","v48","v49","v50","v51","v52","v53","v54","v55","v56","v57","v58","v59","v60","v61","v62","v63","v64","v65","v66","v67","v68","v69","v70","v71","v72","v73","v74","v75","v76","v77","v78","v79","v80","v81","v82","v83","v84","v85","v86","v87","v88","v89","v90","v91","v92","v93","v94","v95","v96","v97","v98","v99","v100","v101","v102","v103","v104","v105","v106","v107","v108","v109","v110","v111","v112","v113","v114","v115","v116","v117","v118","v119","v120","v121","v122","v123","v124","v125","v126","v127","v128","v129","v130","v131","v132","v133","v134","v135","v136","v137","v138","v139","v140","v141","v142","v143","v144","v145","v146","v147","v148","v149","v150","v151","v152","v153","v154","v155","v156","v157","v158","v159","v160","v161","v162","v163","v164","v165","v166","v167","v168","v169","v170","v171","v172","v173","v174","v175","v176","v177","v178","v179","v180","v181","v182","v183","v184","v185","v186","v187","v188","v189","v190","v191","v192","v193","v194","v195","v196","v197","v198","v199","v200","v201","v202","v203","v204","v205","v206","v207","v208","v209","v210","v211","v212","v213","v214","v215","v216","v217","v218","v219","v220","v221","v222","v223","v224","v225","v226","v227","v228","v229","v230","v231","v232","v233","v234","v235","v236","v237","v238","v239","v240","v241","v242","v243","v244","v245","v246","v247","v248","v249","v250","v251","v252","v253","v254","v255","s4","s5","s6","s7","s8","s9","s10","s11","s12","s13","s14","s15","s16","s17","s18","s19","s20","s21","s22","s23","s24","s25","s26","s27","s28","s29","s30","s31","s33","s34","s35","s36","s37","s38","s39","s40","s41","s42","s43","s44","s45","s46","s47","s48","s49","s50","s51","s52","s53","s54","s55","s56","s57","s58","s59","s60","s61","s62","s63","s64","s65","s66","s67","s68","s69","s70","s71","s72","s73","s74","s75","s76","s77","s78","s79","s80","s81","s82","s83","s84","s85","s86","s87","s88","s89","s90","s91","s92","s93","s94","s95","s96","s97","s98","s99","vcc","scc","memory");
}

extern "C" void kernel_launch(void* const* d_in, const int* in_sizes, int n_in, void* d_out, int out_size, void* d_ws,
                              size_t ws_size, hipStream_t stream) {
  fused_kernel<<<256, 512, 0, stream>>>(d_in[0], d_in[1], d_in[2], d_in[3], d_in[4], d_in[6], d_in[7], d_in[8], d_in[9], d_out, d_ws);
}
```
